```python
import torch

def _softmax(x, axis=-1):
    return torch.softmax(x, dim=axis)
def _gelu(x):
    return torch.nn.functional.gelu(x, approximate="tanh")
def _silu(x):
    return torch.nn.functional.silu(x)
def _sigmoid(x):
    return torch.sigmoid(x)
def _layernorm(x, axis=-1, eps=1e-5):
    m = x.mean(dim=axis, keepdim=True)
    v = x.var(dim=axis, keepdim=True, unbiased=False)
    return (x - m) / torch.sqrt(v + eps)
def _scatter_add(tgt, idx, src, axis=0):
    return tgt.clone().index_add_(axis, idx, src)
def _segment_sum(src, seg, n):
    out = torch.zeros((n,) + src.shape[1:], dtype=src.dtype)
    return out.index_add_(0, seg, src)
def _pad(x, pw):
    flat = tuple(v for pair in reversed(pw) for v in pair)
    return torch.nn.functional.pad(x, flat)

kernel_name = 'recurrent_scan__gqa_block_cached__glu_gated_geglu__prompt'

def setup_inputs():

    _w = (lambda a: (lambda t: t.to(torch.bfloat16) if t.is_floating_point() else t)(torch.as_tensor(a)))
    def _A_setup():
        B, Sq, D = 1, 2048, 448
        Hq, Hkv, Sk, Dh = 16, 8, 2048, 256
        assert Hq % Hkv == 0, (Hq, Hkv)
        return {
            "x": _w((lambda *s: torch.randn(*s, dtype=torch.bfloat16) * 0.05)(B, Sq, D)),
            "k_cache": _w((lambda *s: torch.randn(*s, dtype=torch.bfloat16) * 3.2)(B, Hkv, Sk, Dh)),
            "v_cache": _w((lambda *s: torch.randn(*s, dtype=torch.bfloat16) * 3.2)(B, Hkv, Sk, Dh)),
            "wq": _w((lambda *s: torch.randn(*s, dtype=torch.bfloat16) * 3.2)(D, Hq * Dh) * (D ** -0.5)),
            "wo": _w((lambda *s: torch.randn(*s, dtype=torch.bfloat16) * 3.2)(Hq * Dh, D) * ((Hq * Dh) ** -0.5)),
        }
    _A_in = _A_setup()

    def _B_setup():
        B, S, Dm, Df = 1, 2048, 448, 1792
        return {
            "x": _w((lambda *s: torch.randn(*s, dtype=torch.bfloat16) * 0.05)(B, S, Dm)),
            "w_gate": _w((lambda *s: torch.randn(*s, dtype=torch.bfloat16) * 0.4)(Dm, Df) * (Dm ** -0.5)),
            "w_up": _w((lambda *s: torch.randn(*s, dtype=torch.bfloat16) * 0.4)(Dm, Df) * (Dm ** -0.5)),
            "w_down": _w((lambda *s: torch.randn(*s, dtype=torch.bfloat16) * 0.4)(Df, Dm) * (Df ** -0.5)),
        }
    _B_in = _B_setup()

    return {
        "A_x": _A_in["x"],
        "A_k_cache": _A_in["k_cache"],
        "A_v_cache": _A_in["v_cache"],
        "A_wq": _A_in["wq"],
        "A_wo": _A_in["wo"],
        "B_x": _B_in["x"],
        "B_w_gate": _B_in["w_gate"],
        "B_w_up": _B_in["w_up"],
        "B_w_down": _B_in["w_down"],
    }

def reference(A_x, A_k_cache, A_v_cache, A_wq, A_wo, B_x, B_w_gate, B_w_up, B_w_down):
    def _A_ref(x, k_cache, v_cache, wq, wo):
        B, Sq, D = x.shape
        Hq, Dh = 16, 256
        Hkv = k_cache.shape[1]
        rep = Hq // Hkv
        q = (lambda x, *d: x.permute(*d))((x @ wq).reshape(B, Sq, Hq, Dh), 0, 2, 1, 3)
        k = (lambda x, n, axis: torch.repeat_interleave(x, n, dim=axis))(k_cache, rep, axis=1)
        v = (lambda x, n, axis: torch.repeat_interleave(x, n, dim=axis))(v_cache, rep, axis=1)
        scores = torch.einsum("bhqd,bhkd->bhqk", q, k) * (Dh ** -0.5)
        p = _softmax(scores, axis=-1)
        o = torch.einsum("bhqk,bhkd->bhqd", p, v)
        return (lambda x, *d: x.permute(*d))(o, 0, 2, 1, 3).reshape(B, Sq, Hq * Dh) @ wo

    def _B_ref(x, w_gate, w_up, w_down):
        g = torch.einsum("bsd,df->bsf", x, w_gate)
        u = torch.einsum("bsd,df->bsf", x, w_up)
        h = _gelu(g) * u
        return torch.einsum("bsf,fd->bsd", h, w_down)

    x = A_x
    xn = _layernorm(x, axis=-1)
    s = _A_ref(xn, A_k_cache, A_v_cache, A_wq, A_wo)
    g = _silu(_B_ref(xn, B_w_gate, B_w_up, B_w_down))
    return x + s * g
```

```cpp
#include <hip/hip_runtime.h>
#include <hip/hip_bf16.h>
#include <stdint.h>

typedef unsigned short u16;
typedef unsigned u32x4 __attribute__((ext_vector_type(4)));
typedef unsigned u32x2 __attribute__((ext_vector_type(2)));

__global__ __launch_bounds__(256, 1) void fused_main(const void* x, const void* kc, const void* vc, const void* wq, const void* wo,
                                                     const void* wg, const void* wu, void* hbuf, void* sbuf, void* dbg, const void* wd, void* sy, void* out) {
  __shared__ __attribute__((aligned(16))) char lds[163840];
  unsigned tid = threadIdx.x;
  asm volatile(
"s_load_dwordx16 s[8:23], %2, 0x0\ns_load_dwordx4 s[24:27], %2, 0x40\ns_load_dwordx2 s[28:29], %2, 0x50\ns_load_dwordx2 s[4:5], %2, 0x58\n"
"s_getpc_b64 s[96:97]\nv_lshlrev_b32 v253, 5, %0\nglobal_load_dword a127, v253, s[96:97]\nv_mov_b32 v255, %0\n"
"v_lshrrev_b32 v245, 6, v255\nv_and_b32 v254, 63, v255\ns_mov_b32 s70, 0x1c0000\ns_mov_b32 s71, 0x20000\n"
"v_readfirstlane_b32 s72, v245\ns_and_b32 s74, %1, 7\ns_lshr_b32 s75, %1, 3\ns_mov_b32 s84, s75\n"
"v_lshrrev_b32 v200, 3, v254\nv_and_b32 v201, 7, v254\ns_lshl_b32 s85, s72, 5\ns_add_u32 s85, s85, s84\n"
"s_mul_i32 s76, s74, 7\ns_add_u32 s76, s76, 1\ns_lshr_b32 s76, s76, 1\ns_lshl_b32 s76, s76, 7\n"
"s_cmp_eq_u32 s72, 0\ns_mov_b32 s94, 0x188000\ns_lshl_b32 s86, s74, 1\ns_add_u32 s86, s86, s72\n"
"s_sub_u32 s86, s86, 2\ns_lshl_b32 s86, s86, 9\ns_cmp_lt_u32 s72, 2\ns_cselect_b32 s86, s76, s86\n"
"s_mov_b32 s77, s86\ns_movk_i32 s33, 0x2000\ns_cselect_b32 s33, 3584, s33\ns_mov_b32 s95, 0x20000\n"
"v_mul_u32_u24 v199, s33, v254\ns_lshl_b32 s33, s33, 6\ns_and_b32 s91, s84, 7\ns_mul_i32 s91, s91, s33\n"
"s_add_u32 s91, s91, s86\ns_lshr_b32 s34, s84, 3\ns_lshl_b32 s86, s34, 7\ns_add_u32 s91, s91, s86\n"
"v_mul_u32_u24 v246, 896, v200\nv_lshl_add_u32 v246, v201, 4, v246\ns_lshl_b32 s90, s75, 2\ns_add_u32 s90, s90, s72\n"
"s_mul_i32 s90, s90, 14336\ns_add_u32 s87, s90, 7168\ns_cmp_eq_u32 s34, 0\ns_cselect_b32 s35, s91, 0x7f000000\ns_waitcnt lgkmcnt(0)\n"
"s_mov_b64 s[68:69], s[8:9]\ns_and_b32 s69, s69, 0xffff\ns_cmp_eq_u32 s72, 0\ns_cselect_b64 s[92:93], s[18:19], s[20:21]\n"
"s_cmp_lt_u32 s72, 2\ns_cselect_b64 s[92:93], s[92:93], s[14:15]\ns_cselect_b32 s94, s94, 0x380000\ns_and_b32 s93, s93, 0xffff\n"
"buffer_load_dword a127, v199, s[92:95], s35 offen offset:0\ns_and_b32 s5, s5, 0xffff ; s[8:9]\ns_mov_b32 s6, 0x10000\ns_mov_b32 s7, 0x20000\n"
"s_lshl_b32 s30, %1, 8\nv_lshl_add_u32 v190, v254, 2, s30\nbuffer_load_dword v191, v190, s[4:7], 0 offen sc1\nbuffer_load_dwordx4 v[0:3], v246, s[68:71], s90 offen offset:0\n"
"buffer_load_dwordx4 v[4:7], v246, s[68:71], s90 offen offset:128\nbuffer_load_dwordx4 v[8:11], v246, s[68:71], s90 offen offset:256\nbuffer_load_dwordx4 v[12:15], v246, s[68:71], s90 offen offset:384\nbuffer_load_dwordx4 v[16:19], v246, s[68:71], s90 offen offset:512\n"
"buffer_load_dwordx4 v[20:23], v246, s[68:71], s90 offen offset:640\nbuffer_load_dwordx4 v[24:27], v246, s[68:71], s90 offen offset:768\nbuffer_load_dwordx4 v[28:31], v246, s[68:71], s87 offen offset:0\nbuffer_load_dwordx4 v[32:35], v246, s[68:71], s87 offen offset:128\n"
"buffer_load_dwordx4 v[36:39], v246, s[68:71], s87 offen offset:256\nbuffer_load_dwordx4 v[40:43], v246, s[68:71], s87 offen offset:384\nbuffer_load_dwordx4 v[44:47], v246, s[68:71], s87 offen offset:512\nbuffer_load_dwordx4 v[48:51], v246, s[68:71], s87 offen offset:640\n"
"buffer_load_dwordx4 v[52:55], v246, s[68:71], s87 offen offset:768\ns_mov_b64 s[36:37], s[18:19]\ns_and_b32 s37, s37, 0xffff\ns_mov_b32 s38, 0x188000\n"
"s_mov_b32 s39, 0x20000\ns_mov_b64 s[40:41], s[20:21]\ns_and_b32 s41, s41, 0xffff\ns_mov_b32 s42, 0x188000\n"
"s_mov_b32 s43, 0x20000\ns_mov_b64 s[60:61], s[22:23]\ns_and_b32 s61, s61, 0xffff\ns_mov_b32 s62, 0x700000\n"
"s_mov_b32 s63, 0x20000\ns_lshl_b32 s73, s72, 10\ns_and_b32 s89, s72, 1\ns_mov_b32 s77, s76\n"
"s_lshl_b32 s90, s72, 3\nv_add_u32 v202, s90, v200\nv_and_b32 v204, 6, v200\nv_xor_b32 v204, v204, v201\n"
"v_and_b32 v203, 2, v200\ns_lshl_b32 s90, s89, 2\nv_or_b32 v203, s90, v203\nv_xor_b32 v203, v203, v201\n"
"v_lshrrev_b32 v205, 1, v200\nv_or_b32 v205, s90, v205\nv_xor_b32 v205, v205, v201\nv_mul_u32_u24 v250, 3584, v202\n"
"v_lshl_add_u32 v250, v203, 4, v250\nv_mul_u32_u24 v251, 8192, v202\nv_lshl_add_u32 v251, v203, 4, v251\nv_mul_u32_u24 v231, 512, v202\n"
"v_lshl_add_u32 v231, v205, 4, v231\nv_mul_u32_u24 v232, 512, v202\nv_lshl_add_u32 v232, v204, 4, v232\nv_mul_u32_u24 v235, 896, v202\n"
"v_lshl_add_u32 v235, v204, 4, v235\ns_mov_b64 s[44:45], s[14:15]\ns_and_b32 s45, s45, 0xffff\ns_mov_b32 s46, 0x380000\n"
"s_mov_b32 s47, 0x20000\ns_mov_b64 s[48:49], s[10:11]\ns_and_b32 s49, s49, 0xffff\ns_mov_b32 s50, 0x800000\n"
"s_mov_b32 s51, 0x20000\ns_mov_b64 s[52:53], s[12:13]\ns_and_b32 s53, s53, 0xffff\ns_mov_b32 s54, 0x800000\n"
"s_mov_b32 s55, 0x20000\ns_mov_b64 s[56:57], s[16:17]\ns_and_b32 s57, s57, 0xffff\ns_mov_b32 s58, 0x380000\n"
"s_mov_b32 s59, 0x20000\ns_mov_b64 s[64:65], s[24:25]\ns_and_b32 s65, s65, 0xffff\ns_mov_b32 s66, 0xe00000\n"
"s_mov_b32 s67, 0x20000\ns_load_dwordx2 s[10:11], %2, 0x60\ns_lshl_b32 s78, s74, 10\ns_lshl_b32 s88, s74, 20\n"
"s_mov_b32 s79, s88\ns_mov_b32 s80, s88\ns_mul_i32 s81, s74, 458752\ns_mul_i32 s82, s74, 3\n"
"s_add_u32 s82, s82, 1\ns_lshr_b32 s82, s82, 1\ns_mul_i32 s86, s75, 14\ns_add_u32 s82, s82, s86\n"
"s_lshl_b32 s82, s82, 2\ns_add_u32 s82, s82, s72\ns_lshl_b32 s82, s82, 12\ns_mul_i32 s83, s75, 56\n"
"s_add_u32 s83, s83, s74\ns_lshl_b32 s83, s83, 2\ns_add_u32 s83, s83, s72\ns_lshl_b32 s83, s83, 11\n"
"s_mov_b64 s[96:97], -1\nv_lshlrev_b32 v224, 4, v254\nv_and_b32 v206, 15, v254\nv_lshrrev_b32 v207, 4, v254\n"
"v_bfe_u32 v208, v254, 2, 2\nv_bfe_u32 v209, v254, 3, 2\nv_and_b32 v245, 3, v254\nv_lshlrev_b32 v245, 3, v245\n"
"v_lshl_add_u32 v245, v208, 7, v245\nv_xor_b32 v247, s72, v209\nv_lshl_add_u32 v247, v247, 5, v245\nv_lshl_add_u32 v248, v207, 10, v247\n"
"v_add_u32 v249, 0x10000, v248\nv_lshl_add_u32 v210, v207, 9, v245\ns_lshl_b32 s90, s89, 14\nv_xor_b32 v247, 0, v209\n"
"v_lshl_add_u32 v227, v247, 5, v210\nv_add_u32 v227, s90, v227\nv_xor_b32 v247, 1, v209\nv_lshl_add_u32 v228, v247, 5, v210\n"
"v_add_u32 v228, s90, v228\nv_xor_b32 v247, 2, v209\nv_lshl_add_u32 v229, v247, 5, v210\nv_add_u32 v229, s90, v229\n"
"v_xor_b32 v247, 3, v209\nv_lshl_add_u32 v230, v247, 5, v210\nv_add_u32 v230, s90, v230\nv_lshrrev_b32 v247, 1, v206\n"
"v_xor_b32 v247, v247, v207\nv_lshlrev_b32 v247, 4, v247\nv_lshl_add_u32 v247, v206, 7, v247\ns_lshl_b32 s90, s89, 12\n"
"v_add_u32 v225, s90, v247\nv_xor_b32 v226, 64, v225\nv_add_u32 v212, 0x1a000, v247\nv_xor_b32 v213, 64, v212\n"
"s_lshl_b32 s90, s72, 12\ns_add_u32 s90, s90, 0x20000\nv_add_u32 v233, s90, v224\nv_xor_b32 v234, 0x1000, v233\n"
"s_lshr_b32 s86, s72, 1\ns_lshl_b32 s90, s86, 12\ns_lshl_b32 s87, s89, 9\ns_add_u32 s90, s90, s87\n"
"s_add_u32 s90, s90, 0x20000\nv_lshrrev_b32 v247, 1, v207\nv_lshlrev_b32 v247, 8, v247\nv_lshl_add_u32 v247, v206, 4, v247\n"
"v_and_b32 v245, 1, v207\nv_lshl_add_u32 v247, v245, 10, v247\nv_add_u32 v252, s90, v247\ns_lshl_b32 s90, s86, 13\n"
"s_add_u32 s90, s90, 0x20000\nv_add_u32 v253, s90, v224\ns_lshl_b32 s90, s86, 11\ns_add_u32 s90, s90, 0x20000\n"
"v_lshlrev_b32 v244, 4, v206\nv_add_u32 v244, s90, v244\nv_mov_b32 v240, 0xff800000\nv_mov_b32 v241, 0xff800000\n"
"v_mov_b32 v242, 0xff800000\nv_mov_b32 v243, 0xff800000\nv_lshrrev_b32 v247, 1, v200\nv_xor_b32 v247, v247, v201\n"
"v_lshlrev_b32 v247, 4, v247\nv_lshl_add_u32 v247, v200, 7, v247\ns_lshl_b32 s90, s72, 11\ns_add_u32 s90, s90, 0x1a000\n"
"v_add_u32 v214, s90, v247\nv_xor_b32 v215, 64, v214\nv_add_u32 v215, 1024, v215\ns_lshl_b32 s90, s72, 6\n"
"s_add_u32 s90, s90, 0x7000\nv_lshlrev_b32 v216, 2, v200\nv_add_u32 v216, s90, v216\nv_lshlrev_b32 v217, 2, v206\n"
"v_mov_b32 v246, v199\nv_lshlrev_b32 v247, 7, v254\ns_getpc_b64 s[96:97]\nL_pc%=:\ns_lshr_b32 s86, %1, 3\n"
"s_lshl_b32 s86, s86, 13\nv_add_u32 v197, s86, v247\ns_mov_b32 s86, L_end%=-L_pc%=-4\nv_min_u32 v197, s86, v197\n"
"s_min_u32 s34, s34, 1\ns_waitcnt vmcnt(13)\nv_lshlrev_b32 v64, 16, v0\nv_and_b32 v65, 0xffff0000, v0\n"
"v_lshlrev_b32 v66, 16, v1\nv_and_b32 v67, 0xffff0000, v1\nv_lshlrev_b32 v68, 16, v2\nv_and_b32 v69, 0xffff0000, v2\n"
"v_pk_add_f32 v[220:221], v[64:65], v[68:69]\nv_lshlrev_b32 v70, 16, v3\nv_and_b32 v71, 0xffff0000, v3\nv_pk_add_f32 v[222:223], v[66:67], v[70:71]\n"
"s_waitcnt vmcnt(12)\nv_lshlrev_b32 v72, 16, v4\nv_and_b32 v73, 0xffff0000, v4\nv_pk_add_f32 v[220:221], v[220:221], v[72:73]\n"
"v_lshlrev_b32 v74, 16, v5\nv_and_b32 v75, 0xffff0000, v5\nv_pk_add_f32 v[222:223], v[222:223], v[74:75]\nv_lshlrev_b32 v76, 16, v6\n"
"v_and_b32 v77, 0xffff0000, v6\nv_pk_add_f32 v[220:221], v[220:221], v[76:77]\nv_lshlrev_b32 v78, 16, v7\nv_and_b32 v79, 0xffff0000, v7\n"
"v_pk_add_f32 v[222:223], v[222:223], v[78:79]\ns_waitcnt vmcnt(11)\nv_lshlrev_b32 v80, 16, v8\nv_and_b32 v81, 0xffff0000, v8\n"
"v_pk_add_f32 v[220:221], v[220:221], v[80:81]\ns_add_u32 m0, s73, 0x0\ns_add_u32 s76, s76, 0x0\nbuffer_load_dwordx4 v250, s[36:39], s76 offen lds\n"
"v_lshlrev_b32 v82, 16, v9\nv_and_b32 v83, 0xffff0000, v9\nv_pk_add_f32 v[222:223], v[222:223], v[82:83]\nv_lshlrev_b32 v84, 16, v10\n"
"v_and_b32 v85, 0xffff0000, v10\nv_pk_add_f32 v[220:221], v[220:221], v[84:85]\nv_lshlrev_b32 v86, 16, v11\nv_and_b32 v87, 0xffff0000, v11\n"
"v_pk_add_f32 v[222:223], v[222:223], v[86:87]\ns_waitcnt vmcnt(11)\nv_lshlrev_b32 v88, 16, v12\nv_and_b32 v89, 0xffff0000, v12\n"
"v_pk_add_f32 v[220:221], v[220:221], v[88:89]\nv_lshlrev_b32 v90, 16, v13\nv_and_b32 v91, 0xffff0000, v13\nv_pk_add_f32 v[222:223], v[222:223], v[90:91]\n"
"v_lshlrev_b32 v92, 16, v14\nv_and_b32 v93, 0xffff0000, v14\nv_pk_add_f32 v[220:221], v[220:221], v[92:93]\nv_lshlrev_b32 v94, 16, v15\n"
"v_and_b32 v95, 0xffff0000, v15\nv_pk_add_f32 v[222:223], v[222:223], v[94:95]\ns_waitcnt vmcnt(10)\nv_lshlrev_b32 v96, 16, v16\n"
"v_and_b32 v97, 0xffff0000, v16\nv_pk_add_f32 v[220:221], v[220:221], v[96:97]\nv_lshlrev_b32 v98, 16, v17\ns_add_u32 m0, s73, 0x1000\n"
"s_add_u32 s76, s76, 0x1c000\nbuffer_load_dwordx4 v250, s[36:39], s76 offen lds\nv_and_b32 v99, 0xffff0000, v17\nv_pk_add_f32 v[222:223], v[222:223], v[98:99]\n"
"v_lshlrev_b32 v100, 16, v18\nv_and_b32 v101, 0xffff0000, v18\nv_pk_add_f32 v[220:221], v[220:221], v[100:101]\nv_lshlrev_b32 v102, 16, v19\n"
"v_and_b32 v103, 0xffff0000, v19\nv_pk_add_f32 v[222:223], v[222:223], v[102:103]\ns_waitcnt vmcnt(10)\nv_lshlrev_b32 v104, 16, v20\n"
"v_and_b32 v105, 0xffff0000, v20\nv_pk_add_f32 v[220:221], v[220:221], v[104:105]\nv_lshlrev_b32 v106, 16, v21\nv_and_b32 v107, 0xffff0000, v21\n"
"v_pk_add_f32 v[222:223], v[222:223], v[106:107]\nv_lshlrev_b32 v108, 16, v22\nv_and_b32 v109, 0xffff0000, v22\nv_pk_add_f32 v[220:221], v[220:221], v[108:109]\n"
"v_lshlrev_b32 v110, 16, v23\nv_and_b32 v111, 0xffff0000, v23\nv_pk_add_f32 v[222:223], v[222:223], v[110:111]\ns_waitcnt vmcnt(9)\n"
"v_lshlrev_b32 v112, 16, v24\nv_and_b32 v113, 0xffff0000, v24\nv_pk_add_f32 v[220:221], v[220:221], v[112:113]\nv_lshlrev_b32 v114, 16, v25\n"
"v_and_b32 v115, 0xffff0000, v25\ns_add_u32 m0, s73, 0x2000\ns_add_u32 s76, s76, 0x1c000\nbuffer_load_dwordx4 v250, s[36:39], s76 offen lds\n"
"v_pk_add_f32 v[222:223], v[222:223], v[114:115]\nv_lshlrev_b32 v116, 16, v26\nv_and_b32 v117, 0xffff0000, v26\nv_pk_add_f32 v[220:221], v[220:221], v[116:117]\n"
"v_lshlrev_b32 v118, 16, v27\nv_and_b32 v119, 0xffff0000, v27\nv_pk_add_f32 v[222:223], v[222:223], v[118:119]\nv_pk_add_f32 v[220:221], v[220:221], v[222:223]\n"
"v_add_f32 v192, v220, v221\ns_nop 1\nv_add_f32_dpp v192, v192, v192 quad_perm:[1,0,3,2] row_mask:0xf bank_mask:0xf\ns_nop 1\n"
"v_add_f32_dpp v192, v192, v192 quad_perm:[2,3,0,1] row_mask:0xf bank_mask:0xf\ns_nop 1\nv_add_f32_dpp v192, v192, v192 row_half_mirror row_mask:0xf bank_mask:0xf\nv_mul_f32 v192, 0x3b124925, v192\n"
"v_cvt_pk_bf16_f32 v194, v192, v192\nv_lshlrev_b32 v194, 16, v194\nv_pk_add_f32 v[64:65], v[64:65], v[194:195] op_sel_hi:[1,0] neg_lo:[0,1] neg_hi:[0,1]\nv_cvt_pk_bf16_f32 v0, v64, v65\n"
"v_pk_add_f32 v[66:67], v[66:67], v[194:195] op_sel_hi:[1,0] neg_lo:[0,1] neg_hi:[0,1]\nv_cvt_pk_bf16_f32 v1, v66, v67\nv_pk_add_f32 v[68:69], v[68:69], v[194:195] op_sel_hi:[1,0] neg_lo:[0,1] neg_hi:[0,1]\nv_cvt_pk_bf16_f32 v2, v68, v69\n"
"v_pk_add_f32 v[70:71], v[70:71], v[194:195] op_sel_hi:[1,0] neg_lo:[0,1] neg_hi:[0,1]\nv_cvt_pk_bf16_f32 v3, v70, v71\nds_write_b128 v214, v[0:3] offset:0\nv_pk_add_f32 v[72:73], v[72:73], v[194:195] op_sel_hi:[1,0] neg_lo:[0,1] neg_hi:[0,1]\n"
"s_add_u32 m0, s73, 0x3000\ns_add_u32 s76, s76, 0x1c000\nbuffer_load_dwordx4 v250, s[36:39], s76 offen lds\nv_cvt_pk_bf16_f32 v4, v72, v73\n"
"v_pk_add_f32 v[74:75], v[74:75], v[194:195] op_sel_hi:[1,0] neg_lo:[0,1] neg_hi:[0,1]\nv_cvt_pk_bf16_f32 v5, v74, v75\nv_pk_add_f32 v[76:77], v[76:77], v[194:195] op_sel_hi:[1,0] neg_lo:[0,1] neg_hi:[0,1]\nv_cvt_pk_bf16_f32 v6, v76, v77\n"
"v_pk_add_f32 v[78:79], v[78:79], v[194:195] op_sel_hi:[1,0] neg_lo:[0,1] neg_hi:[0,1]\nv_cvt_pk_bf16_f32 v7, v78, v79\nds_write_b128 v214, v[4:7] offset:8192\nv_pk_add_f32 v[80:81], v[80:81], v[194:195] op_sel_hi:[1,0] neg_lo:[0,1] neg_hi:[0,1]\n"
"v_cvt_pk_bf16_f32 v8, v80, v81\nv_pk_add_f32 v[82:83], v[82:83], v[194:195] op_sel_hi:[1,0] neg_lo:[0,1] neg_hi:[0,1]\nv_cvt_pk_bf16_f32 v9, v82, v83\nv_pk_add_f32 v[84:85], v[84:85], v[194:195] op_sel_hi:[1,0] neg_lo:[0,1] neg_hi:[0,1]\n"
"v_cvt_pk_bf16_f32 v10, v84, v85\nv_pk_add_f32 v[86:87], v[86:87], v[194:195] op_sel_hi:[1,0] neg_lo:[0,1] neg_hi:[0,1]\nv_cvt_pk_bf16_f32 v11, v86, v87\nds_write_b128 v214, v[8:11] offset:16384\n"
"v_pk_add_f32 v[88:89], v[88:89], v[194:195] op_sel_hi:[1,0] neg_lo:[0,1] neg_hi:[0,1]\nv_cvt_pk_bf16_f32 v12, v88, v89\nv_pk_add_f32 v[90:91], v[90:91], v[194:195] op_sel_hi:[1,0] neg_lo:[0,1] neg_hi:[0,1]\nv_cvt_pk_bf16_f32 v13, v90, v91\n"
"v_pk_add_f32 v[92:93], v[92:93], v[194:195] op_sel_hi:[1,0] neg_lo:[0,1] neg_hi:[0,1]\nv_cvt_pk_bf16_f32 v14, v92, v93\nv_pk_add_f32 v[94:95], v[94:95], v[194:195] op_sel_hi:[1,0] neg_lo:[0,1] neg_hi:[0,1]\nv_cvt_pk_bf16_f32 v15, v94, v95\n"
"s_add_u32 m0, s73, 0x4000\ns_add_u32 s76, s76, 0x1c000\nbuffer_load_dwordx4 v250, s[36:39], s76 offen lds\nds_write_b128 v214, v[12:15] offset:24576\n"
"v_pk_add_f32 v[96:97], v[96:97], v[194:195] op_sel_hi:[1,0] neg_lo:[0,1] neg_hi:[0,1]\nv_cvt_pk_bf16_f32 v16, v96, v97\nv_pk_add_f32 v[98:99], v[98:99], v[194:195] op_sel_hi:[1,0] neg_lo:[0,1] neg_hi:[0,1]\nv_cvt_pk_bf16_f32 v17, v98, v99\n"
"v_pk_add_f32 v[100:101], v[100:101], v[194:195] op_sel_hi:[1,0] neg_lo:[0,1] neg_hi:[0,1]\nv_cvt_pk_bf16_f32 v18, v100, v101\nv_pk_add_f32 v[102:103], v[102:103], v[194:195] op_sel_hi:[1,0] neg_lo:[0,1] neg_hi:[0,1]\nv_cvt_pk_bf16_f32 v19, v102, v103\n"
"ds_write_b128 v214, v[16:19] offset:32768\nv_pk_add_f32 v[104:105], v[104:105], v[194:195] op_sel_hi:[1,0] neg_lo:[0,1] neg_hi:[0,1]\nv_cvt_pk_bf16_f32 v20, v104, v105\nv_pk_add_f32 v[106:107], v[106:107], v[194:195] op_sel_hi:[1,0] neg_lo:[0,1] neg_hi:[0,1]\n"
"v_cvt_pk_bf16_f32 v21, v106, v107\nv_pk_add_f32 v[108:109], v[108:109], v[194:195] op_sel_hi:[1,0] neg_lo:[0,1] neg_hi:[0,1]\nv_cvt_pk_bf16_f32 v22, v108, v109\nv_pk_add_f32 v[110:111], v[110:111], v[194:195] op_sel_hi:[1,0] neg_lo:[0,1] neg_hi:[0,1]\n"
"v_cvt_pk_bf16_f32 v23, v110, v111\nds_write_b128 v214, v[20:23] offset:40960\nv_pk_add_f32 v[112:113], v[112:113], v[194:195] op_sel_hi:[1,0] neg_lo:[0,1] neg_hi:[0,1]\nv_cvt_pk_bf16_f32 v24, v112, v113\n"
"v_pk_add_f32 v[114:115], v[114:115], v[194:195] op_sel_hi:[1,0] neg_lo:[0,1] neg_hi:[0,1]\nv_cvt_pk_bf16_f32 v25, v114, v115\nv_pk_add_f32 v[116:117], v[116:117], v[194:195] op_sel_hi:[1,0] neg_lo:[0,1] neg_hi:[0,1]\nv_cvt_pk_bf16_f32 v26, v116, v117\n"
"s_add_u32 m0, s73, 0x5000\ns_add_u32 s76, s76, 0x1c000\nbuffer_load_dwordx4 v250, s[36:39], s76 offen lds\nv_pk_add_f32 v[118:119], v[118:119], v[194:195] op_sel_hi:[1,0] neg_lo:[0,1] neg_hi:[0,1]\n"
"v_cvt_pk_bf16_f32 v27, v118, v119\nds_write_b128 v214, v[24:27] offset:49152\nv_pk_mul_f32 v[220:221], v[64:65], v[64:65]\nv_pk_mul_f32 v[222:223], v[66:67], v[66:67]\n"
"v_pk_fma_f32 v[220:221], v[68:69], v[68:69], v[220:221]\nv_pk_fma_f32 v[222:223], v[70:71], v[70:71], v[222:223]\nv_pk_fma_f32 v[220:221], v[72:73], v[72:73], v[220:221]\nv_pk_fma_f32 v[222:223], v[74:75], v[74:75], v[222:223]\n"
"v_pk_fma_f32 v[220:221], v[76:77], v[76:77], v[220:221]\nv_pk_fma_f32 v[222:223], v[78:79], v[78:79], v[222:223]\nv_pk_fma_f32 v[220:221], v[80:81], v[80:81], v[220:221]\nv_pk_fma_f32 v[222:223], v[82:83], v[82:83], v[222:223]\n"
"v_pk_fma_f32 v[220:221], v[84:85], v[84:85], v[220:221]\nv_pk_fma_f32 v[222:223], v[86:87], v[86:87], v[222:223]\nv_pk_fma_f32 v[220:221], v[88:89], v[88:89], v[220:221]\nv_pk_fma_f32 v[222:223], v[90:91], v[90:91], v[222:223]\n"
"v_pk_fma_f32 v[220:221], v[92:93], v[92:93], v[220:221]\nv_pk_fma_f32 v[222:223], v[94:95], v[94:95], v[222:223]\nv_pk_fma_f32 v[220:221], v[96:97], v[96:97], v[220:221]\nv_pk_fma_f32 v[222:223], v[98:99], v[98:99], v[222:223]\n"
"v_pk_fma_f32 v[220:221], v[100:101], v[100:101], v[220:221]\nv_pk_fma_f32 v[222:223], v[102:103], v[102:103], v[222:223]\nv_pk_fma_f32 v[220:221], v[104:105], v[104:105], v[220:221]\nv_pk_fma_f32 v[222:223], v[106:107], v[106:107], v[222:223]\n"
"s_add_u32 m0, s73, 0x6000\ns_add_u32 s76, s76, 0x1c000\nbuffer_load_dwordx4 v250, s[36:39], s76 offen lds\nv_pk_fma_f32 v[220:221], v[108:109], v[108:109], v[220:221]\n"
"v_pk_fma_f32 v[222:223], v[110:111], v[110:111], v[222:223]\nv_pk_fma_f32 v[220:221], v[112:113], v[112:113], v[220:221]\nv_pk_fma_f32 v[222:223], v[114:115], v[114:115], v[222:223]\nv_pk_fma_f32 v[220:221], v[116:117], v[116:117], v[220:221]\n"
"v_pk_fma_f32 v[222:223], v[118:119], v[118:119], v[222:223]\nv_pk_add_f32 v[220:221], v[220:221], v[222:223]\nv_add_f32 v196, v220, v221\ns_nop 1\n"
"v_add_f32_dpp v196, v196, v196 quad_perm:[1,0,3,2] row_mask:0xf bank_mask:0xf\ns_nop 1\nv_add_f32_dpp v196, v196, v196 quad_perm:[2,3,0,1] row_mask:0xf bank_mask:0xf\ns_nop 1\n"
"v_add_f32_dpp v196, v196, v196 row_half_mirror row_mask:0xf bank_mask:0xf\nv_sub_f32 v198, v192, v194\nv_mul_f32 v198, v198, v198\nv_mul_f32 v196, 0x3b124925, v196\n"
"v_sub_f32 v196, v196, v198\nv_max_f32 v196, 0, v196\nv_cvt_pk_bf16_f32 v196, v196, v196\nv_lshlrev_b32 v196, 16, v196\n"
"v_add_f32 v196, 0x3727c5ac, v196\nv_cvt_pk_bf16_f32 v196, v196, v196\nv_lshlrev_b32 v196, 16, v196\nv_sqrt_f32 v196, v196\n"
"s_nop 0\nv_cvt_pk_bf16_f32 v196, v196, v196\nv_lshlrev_b32 v196, 16, v196\nv_rcp_f32 v196, v196\n"
"s_nop 0\nds_write_b32 v216, v196 offset:0\nv_lshrrev_b32 v191, v254, v191\nv_and_b32 v191, 1, v191\n"
"v_cmp_eq_u32 vcc, 0, v191\ns_nop 1\nv_mov_b32 v191, vcc_lo\ns_lshl_b32 s31, s72, 2\n"
"s_add_u32 s31, s31, 0x7100\nv_mov_b32 v190, s31\nds_write_b32 v190, v191 offset:0\nv_lshlrev_b32 v190, 24, v254\n"
"s_lshl_b32 s31, s72, 28\ns_add_u32 s31, s31, s30\nbuffer_store_dword v191, v190, s[4:7], s31 offen offset:128 sc1\ns_nop 1\n"
"s_mov_b32 s31, 0\ns_waitcnt vmcnt(14)\nv_lshlrev_b32 v120, 16, v28\nv_and_b32 v121, 0xffff0000, v28\n"
"v_lshlrev_b32 v122, 16, v29\nv_and_b32 v123, 0xffff0000, v29\nv_lshlrev_b32 v124, 16, v30\nv_and_b32 v125, 0xffff0000, v30\n"
"v_pk_add_f32 v[220:221], v[120:121], v[124:125]\nv_lshlrev_b32 v126, 16, v31\nv_and_b32 v127, 0xffff0000, v31\nv_pk_add_f32 v[222:223], v[122:123], v[126:127]\n"
"s_waitcnt vmcnt(13)\nv_lshlrev_b32 v128, 16, v32\nv_and_b32 v129, 0xffff0000, v32\nv_pk_add_f32 v[220:221], v[220:221], v[128:129]\n"
"v_lshlrev_b32 v130, 16, v33\nv_and_b32 v131, 0xffff0000, v33\nv_pk_add_f32 v[222:223], v[222:223], v[130:131]\nv_lshlrev_b32 v132, 16, v34\n"
"v_and_b32 v133, 0xffff0000, v34\nv_pk_add_f32 v[220:221], v[220:221], v[132:133]\nv_lshlrev_b32 v134, 16, v35\nv_and_b32 v135, 0xffff0000, v35\n"
"v_pk_add_f32 v[222:223], v[222:223], v[134:135]\ns_waitcnt vmcnt(12)\nv_lshlrev_b32 v136, 16, v36\nv_and_b32 v137, 0xffff0000, v36\n"
"v_pk_add_f32 v[220:221], v[220:221], v[136:137]\nv_lshlrev_b32 v138, 16, v37\nv_and_b32 v139, 0xffff0000, v37\nv_pk_add_f32 v[222:223], v[222:223], v[138:139]\n"
"v_lshlrev_b32 v140, 16, v38\nv_and_b32 v141, 0xffff0000, v38\nv_pk_add_f32 v[220:221], v[220:221], v[140:141]\nv_lshlrev_b32 v142, 16, v39\n"
"v_and_b32 v143, 0xffff0000, v39\nv_pk_add_f32 v[222:223], v[222:223], v[142:143]\ns_waitcnt vmcnt(11)\nv_lshlrev_b32 v144, 16, v40\n"
"v_and_b32 v145, 0xffff0000, v40\nv_pk_add_f32 v[220:221], v[220:221], v[144:145]\nv_lshlrev_b32 v146, 16, v41\nv_and_b32 v147, 0xffff0000, v41\n"
"v_pk_add_f32 v[222:223], v[222:223], v[146:147]\nv_lshlrev_b32 v148, 16, v42\nv_and_b32 v149, 0xffff0000, v42\nv_pk_add_f32 v[220:221], v[220:221], v[148:149]\n"
"v_lshlrev_b32 v150, 16, v43\nv_and_b32 v151, 0xffff0000, v43\nv_pk_add_f32 v[222:223], v[222:223], v[150:151]\ns_waitcnt vmcnt(10)\n"
"v_lshlrev_b32 v152, 16, v44\nv_and_b32 v153, 0xffff0000, v44\nv_pk_add_f32 v[220:221], v[220:221], v[152:153]\nv_lshlrev_b32 v154, 16, v45\n"
"v_and_b32 v155, 0xffff0000, v45\nv_pk_add_f32 v[222:223], v[222:223], v[154:155]\nv_lshlrev_b32 v156, 16, v46\nv_and_b32 v157, 0xffff0000, v46\n"
"v_pk_add_f32 v[220:221], v[220:221], v[156:157]\nv_lshlrev_b32 v158, 16, v47\nv_and_b32 v159, 0xffff0000, v47\nv_pk_add_f32 v[222:223], v[222:223], v[158:159]\n"
"s_waitcnt vmcnt(9)\nv_lshlrev_b32 v160, 16, v48\nv_and_b32 v161, 0xffff0000, v48\nv_pk_add_f32 v[220:221], v[220:221], v[160:161]\n"
"v_lshlrev_b32 v162, 16, v49\nv_and_b32 v163, 0xffff0000, v49\nv_pk_add_f32 v[222:223], v[222:223], v[162:163]\nv_lshlrev_b32 v164, 16, v50\n"
"v_and_b32 v165, 0xffff0000, v50\nv_pk_add_f32 v[220:221], v[220:221], v[164:165]\nv_lshlrev_b32 v166, 16, v51\nv_and_b32 v167, 0xffff0000, v51\n"
"v_pk_add_f32 v[222:223], v[222:223], v[166:167]\ns_waitcnt vmcnt(8)\nv_lshlrev_b32 v168, 16, v52\nv_and_b32 v169, 0xffff0000, v52\n"
"v_pk_add_f32 v[220:221], v[220:221], v[168:169]\nv_lshlrev_b32 v170, 16, v53\nv_and_b32 v171, 0xffff0000, v53\nv_pk_add_f32 v[222:223], v[222:223], v[170:171]\n"
"v_lshlrev_b32 v172, 16, v54\nv_and_b32 v173, 0xffff0000, v54\nv_pk_add_f32 v[220:221], v[220:221], v[172:173]\nv_lshlrev_b32 v174, 16, v55\n"
"v_and_b32 v175, 0xffff0000, v55\nv_pk_add_f32 v[222:223], v[222:223], v[174:175]\nv_pk_add_f32 v[220:221], v[220:221], v[222:223]\nv_add_f32 v193, v220, v221\n"
"s_nop 1\nv_add_f32_dpp v193, v193, v193 quad_perm:[1,0,3,2] row_mask:0xf bank_mask:0xf\ns_nop 1\nv_add_f32_dpp v193, v193, v193 quad_perm:[2,3,0,1] row_mask:0xf bank_mask:0xf\n"
"s_nop 1\nv_add_f32_dpp v193, v193, v193 row_half_mirror row_mask:0xf bank_mask:0xf\nv_mul_f32 v193, 0x3b124925, v193\nv_cvt_pk_bf16_f32 v218, v193, v193\n"
"v_lshlrev_b32 v218, 16, v218\nv_pk_add_f32 v[120:121], v[120:121], v[218:219] op_sel_hi:[1,0] neg_lo:[0,1] neg_hi:[0,1]\nv_cvt_pk_bf16_f32 v28, v120, v121\nv_pk_add_f32 v[122:123], v[122:123], v[218:219] op_sel_hi:[1,0] neg_lo:[0,1] neg_hi:[0,1]\n"
"v_cvt_pk_bf16_f32 v29, v122, v123\nv_pk_add_f32 v[124:125], v[124:125], v[218:219] op_sel_hi:[1,0] neg_lo:[0,1] neg_hi:[0,1]\nv_cvt_pk_bf16_f32 v30, v124, v125\nv_pk_add_f32 v[126:127], v[126:127], v[218:219] op_sel_hi:[1,0] neg_lo:[0,1] neg_hi:[0,1]\n"
"v_cvt_pk_bf16_f32 v31, v126, v127\nds_write_b128 v215, v[28:31] offset:0\nv_pk_add_f32 v[128:129], v[128:129], v[218:219] op_sel_hi:[1,0] neg_lo:[0,1] neg_hi:[0,1]\nv_cvt_pk_bf16_f32 v32, v128, v129\n"
"v_pk_add_f32 v[130:131], v[130:131], v[218:219] op_sel_hi:[1,0] neg_lo:[0,1] neg_hi:[0,1]\nv_cvt_pk_bf16_f32 v33, v130, v131\nv_pk_add_f32 v[132:133], v[132:133], v[218:219] op_sel_hi:[1,0] neg_lo:[0,1] neg_hi:[0,1]\nv_cvt_pk_bf16_f32 v34, v132, v133\n"
"v_pk_add_f32 v[134:135], v[134:135], v[218:219] op_sel_hi:[1,0] neg_lo:[0,1] neg_hi:[0,1]\nv_cvt_pk_bf16_f32 v35, v134, v135\nds_write_b128 v215, v[32:35] offset:8192\nv_pk_add_f32 v[136:137], v[136:137], v[218:219] op_sel_hi:[1,0] neg_lo:[0,1] neg_hi:[0,1]\n"
"v_cvt_pk_bf16_f32 v36, v136, v137\nv_pk_add_f32 v[138:139], v[138:139], v[218:219] op_sel_hi:[1,0] neg_lo:[0,1] neg_hi:[0,1]\nv_cvt_pk_bf16_f32 v37, v138, v139\nv_pk_add_f32 v[140:141], v[140:141], v[218:219] op_sel_hi:[1,0] neg_lo:[0,1] neg_hi:[0,1]\n"
"v_cvt_pk_bf16_f32 v38, v140, v141\nv_pk_add_f32 v[142:143], v[142:143], v[218:219] op_sel_hi:[1,0] neg_lo:[0,1] neg_hi:[0,1]\nv_cvt_pk_bf16_f32 v39, v142, v143\nds_write_b128 v215, v[36:39] offset:16384\n"
"v_pk_add_f32 v[144:145], v[144:145], v[218:219] op_sel_hi:[1,0] neg_lo:[0,1] neg_hi:[0,1]\nv_cvt_pk_bf16_f32 v40, v144, v145\nv_pk_add_f32 v[146:147], v[146:147], v[218:219] op_sel_hi:[1,0] neg_lo:[0,1] neg_hi:[0,1]\nv_cvt_pk_bf16_f32 v41, v146, v147\n"
"v_pk_add_f32 v[148:149], v[148:149], v[218:219] op_sel_hi:[1,0] neg_lo:[0,1] neg_hi:[0,1]\nv_cvt_pk_bf16_f32 v42, v148, v149\nv_pk_add_f32 v[150:151], v[150:151], v[218:219] op_sel_hi:[1,0] neg_lo:[0,1] neg_hi:[0,1]\nv_cvt_pk_bf16_f32 v43, v150, v151\n"
"ds_write_b128 v215, v[40:43] offset:24576\nv_pk_add_f32 v[152:153], v[152:153], v[218:219] op_sel_hi:[1,0] neg_lo:[0,1] neg_hi:[0,1]\nv_cvt_pk_bf16_f32 v44, v152, v153\nv_pk_add_f32 v[154:155], v[154:155], v[218:219] op_sel_hi:[1,0] neg_lo:[0,1] neg_hi:[0,1]\n"
"v_cvt_pk_bf16_f32 v45, v154, v155\nv_pk_add_f32 v[156:157], v[156:157], v[218:219] op_sel_hi:[1,0] neg_lo:[0,1] neg_hi:[0,1]\nv_cvt_pk_bf16_f32 v46, v156, v157\nv_pk_add_f32 v[158:159], v[158:159], v[218:219] op_sel_hi:[1,0] neg_lo:[0,1] neg_hi:[0,1]\n"
"v_cvt_pk_bf16_f32 v47, v158, v159\nds_write_b128 v215, v[44:47] offset:32768\nv_pk_add_f32 v[160:161], v[160:161], v[218:219] op_sel_hi:[1,0] neg_lo:[0,1] neg_hi:[0,1]\nv_cvt_pk_bf16_f32 v48, v160, v161\n"
"v_pk_add_f32 v[162:163], v[162:163], v[218:219] op_sel_hi:[1,0] neg_lo:[0,1] neg_hi:[0,1]\nv_cvt_pk_bf16_f32 v49, v162, v163\nv_pk_add_f32 v[164:165], v[164:165], v[218:219] op_sel_hi:[1,0] neg_lo:[0,1] neg_hi:[0,1]\nv_cvt_pk_bf16_f32 v50, v164, v165\n"
"v_pk_add_f32 v[166:167], v[166:167], v[218:219] op_sel_hi:[1,0] neg_lo:[0,1] neg_hi:[0,1]\nv_cvt_pk_bf16_f32 v51, v166, v167\nds_write_b128 v215, v[48:51] offset:40960\nv_pk_add_f32 v[168:169], v[168:169], v[218:219] op_sel_hi:[1,0] neg_lo:[0,1] neg_hi:[0,1]\n"
"v_cvt_pk_bf16_f32 v52, v168, v169\nv_pk_add_f32 v[170:171], v[170:171], v[218:219] op_sel_hi:[1,0] neg_lo:[0,1] neg_hi:[0,1]\nv_cvt_pk_bf16_f32 v53, v170, v171\nv_pk_add_f32 v[172:173], v[172:173], v[218:219] op_sel_hi:[1,0] neg_lo:[0,1] neg_hi:[0,1]\n"
"v_cvt_pk_bf16_f32 v54, v172, v173\nv_pk_add_f32 v[174:175], v[174:175], v[218:219] op_sel_hi:[1,0] neg_lo:[0,1] neg_hi:[0,1]\nv_cvt_pk_bf16_f32 v55, v174, v175\nds_write_b128 v215, v[52:55] offset:49152\n"
"s_add_u32 m0, s73, 0x10000\ns_add_u32 s76, s76, 0x1c000\nbuffer_load_dwordx4 v250, s[36:39], s76 offen lds\ns_add_u32 m0, s73, 0x11000\n"
"s_add_u32 s76, s76, 0x1c000\nbuffer_load_dwordx4 v250, s[36:39], s76 offen lds\ns_add_u32 m0, s73, 0x12000\ns_add_u32 s76, s76, 0x1c000\n"
"buffer_load_dwordx4 v250, s[36:39], s76 offen lds\ns_add_u32 m0, s73, 0x13000\ns_add_u32 s76, s76, 0x1c000\nbuffer_load_dwordx4 v250, s[36:39], s76 offen lds\n"
"s_add_u32 m0, s73, 0x14000\ns_add_u32 s76, s76, 0x1c000\nbuffer_load_dwordx4 v250, s[36:39], s76 offen lds\ns_add_u32 m0, s73, 0x15000\n"
"s_add_u32 s76, s76, 0x1c000\nbuffer_load_dwordx4 v250, s[36:39], s76 offen lds\ns_add_u32 m0, s73, 0x16000\ns_add_u32 s76, s76, 0x1c000\n"
"buffer_load_dwordx4 v250, s[36:39], s76 offen lds\nglobal_load_dword a127, v197, s[96:97]\ns_mov_b64 s[96:97], -1\ns_cmp_eq_u32 s34, 1\ns_cselect_b32 s35, s91, 0x7f000000\n"
"buffer_load_dword a127, v246, s[92:95], s35 offen offset:0\ns_sub_u32 s35, s84, 28\ns_lshl_b32 s35, s35, 1\ns_add_u32 s35, s35, s89\n"
"s_lshl_b32 s35, s35, 13\ns_add_u32 s35, s35, s88\ns_cmp_lt_u32 s72, 2\ns_cselect_b64 s[92:93], s[48:49], s[52:53]\n"
"s_mov_b32 s94, s50\ns_mov_b32 s90, 0x6f800000\ns_waitcnt vmcnt(10) lgkmcnt(0)\ns_barrier\n"
"s_bitcmp1_b32 s74, 0\ns_cbranch_scc1 L_varB%=\nds_read_b64_tr_b16 a[64:65], v248 offset:0\nds_read_b64_tr_b16 a[66:67], v248 offset:512\n"
"ds_read_b128 a[128:131], v212 offset:0\nds_read_b64_tr_b16 a[68:69], v248 offset:4096\nds_read_b64_tr_b16 a[70:71], v248 offset:4608\nds_read_b128 a[144:147], v213 offset:0\n"
"ds_read_b64_tr_b16 a[72:73], v248 offset:8192\nds_read_b64_tr_b16 a[74:75], v248 offset:8704\nds_read_b128 a[160:163], v212 offset:8192\nds_read_b64_tr_b16 a[76:77], v248 offset:12288\n"
"ds_read_b64_tr_b16 a[78:79], v248 offset:12800\nds_read_b128 a[176:179], v213 offset:8192\nds_read_b64_tr_b16 a[80:81], v248 offset:16384\nds_read_b64_tr_b16 a[82:83], v248 offset:16896\n"
"s_waitcnt lgkmcnt(11)\nv_mfma_f32_16x16x32_bf16 v[176:179], a[64:67], a[128:131], 0\ns_add_u32 m0, s73, 0x8000\nds_read_b128 a[0:3], v212 offset:16384\n"
"ds_read_b64_tr_b16 a[84:85], v248 offset:20480\nds_read_b64_tr_b16 a[86:87], v248 offset:20992\nv_pk_mul_f32 v[220:221], v[120:121], v[120:121]\ns_waitcnt lgkmcnt(11)\n"
"v_mfma_f32_16x16x32_bf16 v[176:179], a[68:71], a[144:147], v[176:179]\ns_add_u32 s77, s77, 0x0\nv_pk_mul_f32 v[222:223], v[122:123], v[122:123]\nv_pk_fma_f32 v[220:221], v[124:125], v[124:125], v[220:221]\n"
"buffer_load_dwordx4 v250, s[40:43], s77 offen lds\ns_waitcnt lgkmcnt(8)\nv_mfma_f32_16x16x32_bf16 v[176:179], a[72:75], a[160:163], v[176:179]\ns_add_u32 m0, s73, 0x9000\n"
"ds_read_b128 a[16:19], v213 offset:16384\nds_read_b64_tr_b16 a[88:89], v248 offset:24576\nds_read_b64_tr_b16 a[90:91], v248 offset:25088\nv_pk_fma_f32 v[222:223], v[126:127], v[126:127], v[222:223]\n"
"s_waitcnt lgkmcnt(8)\nv_mfma_f32_16x16x32_bf16 v[176:179], a[76:79], a[176:179], v[176:179]\ns_add_u32 s77, s77, 0x1c000\nv_pk_fma_f32 v[220:221], v[128:129], v[128:129], v[220:221]\n"
"v_pk_fma_f32 v[222:223], v[130:131], v[130:131], v[222:223]\nbuffer_load_dwordx4 v250, s[40:43], s77 offen lds\ns_waitcnt lgkmcnt(5)\nv_mfma_f32_16x16x32_bf16 v[176:179], a[80:83], a[0:3], v[176:179]\n"
"s_add_u32 m0, s73, 0xa000\nds_read_b128 a[32:35], v212 offset:24576\nds_read_b128 a[132:135], v212 offset:2048\nds_read_b128 a[148:151], v213 offset:2048\n"
"v_pk_fma_f32 v[220:221], v[132:133], v[132:133], v[220:221]\ns_waitcnt lgkmcnt(5)\nv_mfma_f32_16x16x32_bf16 v[176:179], a[84:87], a[16:19], v[176:179]\ns_add_u32 s77, s77, 0x1c000\n"
"v_pk_fma_f32 v[222:223], v[134:135], v[134:135], v[222:223]\nv_pk_fma_f32 v[220:221], v[136:137], v[136:137], v[220:221]\nbuffer_load_dwordx4 v250, s[40:43], s77 offen lds\ns_waitcnt lgkmcnt(2)\n"
"v_mfma_f32_16x16x32_bf16 v[176:179], a[88:91], a[32:35], v[176:179]\ns_add_u32 m0, s73, 0xb000\nds_read_b128 a[164:167], v212 offset:10240\nds_read_b128 a[180:183], v213 offset:10240\n"
"ds_read_b128 a[4:7], v212 offset:18432\nv_pk_fma_f32 v[222:223], v[138:139], v[138:139], v[222:223]\ns_waitcnt lgkmcnt(4)\nv_mfma_f32_16x16x32_bf16 v[180:183], a[64:67], a[132:135], 0\n"
"s_add_u32 s77, s77, 0x1c000\nv_pk_fma_f32 v[220:221], v[140:141], v[140:141], v[220:221]\nv_pk_fma_f32 v[222:223], v[142:143], v[142:143], v[222:223]\nbuffer_load_dwordx4 v250, s[40:43], s77 offen lds\n"
"s_waitcnt lgkmcnt(3)\nv_mfma_f32_16x16x32_bf16 v[180:183], a[68:71], a[148:151], v[180:183]\ns_add_u32 m0, s73, 0xc000\nds_read_b128 a[20:23], v213 offset:18432\n"
"ds_read_b128 a[36:39], v212 offset:26624\nds_read_b128 a[136:139], v212 offset:4096\nv_pk_fma_f32 v[220:221], v[144:145], v[144:145], v[220:221]\ns_waitcnt lgkmcnt(5)\n"
"v_mfma_f32_16x16x32_bf16 v[180:183], a[72:75], a[164:167], v[180:183]\ns_add_u32 s77, s77, 0x1c000\nv_pk_fma_f32 v[222:223], v[146:147], v[146:147], v[222:223]\nv_pk_fma_f32 v[220:221], v[148:149], v[148:149], v[220:221]\n"
"buffer_load_dwordx4 v250, s[40:43], s77 offen lds\ns_waitcnt lgkmcnt(4)\nv_mfma_f32_16x16x32_bf16 v[180:183], a[76:79], a[180:183], v[180:183]\ns_add_u32 m0, s73, 0xd000\n"
"ds_read_b128 a[152:155], v213 offset:4096\nds_read_b128 a[168:171], v212 offset:12288\nds_read_b128 a[184:187], v213 offset:12288\nv_pk_fma_f32 v[222:223], v[150:151], v[150:151], v[222:223]\n"
"s_waitcnt lgkmcnt(6)\nv_mfma_f32_16x16x32_bf16 v[180:183], a[80:83], a[4:7], v[180:183]\ns_add_u32 s77, s77, 0x1c000\nv_pk_fma_f32 v[220:221], v[152:153], v[152:153], v[220:221]\n"
"v_pk_fma_f32 v[222:223], v[154:155], v[154:155], v[222:223]\nbuffer_load_dwordx4 v250, s[40:43], s77 offen lds\ns_waitcnt lgkmcnt(5)\nv_mfma_f32_16x16x32_bf16 v[180:183], a[84:87], a[20:23], v[180:183]\n"
"s_add_u32 m0, s73, 0xe000\nds_read_b128 a[8:11], v212 offset:20480\nds_read_b128 a[24:27], v213 offset:20480\nds_read_b128 a[40:43], v212 offset:28672\n"
"v_pk_fma_f32 v[220:221], v[156:157], v[156:157], v[220:221]\ns_waitcnt lgkmcnt(7)\nv_mfma_f32_16x16x32_bf16 v[180:183], a[88:91], a[36:39], v[180:183]\ns_add_u32 s77, s77, 0x1c000\n"
"v_pk_fma_f32 v[222:223], v[158:159], v[158:159], v[222:223]\nv_pk_fma_f32 v[220:221], v[160:161], v[160:161], v[220:221]\nbuffer_load_dwordx4 v250, s[40:43], s77 offen lds\nds_read_b128 a[140:143], v212 offset:6144\n"
"ds_read_b128 a[156:159], v213 offset:6144\nds_read_b128 a[172:175], v212 offset:14336\nds_read_b128 a[188:191], v213 offset:14336\nds_read_b128 a[12:15], v212 offset:22528\n"
"ds_read_b128 a[28:31], v213 offset:22528\nds_read_b128 a[44:47], v212 offset:30720\ns_waitcnt vmcnt(9) lgkmcnt(0)\ns_barrier\n"
"v_mfma_f32_16x16x32_bf16 v[184:187], a[64:67], a[136:139], 0\ns_add_u32 m0, s73, 0x18000\nds_read_b64_tr_b16 a[96:97], v249 offset:0\nds_read_b64_tr_b16 a[98:99], v249 offset:512\n"
"ds_read_b64_tr_b16 a[100:101], v249 offset:4096\nv_pk_fma_f32 v[222:223], v[162:163], v[162:163], v[222:223]\nv_mfma_f32_16x16x32_bf16 v[184:187], a[68:71], a[152:155], v[184:187]\ns_add_u32 s77, s77, 0x1c000\n"
"buffer_load_dwordx4 v250, s[40:43], s77 offen lds\nv_mfma_f32_16x16x32_bf16 v[184:187], a[72:75], a[168:171], v[184:187]\ns_add_u32 m0, s73, 0x19000\nds_read_b64_tr_b16 a[102:103], v249 offset:4608\n"
"ds_read_b64_tr_b16 a[104:105], v249 offset:8192\nds_read_b64_tr_b16 a[106:107], v249 offset:8704\nv_pk_fma_f32 v[220:221], v[164:165], v[164:165], v[220:221]\nv_mfma_f32_16x16x32_bf16 v[184:187], a[76:79], a[184:187], v[184:187]\n"
"s_add_u32 s77, s77, 0x1c000\nbuffer_load_dwordx4 v250, s[40:43], s77 offen lds\nv_mfma_f32_16x16x32_bf16 v[184:187], a[80:83], a[8:11], v[184:187]\ns_add_u32 m0, s73, 0x1a000\n"
"ds_read_b64_tr_b16 a[108:109], v249 offset:12288\nds_read_b64_tr_b16 a[110:111], v249 offset:12800\nds_read_b64_tr_b16 a[112:113], v249 offset:16384\nv_pk_fma_f32 v[222:223], v[166:167], v[166:167], v[222:223]\n"
"v_mfma_f32_16x16x32_bf16 v[184:187], a[84:87], a[24:27], v[184:187]\ns_add_u32 s77, s77, 0x1c000\nbuffer_load_dwordx4 v250, s[40:43], s77 offen lds\nv_mfma_f32_16x16x32_bf16 v[184:187], a[88:91], a[40:43], v[184:187]\n"
"s_add_u32 m0, s73, 0x1b000\nds_read_b64_tr_b16 a[114:115], v249 offset:16896\nds_read_b64_tr_b16 a[116:117], v249 offset:20480\nds_read_b64_tr_b16 a[118:119], v249 offset:20992\n"
"v_pk_fma_f32 v[220:221], v[168:169], v[168:169], v[220:221]\nv_mfma_f32_16x16x32_bf16 v[188:191], a[64:67], a[140:143], 0\ns_add_u32 s77, s77, 0x1c000\nbuffer_load_dwordx4 v250, s[40:43], s77 offen lds\n"
"v_mfma_f32_16x16x32_bf16 v[188:191], a[68:71], a[156:159], v[188:191]\ns_add_u32 m0, s73, 0x1c000\nds_read_b64_tr_b16 a[120:121], v249 offset:24576\nds_read_b64_tr_b16 a[122:123], v249 offset:25088\n"
"ds_read_b128 a[192:195], v213 offset:24576\nv_pk_fma_f32 v[222:223], v[170:171], v[170:171], v[222:223]\nv_mfma_f32_16x16x32_bf16 v[188:191], a[72:75], a[172:175], v[188:191]\ns_add_u32 s77, s77, 0x1c000\n"
"buffer_load_dwordx4 v250, s[40:43], s77 offen lds\nv_mfma_f32_16x16x32_bf16 v[188:191], a[76:79], a[188:191], v[188:191]\ns_add_u32 m0, s73, 0x1d000\nds_read_b128 a[208:211], v212 offset:32768\n"
"ds_read_b128 a[224:227], v213 offset:32768\nds_read_b128 a[240:243], v212 offset:40960\nv_pk_fma_f32 v[220:221], v[172:173], v[172:173], v[220:221]\nv_mfma_f32_16x16x32_bf16 v[188:191], a[80:83], a[12:15], v[188:191]\n"
"s_add_u32 s77, s77, 0x1c000\nbuffer_load_dwordx4 v250, s[40:43], s77 offen lds\nv_mfma_f32_16x16x32_bf16 v[188:191], a[84:87], a[28:31], v[188:191]\ns_add_u32 m0, s73, 0x1e000\n"
"ds_read_b128 v[0:3], v213 offset:40960\nds_read_b128 v[16:19], v212 offset:49152\nds_read_b128 v[32:35], v213 offset:49152\nv_pk_fma_f32 v[222:223], v[174:175], v[174:175], v[222:223]\n"
"v_mfma_f32_16x16x32_bf16 v[188:191], a[88:91], a[44:47], v[188:191]\ns_add_u32 s77, s77, 0x1c000\nbuffer_load_dwordx4 v250, s[40:43], s77 offen lds\nv_sub_f32 v198, v193, v218\n"
"v_mul_f32 v198, v198, v198\nv_pk_add_f32 v[220:221], v[220:221], v[222:223]\nv_add_f32 v196, v220, v221\ns_nop 1\n"
"v_add_f32_dpp v196, v196, v196 quad_perm:[1,0,3,2] row_mask:0xf bank_mask:0xf\ns_nop 1\nv_add_f32_dpp v196, v196, v196 quad_perm:[2,3,0,1] row_mask:0xf bank_mask:0xf\ns_nop 1\n"
"v_add_f32_dpp v196, v196, v196 row_half_mirror row_mask:0xf bank_mask:0xf\nv_mul_f32 v196, 0x3b124925, v196\nv_sub_f32 v196, v196, v198\nv_max_f32 v196, 0, v196\n"
"v_cvt_pk_bf16_f32 v196, v196, v196\nv_lshlrev_b32 v196, 16, v196\nv_add_f32 v196, 0x3727c5ac, v196\nv_cvt_pk_bf16_f32 v196, v196, v196\n"
"v_lshlrev_b32 v196, 16, v196\nv_sqrt_f32 v196, v196\ns_nop 0\nv_cvt_pk_bf16_f32 v196, v196, v196\n"
"v_lshlrev_b32 v196, 16, v196\nv_rcp_f32 v196, v196\ns_nop 0\nds_write_b32 v216, v196 offset:32\n"
"s_waitcnt vmcnt(7) lgkmcnt(0)\ns_barrier\nv_mfma_f32_16x16x32_bf16 v[176:179], a[96:99], a[192:195], v[176:179]\ns_add_u32 m0, s73, 0x0\n"
"ds_read_b128 a[196:199], v213 offset:26624\nds_read_b128 a[212:215], v212 offset:34816\nds_read_b128 a[228:231], v213 offset:34816\nv_mfma_f32_16x16x32_bf16 v[176:179], a[100:103], a[208:211], v[176:179]\n"
"s_add_u32 s78, s78, 0x0\nbuffer_load_dwordx4 v251, s[44:47], s78 offen lds\nv_mfma_f32_16x16x32_bf16 v[176:179], a[104:107], a[224:227], v[176:179]\ns_add_u32 m0, s73, 0x1000\n"
"ds_read_b128 a[244:247], v212 offset:43008\nds_read_b128 v[4:7], v213 offset:43008\nds_read_b128 v[20:23], v212 offset:51200\nv_mfma_f32_16x16x32_bf16 v[176:179], a[108:111], a[240:243], v[176:179]\n"
"s_add_u32 s78, s78, 0x40000\nbuffer_load_dwordx4 v251, s[44:47], s78 offen lds\nv_mfma_f32_16x16x32_bf16 v[176:179], a[112:115], v[0:3], v[176:179]\ns_add_u32 m0, s73, 0x2000\n"
"ds_read_b128 v[36:39], v213 offset:51200\nds_read_b128 a[200:203], v213 offset:28672\nds_read_b128 a[216:219], v212 offset:36864\nv_mfma_f32_16x16x32_bf16 v[176:179], a[116:119], v[16:19], v[176:179]\n"
"s_add_u32 s78, s78, 0x40000\nbuffer_load_dwordx4 v251, s[44:47], s78 offen lds\nv_mfma_f32_16x16x32_bf16 v[176:179], a[120:123], v[32:35], v[176:179]\ns_add_u32 m0, s73, 0x3000\n"
"ds_read_b128 a[232:235], v213 offset:36864\nds_read_b128 a[248:251], v212 offset:45056\nds_read_b128 v[8:11], v213 offset:45056\ns_waitcnt lgkmcnt(11)\n"
"v_mfma_f32_16x16x32_bf16 v[180:183], a[96:99], a[196:199], v[180:183]\ns_add_u32 s78, s78, 0x40000\nbuffer_load_dwordx4 v251, s[44:47], s78 offen lds\ns_waitcnt lgkmcnt(9)\n"
"v_mfma_f32_16x16x32_bf16 v[180:183], a[100:103], a[212:215], v[180:183]\ns_add_u32 m0, s73, 0x4000\nds_read_b128 v[24:27], v212 offset:53248\nds_read_b128 v[40:43], v213 offset:53248\n"
"ds_read_b128 a[204:207], v213 offset:30720\nv_mfma_f32_16x16x32_bf16 v[180:183], a[104:107], a[228:231], v[180:183]\ns_add_u32 s78, s78, 0x40000\nbuffer_load_dwordx4 v251, s[44:47], s78 offen lds\n"
"s_waitcnt lgkmcnt(9)\nv_mfma_f32_16x16x32_bf16 v[180:183], a[108:111], a[244:247], v[180:183]\ns_add_u32 m0, s73, 0x5000\nds_read_b128 a[220:223], v212 offset:38912\n"
"ds_read_b128 a[236:239], v213 offset:38912\nds_read_b128 a[252:255], v212 offset:47104\nv_mfma_f32_16x16x32_bf16 v[180:183], a[112:115], v[4:7], v[180:183]\ns_add_u32 s78, s78, 0x40000\n"
"buffer_load_dwordx4 v251, s[44:47], s78 offen lds\nv_mfma_f32_16x16x32_bf16 v[180:183], a[116:119], v[20:23], v[180:183]\ns_add_u32 m0, s73, 0x6000\nds_read_b128 v[12:15], v213 offset:47104\n"
"ds_read_b128 v[28:31], v212 offset:55296\nds_read_b128 v[44:47], v213 offset:55296\ns_waitcnt lgkmcnt(12)\nv_mfma_f32_16x16x32_bf16 v[180:183], a[120:123], v[36:39], v[180:183]\n"
"s_add_u32 s78, s78, 0x40000\nbuffer_load_dwordx4 v251, s[44:47], s78 offen lds\nv_mfma_f32_16x16x32_bf16 v[184:187], a[96:99], a[200:203], v[184:187]\ns_add_u32 m0, s73, 0x10000\n"
"ds_read_b64_tr_b16 a[64:65], v248 offset:32768\nds_read_b64_tr_b16 a[66:67], v248 offset:33280\nds_read_b64_tr_b16 a[68:69], v248 offset:36864\nv_mfma_f32_16x16x32_bf16 v[184:187], a[100:103], a[216:219], v[184:187]\n"
"s_add_u32 s78, s78, 0x40000\nbuffer_load_dwordx4 v251, s[44:47], s78 offen lds\ns_waitcnt lgkmcnt(9)\nv_mfma_f32_16x16x32_bf16 v[184:187], a[104:107], a[232:235], v[184:187]\n"
"s_add_u32 m0, s73, 0x11000\nds_read_b64_tr_b16 a[70:71], v248 offset:37376\nds_read_b64_tr_b16 a[72:73], v248 offset:40960\nds_read_b64_tr_b16 a[74:75], v248 offset:41472\n"
"v_mfma_f32_16x16x32_bf16 v[184:187], a[108:111], a[248:251], v[184:187]\ns_add_u32 s78, s78, 0x40000\nbuffer_load_dwordx4 v251, s[44:47], s78 offen lds\nv_mfma_f32_16x16x32_bf16 v[184:187], a[112:115], v[8:11], v[184:187]\n"
"s_add_u32 m0, s73, 0x12000\nds_read_b64_tr_b16 a[76:77], v248 offset:45056\nds_read_b64_tr_b16 a[78:79], v248 offset:45568\nds_read_b64_tr_b16 a[80:81], v248 offset:49152\n"
"v_mfma_f32_16x16x32_bf16 v[184:187], a[116:119], v[24:27], v[184:187]\ns_add_u32 s78, s78, 0x40000\nbuffer_load_dwordx4 v251, s[44:47], s78 offen lds\nv_mfma_f32_16x16x32_bf16 v[184:187], a[120:123], v[40:43], v[184:187]\n"
"s_add_u32 m0, s73, 0x13000\nds_read_b64_tr_b16 a[82:83], v248 offset:49664\nds_read_b64_tr_b16 a[84:85], v248 offset:53248\nds_read_b64_tr_b16 a[86:87], v248 offset:53760\n"
"v_mfma_f32_16x16x32_bf16 v[188:191], a[96:99], a[204:207], v[188:191]\ns_add_u32 s78, s78, 0x40000\nbuffer_load_dwordx4 v251, s[44:47], s78 offen lds\ns_waitcnt lgkmcnt(9)\n"
"v_mfma_f32_16x16x32_bf16 v[188:191], a[100:103], a[220:223], v[188:191]\ns_add_u32 m0, s73, 0x14000\nds_read_b64_tr_b16 a[88:89], v248 offset:57344\nds_read_b64_tr_b16 a[90:91], v248 offset:57856\n"
"ds_read_b32 v144, v217 offset:28672\nv_mfma_f32_16x16x32_bf16 v[188:191], a[104:107], a[236:239], v[188:191]\ns_add_u32 s78, s78, 0x40000\nbuffer_load_dwordx4 v251, s[44:47], s78 offen lds\n"
"v_mfma_f32_16x16x32_bf16 v[188:191], a[108:111], a[252:255], v[188:191]\ns_add_u32 m0, s73, 0x15000\nds_read_b32 v145, v217 offset:28736\nds_read_b32 v146, v217 offset:28800\n"
"ds_read_b32 v147, v217 offset:28864\nv_mov_b32 v106, 0\nv_mfma_f32_16x16x32_bf16 v[188:191], a[112:115], v[12:15], v[188:191]\ns_add_u32 s78, s78, 0x40000\n"
"buffer_load_dwordx4 v251, s[44:47], s78 offen lds\nv_mfma_f32_16x16x32_bf16 v[188:191], a[116:119], v[28:31], v[188:191]\ns_add_u32 m0, s73, 0x16000\nds_read_b32 v106, v106 offset:28928\n"
"v_mul_f32 v128, v176, v176\nv_mfma_f32_16x16x32_bf16 v[188:191], a[120:123], v[44:47], v[188:191]\ns_add_u32 s78, s78, 0x40000\nbuffer_load_dwordx4 v251, s[44:47], s78 offen lds\n"
"s_waitcnt vmcnt(14) lgkmcnt(0)\ns_barrier\nv_mfma_f32_16x16x32_bf16 v[160:163], a[64:67], a[128:131], 0\ns_add_u32 s78, s78, 0xffcc0200\n"
"ds_read_b64_tr_b16 a[96:97], v249 offset:32768\nv_mul_f32 v129, v177, v177\nv_mfma_f32_16x16x32_bf16 v[160:163], a[68:71], a[144:147], v[160:163]\ns_add_u32 m0, s73, 0x8000\n"
"ds_read_b64_tr_b16 a[98:99], v249 offset:33280\nv_mul_f32 v130, v178, v178\nv_mfma_f32_16x16x32_bf16 v[160:163], a[72:75], a[160:163], v[160:163]\nbuffer_load_dwordx4 v251, s[44:47], s78 offen lds\n"
"v_mfma_f32_16x16x32_bf16 v[160:163], a[76:79], a[176:179], v[160:163]\ns_add_u32 s78, s78, 0x40000\nds_read_b64_tr_b16 a[100:101], v249 offset:36864\nv_mul_f32 v152, 0xc0135761, v144\n"
"v_mfma_f32_16x16x32_bf16 v[160:163], a[80:83], a[0:3], v[160:163]\ns_add_u32 m0, s73, 0x9000\nds_read_b64_tr_b16 a[102:103], v249 offset:37376\nv_mul_f32 v156, v144, v144\n"
"v_mfma_f32_16x16x32_bf16 v[160:163], a[84:87], a[16:19], v[160:163]\nbuffer_load_dwordx4 v251, s[44:47], s78 offen lds\nv_mfma_f32_16x16x32_bf16 v[160:163], a[88:91], a[32:35], v[160:163]\nds_read_b64_tr_b16 a[104:105], v249 offset:40960\n"
"v_mul_f32 v148, v152, v156\nv_mfma_f32_16x16x32_bf16 v[164:167], a[88:91], a[36:39], 0\ns_add_u32 s78, s78, 0x40000\nds_read_b64_tr_b16 a[106:107], v249 offset:41472\n"
"v_mul_f32 v153, 0xc0135761, v145\nv_mfma_f32_16x16x32_bf16 v[164:167], a[84:87], a[20:23], v[164:167]\ns_add_u32 m0, s73, 0xa000\nds_read_b64_tr_b16 a[108:109], v249 offset:45056\n"
"v_mul_f32 v157, v145, v145\nv_mfma_f32_16x16x32_bf16 v[164:167], a[80:83], a[4:7], v[164:167]\nbuffer_load_dwordx4 v251, s[44:47], s78 offen lds\nv_mfma_f32_16x16x32_bf16 v[164:167], a[76:79], a[180:183], v[164:167]\n"
"ds_read_b64_tr_b16 a[110:111], v249 offset:45568\nv_mul_f32 v154, 0xc0135761, v146\nv_mfma_f32_16x16x32_bf16 v[164:167], a[72:75], a[164:167], v[164:167]\ns_add_u32 s78, s78, 0x40000\n"
"ds_read_b64_tr_b16 a[112:113], v249 offset:49152\nv_mul_f32 v148, 0x3d372713, v148\nv_mfma_f32_16x16x32_bf16 v[164:167], a[68:71], a[148:151], v[164:167]\ns_add_u32 m0, s73, 0xb000\n"
"ds_read_b64_tr_b16 a[114:115], v249 offset:49664\nv_mul_f32 v158, v146, v146\nv_mfma_f32_16x16x32_bf16 v[164:167], a[64:67], a[132:135], v[164:167]\nbuffer_load_dwordx4 v251, s[44:47], s78 offen lds\n"
"v_mfma_f32_16x16x32_bf16 v[168:171], a[64:67], a[136:139], 0\ns_add_u32 s78, s78, 0x40000\nds_read_b64_tr_b16 a[116:117], v249 offset:53248\nv_mul_f32 v155, 0xc0135761, v147\n"
"v_mfma_f32_16x16x32_bf16 v[168:171], a[68:71], a[152:155], v[168:171]\ns_add_u32 m0, s73, 0xc000\nds_read_b64_tr_b16 a[118:119], v249 offset:53760\nv_mul_f32 v159, v147, v147\n"
"v_mfma_f32_16x16x32_bf16 v[168:171], a[72:75], a[168:171], v[168:171]\nbuffer_load_dwordx4 v251, s[44:47], s78 offen lds\nv_mfma_f32_16x16x32_bf16 v[168:171], a[76:79], a[184:187], v[168:171]\nds_read_b64_tr_b16 a[120:121], v249 offset:57344\n"
"v_mul_f32 v149, v153, v157\nv_mfma_f32_16x16x32_bf16 v[168:171], a[80:83], a[8:11], v[168:171]\ns_add_u32 s78, s78, 0x40000\nds_read_b64_tr_b16 a[122:123], v249 offset:57856\n"
"v_mul_f32 v131, v179, v179\nv_mfma_f32_16x16x32_bf16 v[168:171], a[84:87], a[24:27], v[168:171]\ns_add_u32 m0, s73, 0xd000\nv_mul_f32 v150, v154, v158\n"
"v_mul_f32 v151, v155, v159\nv_mfma_f32_16x16x32_bf16 v[168:171], a[88:91], a[40:43], v[168:171]\nbuffer_load_dwordx4 v251, s[44:47], s78 offen lds\nv_mfma_f32_16x16x32_bf16 v[172:175], a[88:91], a[44:47], 0\n"
"v_fma_f32 v128, v128, v148, v152\nv_fma_f32 v129, v129, v148, v152\nv_mfma_f32_16x16x32_bf16 v[172:175], a[84:87], a[28:31], v[172:175]\ns_add_u32 s78, s78, 0x40000\n"
"v_fma_f32 v130, v130, v148, v152\nv_mul_f32 v149, 0x3d372713, v149\nv_mfma_f32_16x16x32_bf16 v[172:175], a[80:83], a[12:15], v[172:175]\ns_add_u32 m0, s73, 0xe000\n"
"v_fma_f32 v131, v131, v148, v152\nv_mul_f32 v132, v180, v180\nv_mfma_f32_16x16x32_bf16 v[172:175], a[76:79], a[188:191], v[172:175]\nbuffer_load_dwordx4 v251, s[44:47], s78 offen lds\n"
"v_mfma_f32_16x16x32_bf16 v[172:175], a[72:75], a[172:175], v[172:175]\nv_mul_f32 v133, v181, v181\nv_mul_f32 v134, v182, v182\nv_mfma_f32_16x16x32_bf16 v[172:175], a[68:71], a[156:159], v[172:175]\n"
"v_mul_f32 v135, v183, v183\nv_mul_f32 v150, 0x3d372713, v150\nv_mfma_f32_16x16x32_bf16 v[172:175], a[64:67], a[140:143], v[172:175]\nv_mul_f32 v136, v184, v184\n"
"v_mul_f32 v137, v185, v185\ns_waitcnt vmcnt(14) lgkmcnt(0)\ns_barrier\nv_mfma_f32_16x16x32_bf16 v[160:163], a[96:99], a[192:195], v[160:163]\n"
"s_add_u32 s78, s78, 0x40000\nds_read_b64_tr_b16 a[64:65], v248 offset:0\nv_mul_f32 v138, v186, v186\nv_mfma_f32_16x16x32_bf16 v[160:163], a[100:103], a[208:211], v[160:163]\n"
"s_add_u32 m0, s73, 0x18000\nds_read_b64_tr_b16 a[66:67], v248 offset:512\nv_mul_f32 v151, 0x3d372713, v151\nv_mfma_f32_16x16x32_bf16 v[160:163], a[104:107], a[224:227], v[160:163]\n"
"buffer_load_dwordx4 v251, s[44:47], s78 offen lds\nv_mfma_f32_16x16x32_bf16 v[160:163], a[108:111], a[240:243], v[160:163]\ns_add_u32 s78, s78, 0x40000\nds_read_b64_tr_b16 a[68:69], v248 offset:4096\n"
"v_mul_f32 v139, v187, v187\nv_mfma_f32_16x16x32_bf16 v[160:163], a[112:115], v[0:3], v[160:163]\ns_add_u32 m0, s73, 0x19000\nds_read_b64_tr_b16 a[70:71], v248 offset:4608\n"
"v_mul_f32 v140, v188, v188\nv_mfma_f32_16x16x32_bf16 v[160:163], a[116:119], v[16:19], v[160:163]\nbuffer_load_dwordx4 v251, s[44:47], s78 offen lds\nv_mfma_f32_16x16x32_bf16 v[160:163], a[120:123], v[32:35], v[160:163]\n"
"ds_read_b64_tr_b16 a[72:73], v248 offset:8192\nv_mul_f32 v141, v189, v189\nv_mfma_f32_16x16x32_bf16 v[164:167], a[120:123], v[36:39], v[164:167]\ns_add_u32 s78, s78, 0x40000\n"
"ds_read_b64_tr_b16 a[74:75], v248 offset:8704\nv_mul_f32 v156, 0xbfb8aa3b, v156\nv_mfma_f32_16x16x32_bf16 v[164:167], a[116:119], v[20:23], v[164:167]\ns_add_u32 m0, s73, 0x1a000\n"
"ds_read_b64_tr_b16 a[76:77], v248 offset:12288\nv_mul_f32 v142, v190, v190\nv_mfma_f32_16x16x32_bf16 v[164:167], a[112:115], v[4:7], v[164:167]\nbuffer_load_dwordx4 v251, s[44:47], s78 offen lds\n"
"v_mfma_f32_16x16x32_bf16 v[164:167], a[108:111], a[244:247], v[164:167]\nds_read_b64_tr_b16 a[78:79], v248 offset:12800\nv_mul_f32 v128, v128, v176\nv_mfma_f32_16x16x32_bf16 v[164:167], a[104:107], a[228:231], v[164:167]\n"
"s_add_u32 s78, s78, 0x40000\nds_read_b64_tr_b16 a[80:81], v248 offset:16384\nv_mul_f32 v129, v129, v177\nv_mfma_f32_16x16x32_bf16 v[164:167], a[100:103], a[212:215], v[164:167]\n"
"s_add_u32 m0, s73, 0x1b000\nds_read_b64_tr_b16 a[82:83], v248 offset:16896\nv_mul_f32 v130, v130, v178\nv_mfma_f32_16x16x32_bf16 v[164:167], a[96:99], a[196:199], v[164:167]\n"
"buffer_load_dwordx4 v251, s[44:47], s78 offen lds\nv_mfma_f32_16x16x32_bf16 v[168:171], a[96:99], a[200:203], v[168:171]\ns_add_u32 s78, s78, 0x40000\nds_read_b64_tr_b16 a[84:85], v248 offset:20480\n"
"v_mul_f32 v131, v131, v179\nv_mfma_f32_16x16x32_bf16 v[168:171], a[100:103], a[216:219], v[168:171]\ns_add_u32 m0, s73, 0x1c000\nds_read_b64_tr_b16 a[86:87], v248 offset:20992\n"
"v_fma_f32 v132, v132, v149, v153\nv_mfma_f32_16x16x32_bf16 v[168:171], a[104:107], a[232:235], v[168:171]\nbuffer_load_dwordx4 v251, s[44:47], s78 offen lds\nv_mfma_f32_16x16x32_bf16 v[168:171], a[108:111], a[248:251], v[168:171]\n"
"ds_read_b64_tr_b16 a[88:89], v248 offset:24576\nv_mul_f32 v143, v191, v191\nv_mfma_f32_16x16x32_bf16 v[168:171], a[112:115], v[8:11], v[168:171]\ns_add_u32 s78, s78, 0x40000\n"
"ds_read_b64_tr_b16 a[90:91], v248 offset:25088\nv_fma_f32 v133, v133, v149, v153\nv_mfma_f32_16x16x32_bf16 v[168:171], a[116:119], v[24:27], v[168:171]\ns_add_u32 m0, s73, 0x1d000\n"
"v_fma_f32 v134, v134, v149, v153\nv_fma_f32 v135, v135, v149, v153\nv_mfma_f32_16x16x32_bf16 v[168:171], a[120:123], v[40:43], v[168:171]\nbuffer_load_dwordx4 v251, s[44:47], s78 offen lds\n"
"v_mfma_f32_16x16x32_bf16 v[172:175], a[120:123], v[44:47], v[172:175]\nv_fma_f32 v136, v136, v150, v154\nv_fma_f32 v137, v137, v150, v154\nv_mfma_f32_16x16x32_bf16 v[172:175], a[116:119], v[28:31], v[172:175]\n"
"s_add_u32 s78, s78, 0x40000\nv_fma_f32 v138, v138, v150, v154\nv_fma_f32 v139, v139, v150, v154\nv_mfma_f32_16x16x32_bf16 v[172:175], a[112:115], v[12:15], v[172:175]\n"
"s_add_u32 m0, s73, 0x1e000\nv_fma_f32 v140, v140, v151, v155\nv_fma_f32 v141, v141, v151, v155\nv_mfma_f32_16x16x32_bf16 v[172:175], a[108:111], a[252:255], v[172:175]\n"
"buffer_load_dwordx4 v251, s[44:47], s78 offen lds\nv_mfma_f32_16x16x32_bf16 v[172:175], a[104:107], a[236:239], v[172:175]\nv_fma_f32 v142, v142, v151, v155\nv_mul_f32 v157, 0xbfb8aa3b, v157\n"
"v_mfma_f32_16x16x32_bf16 v[172:175], a[100:103], a[220:223], v[172:175]\nv_rcp_f32 v156, v156\nv_mfma_f32_16x16x32_bf16 v[172:175], a[96:99], a[204:207], v[172:175]\nv_exp_f32 v128, v128\n"
"s_waitcnt vmcnt(14) lgkmcnt(0)\ns_barrier\nv_mfma_f32_16x16x32_bf16 v[192:195], a[64:67], a[128:131], 0\ns_add_u32 s76, s76, 0xffe94080\n"
"ds_read_b64_tr_b16 a[96:97], v249 offset:0\nv_fma_f32 v143, v143, v151, v155\nv_mfma_f32_16x16x32_bf16 v[192:195], a[68:71], a[144:147], v[192:195]\ns_add_u32 m0, s73, 0x0\n"
"ds_read_b64_tr_b16 a[98:99], v249 offset:512\nv_mul_f32 v132, v132, v180\nv_mfma_f32_16x16x32_bf16 v[192:195], a[72:75], a[160:163], v[192:195]\nbuffer_load_dwordx4 v250, s[36:39], s76 offen lds\n"
"v_mfma_f32_16x16x32_bf16 v[192:195], a[76:79], a[176:179], v[192:195]\ns_add_u32 s76, s76, 0x1c000\nds_read_b64_tr_b16 a[100:101], v249 offset:4096\nv_mul_f32 v133, v133, v181\n"
"v_mfma_f32_16x16x32_bf16 v[192:195], a[80:83], a[0:3], v[192:195]\ns_add_u32 m0, s73, 0x1000\nds_read_b64_tr_b16 a[102:103], v249 offset:4608\nv_mul_f32 v134, v134, v182\n"
"v_mfma_f32_16x16x32_bf16 v[192:195], a[84:87], a[16:19], v[192:195]\nbuffer_load_dwordx4 v250, s[36:39], s76 offen lds\nv_mfma_f32_16x16x32_bf16 v[192:195], a[88:91], a[32:35], v[192:195]\nds_read_b64_tr_b16 a[104:105], v249 offset:8192\n"
"v_mul_f32 v135, v135, v183\nv_mfma_f32_16x16x32_bf16 v[196:199], a[88:91], a[36:39], 0\ns_add_u32 s76, s76, 0x1c000\nds_read_b64_tr_b16 a[106:107], v249 offset:8704\n"
"v_mul_f32 v158, 0xbfb8aa3b, v158\nv_mfma_f32_16x16x32_bf16 v[196:199], a[84:87], a[20:23], v[196:199]\ns_add_u32 m0, s73, 0x2000\nds_read_b64_tr_b16 a[108:109], v249 offset:12288\n"
"v_mul_f32 v136, v136, v184\nv_mfma_f32_16x16x32_bf16 v[196:199], a[80:83], a[4:7], v[196:199]\nbuffer_load_dwordx4 v250, s[36:39], s76 offen lds\nv_mfma_f32_16x16x32_bf16 v[196:199], a[76:79], a[180:183], v[196:199]\n"
"ds_read_b64_tr_b16 a[110:111], v249 offset:12800\nv_mul_f32 v137, v137, v185\nv_mfma_f32_16x16x32_bf16 v[196:199], a[72:75], a[164:167], v[196:199]\ns_add_u32 s76, s76, 0x1c000\n"
"ds_read_b64_tr_b16 a[112:113], v249 offset:16384\nv_mul_f32 v138, v138, v186\nv_mfma_f32_16x16x32_bf16 v[196:199], a[68:71], a[148:151], v[196:199]\ns_add_u32 m0, s73, 0x3000\n"
"ds_read_b64_tr_b16 a[114:115], v249 offset:16896\nv_mul_f32 v159, 0xbfb8aa3b, v159\nv_mfma_f32_16x16x32_bf16 v[196:199], a[64:67], a[132:135], v[196:199]\nbuffer_load_dwordx4 v250, s[36:39], s76 offen lds\n"
"v_mfma_f32_16x16x32_bf16 v[200:203], a[64:67], a[136:139], 0\ns_add_u32 s76, s76, 0x1c000\nds_read_b64_tr_b16 a[116:117], v249 offset:20480\nv_mul_f32 v139, v139, v187\n"
"v_mfma_f32_16x16x32_bf16 v[200:203], a[68:71], a[152:155], v[200:203]\ns_add_u32 m0, s73, 0x4000\nds_read_b64_tr_b16 a[118:119], v249 offset:20992\nv_mul_f32 v140, v140, v188\n"
"v_mfma_f32_16x16x32_bf16 v[200:203], a[72:75], a[168:171], v[200:203]\nbuffer_load_dwordx4 v250, s[36:39], s76 offen lds\nv_mfma_f32_16x16x32_bf16 v[200:203], a[76:79], a[184:187], v[200:203]\nds_read_b64_tr_b16 a[120:121], v249 offset:24576\n"
"v_mul_f32 v141, v141, v189\nv_mfma_f32_16x16x32_bf16 v[200:203], a[80:83], a[8:11], v[200:203]\ns_add_u32 s76, s76, 0x1c000\nds_read_b64_tr_b16 a[122:123], v249 offset:25088\n"
"v_mul_f32 v142, v142, v190\nv_mfma_f32_16x16x32_bf16 v[200:203], a[84:87], a[24:27], v[200:203]\ns_add_u32 m0, s73, 0x5000\nv_exp_f32 v129, v129\n"
"v_mfma_f32_16x16x32_bf16 v[200:203], a[88:91], a[40:43], v[200:203]\nbuffer_load_dwordx4 v250, s[36:39], s76 offen lds\nv_mfma_f32_16x16x32_bf16 v[204:207], a[88:91], a[44:47], 0\nv_exp_f32 v130, v130\n"
"v_mfma_f32_16x16x32_bf16 v[204:207], a[84:87], a[28:31], v[204:207]\ns_add_u32 s76, s76, 0x1c000\nv_exp_f32 v131, v131\nv_mfma_f32_16x16x32_bf16 v[204:207], a[80:83], a[12:15], v[204:207]\n"
"s_add_u32 m0, s73, 0x6000\nv_mul_f32 v143, v143, v191\nv_fma_f32 v128, v128, v156, v156\nv_mfma_f32_16x16x32_bf16 v[204:207], a[76:79], a[188:191], v[204:207]\n"
"buffer_load_dwordx4 v250, s[36:39], s76 offen lds\nv_mfma_f32_16x16x32_bf16 v[204:207], a[72:75], a[172:175], v[204:207]\nv_fma_f32 v129, v129, v156, v156\nv_fma_f32 v130, v130, v156, v156\n"
"v_mfma_f32_16x16x32_bf16 v[204:207], a[68:71], a[156:159], v[204:207]\nv_fma_f32 v131, v131, v156, v156\nv_mul_f32 v236, 0x3db8aa3b, v144\nv_mfma_f32_16x16x32_bf16 v[204:207], a[64:67], a[140:143], v[204:207]\n"
"v_rcp_f32 v157, v157\ns_waitcnt vmcnt(14) lgkmcnt(0)\ns_barrier\nv_mfma_f32_16x16x32_bf16 v[192:195], a[96:99], a[192:195], v[192:195]\n"
"s_add_u32 s76, s76, 0x1c000\nds_read_b64_tr_b16 a[64:65], v248 offset:32768\nv_mul_f32 v237, 0x3db8aa3b, v145\nv_mfma_f32_16x16x32_bf16 v[192:195], a[100:103], a[208:211], v[192:195]\n"
"s_add_u32 m0, s73, 0x10000\nds_read_b64_tr_b16 a[66:67], v248 offset:33280\nv_mul_f32 v238, 0x3db8aa3b, v146\nv_mfma_f32_16x16x32_bf16 v[192:195], a[104:107], a[224:227], v[192:195]\n"
"buffer_load_dwordx4 v250, s[36:39], s76 offen lds\nv_mfma_f32_16x16x32_bf16 v[192:195], a[108:111], a[240:243], v[192:195]\ns_add_u32 s76, s76, 0x1c000\nds_read_b64_tr_b16 a[68:69], v248 offset:36864\n"
"v_mul_f32 v239, 0x3db8aa3b, v147\nv_mfma_f32_16x16x32_bf16 v[192:195], a[112:115], v[0:3], v[192:195]\ns_add_u32 m0, s73, 0x11000\nds_read_b64_tr_b16 a[70:71], v248 offset:37376\n"
"v_subrev_u32 v104, 0x18000, v252\nv_mfma_f32_16x16x32_bf16 v[192:195], a[116:119], v[16:19], v[192:195]\nbuffer_load_dwordx4 v250, s[36:39], s76 offen lds\nv_mfma_f32_16x16x32_bf16 v[192:195], a[120:123], v[32:35], v[192:195]\n"
"ds_read_b64_tr_b16 a[72:73], v248 offset:40960\nv_subrev_u32 v105, 0x18000, v253\nv_mfma_f32_16x16x32_bf16 v[196:199], a[120:123], v[36:39], v[196:199]\ns_add_u32 s76, s76, 0x1c000\n"
"ds_read_b64_tr_b16 a[74:75], v248 offset:41472\nv_readfirstlane_b32 s30, v106\nv_mfma_f32_16x16x32_bf16 v[196:199], a[116:119], v[20:23], v[196:199]\ns_add_u32 m0, s73, 0x12000\n"
"ds_read_b64_tr_b16 a[76:77], v248 offset:45056\nds_read_b64_tr_b16 a[78:79], v248 offset:45568\nv_mfma_f32_16x16x32_bf16 v[196:199], a[112:115], v[4:7], v[196:199]\nbuffer_load_dwordx4 v250, s[36:39], s76 offen lds\n"
"v_mfma_f32_16x16x32_bf16 v[196:199], a[108:111], a[244:247], v[196:199]\nds_read_b64_tr_b16 a[80:81], v248 offset:49152\nv_mul_f32 v192, v192, v236\nv_mfma_f32_16x16x32_bf16 v[196:199], a[104:107], a[228:231], v[196:199]\n"
"s_add_u32 s76, s76, 0x1c000\nds_read_b64_tr_b16 a[82:83], v248 offset:49664\nv_mul_f32 v193, v193, v236\nv_mfma_f32_16x16x32_bf16 v[196:199], a[100:103], a[212:215], v[196:199]\n"
"s_add_u32 m0, s73, 0x13000\nds_read_b64_tr_b16 a[84:85], v248 offset:53248\nv_mul_f32 v194, v194, v236\nv_mfma_f32_16x16x32_bf16 v[196:199], a[96:99], a[196:199], v[196:199]\n"
"buffer_load_dwordx4 v250, s[36:39], s76 offen lds\nv_mfma_f32_16x16x32_bf16 v[200:203], a[96:99], a[200:203], v[200:203]\ns_add_u32 s76, s76, 0x1c000\nds_read_b64_tr_b16 a[86:87], v248 offset:53760\n"
"v_mul_f32 v195, v195, v236\nv_mfma_f32_16x16x32_bf16 v[200:203], a[100:103], a[216:219], v[200:203]\ns_add_u32 m0, s73, 0x14000\nds_read_b64_tr_b16 a[88:89], v248 offset:57344\n"
"v_cvt_pk_bf16_f32 v48, v192, v193\nv_mfma_f32_16x16x32_bf16 v[200:203], a[104:107], a[232:235], v[200:203]\nbuffer_load_dwordx4 v250, s[36:39], s76 offen lds\nv_mfma_f32_16x16x32_bf16 v[200:203], a[108:111], a[248:251], v[200:203]\n"
"ds_read_b64_tr_b16 a[90:91], v248 offset:57856\nv_mul_f32 v196, v196, v237\nv_mfma_f32_16x16x32_bf16 v[200:203], a[112:115], v[8:11], v[200:203]\ns_add_u32 s76, s76, 0x1c000\n"
"v_exp_f32 v132, v132\nv_mfma_f32_16x16x32_bf16 v[200:203], a[116:119], v[24:27], v[200:203]\ns_add_u32 m0, s73, 0x15000\nv_exp_f32 v133, v133\n"
"v_mfma_f32_16x16x32_bf16 v[200:203], a[120:123], v[40:43], v[200:203]\nbuffer_load_dwordx4 v250, s[36:39], s76 offen lds\nv_mfma_f32_16x16x32_bf16 v[204:207], a[120:123], v[44:47], v[204:207]\nv_exp_f32 v134, v134\n"
"v_mfma_f32_16x16x32_bf16 v[204:207], a[116:119], v[28:31], v[204:207]\ns_add_u32 s76, s76, 0x1c000\nv_exp_f32 v135, v135\nv_mfma_f32_16x16x32_bf16 v[204:207], a[112:115], v[12:15], v[204:207]\n"
"s_add_u32 m0, s73, 0x16000\nv_rcp_f32 v158, v158\nv_mfma_f32_16x16x32_bf16 v[204:207], a[108:111], a[252:255], v[204:207]\nbuffer_load_dwordx4 v250, s[36:39], s76 offen lds\n"
"v_mfma_f32_16x16x32_bf16 v[204:207], a[104:107], a[236:239], v[204:207]\nv_exp_f32 v136, v136\nv_mfma_f32_16x16x32_bf16 v[204:207], a[100:103], a[220:223], v[204:207]\nv_exp_f32 v137, v137\n"
"v_mfma_f32_16x16x32_bf16 v[204:207], a[96:99], a[204:207], v[204:207]\nv_exp_f32 v138, v138\ns_waitcnt vmcnt(14) lgkmcnt(0)\ns_barrier\n"
"v_mfma_f32_16x16x32_bf16 v[208:211], a[64:67], a[128:131], 0\ns_add_u32 s77, s77, 0xffe94080\nds_read_b64_tr_b16 a[96:97], v249 offset:32768\nv_fma_f32 v132, v132, v157, v157\n"
"v_mfma_f32_16x16x32_bf16 v[208:211], a[68:71], a[144:147], v[208:211]\ns_add_u32 m0, s73, 0x8000\nds_read_b64_tr_b16 a[98:99], v249 offset:33280\nv_fma_f32 v133, v133, v157, v157\n"
"v_mfma_f32_16x16x32_bf16 v[208:211], a[72:75], a[160:163], v[208:211]\nbuffer_load_dwordx4 v250, s[40:43], s77 offen lds\nv_mfma_f32_16x16x32_bf16 v[208:211], a[76:79], a[176:179], v[208:211]\ns_add_u32 s77, s77, 0x1c000\n"
"ds_read_b64_tr_b16 a[100:101], v249 offset:36864\nv_fma_f32 v134, v134, v157, v157\nv_mfma_f32_16x16x32_bf16 v[208:211], a[80:83], a[0:3], v[208:211]\ns_add_u32 m0, s73, 0x9000\n"
"ds_read_b64_tr_b16 a[102:103], v249 offset:37376\nv_fma_f32 v135, v135, v157, v157\nv_mfma_f32_16x16x32_bf16 v[208:211], a[84:87], a[16:19], v[208:211]\nbuffer_load_dwordx4 v250, s[40:43], s77 offen lds\n"
"v_mfma_f32_16x16x32_bf16 v[208:211], a[88:91], a[32:35], v[208:211]\nds_read_b64_tr_b16 a[104:105], v249 offset:40960\nv_fma_f32 v136, v136, v158, v158\nv_mfma_f32_16x16x32_bf16 v[212:215], a[88:91], a[36:39], 0\n"
"s_add_u32 s77, s77, 0x1c000\nds_read_b64_tr_b16 a[106:107], v249 offset:41472\nv_fma_f32 v137, v137, v158, v158\nv_mfma_f32_16x16x32_bf16 v[212:215], a[84:87], a[20:23], v[212:215]\n"
"s_add_u32 m0, s73, 0xa000\nds_read_b64_tr_b16 a[108:109], v249 offset:45056\nv_fma_f32 v138, v138, v158, v158\nv_mfma_f32_16x16x32_bf16 v[212:215], a[80:83], a[4:7], v[212:215]\n"
"buffer_load_dwordx4 v250, s[40:43], s77 offen lds\nv_mfma_f32_16x16x32_bf16 v[212:215], a[76:79], a[180:183], v[212:215]\nds_read_b64_tr_b16 a[110:111], v249 offset:45568\nv_mul_f32 v197, v197, v237\n"
"v_mfma_f32_16x16x32_bf16 v[212:215], a[72:75], a[164:167], v[212:215]\ns_add_u32 s77, s77, 0x1c000\nds_read_b64_tr_b16 a[112:113], v249 offset:49152\nv_mul_f32 v198, v198, v237\n"
"v_mfma_f32_16x16x32_bf16 v[212:215], a[68:71], a[148:151], v[212:215]\ns_add_u32 m0, s73, 0xb000\nds_read_b64_tr_b16 a[114:115], v249 offset:49664\nv_mul_f32 v199, v199, v237\n"
"v_mfma_f32_16x16x32_bf16 v[212:215], a[64:67], a[132:135], v[212:215]\nbuffer_load_dwordx4 v250, s[40:43], s77 offen lds\nv_mfma_f32_16x16x32_bf16 v[216:219], a[64:67], a[136:139], 0\ns_add_u32 s77, s77, 0x1c000\n"
"ds_read_b64_tr_b16 a[116:117], v249 offset:53248\nv_mul_f32 v200, v200, v238\nv_mfma_f32_16x16x32_bf16 v[216:219], a[68:71], a[152:155], v[216:219]\ns_add_u32 m0, s73, 0xc000\n"
"ds_read_b64_tr_b16 a[118:119], v249 offset:53760\nv_mul_f32 v201, v201, v238\nv_mfma_f32_16x16x32_bf16 v[216:219], a[72:75], a[168:171], v[216:219]\nbuffer_load_dwordx4 v250, s[40:43], s77 offen lds\n"
"v_mfma_f32_16x16x32_bf16 v[216:219], a[76:79], a[184:187], v[216:219]\nds_read_b64_tr_b16 a[120:121], v249 offset:57344\nv_mul_f32 v202, v202, v238\nv_mfma_f32_16x16x32_bf16 v[216:219], a[80:83], a[8:11], v[216:219]\n"
"s_add_u32 s77, s77, 0x1c000\nds_read_b64_tr_b16 a[122:123], v249 offset:57856\nv_mul_f32 v203, v203, v238\nv_mfma_f32_16x16x32_bf16 v[216:219], a[84:87], a[24:27], v[216:219]\n"
"s_add_u32 m0, s73, 0xd000\nv_exp_f32 v139, v139\nv_mfma_f32_16x16x32_bf16 v[216:219], a[88:91], a[40:43], v[216:219]\nbuffer_load_dwordx4 v250, s[40:43], s77 offen lds\n"
"v_mfma_f32_16x16x32_bf16 v[220:223], a[88:91], a[44:47], 0\nv_rcp_f32 v159, v159\nv_mfma_f32_16x16x32_bf16 v[220:223], a[84:87], a[28:31], v[220:223]\ns_add_u32 s77, s77, 0x1c000\n"
"v_exp_f32 v140, v140\nv_mfma_f32_16x16x32_bf16 v[220:223], a[80:83], a[12:15], v[220:223]\ns_add_u32 m0, s73, 0xe000\nv_exp_f32 v141, v141\n"
"v_mfma_f32_16x16x32_bf16 v[220:223], a[76:79], a[188:191], v[220:223]\nbuffer_load_dwordx4 v250, s[40:43], s77 offen lds\nv_mfma_f32_16x16x32_bf16 v[220:223], a[72:75], a[172:175], v[220:223]\nv_exp_f32 v142, v142\n"
"v_mfma_f32_16x16x32_bf16 v[220:223], a[68:71], a[156:159], v[220:223]\nv_exp_f32 v143, v143\nv_mfma_f32_16x16x32_bf16 v[220:223], a[64:67], a[140:143], v[220:223]\nv_rcp_f32 v128, v128\n"
"s_waitcnt vmcnt(14) lgkmcnt(0)\ns_barrier\nv_mfma_f32_16x16x32_bf16 v[208:211], a[96:99], a[192:195], v[208:211]\ns_add_u32 s77, s77, 0x1c000\n"
"v_rcp_f32 v129, v129\nv_mfma_f32_16x16x32_bf16 v[208:211], a[100:103], a[208:211], v[208:211]\ns_add_u32 m0, s73, 0x18000\nv_rcp_f32 v130, v130\n"
"v_mfma_f32_16x16x32_bf16 v[208:211], a[104:107], a[224:227], v[208:211]\nbuffer_load_dwordx4 v250, s[40:43], s77 offen lds\nv_mfma_f32_16x16x32_bf16 v[208:211], a[108:111], a[240:243], v[208:211]\ns_add_u32 s77, s77, 0x1c000\n"
"v_rcp_f32 v131, v131\nv_mfma_f32_16x16x32_bf16 v[208:211], a[112:115], v[0:3], v[208:211]\ns_add_u32 m0, s73, 0x19000\nds_read_b64_tr_b16 a[64:65], v248 offset:0\n"
"v_fma_f32 v139, v139, v158, v158\nv_mfma_f32_16x16x32_bf16 v[208:211], a[116:119], v[16:19], v[208:211]\nbuffer_load_dwordx4 v250, s[40:43], s77 offen lds\nv_mfma_f32_16x16x32_bf16 v[208:211], a[120:123], v[32:35], v[208:211]\n"
"s_cmp_lt_u32 s75, 28\ns_cbranch_scc1 L_kvA%=\nbuffer_load_dword a127, v247, s[92:95], s35 offen\ns_add_u32 s35, s35, 0x10000\nbuffer_load_dword a127, v247, s[92:95], s35 offen\ns_add_u32 s35, s35, 0x10000\nbuffer_load_dword a127, v247, s[92:95], s35 offen\ns_add_u32 s35, s35, 0x10000\nL_kvA%=:\nds_read_b64_tr_b16 a[66:67], v248 offset:512\nv_fma_f32 v140, v140, v159, v159\nv_mfma_f32_16x16x32_bf16 v[212:215], a[120:123], v[36:39], v[212:215]\n"
"s_add_u32 s77, s77, 0x1c000\nds_read_b64_tr_b16 a[68:69], v248 offset:4096\nv_fma_f32 v141, v141, v159, v159\nv_mfma_f32_16x16x32_bf16 v[212:215], a[116:119], v[20:23], v[212:215]\n"
"s_add_u32 m0, s73, 0x1a000\nds_read_b64_tr_b16 a[70:71], v248 offset:4608\nv_fma_f32 v142, v142, v159, v159\nv_mfma_f32_16x16x32_bf16 v[212:215], a[112:115], v[4:7], v[212:215]\n"
"buffer_load_dwordx4 v250, s[40:43], s77 offen lds\nv_mfma_f32_16x16x32_bf16 v[212:215], a[108:111], a[244:247], v[212:215]\nds_read_b64_tr_b16 a[72:73], v248 offset:8192\nv_fma_f32 v143, v143, v159, v159\n"
"v_mfma_f32_16x16x32_bf16 v[212:215], a[104:107], a[228:231], v[212:215]\ns_add_u32 s77, s77, 0x1c000\nds_read_b64_tr_b16 a[74:75], v248 offset:8704\nv_mul_f32 v128, v128, v176\n"
"v_mfma_f32_16x16x32_bf16 v[212:215], a[100:103], a[212:215], v[212:215]\ns_add_u32 m0, s73, 0x1b000\nds_read_b64_tr_b16 a[76:77], v248 offset:12288\nv_mul_f32 v129, v129, v177\n"
"v_mfma_f32_16x16x32_bf16 v[212:215], a[96:99], a[196:199], v[212:215]\nbuffer_load_dwordx4 v250, s[40:43], s77 offen lds\nv_mfma_f32_16x16x32_bf16 v[216:219], a[96:99], a[200:203], v[216:219]\ns_add_u32 s77, s77, 0x1c000\n"
"ds_read_b64_tr_b16 a[78:79], v248 offset:12800\nv_mul_f32 v130, v130, v178\nv_mfma_f32_16x16x32_bf16 v[216:219], a[100:103], a[216:219], v[216:219]\ns_add_u32 m0, s73, 0x1c000\n"
"ds_read_b64_tr_b16 a[80:81], v248 offset:16384\nv_mul_f32 v131, v131, v179\nv_mfma_f32_16x16x32_bf16 v[216:219], a[104:107], a[232:235], v[216:219]\nbuffer_load_dwordx4 v250, s[40:43], s77 offen lds\n"
"v_mfma_f32_16x16x32_bf16 v[216:219], a[108:111], a[248:251], v[216:219]\nds_read_b64_tr_b16 a[82:83], v248 offset:16896\nv_mul_f32 v128, v128, v160\nv_mfma_f32_16x16x32_bf16 v[216:219], a[112:115], v[8:11], v[216:219]\n"
"s_add_u32 s77, s77, 0x1c000\nds_read_b64_tr_b16 a[84:85], v248 offset:20480\nv_mul_f32 v129, v129, v161\nv_mfma_f32_16x16x32_bf16 v[216:219], a[116:119], v[24:27], v[216:219]\n"
"s_add_u32 m0, s73, 0x1d000\nds_read_b64_tr_b16 a[86:87], v248 offset:20992\nv_mul_f32 v130, v130, v162\nv_mfma_f32_16x16x32_bf16 v[216:219], a[120:123], v[40:43], v[216:219]\n"
"buffer_load_dwordx4 v250, s[40:43], s77 offen lds\nv_mfma_f32_16x16x32_bf16 v[220:223], a[120:123], v[44:47], v[220:223]\nds_read_b64_tr_b16 a[88:89], v248 offset:24576\nv_mul_f32 v131, v131, v163\n"
"v_mfma_f32_16x16x32_bf16 v[220:223], a[116:119], v[28:31], v[220:223]\ns_add_u32 s77, s77, 0x1c000\nds_read_b64_tr_b16 a[90:91], v248 offset:25088\nv_cvt_pk_bf16_f32 v112, v128, v129\n"
"v_mfma_f32_16x16x32_bf16 v[220:223], a[112:115], v[12:15], v[220:223]\ns_add_u32 m0, s73, 0x1e000\nv_rcp_f32 v132, v132\nv_mfma_f32_16x16x32_bf16 v[220:223], a[108:111], a[252:255], v[220:223]\n"
"buffer_load_dwordx4 v250, s[40:43], s77 offen lds\nv_mfma_f32_16x16x32_bf16 v[220:223], a[104:107], a[236:239], v[220:223]\nv_rcp_f32 v133, v133\nv_mfma_f32_16x16x32_bf16 v[220:223], a[100:103], a[220:223], v[220:223]\n"
"v_rcp_f32 v134, v134\nv_mfma_f32_16x16x32_bf16 v[220:223], a[96:99], a[204:207], v[220:223]\nv_rcp_f32 v135, v135\ns_waitcnt vmcnt(14) lgkmcnt(0)\n"
"s_barrier\nv_mfma_f32_16x16x32_bf16 v[176:179], a[64:67], a[128:131], 0\ns_add_u32 s78, s78, 0xffcbfe80\nv_rcp_f32 v136, v136\n"
"v_mfma_f32_16x16x32_bf16 v[176:179], a[68:71], a[144:147], v[176:179]\ns_add_u32 m0, s73, 0x0\nv_rcp_f32 v137, v137\nv_mfma_f32_16x16x32_bf16 v[176:179], a[72:75], a[160:163], v[176:179]\n"
"buffer_load_dwordx4 v251, s[44:47], s78 offen lds\nv_mfma_f32_16x16x32_bf16 v[176:179], a[76:79], a[176:179], v[176:179]\ns_add_u32 s78, s78, 0x40000\nv_rcp_f32 v138, v138\n"
"v_mfma_f32_16x16x32_bf16 v[176:179], a[80:83], a[0:3], v[176:179]\ns_add_u32 m0, s73, 0x1000\nv_rcp_f32 v139, v139\nv_mfma_f32_16x16x32_bf16 v[176:179], a[84:87], a[16:19], v[176:179]\n"
"buffer_load_dwordx4 v251, s[44:47], s78 offen lds\nv_mfma_f32_16x16x32_bf16 v[176:179], a[88:91], a[32:35], v[176:179]\nv_rcp_f32 v140, v140\nv_mul_f32 v132, v132, v180\n"
"v_mul_f32 v133, v133, v181\nv_mul_f32 v134, v134, v182\nv_mul_f32 v135, v135, v183\nv_mfma_f32_16x16x32_bf16 v[180:183], a[88:91], a[36:39], 0\n"
"s_add_u32 s78, s78, 0x40000\nv_rcp_f32 v141, v141\nv_mfma_f32_16x16x32_bf16 v[180:183], a[84:87], a[20:23], v[180:183]\ns_add_u32 m0, s73, 0x2000\n"
"v_rcp_f32 v142, v142\nv_mfma_f32_16x16x32_bf16 v[180:183], a[80:83], a[4:7], v[180:183]\nbuffer_load_dwordx4 v251, s[44:47], s78 offen lds\nv_mfma_f32_16x16x32_bf16 v[180:183], a[76:79], a[180:183], v[180:183]\n"
"v_rcp_f32 v143, v143\nv_mfma_f32_16x16x32_bf16 v[180:183], a[72:75], a[164:167], v[180:183]\ns_add_u32 s78, s78, 0x40000\nds_read_b64_tr_b16 a[96:97], v249 offset:0\n"
"v_mul_f32 v136, v136, v184\nv_mfma_f32_16x16x32_bf16 v[180:183], a[68:71], a[148:151], v[180:183]\ns_add_u32 m0, s73, 0x3000\nds_read_b64_tr_b16 a[98:99], v249 offset:512\n"
"v_mul_f32 v137, v137, v185\nv_mfma_f32_16x16x32_bf16 v[180:183], a[64:67], a[132:135], v[180:183]\nbuffer_load_dwordx4 v251, s[44:47], s78 offen lds\nv_mul_f32 v138, v138, v186\n"
"v_mul_f32 v139, v139, v187\nv_mfma_f32_16x16x32_bf16 v[184:187], a[64:67], a[136:139], 0\ns_add_u32 s78, s78, 0x40000\nds_read_b64_tr_b16 a[100:101], v249 offset:4096\n"
"v_mul_f32 v140, v140, v188\nv_mfma_f32_16x16x32_bf16 v[184:187], a[68:71], a[152:155], v[184:187]\ns_add_u32 m0, s73, 0x4000\nds_read_b64_tr_b16 a[102:103], v249 offset:4608\n"
"v_mul_f32 v141, v141, v189\nv_mfma_f32_16x16x32_bf16 v[184:187], a[72:75], a[168:171], v[184:187]\nbuffer_load_dwordx4 v251, s[44:47], s78 offen lds\nv_mfma_f32_16x16x32_bf16 v[184:187], a[76:79], a[184:187], v[184:187]\n"
"ds_read_b64_tr_b16 a[104:105], v249 offset:8192\nv_mul_f32 v142, v142, v190\nv_mfma_f32_16x16x32_bf16 v[184:187], a[80:83], a[8:11], v[184:187]\ns_add_u32 s78, s78, 0x40000\n"
"ds_read_b64_tr_b16 a[106:107], v249 offset:8704\nv_mul_f32 v143, v143, v191\nv_mfma_f32_16x16x32_bf16 v[184:187], a[84:87], a[24:27], v[184:187]\ns_add_u32 m0, s73, 0x5000\n"
"ds_read_b64_tr_b16 a[108:109], v249 offset:12288\nv_cvt_pk_bf16_f32 v113, v130, v131\nv_mfma_f32_16x16x32_bf16 v[184:187], a[88:91], a[40:43], v[184:187]\nbuffer_load_dwordx4 v251, s[44:47], s78 offen lds\n"
"v_mfma_f32_16x16x32_bf16 v[188:191], a[88:91], a[44:47], 0\nds_read_b64_tr_b16 a[110:111], v249 offset:12800\nv_mul_f32 v132, v132, v164\nv_mfma_f32_16x16x32_bf16 v[188:191], a[84:87], a[28:31], v[188:191]\n"
"s_add_u32 s78, s78, 0x40000\nds_read_b64_tr_b16 a[112:113], v249 offset:16384\nv_mul_f32 v133, v133, v165\nv_mfma_f32_16x16x32_bf16 v[188:191], a[80:83], a[12:15], v[188:191]\n"
"s_add_u32 m0, s73, 0x6000\nds_read_b64_tr_b16 a[114:115], v249 offset:16896\nv_mul_f32 v134, v134, v166\nv_mfma_f32_16x16x32_bf16 v[188:191], a[76:79], a[188:191], v[188:191]\n"
"buffer_load_dwordx4 v251, s[44:47], s78 offen lds\nv_mfma_f32_16x16x32_bf16 v[188:191], a[72:75], a[172:175], v[188:191]\nds_read_b64_tr_b16 a[116:117], v249 offset:20480\nv_mul_f32 v135, v135, v167\n"
"v_mfma_f32_16x16x32_bf16 v[188:191], a[68:71], a[156:159], v[188:191]\nds_read_b64_tr_b16 a[118:119], v249 offset:20992\nv_mul_f32 v136, v136, v168\nv_mfma_f32_16x16x32_bf16 v[188:191], a[64:67], a[140:143], v[188:191]\n"
"ds_read_b64_tr_b16 a[120:121], v249 offset:24576\nv_mul_f32 v137, v137, v169\nds_read_b64_tr_b16 a[122:123], v249 offset:25088\ns_waitcnt vmcnt(14) lgkmcnt(0)\n"
"s_barrier\nv_mfma_f32_16x16x32_bf16 v[176:179], a[96:99], a[192:195], v[176:179]\ns_add_u32 s78, s78, 0x40000\nds_read_b64_tr_b16 a[64:65], v248 offset:32768\n"
"v_mul_f32 v138, v138, v170\nv_mfma_f32_16x16x32_bf16 v[176:179], a[100:103], a[208:211], v[176:179]\ns_add_u32 m0, s73, 0x10000\nds_read_b64_tr_b16 a[66:67], v248 offset:33280\n"
"v_mul_f32 v139, v139, v171\nv_mfma_f32_16x16x32_bf16 v[176:179], a[104:107], a[224:227], v[176:179]\nbuffer_load_dwordx4 v251, s[44:47], s78 offen lds\nv_mfma_f32_16x16x32_bf16 v[176:179], a[108:111], a[240:243], v[176:179]\n"
"s_add_u32 s78, s78, 0x40000\nds_read_b64_tr_b16 a[68:69], v248 offset:36864\nv_mul_f32 v140, v140, v172\nv_mfma_f32_16x16x32_bf16 v[176:179], a[112:115], v[0:3], v[176:179]\n"
"s_add_u32 m0, s73, 0x11000\nds_read_b64_tr_b16 a[70:71], v248 offset:37376\nv_mul_f32 v141, v141, v173\nv_mfma_f32_16x16x32_bf16 v[176:179], a[116:119], v[16:19], v[176:179]\n"
"buffer_load_dwordx4 v251, s[44:47], s78 offen lds\nv_mfma_f32_16x16x32_bf16 v[176:179], a[120:123], v[32:35], v[176:179]\nds_read_b64_tr_b16 a[72:73], v248 offset:40960\nv_mul_f32 v142, v142, v174\n"
"v_mfma_f32_16x16x32_bf16 v[180:183], a[120:123], v[36:39], v[180:183]\ns_add_u32 s78, s78, 0x40000\nds_read_b64_tr_b16 a[74:75], v248 offset:41472\nv_mul_f32 v143, v143, v175\n"
"v_mfma_f32_16x16x32_bf16 v[180:183], a[116:119], v[20:23], v[180:183]\ns_add_u32 m0, s73, 0x12000\nds_read_b64_tr_b16 a[76:77], v248 offset:45056\nv_cvt_pk_bf16_f32 v116, v132, v133\n"
"v_mfma_f32_16x16x32_bf16 v[180:183], a[112:115], v[4:7], v[180:183]\nbuffer_load_dwordx4 v251, s[44:47], s78 offen lds\nv_mfma_f32_16x16x32_bf16 v[180:183], a[108:111], a[244:247], v[180:183]\nds_read_b64_tr_b16 a[78:79], v248 offset:45568\n"
"v_mul_f32 v128, v176, v176\nv_mfma_f32_16x16x32_bf16 v[180:183], a[104:107], a[228:231], v[180:183]\ns_add_u32 s78, s78, 0x40000\nds_read_b64_tr_b16 a[80:81], v248 offset:49152\n"
"v_mul_f32 v129, v177, v177\nv_mfma_f32_16x16x32_bf16 v[180:183], a[100:103], a[212:215], v[180:183]\ns_add_u32 m0, s73, 0x13000\nds_read_b64_tr_b16 a[82:83], v248 offset:49664\n"
"v_mul_f32 v130, v178, v178\nv_mfma_f32_16x16x32_bf16 v[180:183], a[96:99], a[196:199], v[180:183]\nbuffer_load_dwordx4 v251, s[44:47], s78 offen lds\nv_mfma_f32_16x16x32_bf16 v[184:187], a[96:99], a[200:203], v[184:187]\n"
"s_add_u32 s78, s78, 0x40000\nds_read_b64_tr_b16 a[84:85], v248 offset:53248\nv_fma_f32 v128, v128, v148, v152\nv_mfma_f32_16x16x32_bf16 v[184:187], a[100:103], a[216:219], v[184:187]\n"
"s_add_u32 m0, s73, 0x14000\nds_read_b64_tr_b16 a[86:87], v248 offset:53760\nv_fma_f32 v129, v129, v148, v152\nv_mfma_f32_16x16x32_bf16 v[184:187], a[104:107], a[232:235], v[184:187]\n"
"buffer_load_dwordx4 v251, s[44:47], s78 offen lds\nv_mfma_f32_16x16x32_bf16 v[184:187], a[108:111], a[248:251], v[184:187]\nv_mul_f32 v131, v179, v179\nds_read_b64_tr_b16 a[88:89], v248 offset:57344\n"
"v_mfma_f32_16x16x32_bf16 v[184:187], a[112:115], v[8:11], v[184:187]\ns_add_u32 s78, s78, 0x40000\nv_fma_f32 v130, v130, v148, v152\nds_read_b64_tr_b16 a[90:91], v248 offset:57856\n"
"v_mfma_f32_16x16x32_bf16 v[184:187], a[116:119], v[24:27], v[184:187]\ns_add_u32 m0, s73, 0x15000\nv_mul_f32 v128, v128, v176\nv_mul_f32 v129, v129, v177\n"
"v_mfma_f32_16x16x32_bf16 v[184:187], a[120:123], v[40:43], v[184:187]\nbuffer_load_dwordx4 v251, s[44:47], s78 offen lds\nv_mfma_f32_16x16x32_bf16 v[188:191], a[120:123], v[44:47], v[188:191]\nv_fma_f32 v131, v131, v148, v152\n"
"v_mul_f32 v130, v130, v178\nv_mfma_f32_16x16x32_bf16 v[188:191], a[116:119], v[28:31], v[188:191]\ns_add_u32 s78, s78, 0x40000\nv_exp_f32 v128, v128\n"
"v_mfma_f32_16x16x32_bf16 v[188:191], a[112:115], v[12:15], v[188:191]\ns_add_u32 m0, s73, 0x16000\nv_mul_f32 v131, v131, v179\nv_cvt_pk_bf16_f32 v117, v134, v135\n"
"v_mfma_f32_16x16x32_bf16 v[188:191], a[108:111], a[252:255], v[188:191]\nbuffer_load_dwordx4 v251, s[44:47], s78 offen lds\nv_mfma_f32_16x16x32_bf16 v[188:191], a[104:107], a[236:239], v[188:191]\nv_exp_f32 v129, v129\n"
"v_mfma_f32_16x16x32_bf16 v[188:191], a[100:103], a[220:223], v[188:191]\nv_exp_f32 v130, v130\nv_mfma_f32_16x16x32_bf16 v[188:191], a[96:99], a[204:207], v[188:191]\nv_cvt_pk_bf16_f32 v120, v136, v137\n"
"v_cvt_pk_bf16_f32 v121, v138, v139\ns_waitcnt vmcnt(14) lgkmcnt(0)\ns_barrier\nv_mfma_f32_16x16x32_bf16 v[160:163], a[64:67], a[128:131], 0\n"
"s_add_u32 s76, s76, 0xffe94080\nds_read_b64_tr_b16 a[96:97], v249 offset:32768\nv_cvt_pk_bf16_f32 v124, v140, v141\nv_mfma_f32_16x16x32_bf16 v[160:163], a[68:71], a[144:147], v[160:163]\n"
"s_add_u32 m0, s73, 0x8000\nds_read_b64_tr_b16 a[98:99], v249 offset:33280\nv_cvt_pk_bf16_f32 v125, v142, v143\nv_mfma_f32_16x16x32_bf16 v[160:163], a[72:75], a[160:163], v[160:163]\n"
"buffer_load_dwordx4 v250, s[36:39], s76 offen lds\nv_mfma_f32_16x16x32_bf16 v[160:163], a[76:79], a[176:179], v[160:163]\ns_add_u32 s76, s76, 0x1c000\nds_read_b64_tr_b16 a[100:101], v249 offset:36864\n"
"v_fma_f32 v128, v128, v156, v156\nv_mfma_f32_16x16x32_bf16 v[160:163], a[80:83], a[0:3], v[160:163]\ns_add_u32 m0, s73, 0x9000\nds_read_b64_tr_b16 a[102:103], v249 offset:37376\n"
"v_fma_f32 v129, v129, v156, v156\nv_mfma_f32_16x16x32_bf16 v[160:163], a[84:87], a[16:19], v[160:163]\nbuffer_load_dwordx4 v250, s[36:39], s76 offen lds\nv_mfma_f32_16x16x32_bf16 v[160:163], a[88:91], a[32:35], v[160:163]\n"
"ds_read_b64_tr_b16 a[104:105], v249 offset:40960\nv_mul_f32 v132, v180, v180\nv_mfma_f32_16x16x32_bf16 v[164:167], a[88:91], a[36:39], 0\ns_add_u32 s76, s76, 0x1c000\n"
"ds_read_b64_tr_b16 a[106:107], v249 offset:41472\nv_fma_f32 v130, v130, v156, v156\nv_mfma_f32_16x16x32_bf16 v[164:167], a[84:87], a[20:23], v[164:167]\ns_add_u32 m0, s73, 0xa000\n"
"ds_read_b64_tr_b16 a[108:109], v249 offset:45056\nv_mul_f32 v133, v181, v181\nv_mfma_f32_16x16x32_bf16 v[164:167], a[80:83], a[4:7], v[164:167]\nbuffer_load_dwordx4 v250, s[36:39], s76 offen lds\n"
"v_mfma_f32_16x16x32_bf16 v[164:167], a[76:79], a[180:183], v[164:167]\nds_read_b64_tr_b16 a[110:111], v249 offset:45568\nv_mul_f32 v134, v182, v182\nv_mfma_f32_16x16x32_bf16 v[164:167], a[72:75], a[164:167], v[164:167]\n"
"s_add_u32 s76, s76, 0x1c000\nds_read_b64_tr_b16 a[112:113], v249 offset:49152\nv_mul_f32 v135, v183, v183\nv_mfma_f32_16x16x32_bf16 v[164:167], a[68:71], a[148:151], v[164:167]\n"
"s_add_u32 m0, s73, 0xb000\nds_read_b64_tr_b16 a[114:115], v249 offset:49664\nv_mul_f32 v136, v184, v184\nv_mfma_f32_16x16x32_bf16 v[164:167], a[64:67], a[132:135], v[164:167]\n"
"buffer_load_dwordx4 v250, s[36:39], s76 offen lds\nv_mfma_f32_16x16x32_bf16 v[168:171], a[64:67], a[136:139], 0\ns_add_u32 s76, s76, 0x1c000\nds_read_b64_tr_b16 a[116:117], v249 offset:53248\n"
"v_mul_f32 v137, v185, v185\nv_mfma_f32_16x16x32_bf16 v[168:171], a[68:71], a[152:155], v[168:171]\ns_add_u32 m0, s73, 0xc000\nds_read_b64_tr_b16 a[118:119], v249 offset:53760\n"
"v_mul_f32 v138, v186, v186\nv_mfma_f32_16x16x32_bf16 v[168:171], a[72:75], a[168:171], v[168:171]\nbuffer_load_dwordx4 v250, s[36:39], s76 offen lds\nv_mfma_f32_16x16x32_bf16 v[168:171], a[76:79], a[184:187], v[168:171]\n"
"ds_read_b64_tr_b16 a[120:121], v249 offset:57344\nv_mul_f32 v139, v187, v187\nv_mfma_f32_16x16x32_bf16 v[168:171], a[80:83], a[8:11], v[168:171]\ns_add_u32 s76, s76, 0x1c000\n"
"ds_read_b64_tr_b16 a[122:123], v249 offset:57856\nv_mul_f32 v140, v188, v188\nv_mfma_f32_16x16x32_bf16 v[168:171], a[84:87], a[24:27], v[168:171]\ns_add_u32 m0, s73, 0xd000\n"
"v_exp_f32 v131, v131\nv_mfma_f32_16x16x32_bf16 v[168:171], a[88:91], a[40:43], v[168:171]\nbuffer_load_dwordx4 v250, s[36:39], s76 offen lds\nv_mfma_f32_16x16x32_bf16 v[172:175], a[88:91], a[44:47], 0\n"
"v_mul_f32 v141, v189, v189\nv_mul_f32 v142, v190, v190\nv_mfma_f32_16x16x32_bf16 v[172:175], a[84:87], a[28:31], v[172:175]\ns_add_u32 s76, s76, 0x1c000\n"
"v_mul_f32 v143, v191, v191\nv_fma_f32 v131, v131, v156, v156\nv_mfma_f32_16x16x32_bf16 v[172:175], a[80:83], a[12:15], v[172:175]\ns_add_u32 m0, s73, 0xe000\n"
"v_rcp_f32 v128, v128\nv_mfma_f32_16x16x32_bf16 v[172:175], a[76:79], a[188:191], v[172:175]\nbuffer_load_dwordx4 v250, s[36:39], s76 offen lds\nv_mfma_f32_16x16x32_bf16 v[172:175], a[72:75], a[172:175], v[172:175]\n"
"v_rcp_f32 v129, v129\nv_mfma_f32_16x16x32_bf16 v[172:175], a[68:71], a[156:159], v[172:175]\nv_fma_f32 v132, v132, v149, v153\nv_fma_f32 v133, v133, v149, v153\n"
"v_mfma_f32_16x16x32_bf16 v[172:175], a[64:67], a[140:143], v[172:175]\nv_rcp_f32 v130, v130\ns_waitcnt vmcnt(14) lgkmcnt(0)\ns_barrier\n"
"v_mfma_f32_16x16x32_bf16 v[160:163], a[96:99], a[192:195], v[160:163]\ns_add_u32 s76, s76, 0x1c000\nds_read_b64_tr_b16 a[64:65], v248 offset:0\nv_fma_f32 v134, v134, v149, v153\n"
"v_mfma_f32_16x16x32_bf16 v[160:163], a[100:103], a[208:211], v[160:163]\ns_add_u32 m0, s73, 0x18000\nds_read_b64_tr_b16 a[66:67], v248 offset:512\nv_fma_f32 v135, v135, v149, v153\n"
"v_mfma_f32_16x16x32_bf16 v[160:163], a[104:107], a[224:227], v[160:163]\nbuffer_load_dwordx4 v250, s[36:39], s76 offen lds\nv_mfma_f32_16x16x32_bf16 v[160:163], a[108:111], a[240:243], v[160:163]\ns_add_u32 s76, s76, 0x1c000\n"
"ds_read_b64_tr_b16 a[68:69], v248 offset:4096\nv_fma_f32 v136, v136, v150, v154\nv_mfma_f32_16x16x32_bf16 v[160:163], a[112:115], v[0:3], v[160:163]\ns_add_u32 m0, s73, 0x19000\n"
"ds_read_b64_tr_b16 a[70:71], v248 offset:4608\nv_fma_f32 v137, v137, v150, v154\nv_mfma_f32_16x16x32_bf16 v[160:163], a[116:119], v[16:19], v[160:163]\nbuffer_load_dwordx4 v250, s[36:39], s76 offen lds\n"
"v_mfma_f32_16x16x32_bf16 v[160:163], a[120:123], v[32:35], v[160:163]\nds_read_b64_tr_b16 a[72:73], v248 offset:8192\nv_cvt_pk_bf16_f32 v49, v194, v195\nv_mfma_f32_16x16x32_bf16 v[164:167], a[120:123], v[36:39], v[164:167]\n"
"s_add_u32 s76, s76, 0x1c000\nds_read_b64_tr_b16 a[74:75], v248 offset:8704\nv_fma_f32 v138, v138, v150, v154\nv_mfma_f32_16x16x32_bf16 v[164:167], a[116:119], v[20:23], v[164:167]\n"
"s_add_u32 m0, s73, 0x1a000\nds_read_b64_tr_b16 a[76:77], v248 offset:12288\nv_mul_f32 v204, v204, v239\nv_mfma_f32_16x16x32_bf16 v[164:167], a[112:115], v[4:7], v[164:167]\n"
"buffer_load_dwordx4 v250, s[36:39], s76 offen lds\nv_mfma_f32_16x16x32_bf16 v[164:167], a[108:111], a[244:247], v[164:167]\nds_read_b64_tr_b16 a[78:79], v248 offset:12800\nv_fma_f32 v139, v139, v150, v154\n"
"v_mfma_f32_16x16x32_bf16 v[164:167], a[104:107], a[228:231], v[164:167]\ns_add_u32 s76, s76, 0x1c000\nds_read_b64_tr_b16 a[80:81], v248 offset:16384\nv_mul_f32 v205, v205, v239\n"
"v_mfma_f32_16x16x32_bf16 v[164:167], a[100:103], a[212:215], v[164:167]\ns_add_u32 m0, s73, 0x1b000\nds_read_b64_tr_b16 a[82:83], v248 offset:16896\nv_mul_f32 v206, v206, v239\n"
"v_mfma_f32_16x16x32_bf16 v[164:167], a[96:99], a[196:199], v[164:167]\nbuffer_load_dwordx4 v250, s[36:39], s76 offen lds\nv_mfma_f32_16x16x32_bf16 v[168:171], a[96:99], a[200:203], v[168:171]\ns_add_u32 s76, s76, 0x1c000\n"
"ds_read_b64_tr_b16 a[84:85], v248 offset:20480\nv_mul_f32 v207, v207, v239\nv_mfma_f32_16x16x32_bf16 v[168:171], a[100:103], a[216:219], v[168:171]\ns_add_u32 m0, s73, 0x1c000\n"
"ds_read_b64_tr_b16 a[86:87], v248 offset:20992\nv_fma_f32 v140, v140, v151, v155\nv_mfma_f32_16x16x32_bf16 v[168:171], a[104:107], a[232:235], v[168:171]\nbuffer_load_dwordx4 v250, s[36:39], s76 offen lds\n"
"v_mfma_f32_16x16x32_bf16 v[168:171], a[108:111], a[248:251], v[168:171]\nds_read_b64_tr_b16 a[88:89], v248 offset:24576\nv_fma_f32 v141, v141, v151, v155\nv_mfma_f32_16x16x32_bf16 v[168:171], a[112:115], v[8:11], v[168:171]\n"
"s_add_u32 s76, s76, 0x1c000\nds_read_b64_tr_b16 a[90:91], v248 offset:25088\nv_fma_f32 v142, v142, v151, v155\nv_mfma_f32_16x16x32_bf16 v[168:171], a[116:119], v[24:27], v[168:171]\n"
"s_add_u32 m0, s73, 0x1d000\nv_cvt_pk_bf16_f32 v50, v196, v197\nv_fma_f32 v143, v143, v151, v155\nv_mfma_f32_16x16x32_bf16 v[168:171], a[120:123], v[40:43], v[168:171]\n"
"buffer_load_dwordx4 v250, s[36:39], s76 offen lds\nv_mfma_f32_16x16x32_bf16 v[172:175], a[120:123], v[44:47], v[172:175]\nv_cvt_pk_bf16_f32 v51, v198, v199\nv_mul_f32 v128, v128, v176\n"
"v_mfma_f32_16x16x32_bf16 v[172:175], a[116:119], v[28:31], v[172:175]\ns_add_u32 s76, s76, 0x1c000\nv_rcp_f32 v131, v131\nv_mfma_f32_16x16x32_bf16 v[172:175], a[112:115], v[12:15], v[172:175]\n"
"s_add_u32 m0, s73, 0x1e000\nv_mul_f32 v129, v129, v177\nv_mul_f32 v132, v132, v180\nv_mfma_f32_16x16x32_bf16 v[172:175], a[108:111], a[252:255], v[172:175]\n"
"buffer_load_dwordx4 v250, s[36:39], s76 offen lds\nv_mfma_f32_16x16x32_bf16 v[172:175], a[104:107], a[236:239], v[172:175]\nv_mul_f32 v130, v130, v178\nv_mul_f32 v133, v133, v181\n"
"v_mfma_f32_16x16x32_bf16 v[172:175], a[100:103], a[220:223], v[172:175]\nv_cvt_pk_bf16_f32 v52, v200, v201\nv_mul_f32 v134, v134, v182\nv_mfma_f32_16x16x32_bf16 v[172:175], a[96:99], a[204:207], v[172:175]\n"
"v_cvt_pk_bf16_f32 v53, v202, v203\nv_mul_f32 v135, v135, v183\ns_waitcnt vmcnt(14) lgkmcnt(0)\ns_barrier\n"
"v_mfma_f32_16x16x32_bf16 v[192:195], a[64:67], a[128:131], 0\ns_add_u32 s77, s77, 0xffe94080\nv_mul_f32 v136, v136, v184\nv_mul_f32 v137, v137, v185\n"
"v_mfma_f32_16x16x32_bf16 v[192:195], a[68:71], a[144:147], v[192:195]\ns_add_u32 m0, s73, 0x0\nds_read_b64_tr_b16 a[96:97], v249 offset:0\nv_mul_f32 v138, v138, v186\n"
"v_mfma_f32_16x16x32_bf16 v[192:195], a[72:75], a[160:163], v[192:195]\nbuffer_load_dwordx4 v250, s[40:43], s77 offen lds\nv_mfma_f32_16x16x32_bf16 v[192:195], a[76:79], a[176:179], v[192:195]\ns_add_u32 s77, s77, 0x1c000\n"
"ds_read_b64_tr_b16 a[98:99], v249 offset:512\nv_mul_f32 v139, v139, v187\nv_mfma_f32_16x16x32_bf16 v[192:195], a[80:83], a[0:3], v[192:195]\ns_add_u32 m0, s73, 0x1000\n"
"ds_read_b64_tr_b16 a[100:101], v249 offset:4096\nv_cvt_pk_bf16_f32 v54, v204, v205\nv_mfma_f32_16x16x32_bf16 v[192:195], a[84:87], a[16:19], v[192:195]\nbuffer_load_dwordx4 v250, s[40:43], s77 offen lds\n"
"v_mfma_f32_16x16x32_bf16 v[192:195], a[88:91], a[32:35], v[192:195]\nds_read_b64_tr_b16 a[102:103], v249 offset:4608\nv_cvt_pk_bf16_f32 v55, v206, v207\nv_mfma_f32_16x16x32_bf16 v[196:199], a[88:91], a[36:39], 0\n"
"s_add_u32 s77, s77, 0x1c000\nds_read_b64_tr_b16 a[104:105], v249 offset:8192\nv_mul_f32 v140, v140, v188\nv_mfma_f32_16x16x32_bf16 v[196:199], a[84:87], a[20:23], v[196:199]\n"
"s_add_u32 m0, s73, 0x2000\nds_read_b64_tr_b16 a[106:107], v249 offset:8704\nv_mul_f32 v141, v141, v189\nv_mfma_f32_16x16x32_bf16 v[196:199], a[80:83], a[4:7], v[196:199]\n"
"buffer_load_dwordx4 v250, s[40:43], s77 offen lds\nv_mfma_f32_16x16x32_bf16 v[196:199], a[76:79], a[180:183], v[196:199]\nds_read_b64_tr_b16 a[108:109], v249 offset:12288\nv_mul_f32 v142, v142, v190\n"
"v_mfma_f32_16x16x32_bf16 v[196:199], a[72:75], a[164:167], v[196:199]\ns_add_u32 s77, s77, 0x1c000\nds_read_b64_tr_b16 a[110:111], v249 offset:12800\nv_mul_f32 v143, v143, v191\n"
"v_mfma_f32_16x16x32_bf16 v[196:199], a[68:71], a[148:151], v[196:199]\ns_add_u32 m0, s73, 0x3000\nds_read_b64_tr_b16 a[112:113], v249 offset:16384\nv_mul_f32 v131, v131, v179\n"
"v_mfma_f32_16x16x32_bf16 v[196:199], a[64:67], a[132:135], v[196:199]\nbuffer_load_dwordx4 v250, s[40:43], s77 offen lds\nv_mfma_f32_16x16x32_bf16 v[200:203], a[64:67], a[136:139], 0\ns_add_u32 s77, s77, 0x1c000\n"
"ds_read_b64_tr_b16 a[114:115], v249 offset:16896\nv_mul_f32 v128, v128, v160\nv_mfma_f32_16x16x32_bf16 v[200:203], a[68:71], a[152:155], v[200:203]\ns_add_u32 m0, s73, 0x4000\n"
"ds_read_b64_tr_b16 a[116:117], v249 offset:20480\nv_mul_f32 v129, v129, v161\nv_mfma_f32_16x16x32_bf16 v[200:203], a[72:75], a[168:171], v[200:203]\nbuffer_load_dwordx4 v250, s[40:43], s77 offen lds\n"
"v_mfma_f32_16x16x32_bf16 v[200:203], a[76:79], a[184:187], v[200:203]\nds_read_b64_tr_b16 a[118:119], v249 offset:20992\nv_mul_f32 v130, v130, v162\nv_mfma_f32_16x16x32_bf16 v[200:203], a[80:83], a[8:11], v[200:203]\n"
"s_add_u32 s77, s77, 0x1c000\nds_read_b64_tr_b16 a[120:121], v249 offset:24576\nv_mul_f32 v131, v131, v163\nv_mfma_f32_16x16x32_bf16 v[200:203], a[84:87], a[24:27], v[200:203]\n"
"s_add_u32 m0, s73, 0x5000\nds_read_b64_tr_b16 a[122:123], v249 offset:25088\nv_cvt_pk_bf16_f32 v114, v128, v129\nv_mfma_f32_16x16x32_bf16 v[200:203], a[88:91], a[40:43], v[200:203]\n"
"buffer_load_dwordx4 v250, s[40:43], s77 offen lds\nv_mfma_f32_16x16x32_bf16 v[204:207], a[88:91], a[44:47], 0\nv_exp_f32 v132, v132\nv_mfma_f32_16x16x32_bf16 v[204:207], a[84:87], a[28:31], v[204:207]\n"
"s_add_u32 s77, s77, 0x1c000\nv_exp_f32 v133, v133\nv_mfma_f32_16x16x32_bf16 v[204:207], a[80:83], a[12:15], v[204:207]\ns_add_u32 m0, s73, 0x6000\n"
"v_exp_f32 v134, v134\nv_mfma_f32_16x16x32_bf16 v[204:207], a[76:79], a[188:191], v[204:207]\nbuffer_load_dwordx4 v250, s[40:43], s77 offen lds\nv_mfma_f32_16x16x32_bf16 v[204:207], a[72:75], a[172:175], v[204:207]\n"
"v_exp_f32 v135, v135\nv_mfma_f32_16x16x32_bf16 v[204:207], a[68:71], a[156:159], v[204:207]\nv_exp_f32 v136, v136\nv_mfma_f32_16x16x32_bf16 v[204:207], a[64:67], a[140:143], v[204:207]\n"
"v_exp_f32 v137, v137\ns_waitcnt vmcnt(14) lgkmcnt(0)\ns_barrier\nv_mfma_f32_16x16x32_bf16 v[192:195], a[96:99], a[192:195], v[192:195]\n"
"s_add_u32 s77, s77, 0x1c000\nv_exp_f32 v138, v138\nv_mfma_f32_16x16x32_bf16 v[192:195], a[100:103], a[208:211], v[192:195]\ns_add_u32 m0, s73, 0x10000\n"
"v_exp_f32 v139, v139\nv_mfma_f32_16x16x32_bf16 v[192:195], a[104:107], a[224:227], v[192:195]\nbuffer_load_dwordx4 v250, s[40:43], s77 offen lds\nv_mfma_f32_16x16x32_bf16 v[192:195], a[108:111], a[240:243], v[192:195]\n"
"s_add_u32 s77, s77, 0x1c000\nv_exp_f32 v140, v140\nv_mfma_f32_16x16x32_bf16 v[192:195], a[112:115], v[0:3], v[192:195]\ns_add_u32 m0, s73, 0x11000\n"
"v_exp_f32 v141, v141\nv_mfma_f32_16x16x32_bf16 v[192:195], a[116:119], v[16:19], v[192:195]\nbuffer_load_dwordx4 v250, s[40:43], s77 offen lds\nv_mfma_f32_16x16x32_bf16 v[192:195], a[120:123], v[32:35], v[192:195]\n"
"v_exp_f32 v142, v142\nv_mfma_f32_16x16x32_bf16 v[196:199], a[120:123], v[36:39], v[196:199]\ns_add_u32 s77, s77, 0x1c000\nv_exp_f32 v143, v143\n"
"v_mfma_f32_16x16x32_bf16 v[196:199], a[116:119], v[20:23], v[196:199]\ns_add_u32 m0, s73, 0x12000\nv_fma_f32 v132, v132, v157, v157\nv_cvt_pk_bf16_f32 v115, v130, v131\n"
"v_mfma_f32_16x16x32_bf16 v[196:199], a[112:115], v[4:7], v[196:199]\nbuffer_load_dwordx4 v250, s[40:43], s77 offen lds\nv_mfma_f32_16x16x32_bf16 v[196:199], a[108:111], a[244:247], v[196:199]\nds_read_b64_tr_b16 a[64:65], v248 offset:32768\n"
"v_fma_f32 v133, v133, v157, v157\nv_mfma_f32_16x16x32_bf16 v[196:199], a[104:107], a[228:231], v[196:199]\ns_add_u32 s77, s77, 0x1c000\nds_read_b64_tr_b16 a[66:67], v248 offset:33280\n"
"v_fma_f32 v134, v134, v157, v157\nv_mfma_f32_16x16x32_bf16 v[196:199], a[100:103], a[212:215], v[196:199]\ns_add_u32 m0, s73, 0x13000\nds_read_b64_tr_b16 a[68:69], v248 offset:36864\n"
"v_fma_f32 v135, v135, v157, v157\nv_mfma_f32_16x16x32_bf16 v[196:199], a[96:99], a[196:199], v[196:199]\nbuffer_load_dwordx4 v250, s[40:43], s77 offen lds\nv_mfma_f32_16x16x32_bf16 v[200:203], a[96:99], a[200:203], v[200:203]\n"
"s_add_u32 s77, s77, 0x1c000\nds_read_b64_tr_b16 a[70:71], v248 offset:37376\nv_fma_f32 v136, v136, v158, v158\nv_mfma_f32_16x16x32_bf16 v[200:203], a[100:103], a[216:219], v[200:203]\n"
"s_add_u32 m0, s73, 0x14000\nds_read_b64_tr_b16 a[72:73], v248 offset:40960\nv_fma_f32 v137, v137, v158, v158\nv_mfma_f32_16x16x32_bf16 v[200:203], a[104:107], a[232:235], v[200:203]\n"
"buffer_load_dwordx4 v250, s[40:43], s77 offen lds\nv_mfma_f32_16x16x32_bf16 v[200:203], a[108:111], a[248:251], v[200:203]\nds_read_b64_tr_b16 a[74:75], v248 offset:41472\nv_fma_f32 v138, v138, v158, v158\n"
"v_mfma_f32_16x16x32_bf16 v[200:203], a[112:115], v[8:11], v[200:203]\ns_add_u32 s77, s77, 0x1c000\nds_read_b64_tr_b16 a[76:77], v248 offset:45056\nv_fma_f32 v139, v139, v158, v158\n"
"v_mfma_f32_16x16x32_bf16 v[200:203], a[116:119], v[24:27], v[200:203]\ns_add_u32 m0, s73, 0x15000\nds_read_b64_tr_b16 a[78:79], v248 offset:45568\nv_fma_f32 v140, v140, v159, v159\n"
"v_mfma_f32_16x16x32_bf16 v[200:203], a[120:123], v[40:43], v[200:203]\nbuffer_load_dwordx4 v250, s[40:43], s77 offen lds\nv_mfma_f32_16x16x32_bf16 v[204:207], a[120:123], v[44:47], v[204:207]\nds_read_b64_tr_b16 a[80:81], v248 offset:49152\n"
"v_fma_f32 v141, v141, v159, v159\nv_mfma_f32_16x16x32_bf16 v[204:207], a[116:119], v[28:31], v[204:207]\ns_add_u32 s77, s77, 0x1c000\nds_read_b64_tr_b16 a[82:83], v248 offset:49664\n"
"v_fma_f32 v142, v142, v159, v159\nv_mfma_f32_16x16x32_bf16 v[204:207], a[112:115], v[12:15], v[204:207]\ns_add_u32 m0, s73, 0x16000\nds_read_b64_tr_b16 a[84:85], v248 offset:53248\n"
"v_fma_f32 v143, v143, v159, v159\nv_mfma_f32_16x16x32_bf16 v[204:207], a[108:111], a[252:255], v[204:207]\nbuffer_load_dwordx4 v250, s[40:43], s77 offen lds\nv_mfma_f32_16x16x32_bf16 v[204:207], a[104:107], a[236:239], v[204:207]\n"
"ds_read_b64_tr_b16 a[86:87], v248 offset:53760\nv_mul_f32 v208, v208, v236\nv_mfma_f32_16x16x32_bf16 v[204:207], a[100:103], a[220:223], v[204:207]\nds_read_b64_tr_b16 a[88:89], v248 offset:57344\n"
"v_mul_f32 v209, v209, v236\nv_mfma_f32_16x16x32_bf16 v[204:207], a[96:99], a[204:207], v[204:207]\nds_read_b64_tr_b16 a[90:91], v248 offset:57856\nv_mul_f32 v210, v210, v236\n"
"s_waitcnt vmcnt(14) lgkmcnt(0)\ns_barrier\nv_mfma_f32_16x16x32_bf16 v[176:179], a[64:67], a[128:131], 0\ns_add_u32 s78, s78, 0xffcc0200\n"
"v_rcp_f32 v132, v132\nv_mfma_f32_16x16x32_bf16 v[176:179], a[68:71], a[144:147], v[176:179]\ns_add_u32 m0, s73, 0x8000\nv_rcp_f32 v133, v133\n"
"v_mfma_f32_16x16x32_bf16 v[176:179], a[72:75], a[160:163], v[176:179]\nbuffer_load_dwordx4 v251, s[44:47], s78 offen lds\nv_mfma_f32_16x16x32_bf16 v[176:179], a[76:79], a[176:179], v[176:179]\ns_add_u32 s78, s78, 0x40000\n"
"v_rcp_f32 v134, v134\nv_mfma_f32_16x16x32_bf16 v[176:179], a[80:83], a[0:3], v[176:179]\ns_add_u32 m0, s73, 0x9000\nv_rcp_f32 v135, v135\n"
"v_mfma_f32_16x16x32_bf16 v[176:179], a[84:87], a[16:19], v[176:179]\nbuffer_load_dwordx4 v251, s[44:47], s78 offen lds\nv_mfma_f32_16x16x32_bf16 v[176:179], a[88:91], a[32:35], v[176:179]\nv_rcp_f32 v136, v136\n"
"v_mul_f32 v132, v132, v180\nv_mul_f32 v133, v133, v181\nv_mul_f32 v134, v134, v182\nv_mul_f32 v135, v135, v183\n"
"v_mfma_f32_16x16x32_bf16 v[180:183], a[88:91], a[36:39], 0\ns_add_u32 s78, s78, 0x40000\nv_rcp_f32 v137, v137\nv_mfma_f32_16x16x32_bf16 v[180:183], a[84:87], a[20:23], v[180:183]\n"
"s_add_u32 m0, s73, 0xa000\nv_rcp_f32 v138, v138\nv_mfma_f32_16x16x32_bf16 v[180:183], a[80:83], a[4:7], v[180:183]\nbuffer_load_dwordx4 v251, s[44:47], s78 offen lds\n"
"v_mfma_f32_16x16x32_bf16 v[180:183], a[76:79], a[180:183], v[180:183]\nv_rcp_f32 v139, v139\nv_mfma_f32_16x16x32_bf16 v[180:183], a[72:75], a[164:167], v[180:183]\ns_add_u32 s78, s78, 0x40000\n"
"v_rcp_f32 v140, v140\nv_mfma_f32_16x16x32_bf16 v[180:183], a[68:71], a[148:151], v[180:183]\ns_add_u32 m0, s73, 0xb000\nv_rcp_f32 v141, v141\n"
"v_mfma_f32_16x16x32_bf16 v[180:183], a[64:67], a[132:135], v[180:183]\nbuffer_load_dwordx4 v251, s[44:47], s78 offen lds\nv_mul_f32 v136, v136, v184\nv_mul_f32 v137, v137, v185\n"
"v_mul_f32 v138, v138, v186\nv_mul_f32 v139, v139, v187\nv_mfma_f32_16x16x32_bf16 v[184:187], a[64:67], a[136:139], 0\ns_add_u32 s78, s78, 0x40000\n"
"v_rcp_f32 v142, v142\nv_mfma_f32_16x16x32_bf16 v[184:187], a[68:71], a[152:155], v[184:187]\ns_add_u32 m0, s73, 0xc000\nv_rcp_f32 v143, v143\n"
"v_mfma_f32_16x16x32_bf16 v[184:187], a[72:75], a[168:171], v[184:187]\nbuffer_load_dwordx4 v251, s[44:47], s78 offen lds\nv_mfma_f32_16x16x32_bf16 v[184:187], a[76:79], a[184:187], v[184:187]\nds_read_b64_tr_b16 a[96:97], v249 offset:32768\n"
"v_mul_f32 v140, v140, v188\nv_mfma_f32_16x16x32_bf16 v[184:187], a[80:83], a[8:11], v[184:187]\ns_add_u32 s78, s78, 0x40000\nds_read_b64_tr_b16 a[98:99], v249 offset:33280\n"
"v_mul_f32 v141, v141, v189\nv_mfma_f32_16x16x32_bf16 v[184:187], a[84:87], a[24:27], v[184:187]\ns_add_u32 m0, s73, 0xd000\nds_read_b64_tr_b16 a[100:101], v249 offset:36864\n"
"v_mul_f32 v142, v142, v190\nv_mfma_f32_16x16x32_bf16 v[184:187], a[88:91], a[40:43], v[184:187]\nbuffer_load_dwordx4 v251, s[44:47], s78 offen lds\nv_mul_f32 v143, v143, v191\n"
"v_mfma_f32_16x16x32_bf16 v[188:191], a[88:91], a[44:47], 0\nds_read_b64_tr_b16 a[102:103], v249 offset:37376\nv_mul_f32 v132, v132, v164\nv_mfma_f32_16x16x32_bf16 v[188:191], a[84:87], a[28:31], v[188:191]\n"
"s_add_u32 s78, s78, 0x40000\nds_read_b64_tr_b16 a[104:105], v249 offset:40960\nv_mul_f32 v133, v133, v165\nv_mfma_f32_16x16x32_bf16 v[188:191], a[80:83], a[12:15], v[188:191]\n"
"s_add_u32 m0, s73, 0xe000\nds_read_b64_tr_b16 a[106:107], v249 offset:41472\nv_mul_f32 v134, v134, v166\nv_mfma_f32_16x16x32_bf16 v[188:191], a[76:79], a[188:191], v[188:191]\n"
"buffer_load_dwordx4 v251, s[44:47], s78 offen lds\nv_mfma_f32_16x16x32_bf16 v[188:191], a[72:75], a[172:175], v[188:191]\nbuffer_store_dwordx4 v[112:115], v224, s[60:63], s82 offen offset:0 sc1\nv_mfma_f32_16x16x32_bf16 v[188:191], a[68:71], a[156:159], v[188:191]\n"
"ds_read_b64_tr_b16 a[108:109], v249 offset:45056\nv_mul_f32 v135, v135, v167\nv_mfma_f32_16x16x32_bf16 v[188:191], a[64:67], a[140:143], v[188:191]\nds_read_b64_tr_b16 a[110:111], v249 offset:45568\n"
"v_cvt_pk_bf16_f32 v118, v132, v133\nds_read_b64_tr_b16 a[112:113], v249 offset:49152\nds_read_b64_tr_b16 a[114:115], v249 offset:49664\nds_read_b64_tr_b16 a[116:117], v249 offset:53248\n"
"ds_read_b64_tr_b16 a[118:119], v249 offset:53760\nds_read_b64_tr_b16 a[120:121], v249 offset:57344\nds_read_b64_tr_b16 a[122:123], v249 offset:57856\ns_waitcnt vmcnt(15) lgkmcnt(0)\n"
"s_barrier\nv_mfma_f32_16x16x32_bf16 v[176:179], a[96:99], a[192:195], v[176:179]\ns_add_u32 s78, s78, 0x40000\nv_cvt_pk_bf16_f32 v119, v134, v135\n"
"ds_read_b64_tr_b16 a[64:65], v248 offset:0\nv_mfma_f32_16x16x32_bf16 v[176:179], a[100:103], a[208:211], v[176:179]\ns_add_u32 m0, s73, 0x18000\nds_read_b64_tr_b16 a[66:67], v248 offset:512\n"
"v_mul_f32 v136, v136, v168\nv_mfma_f32_16x16x32_bf16 v[176:179], a[104:107], a[224:227], v[176:179]\nbuffer_load_dwordx4 v251, s[44:47], s78 offen lds\nv_mfma_f32_16x16x32_bf16 v[176:179], a[108:111], a[240:243], v[176:179]\n"
"s_add_u32 s78, s78, 0x40000\nds_read_b64_tr_b16 a[68:69], v248 offset:4096\nv_mul_f32 v137, v137, v169\nv_mfma_f32_16x16x32_bf16 v[176:179], a[112:115], v[0:3], v[176:179]\n"
"s_add_u32 m0, s73, 0x19000\nds_read_b64_tr_b16 a[70:71], v248 offset:4608\nv_mul_f32 v138, v138, v170\nv_mfma_f32_16x16x32_bf16 v[176:179], a[116:119], v[16:19], v[176:179]\n"
"buffer_load_dwordx4 v251, s[44:47], s78 offen lds\nv_mfma_f32_16x16x32_bf16 v[176:179], a[120:123], v[32:35], v[176:179]\nds_read_b64_tr_b16 a[72:73], v248 offset:8192\nv_mul_f32 v139, v139, v171\n"
"v_mfma_f32_16x16x32_bf16 v[180:183], a[120:123], v[36:39], v[180:183]\ns_add_u32 s78, s78, 0x40000\nds_read_b64_tr_b16 a[74:75], v248 offset:8704\nv_cvt_pk_bf16_f32 v122, v136, v137\n"
"v_mfma_f32_16x16x32_bf16 v[180:183], a[116:119], v[20:23], v[180:183]\ns_add_u32 m0, s73, 0x1a000\nds_read_b64_tr_b16 a[76:77], v248 offset:12288\nv_cvt_pk_bf16_f32 v123, v138, v139\n"
"v_mfma_f32_16x16x32_bf16 v[180:183], a[112:115], v[4:7], v[180:183]\nbuffer_load_dwordx4 v251, s[44:47], s78 offen lds\nv_mfma_f32_16x16x32_bf16 v[180:183], a[108:111], a[244:247], v[180:183]\nds_read_b64_tr_b16 a[78:79], v248 offset:12800\n"
"v_mul_f32 v140, v140, v172\nv_mfma_f32_16x16x32_bf16 v[180:183], a[104:107], a[228:231], v[180:183]\ns_add_u32 s78, s78, 0x40000\nds_read_b64_tr_b16 a[80:81], v248 offset:16384\n"
"v_mul_f32 v141, v141, v173\nv_mfma_f32_16x16x32_bf16 v[180:183], a[100:103], a[212:215], v[180:183]\ns_add_u32 m0, s73, 0x1b000\nds_read_b64_tr_b16 a[82:83], v248 offset:16896\n"
"v_mul_f32 v142, v142, v174\nv_mfma_f32_16x16x32_bf16 v[180:183], a[96:99], a[196:199], v[180:183]\nbuffer_load_dwordx4 v251, s[44:47], s78 offen lds\nv_mfma_f32_16x16x32_bf16 v[184:187], a[96:99], a[200:203], v[184:187]\n"
"s_add_u32 s78, s78, 0x40000\nds_read_b64_tr_b16 a[84:85], v248 offset:20480\nv_mul_f32 v143, v143, v175\nv_mfma_f32_16x16x32_bf16 v[184:187], a[100:103], a[216:219], v[184:187]\n"
"s_add_u32 m0, s73, 0x1c000\nds_read_b64_tr_b16 a[86:87], v248 offset:20992\nv_cvt_pk_bf16_f32 v126, v140, v141\nv_mfma_f32_16x16x32_bf16 v[184:187], a[104:107], a[232:235], v[184:187]\n"
"buffer_load_dwordx4 v251, s[44:47], s78 offen lds\nv_mfma_f32_16x16x32_bf16 v[184:187], a[108:111], a[248:251], v[184:187]\nds_read_b64_tr_b16 a[88:89], v248 offset:24576\nv_cvt_pk_bf16_f32 v127, v142, v143\n"
"v_mfma_f32_16x16x32_bf16 v[184:187], a[112:115], v[8:11], v[184:187]\ns_add_u32 s78, s78, 0x40000\nds_read_b64_tr_b16 a[90:91], v248 offset:25088\nv_mul_f32 v211, v211, v236\n"
"v_mfma_f32_16x16x32_bf16 v[184:187], a[116:119], v[24:27], v[184:187]\ns_add_u32 m0, s73, 0x1d000\nv_mul_f32 v212, v212, v237\nv_mul_f32 v213, v213, v237\n"
"v_mfma_f32_16x16x32_bf16 v[184:187], a[120:123], v[40:43], v[184:187]\nbuffer_load_dwordx4 v251, s[44:47], s78 offen lds\nv_mfma_f32_16x16x32_bf16 v[188:191], a[120:123], v[44:47], v[188:191]\nv_mul_f32 v214, v214, v237\n"
"v_mul_f32 v215, v215, v237\nv_mfma_f32_16x16x32_bf16 v[188:191], a[116:119], v[28:31], v[188:191]\ns_add_u32 s78, s78, 0x40000\nv_cvt_pk_bf16_f32 v56, v208, v209\n"
"v_cvt_pk_bf16_f32 v57, v210, v211\nv_mfma_f32_16x16x32_bf16 v[188:191], a[112:115], v[12:15], v[188:191]\ns_add_u32 m0, s73, 0x1e000\nv_mul_f32 v216, v216, v238\n"
"v_mul_f32 v217, v217, v238\nv_mfma_f32_16x16x32_bf16 v[188:191], a[108:111], a[252:255], v[188:191]\nbuffer_load_dwordx4 v251, s[44:47], s78 offen lds\nv_mfma_f32_16x16x32_bf16 v[188:191], a[104:107], a[236:239], v[188:191]\n"
"buffer_store_dwordx4 v[116:119], v224, s[60:63], s82 offen offset:1024 sc1\nv_mfma_f32_16x16x32_bf16 v[188:191], a[100:103], a[220:223], v[188:191]\nv_mul_f32 v218, v218, v238\nv_mul_f32 v219, v219, v238\n"
"v_mfma_f32_16x16x32_bf16 v[188:191], a[96:99], a[204:207], v[188:191]\nv_mul_f32 v220, v220, v239\nv_mul_f32 v221, v221, v239\ns_waitcnt vmcnt(16) lgkmcnt(0)\n"
"s_barrier\nv_mfma_f32_16x16x32_bf16 v[160:163], a[64:67], a[128:131], 0\ns_add_u32 s78, s78, 0xffcbfe80\nds_read_b64_tr_b16 a[96:97], v249 offset:0\n"
"v_cvt_pk_bf16_f32 v58, v212, v213\nv_mfma_f32_16x16x32_bf16 v[160:163], a[68:71], a[144:147], v[160:163]\ns_add_u32 m0, s73, 0x0\nds_read_b64_tr_b16 a[98:99], v249 offset:512\n"
"v_cvt_pk_bf16_f32 v59, v214, v215\nv_mfma_f32_16x16x32_bf16 v[160:163], a[72:75], a[160:163], v[160:163]\nbuffer_load_dwordx4 v251, s[44:47], s78 offen lds\nv_mfma_f32_16x16x32_bf16 v[160:163], a[76:79], a[176:179], v[160:163]\n"
"s_add_u32 s78, s78, 0x40000\nds_read_b64_tr_b16 a[100:101], v249 offset:4096\nv_mul_f32 v222, v222, v239\nv_mfma_f32_16x16x32_bf16 v[160:163], a[80:83], a[0:3], v[160:163]\n"
"s_add_u32 m0, s73, 0x1000\nds_read_b64_tr_b16 a[102:103], v249 offset:4608\nv_mul_f32 v223, v223, v239\nv_mfma_f32_16x16x32_bf16 v[160:163], a[84:87], a[16:19], v[160:163]\n"
"buffer_load_dwordx4 v251, s[44:47], s78 offen lds\nv_mfma_f32_16x16x32_bf16 v[160:163], a[88:91], a[32:35], v[160:163]\nds_read_b64_tr_b16 a[104:105], v249 offset:8192\nv_mul_f32 v128, v176, v176\n"
"v_mfma_f32_16x16x32_bf16 v[164:167], a[88:91], a[36:39], 0\ns_add_u32 s78, s78, 0x40000\nds_read_b64_tr_b16 a[106:107], v249 offset:8704\nv_mul_f32 v129, v177, v177\n"
"v_mfma_f32_16x16x32_bf16 v[164:167], a[84:87], a[20:23], v[164:167]\ns_add_u32 m0, s73, 0x2000\nds_read_b64_tr_b16 a[108:109], v249 offset:12288\nv_mul_f32 v130, v178, v178\n"
"v_mfma_f32_16x16x32_bf16 v[164:167], a[80:83], a[4:7], v[164:167]\nbuffer_load_dwordx4 v251, s[44:47], s78 offen lds\nv_mfma_f32_16x16x32_bf16 v[164:167], a[76:79], a[180:183], v[164:167]\nds_read_b64_tr_b16 a[110:111], v249 offset:12800\n"
"v_mul_f32 v131, v179, v179\nv_mfma_f32_16x16x32_bf16 v[164:167], a[72:75], a[164:167], v[164:167]\ns_add_u32 s78, s78, 0x40000\nds_read_b64_tr_b16 a[112:113], v249 offset:16384\n"
"v_cvt_pk_bf16_f32 v60, v216, v217\nv_mfma_f32_16x16x32_bf16 v[164:167], a[68:71], a[148:151], v[164:167]\ns_add_u32 m0, s73, 0x3000\nds_read_b64_tr_b16 a[114:115], v249 offset:16896\n"
"v_cvt_pk_bf16_f32 v61, v218, v219\nv_mfma_f32_16x16x32_bf16 v[164:167], a[64:67], a[132:135], v[164:167]\nbuffer_load_dwordx4 v251, s[44:47], s78 offen lds\nv_mfma_f32_16x16x32_bf16 v[168:171], a[64:67], a[136:139], 0\n"
"s_add_u32 s78, s78, 0x40000\nds_read_b64_tr_b16 a[116:117], v249 offset:20480\nv_cvt_pk_bf16_f32 v62, v220, v221\nv_mfma_f32_16x16x32_bf16 v[168:171], a[68:71], a[152:155], v[168:171]\n"
"s_add_u32 m0, s73, 0x4000\nds_read_b64_tr_b16 a[118:119], v249 offset:20992\nv_cvt_pk_bf16_f32 v63, v222, v223\nv_mfma_f32_16x16x32_bf16 v[168:171], a[72:75], a[168:171], v[168:171]\n"
"buffer_load_dwordx4 v251, s[44:47], s78 offen lds\nv_mfma_f32_16x16x32_bf16 v[168:171], a[76:79], a[184:187], v[168:171]\nds_read_b64_tr_b16 a[120:121], v249 offset:24576\nv_fma_f32 v128, v128, v148, v152\n"
"v_mfma_f32_16x16x32_bf16 v[168:171], a[80:83], a[8:11], v[168:171]\ns_add_u32 s78, s78, 0x40000\nds_read_b64_tr_b16 a[122:123], v249 offset:25088\nv_fma_f32 v129, v129, v148, v152\n"
"v_mfma_f32_16x16x32_bf16 v[168:171], a[84:87], a[24:27], v[168:171]\ns_add_u32 m0, s73, 0x5000\nv_fma_f32 v130, v130, v148, v152\nv_fma_f32 v131, v131, v148, v152\n"
"v_mfma_f32_16x16x32_bf16 v[168:171], a[88:91], a[40:43], v[168:171]\nbuffer_load_dwordx4 v251, s[44:47], s78 offen lds\nv_mfma_f32_16x16x32_bf16 v[172:175], a[88:91], a[44:47], 0\nv_mul_f32 v132, v180, v180\n"
"v_mul_f32 v133, v181, v181\nv_mfma_f32_16x16x32_bf16 v[172:175], a[84:87], a[28:31], v[172:175]\ns_add_u32 s78, s78, 0x40000\nv_mul_f32 v134, v182, v182\n"
"v_mul_f32 v135, v183, v183\nv_mfma_f32_16x16x32_bf16 v[172:175], a[80:83], a[12:15], v[172:175]\ns_add_u32 m0, s73, 0x6000\nv_mul_f32 v136, v184, v184\n"
"v_mul_f32 v137, v185, v185\nv_mfma_f32_16x16x32_bf16 v[172:175], a[76:79], a[188:191], v[172:175]\nbuffer_load_dwordx4 v251, s[44:47], s78 offen lds\nv_mfma_f32_16x16x32_bf16 v[172:175], a[72:75], a[172:175], v[172:175]\n"
"buffer_store_dwordx4 v[120:123], v224, s[60:63], s82 offen offset:2048 sc1\nv_mfma_f32_16x16x32_bf16 v[172:175], a[68:71], a[156:159], v[172:175]\nv_mul_f32 v138, v186, v186\nv_mul_f32 v139, v187, v187\n"
"v_mfma_f32_16x16x32_bf16 v[172:175], a[64:67], a[140:143], v[172:175]\nv_mul_f32 v140, v188, v188\nv_mul_f32 v141, v189, v189\ns_waitcnt vmcnt(17) lgkmcnt(0)\n"
"s_barrier\nv_mfma_f32_16x16x32_bf16 v[160:163], a[96:99], a[192:195], v[160:163]\ns_add_u32 s78, s78, 0x40000\nds_read_b64_tr_b16 a[64:65], v248 offset:32768\n"
"v_mul_f32 v142, v190, v190\nv_mfma_f32_16x16x32_bf16 v[160:163], a[100:103], a[208:211], v[160:163]\ns_add_u32 m0, s73, 0x10000\nds_read_b64_tr_b16 a[66:67], v248 offset:33280\n"
"v_mul_f32 v192, v192, v236\nv_mfma_f32_16x16x32_bf16 v[160:163], a[104:107], a[224:227], v[160:163]\nbuffer_load_dwordx4 v251, s[44:47], s78 offen lds\nv_mfma_f32_16x16x32_bf16 v[160:163], a[108:111], a[240:243], v[160:163]\n"
"s_add_u32 s78, s78, 0x40000\nds_read_b64_tr_b16 a[68:69], v248 offset:36864\nv_mul_f32 v193, v193, v236\nv_mfma_f32_16x16x32_bf16 v[160:163], a[112:115], v[0:3], v[160:163]\n"
"s_add_u32 m0, s73, 0x11000\nds_read_b64_tr_b16 a[70:71], v248 offset:37376\nv_mul_f32 v194, v194, v236\nv_mfma_f32_16x16x32_bf16 v[160:163], a[116:119], v[16:19], v[160:163]\n"
"buffer_load_dwordx4 v251, s[44:47], s78 offen lds\nv_mfma_f32_16x16x32_bf16 v[160:163], a[120:123], v[32:35], v[160:163]\nds_read_b64_tr_b16 a[72:73], v248 offset:40960\nv_mul_f32 v195, v195, v236\n"
"v_mfma_f32_16x16x32_bf16 v[164:167], a[120:123], v[36:39], v[164:167]\ns_add_u32 s78, s78, 0x40000\nds_read_b64_tr_b16 a[74:75], v248 offset:41472\nv_mul_f32 v128, v128, v176\n"
"v_mfma_f32_16x16x32_bf16 v[164:167], a[116:119], v[20:23], v[164:167]\ns_add_u32 m0, s73, 0x12000\nds_read_b64_tr_b16 a[76:77], v248 offset:45056\nv_mul_f32 v129, v129, v177\n"
"v_mfma_f32_16x16x32_bf16 v[164:167], a[112:115], v[4:7], v[164:167]\nbuffer_load_dwordx4 v251, s[44:47], s78 offen lds\nv_mfma_f32_16x16x32_bf16 v[164:167], a[108:111], a[244:247], v[164:167]\nds_read_b64_tr_b16 a[78:79], v248 offset:45568\n"
"v_mul_f32 v130, v130, v178\nv_mfma_f32_16x16x32_bf16 v[164:167], a[104:107], a[228:231], v[164:167]\ns_add_u32 s78, s78, 0x40000\nds_read_b64_tr_b16 a[80:81], v248 offset:49152\n"
"v_mul_f32 v131, v131, v179\nv_mfma_f32_16x16x32_bf16 v[164:167], a[100:103], a[212:215], v[164:167]\ns_add_u32 m0, s73, 0x13000\nds_read_b64_tr_b16 a[82:83], v248 offset:49664\n"
"v_fma_f32 v132, v132, v149, v153\nv_mfma_f32_16x16x32_bf16 v[164:167], a[96:99], a[196:199], v[164:167]\nbuffer_load_dwordx4 v251, s[44:47], s78 offen lds\nv_mfma_f32_16x16x32_bf16 v[168:171], a[96:99], a[200:203], v[168:171]\n"
"s_add_u32 s78, s78, 0x40000\nds_read_b64_tr_b16 a[84:85], v248 offset:53248\nv_mul_f32 v143, v191, v191\nv_mfma_f32_16x16x32_bf16 v[168:171], a[100:103], a[216:219], v[168:171]\n"
"s_add_u32 m0, s73, 0x14000\nds_read_b64_tr_b16 a[86:87], v248 offset:53760\nv_mul_f32 v196, v196, v237\nv_mfma_f32_16x16x32_bf16 v[168:171], a[104:107], a[232:235], v[168:171]\n"
"buffer_load_dwordx4 v251, s[44:47], s78 offen lds\nv_mfma_f32_16x16x32_bf16 v[168:171], a[108:111], a[248:251], v[168:171]\nds_read_b64_tr_b16 a[88:89], v248 offset:57344\nv_mul_f32 v197, v197, v237\n"
"v_mfma_f32_16x16x32_bf16 v[168:171], a[112:115], v[8:11], v[168:171]\ns_add_u32 s78, s78, 0x40000\nds_read_b64_tr_b16 a[90:91], v248 offset:57856\nv_mul_f32 v198, v198, v237\n"
"v_mfma_f32_16x16x32_bf16 v[168:171], a[116:119], v[24:27], v[168:171]\ns_add_u32 m0, s73, 0x15000\nv_mul_f32 v199, v199, v237\nv_fma_f32 v133, v133, v149, v153\n"
"v_mfma_f32_16x16x32_bf16 v[168:171], a[120:123], v[40:43], v[168:171]\nbuffer_load_dwordx4 v251, s[44:47], s78 offen lds\nv_mfma_f32_16x16x32_bf16 v[172:175], a[120:123], v[44:47], v[172:175]\nv_fma_f32 v134, v134, v149, v153\n"
"v_fma_f32 v135, v135, v149, v153\nv_mfma_f32_16x16x32_bf16 v[172:175], a[116:119], v[28:31], v[172:175]\ns_add_u32 s78, s78, 0x40000\nv_fma_f32 v136, v136, v150, v154\n"
"v_mul_f32 v200, v200, v238\nv_mfma_f32_16x16x32_bf16 v[172:175], a[112:115], v[12:15], v[172:175]\ns_add_u32 m0, s73, 0x16000\nv_mul_f32 v201, v201, v238\n"
"v_mul_f32 v202, v202, v238\nv_mfma_f32_16x16x32_bf16 v[172:175], a[108:111], a[252:255], v[172:175]\nbuffer_load_dwordx4 v251, s[44:47], s78 offen lds\nv_mfma_f32_16x16x32_bf16 v[172:175], a[104:107], a[236:239], v[172:175]\n"
"buffer_store_dwordx4 v[124:127], v224, s[60:63], s82 offen offset:3072 sc1\nv_mfma_f32_16x16x32_bf16 v[172:175], a[100:103], a[220:223], v[172:175]\ns_nop 0\ns_add_u32 s82, s82, 0x4000\n"
"v_fma_f32 v137, v137, v150, v154\nv_mul_f32 v203, v203, v238\nv_mfma_f32_16x16x32_bf16 v[172:175], a[96:99], a[204:207], v[172:175]\nv_fma_f32 v138, v138, v150, v154\n"
"v_fma_f32 v139, v139, v150, v154\ns_waitcnt vmcnt(17) lgkmcnt(0)\ns_barrier\nv_mfma_f32_16x16x32_bf16 v[208:211], a[64:67], a[128:131], 0\n"
"s_add_u32 s76, s76, 0xffe94080\nds_read_b64_tr_b16 a[96:97], v249 offset:32768\nv_fma_f32 v140, v140, v151, v155\nv_mfma_f32_16x16x32_bf16 v[208:211], a[68:71], a[144:147], v[208:211]\n"
"s_add_u32 m0, s73, 0x8000\nds_read_b64_tr_b16 a[98:99], v249 offset:33280\nv_fma_f32 v141, v141, v151, v155\nv_mfma_f32_16x16x32_bf16 v[208:211], a[72:75], a[160:163], v[208:211]\n"
"buffer_load_dwordx4 v250, s[36:39], s76 offen lds\nv_mfma_f32_16x16x32_bf16 v[208:211], a[76:79], a[176:179], v[208:211]\ns_add_u32 s76, s76, 0x1c000\nds_read_b64_tr_b16 a[100:101], v249 offset:36864\n"
"v_fma_f32 v142, v142, v151, v155\nv_mfma_f32_16x16x32_bf16 v[208:211], a[80:83], a[0:3], v[208:211]\ns_add_u32 m0, s73, 0x9000\nds_read_b64_tr_b16 a[102:103], v249 offset:37376\n"
"v_cvt_pk_bf16_f32 v64, v192, v193\nv_mfma_f32_16x16x32_bf16 v[208:211], a[84:87], a[16:19], v[208:211]\nbuffer_load_dwordx4 v250, s[36:39], s76 offen lds\nv_mfma_f32_16x16x32_bf16 v[208:211], a[88:91], a[32:35], v[208:211]\n"
"ds_read_b64_tr_b16 a[104:105], v249 offset:40960\nv_cvt_pk_bf16_f32 v65, v194, v195\nv_mfma_f32_16x16x32_bf16 v[212:215], a[88:91], a[36:39], 0\ns_add_u32 s76, s76, 0x1c000\n"
"ds_read_b64_tr_b16 a[106:107], v249 offset:41472\nv_mul_f32 v204, v204, v239\nv_mfma_f32_16x16x32_bf16 v[212:215], a[84:87], a[20:23], v[212:215]\ns_add_u32 m0, s73, 0xa000\n"
"ds_read_b64_tr_b16 a[108:109], v249 offset:45056\nv_mul_f32 v205, v205, v239\nv_mfma_f32_16x16x32_bf16 v[212:215], a[80:83], a[4:7], v[212:215]\nbuffer_load_dwordx4 v250, s[36:39], s76 offen lds\n"
"v_mfma_f32_16x16x32_bf16 v[212:215], a[76:79], a[180:183], v[212:215]\nds_read_b64_tr_b16 a[110:111], v249 offset:45568\nv_mul_f32 v206, v206, v239\nv_mfma_f32_16x16x32_bf16 v[212:215], a[72:75], a[164:167], v[212:215]\n"
"s_add_u32 s76, s76, 0x1c000\nds_read_b64_tr_b16 a[112:113], v249 offset:49152\nv_mul_f32 v207, v207, v239\nv_mfma_f32_16x16x32_bf16 v[212:215], a[68:71], a[148:151], v[212:215]\n"
"s_add_u32 m0, s73, 0xb000\nds_read_b64_tr_b16 a[114:115], v249 offset:49664\nv_mul_f32 v132, v132, v180\nv_mfma_f32_16x16x32_bf16 v[212:215], a[64:67], a[132:135], v[212:215]\n"
"buffer_load_dwordx4 v250, s[36:39], s76 offen lds\nv_mfma_f32_16x16x32_bf16 v[216:219], a[64:67], a[136:139], 0\ns_add_u32 s76, s76, 0x1c000\nds_read_b64_tr_b16 a[116:117], v249 offset:53248\n"
"v_fma_f32 v143, v143, v151, v155\nv_mfma_f32_16x16x32_bf16 v[216:219], a[68:71], a[152:155], v[216:219]\ns_add_u32 m0, s73, 0xc000\nds_read_b64_tr_b16 a[118:119], v249 offset:53760\n"
"v_cvt_pk_bf16_f32 v66, v196, v197\nv_mfma_f32_16x16x32_bf16 v[216:219], a[72:75], a[168:171], v[216:219]\nbuffer_load_dwordx4 v250, s[36:39], s76 offen lds\nv_mfma_f32_16x16x32_bf16 v[216:219], a[76:79], a[184:187], v[216:219]\n"
"ds_read_b64_tr_b16 a[120:121], v249 offset:57344\nv_cvt_pk_bf16_f32 v67, v198, v199\nv_mfma_f32_16x16x32_bf16 v[216:219], a[80:83], a[8:11], v[216:219]\ns_add_u32 s76, s76, 0x1c000\n"
"ds_read_b64_tr_b16 a[122:123], v249 offset:57856\nv_mul_f32 v133, v133, v181\nv_mfma_f32_16x16x32_bf16 v[216:219], a[84:87], a[24:27], v[216:219]\ns_add_u32 m0, s73, 0xd000\n"
"v_exp_f32 v128, v128\nv_mfma_f32_16x16x32_bf16 v[216:219], a[88:91], a[40:43], v[216:219]\nbuffer_load_dwordx4 v250, s[36:39], s76 offen lds\nv_mfma_f32_16x16x32_bf16 v[220:223], a[88:91], a[44:47], 0\n"
"v_exp_f32 v129, v129\nv_mfma_f32_16x16x32_bf16 v[220:223], a[84:87], a[28:31], v[220:223]\ns_add_u32 s76, s76, 0x1c000\nv_exp_f32 v130, v130\n"
"v_mfma_f32_16x16x32_bf16 v[220:223], a[80:83], a[12:15], v[220:223]\ns_add_u32 m0, s73, 0xe000\nv_exp_f32 v131, v131\nv_mfma_f32_16x16x32_bf16 v[220:223], a[76:79], a[188:191], v[220:223]\n"
"buffer_load_dwordx4 v250, s[36:39], s76 offen lds\nv_mfma_f32_16x16x32_bf16 v[220:223], a[72:75], a[172:175], v[220:223]\nv_mul_f32 v134, v134, v182\nv_mul_f32 v135, v135, v183\n"
"v_mfma_f32_16x16x32_bf16 v[220:223], a[68:71], a[156:159], v[220:223]\nv_mul_f32 v136, v136, v184\nv_cvt_pk_bf16_f32 v68, v200, v201\nv_mfma_f32_16x16x32_bf16 v[220:223], a[64:67], a[140:143], v[220:223]\n"
"v_mul_f32 v137, v137, v185\nv_cvt_pk_bf16_f32 v69, v202, v203\ns_waitcnt vmcnt(16) lgkmcnt(0)\ns_barrier\n"
"v_mfma_f32_16x16x32_bf16 v[208:211], a[96:99], a[192:195], v[208:211]\ns_add_u32 s76, s76, 0x1c000\nds_read_b64_tr_b16 a[64:65], v248 offset:0\nv_mul_f32 v138, v138, v186\n"
"v_mfma_f32_16x16x32_bf16 v[208:211], a[100:103], a[208:211], v[208:211]\ns_add_u32 m0, s73, 0x18000\nds_read_b64_tr_b16 a[66:67], v248 offset:512\nv_mul_f32 v139, v139, v187\n"
"v_mfma_f32_16x16x32_bf16 v[208:211], a[104:107], a[224:227], v[208:211]\nbuffer_load_dwordx4 v250, s[36:39], s76 offen lds\nv_mfma_f32_16x16x32_bf16 v[208:211], a[108:111], a[240:243], v[208:211]\ns_add_u32 s76, s76, 0x1c000\n"
"ds_read_b64_tr_b16 a[68:69], v248 offset:4096\nv_mul_f32 v140, v140, v188\nv_mfma_f32_16x16x32_bf16 v[208:211], a[112:115], v[0:3], v[208:211]\ns_add_u32 m0, s73, 0x19000\n"
"ds_read_b64_tr_b16 a[70:71], v248 offset:4608\nv_mul_f32 v141, v141, v189\nv_mfma_f32_16x16x32_bf16 v[208:211], a[116:119], v[16:19], v[208:211]\nbuffer_load_dwordx4 v250, s[36:39], s76 offen lds\n"
"v_mfma_f32_16x16x32_bf16 v[208:211], a[120:123], v[32:35], v[208:211]\nds_read_b64_tr_b16 a[72:73], v248 offset:8192\nv_mul_f32 v142, v142, v190\nv_mfma_f32_16x16x32_bf16 v[212:215], a[120:123], v[36:39], v[212:215]\n"
"s_add_u32 s76, s76, 0x1c000\nds_read_b64_tr_b16 a[74:75], v248 offset:8704\nv_cvt_pk_bf16_f32 v70, v204, v205\nv_mfma_f32_16x16x32_bf16 v[212:215], a[116:119], v[20:23], v[212:215]\n"
"s_add_u32 m0, s73, 0x1a000\nds_read_b64_tr_b16 a[76:77], v248 offset:12288\nv_cvt_pk_bf16_f32 v71, v206, v207\nv_mfma_f32_16x16x32_bf16 v[212:215], a[112:115], v[4:7], v[212:215]\n"
"buffer_load_dwordx4 v250, s[36:39], s76 offen lds\nv_mfma_f32_16x16x32_bf16 v[212:215], a[108:111], a[244:247], v[212:215]\nds_read_b64_tr_b16 a[78:79], v248 offset:12800\nv_mul_f32 v143, v143, v191\n"
"v_mfma_f32_16x16x32_bf16 v[212:215], a[104:107], a[228:231], v[212:215]\ns_add_u32 s76, s76, 0x1c000\nds_read_b64_tr_b16 a[80:81], v248 offset:16384\nv_fma_f32 v128, v128, v156, v156\n"
"v_mfma_f32_16x16x32_bf16 v[212:215], a[100:103], a[212:215], v[212:215]\ns_add_u32 m0, s73, 0x1b000\nds_read_b64_tr_b16 a[82:83], v248 offset:16896\nv_fma_f32 v129, v129, v156, v156\n"
"v_mfma_f32_16x16x32_bf16 v[212:215], a[96:99], a[196:199], v[212:215]\nbuffer_load_dwordx4 v250, s[36:39], s76 offen lds\nv_mfma_f32_16x16x32_bf16 v[216:219], a[96:99], a[200:203], v[216:219]\ns_add_u32 s76, s76, 0x1c000\n"
"ds_read_b64_tr_b16 a[84:85], v248 offset:20480\nv_fma_f32 v130, v130, v156, v156\nv_mfma_f32_16x16x32_bf16 v[216:219], a[100:103], a[216:219], v[216:219]\ns_add_u32 m0, s73, 0x1c000\n"
"ds_read_b64_tr_b16 a[86:87], v248 offset:20992\nv_fma_f32 v131, v131, v156, v156\nv_mfma_f32_16x16x32_bf16 v[216:219], a[104:107], a[232:235], v[216:219]\nbuffer_load_dwordx4 v250, s[36:39], s76 offen lds\n"
"v_mfma_f32_16x16x32_bf16 v[216:219], a[108:111], a[248:251], v[216:219]\nds_read_b64_tr_b16 a[88:89], v248 offset:24576\nv_mul_f32 v208, v208, v236\nv_mfma_f32_16x16x32_bf16 v[216:219], a[112:115], v[8:11], v[216:219]\n"
"s_add_u32 s76, s76, 0x1c000\nds_read_b64_tr_b16 a[90:91], v248 offset:25088\nv_mul_f32 v209, v209, v236\nv_mfma_f32_16x16x32_bf16 v[216:219], a[116:119], v[24:27], v[216:219]\n"
"s_add_u32 m0, s73, 0x1d000\nv_exp_f32 v132, v132\nv_mfma_f32_16x16x32_bf16 v[216:219], a[120:123], v[40:43], v[216:219]\nbuffer_load_dwordx4 v250, s[36:39], s76 offen lds\n"
"v_mfma_f32_16x16x32_bf16 v[220:223], a[120:123], v[44:47], v[220:223]\nv_exp_f32 v133, v133\nv_mfma_f32_16x16x32_bf16 v[220:223], a[116:119], v[28:31], v[220:223]\ns_add_u32 s76, s76, 0x1c000\n"
"v_exp_f32 v134, v134\nv_mfma_f32_16x16x32_bf16 v[220:223], a[112:115], v[12:15], v[220:223]\ns_add_u32 m0, s73, 0x1e000\nv_exp_f32 v135, v135\n"
"v_mfma_f32_16x16x32_bf16 v[220:223], a[108:111], a[252:255], v[220:223]\nbuffer_load_dwordx4 v250, s[36:39], s76 offen lds\nv_mfma_f32_16x16x32_bf16 v[220:223], a[104:107], a[236:239], v[220:223]\nv_exp_f32 v136, v136\n"
"v_mfma_f32_16x16x32_bf16 v[220:223], a[100:103], a[220:223], v[220:223]\nv_exp_f32 v137, v137\nv_mfma_f32_16x16x32_bf16 v[220:223], a[96:99], a[204:207], v[220:223]\nv_exp_f32 v138, v138\n"
"s_waitcnt vmcnt(15) lgkmcnt(0)\ns_barrier\nv_mfma_f32_16x16x32_bf16 v[192:195], a[64:67], a[128:131], 0\ns_add_u32 s77, s77, 0xffe94080\n"
"ds_read_b64_tr_b16 a[96:97], v249 offset:0\nv_fma_f32 v132, v132, v157, v157\nv_mfma_f32_16x16x32_bf16 v[192:195], a[68:71], a[144:147], v[192:195]\ns_add_u32 m0, s73, 0x0\n"
"ds_read_b64_tr_b16 a[98:99], v249 offset:512\nv_fma_f32 v133, v133, v157, v157\nv_mfma_f32_16x16x32_bf16 v[192:195], a[72:75], a[160:163], v[192:195]\nbuffer_load_dwordx4 v250, s[40:43], s77 offen lds\n"
"v_mfma_f32_16x16x32_bf16 v[192:195], a[76:79], a[176:179], v[192:195]\ns_add_u32 s77, s77, 0x1c000\nds_read_b64_tr_b16 a[100:101], v249 offset:4096\nv_fma_f32 v134, v134, v157, v157\n"
"v_mfma_f32_16x16x32_bf16 v[192:195], a[80:83], a[0:3], v[192:195]\ns_add_u32 m0, s73, 0x1000\nds_read_b64_tr_b16 a[102:103], v249 offset:4608\nv_fma_f32 v135, v135, v157, v157\n"
"v_mfma_f32_16x16x32_bf16 v[192:195], a[84:87], a[16:19], v[192:195]\nbuffer_load_dwordx4 v250, s[40:43], s77 offen lds\nv_mfma_f32_16x16x32_bf16 v[192:195], a[88:91], a[32:35], v[192:195]\nds_read_b64_tr_b16 a[104:105], v249 offset:8192\n"
"v_fma_f32 v136, v136, v158, v158\nv_mfma_f32_16x16x32_bf16 v[196:199], a[88:91], a[36:39], 0\ns_add_u32 s77, s77, 0x1c000\nds_read_b64_tr_b16 a[106:107], v249 offset:8704\n"
"v_fma_f32 v137, v137, v158, v158\nv_mfma_f32_16x16x32_bf16 v[196:199], a[84:87], a[20:23], v[196:199]\ns_add_u32 m0, s73, 0x2000\nds_read_b64_tr_b16 a[108:109], v249 offset:12288\n"
"v_fma_f32 v138, v138, v158, v158\nv_mfma_f32_16x16x32_bf16 v[196:199], a[80:83], a[4:7], v[196:199]\nbuffer_load_dwordx4 v250, s[40:43], s77 offen lds\nv_mfma_f32_16x16x32_bf16 v[196:199], a[76:79], a[180:183], v[196:199]\n"
"ds_read_b64_tr_b16 a[110:111], v249 offset:12800\nv_mul_f32 v210, v210, v236\nv_mfma_f32_16x16x32_bf16 v[196:199], a[72:75], a[164:167], v[196:199]\ns_add_u32 s77, s77, 0x1c000\n"
"ds_read_b64_tr_b16 a[112:113], v249 offset:16384\nv_mul_f32 v211, v211, v236\nv_mfma_f32_16x16x32_bf16 v[196:199], a[68:71], a[148:151], v[196:199]\ns_add_u32 m0, s73, 0x3000\n"
"ds_read_b64_tr_b16 a[114:115], v249 offset:16896\nv_cvt_pk_bf16_f32 v72, v208, v209\nv_mfma_f32_16x16x32_bf16 v[196:199], a[64:67], a[132:135], v[196:199]\nbuffer_load_dwordx4 v250, s[40:43], s77 offen lds\n"
"v_mfma_f32_16x16x32_bf16 v[200:203], a[64:67], a[136:139], 0\ns_add_u32 s77, s77, 0x1c000\nds_read_b64_tr_b16 a[116:117], v249 offset:20480\nv_cvt_pk_bf16_f32 v73, v210, v211\n"
"v_mfma_f32_16x16x32_bf16 v[200:203], a[68:71], a[152:155], v[200:203]\ns_add_u32 m0, s73, 0x4000\nds_read_b64_tr_b16 a[118:119], v249 offset:20992\nv_mul_f32 v212, v212, v237\n"
"v_mfma_f32_16x16x32_bf16 v[200:203], a[72:75], a[168:171], v[200:203]\nbuffer_load_dwordx4 v250, s[40:43], s77 offen lds\nv_mfma_f32_16x16x32_bf16 v[200:203], a[76:79], a[184:187], v[200:203]\nds_read_b64_tr_b16 a[120:121], v249 offset:24576\n"
"v_mul_f32 v213, v213, v237\nv_mfma_f32_16x16x32_bf16 v[200:203], a[80:83], a[8:11], v[200:203]\ns_add_u32 s77, s77, 0x1c000\nds_read_b64_tr_b16 a[122:123], v249 offset:25088\n"
"v_mul_f32 v214, v214, v237\nv_mfma_f32_16x16x32_bf16 v[200:203], a[84:87], a[24:27], v[200:203]\ns_add_u32 m0, s73, 0x5000\nv_exp_f32 v139, v139\n"
"v_mfma_f32_16x16x32_bf16 v[200:203], a[88:91], a[40:43], v[200:203]\nbuffer_load_dwordx4 v250, s[40:43], s77 offen lds\nv_mfma_f32_16x16x32_bf16 v[204:207], a[88:91], a[44:47], 0\nv_exp_f32 v140, v140\n"
"v_mfma_f32_16x16x32_bf16 v[204:207], a[84:87], a[28:31], v[204:207]\ns_add_u32 s77, s77, 0x1c000\nv_exp_f32 v141, v141\nv_mfma_f32_16x16x32_bf16 v[204:207], a[80:83], a[12:15], v[204:207]\n"
"s_add_u32 m0, s73, 0x6000\nv_exp_f32 v142, v142\nv_mfma_f32_16x16x32_bf16 v[204:207], a[76:79], a[188:191], v[204:207]\nbuffer_load_dwordx4 v250, s[40:43], s77 offen lds\n"
"v_mfma_f32_16x16x32_bf16 v[204:207], a[72:75], a[172:175], v[204:207]\nv_exp_f32 v143, v143\nv_mfma_f32_16x16x32_bf16 v[204:207], a[68:71], a[156:159], v[204:207]\nv_rcp_f32 v128, v128\n"
"v_mfma_f32_16x16x32_bf16 v[204:207], a[64:67], a[140:143], v[204:207]\nv_rcp_f32 v129, v129\ns_waitcnt vmcnt(14) lgkmcnt(0)\ns_barrier\n"
"v_mfma_f32_16x16x32_bf16 v[192:195], a[96:99], a[192:195], v[192:195]\ns_add_u32 s77, s77, 0x1c000\nv_rcp_f32 v130, v130\nv_mfma_f32_16x16x32_bf16 v[192:195], a[100:103], a[208:211], v[192:195]\n"
"s_add_u32 m0, s73, 0x10000\nv_rcp_f32 v131, v131\nv_mfma_f32_16x16x32_bf16 v[192:195], a[104:107], a[224:227], v[192:195]\nbuffer_load_dwordx4 v250, s[40:43], s77 offen lds\n"
"v_mfma_f32_16x16x32_bf16 v[192:195], a[108:111], a[240:243], v[192:195]\ns_add_u32 s77, s77, 0x1c000\nds_read_b64_tr_b16 a[64:65], v248 offset:32768\nv_fma_f32 v139, v139, v158, v158\n"
"v_mfma_f32_16x16x32_bf16 v[192:195], a[112:115], v[0:3], v[192:195]\ns_add_u32 m0, s73, 0x11000\nds_read_b64_tr_b16 a[66:67], v248 offset:33280\nv_fma_f32 v140, v140, v159, v159\n"
"v_mfma_f32_16x16x32_bf16 v[192:195], a[116:119], v[16:19], v[192:195]\nbuffer_load_dwordx4 v250, s[40:43], s77 offen lds\nv_mfma_f32_16x16x32_bf16 v[192:195], a[120:123], v[32:35], v[192:195]\nds_read_b64_tr_b16 a[68:69], v248 offset:36864\n"
"v_fma_f32 v141, v141, v159, v159\nv_mfma_f32_16x16x32_bf16 v[196:199], a[120:123], v[36:39], v[196:199]\ns_add_u32 s77, s77, 0x1c000\nds_read_b64_tr_b16 a[70:71], v248 offset:37376\n"
"v_fma_f32 v142, v142, v159, v159\nv_mfma_f32_16x16x32_bf16 v[196:199], a[116:119], v[20:23], v[196:199]\ns_add_u32 m0, s73, 0x12000\nds_read_b64_tr_b16 a[72:73], v248 offset:40960\n"
"v_fma_f32 v143, v143, v159, v159\nv_mfma_f32_16x16x32_bf16 v[196:199], a[112:115], v[4:7], v[196:199]\nbuffer_load_dwordx4 v250, s[40:43], s77 offen lds\nv_mfma_f32_16x16x32_bf16 v[196:199], a[108:111], a[244:247], v[196:199]\n"
"ds_read_b64_tr_b16 a[74:75], v248 offset:41472\nv_mul_f32 v128, v128, v176\nv_mfma_f32_16x16x32_bf16 v[196:199], a[104:107], a[228:231], v[196:199]\ns_add_u32 s77, s77, 0x1c000\n"
"ds_read_b64_tr_b16 a[76:77], v248 offset:45056\nv_mul_f32 v129, v129, v177\nv_mfma_f32_16x16x32_bf16 v[196:199], a[100:103], a[212:215], v[196:199]\ns_add_u32 m0, s73, 0x13000\n"
"ds_read_b64_tr_b16 a[78:79], v248 offset:45568\nv_mul_f32 v130, v130, v178\nv_mfma_f32_16x16x32_bf16 v[196:199], a[96:99], a[196:199], v[196:199]\nbuffer_load_dwordx4 v250, s[40:43], s77 offen lds\n"
"v_mfma_f32_16x16x32_bf16 v[200:203], a[96:99], a[200:203], v[200:203]\ns_add_u32 s77, s77, 0x1c000\nds_read_b64_tr_b16 a[80:81], v248 offset:49152\nv_mul_f32 v131, v131, v179\n"
"v_mfma_f32_16x16x32_bf16 v[200:203], a[100:103], a[216:219], v[200:203]\ns_add_u32 m0, s73, 0x14000\nds_read_b64_tr_b16 a[82:83], v248 offset:49664\nv_mul_f32 v128, v128, v160\n"
"v_mfma_f32_16x16x32_bf16 v[200:203], a[104:107], a[232:235], v[200:203]\nbuffer_load_dwordx4 v250, s[40:43], s77 offen lds\nv_mfma_f32_16x16x32_bf16 v[200:203], a[108:111], a[248:251], v[200:203]\nds_read_b64_tr_b16 a[84:85], v248 offset:53248\n"
"v_mul_f32 v129, v129, v161\nv_mfma_f32_16x16x32_bf16 v[200:203], a[112:115], v[8:11], v[200:203]\ns_add_u32 s77, s77, 0x1c000\nds_read_b64_tr_b16 a[86:87], v248 offset:53760\n"
"v_mul_f32 v130, v130, v162\nv_mfma_f32_16x16x32_bf16 v[200:203], a[116:119], v[24:27], v[200:203]\ns_add_u32 m0, s73, 0x15000\nds_read_b64_tr_b16 a[88:89], v248 offset:57344\n"
"v_mul_f32 v131, v131, v163\nv_mfma_f32_16x16x32_bf16 v[200:203], a[120:123], v[40:43], v[200:203]\nbuffer_load_dwordx4 v250, s[40:43], s77 offen lds\nv_mfma_f32_16x16x32_bf16 v[204:207], a[120:123], v[44:47], v[204:207]\n"
"ds_read_b64_tr_b16 a[90:91], v248 offset:57856\nv_mul_f32 v215, v215, v237\nv_mfma_f32_16x16x32_bf16 v[204:207], a[116:119], v[28:31], v[204:207]\ns_add_u32 s77, s77, 0x1c000\n"
"v_rcp_f32 v132, v132\nv_mfma_f32_16x16x32_bf16 v[204:207], a[112:115], v[12:15], v[204:207]\ns_add_u32 m0, s73, 0x16000\nv_rcp_f32 v133, v133\n"
"v_mfma_f32_16x16x32_bf16 v[204:207], a[108:111], a[252:255], v[204:207]\nbuffer_load_dwordx4 v250, s[40:43], s77 offen lds\nv_mfma_f32_16x16x32_bf16 v[204:207], a[104:107], a[236:239], v[204:207]\nv_rcp_f32 v134, v134\n"
"v_mfma_f32_16x16x32_bf16 v[204:207], a[100:103], a[220:223], v[204:207]\nv_rcp_f32 v135, v135\nv_mfma_f32_16x16x32_bf16 v[204:207], a[96:99], a[204:207], v[204:207]\nv_rcp_f32 v136, v136\n"
"s_waitcnt vmcnt(14) lgkmcnt(0)\ns_barrier\nv_mfma_f32_16x16x32_bf16 v[176:179], a[64:67], a[128:131], 0\ns_add_u32 s78, s78, 0xffcc0200\n"
"v_rcp_f32 v137, v137\nv_mfma_f32_16x16x32_bf16 v[176:179], a[68:71], a[144:147], v[176:179]\ns_add_u32 m0, s73, 0x8000\nv_rcp_f32 v138, v138\n"
"v_mfma_f32_16x16x32_bf16 v[176:179], a[72:75], a[160:163], v[176:179]\nbuffer_load_dwordx4 v251, s[44:47], s78 offen lds\nv_mfma_f32_16x16x32_bf16 v[176:179], a[76:79], a[176:179], v[176:179]\ns_add_u32 s78, s78, 0x40000\n"
"v_rcp_f32 v139, v139\nv_mfma_f32_16x16x32_bf16 v[176:179], a[80:83], a[0:3], v[176:179]\ns_add_u32 m0, s73, 0x9000\nv_rcp_f32 v140, v140\n"
"v_mfma_f32_16x16x32_bf16 v[176:179], a[84:87], a[16:19], v[176:179]\nbuffer_load_dwordx4 v251, s[44:47], s78 offen lds\nv_mfma_f32_16x16x32_bf16 v[176:179], a[88:91], a[32:35], v[176:179]\nv_rcp_f32 v141, v141\n"
"v_mul_f32 v132, v132, v180\nv_mul_f32 v133, v133, v181\nv_mul_f32 v134, v134, v182\nv_mul_f32 v135, v135, v183\n"
"v_mfma_f32_16x16x32_bf16 v[180:183], a[88:91], a[36:39], 0\ns_add_u32 s78, s78, 0x40000\nv_rcp_f32 v142, v142\nv_mfma_f32_16x16x32_bf16 v[180:183], a[84:87], a[20:23], v[180:183]\n"
"s_add_u32 m0, s73, 0xa000\nv_rcp_f32 v143, v143\nv_mfma_f32_16x16x32_bf16 v[180:183], a[80:83], a[4:7], v[180:183]\nbuffer_load_dwordx4 v251, s[44:47], s78 offen lds\n"
"v_mfma_f32_16x16x32_bf16 v[180:183], a[76:79], a[180:183], v[180:183]\nds_read_b64_tr_b16 a[96:97], v249 offset:32768\nv_mul_f32 v136, v136, v184\nv_mfma_f32_16x16x32_bf16 v[180:183], a[72:75], a[164:167], v[180:183]\n"
"s_add_u32 s78, s78, 0x40000\nds_read_b64_tr_b16 a[98:99], v249 offset:33280\nv_mul_f32 v137, v137, v185\nv_mfma_f32_16x16x32_bf16 v[180:183], a[68:71], a[148:151], v[180:183]\n"
"s_add_u32 m0, s73, 0xb000\nds_read_b64_tr_b16 a[100:101], v249 offset:36864\nv_mul_f32 v138, v138, v186\nv_mfma_f32_16x16x32_bf16 v[180:183], a[64:67], a[132:135], v[180:183]\n"
"buffer_load_dwordx4 v251, s[44:47], s78 offen lds\nv_mul_f32 v139, v139, v187\nv_mfma_f32_16x16x32_bf16 v[184:187], a[64:67], a[136:139], 0\ns_add_u32 s78, s78, 0x40000\n"
"ds_read_b64_tr_b16 a[102:103], v249 offset:37376\nv_mul_f32 v140, v140, v188\nv_mfma_f32_16x16x32_bf16 v[184:187], a[68:71], a[152:155], v[184:187]\ns_add_u32 m0, s73, 0xc000\n"
"ds_read_b64_tr_b16 a[104:105], v249 offset:40960\nv_mul_f32 v141, v141, v189\nv_mfma_f32_16x16x32_bf16 v[184:187], a[72:75], a[168:171], v[184:187]\nbuffer_load_dwordx4 v251, s[44:47], s78 offen lds\n"
"v_mfma_f32_16x16x32_bf16 v[184:187], a[76:79], a[184:187], v[184:187]\nds_read_b64_tr_b16 a[106:107], v249 offset:41472\nv_mul_f32 v142, v142, v190\nv_mfma_f32_16x16x32_bf16 v[184:187], a[80:83], a[8:11], v[184:187]\n"
"s_add_u32 s78, s78, 0x40000\nds_read_b64_tr_b16 a[108:109], v249 offset:45056\nv_mul_f32 v143, v143, v191\nv_mfma_f32_16x16x32_bf16 v[184:187], a[84:87], a[24:27], v[184:187]\n"
"s_add_u32 m0, s73, 0xd000\nds_read_b64_tr_b16 a[110:111], v249 offset:45568\nv_mul_f32 v132, v132, v164\nv_mfma_f32_16x16x32_bf16 v[184:187], a[88:91], a[40:43], v[184:187]\n"
"buffer_load_dwordx4 v251, s[44:47], s78 offen lds\nv_mfma_f32_16x16x32_bf16 v[188:191], a[88:91], a[44:47], 0\nds_read_b64_tr_b16 a[112:113], v249 offset:49152\nv_mul_f32 v133, v133, v165\n"
"v_mfma_f32_16x16x32_bf16 v[188:191], a[84:87], a[28:31], v[188:191]\ns_add_u32 s78, s78, 0x40000\nds_read_b64_tr_b16 a[114:115], v249 offset:49664\nv_mul_f32 v134, v134, v166\n"
"v_mfma_f32_16x16x32_bf16 v[188:191], a[80:83], a[12:15], v[188:191]\ns_add_u32 m0, s73, 0xe000\nds_read_b64_tr_b16 a[116:117], v249 offset:53248\nv_mul_f32 v135, v135, v167\n"
"v_mfma_f32_16x16x32_bf16 v[188:191], a[76:79], a[188:191], v[188:191]\nbuffer_load_dwordx4 v251, s[44:47], s78 offen lds\nv_mfma_f32_16x16x32_bf16 v[188:191], a[72:75], a[172:175], v[188:191]\nds_read_b64_tr_b16 a[118:119], v249 offset:53760\n"
"v_mul_f32 v136, v136, v168\nv_mfma_f32_16x16x32_bf16 v[188:191], a[68:71], a[156:159], v[188:191]\nds_read_b64_tr_b16 a[120:121], v249 offset:57344\nv_mul_f32 v137, v137, v169\n"
"v_mfma_f32_16x16x32_bf16 v[188:191], a[64:67], a[140:143], v[188:191]\nds_read_b64_tr_b16 a[122:123], v249 offset:57856\nv_mul_f32 v138, v138, v170\ns_waitcnt vmcnt(14) lgkmcnt(0)\n"
"s_barrier\nv_mfma_f32_16x16x32_bf16 v[176:179], a[96:99], a[192:195], v[176:179]\ns_add_u32 s78, s78, 0x40000\nds_read_b64_tr_b16 a[64:65], v248 offset:0\n"
"v_mul_f32 v139, v139, v171\nv_mfma_f32_16x16x32_bf16 v[176:179], a[100:103], a[208:211], v[176:179]\ns_add_u32 m0, s73, 0x18000\nds_read_b64_tr_b16 a[66:67], v248 offset:512\n"
"v_mul_f32 v140, v140, v172\nv_mfma_f32_16x16x32_bf16 v[176:179], a[104:107], a[224:227], v[176:179]\nbuffer_load_dwordx4 v251, s[44:47], s78 offen lds\nv_mfma_f32_16x16x32_bf16 v[176:179], a[108:111], a[240:243], v[176:179]\n"
"s_add_u32 s78, s78, 0x40000\nds_read_b64_tr_b16 a[68:69], v248 offset:4096\nv_mul_f32 v141, v141, v173\nv_mfma_f32_16x16x32_bf16 v[176:179], a[112:115], v[0:3], v[176:179]\n"
"s_add_u32 m0, s73, 0x19000\nds_read_b64_tr_b16 a[70:71], v248 offset:4608\nv_mul_f32 v142, v142, v174\nv_mfma_f32_16x16x32_bf16 v[176:179], a[116:119], v[16:19], v[176:179]\n"
"buffer_load_dwordx4 v251, s[44:47], s78 offen lds\nv_mfma_f32_16x16x32_bf16 v[176:179], a[120:123], v[32:35], v[176:179]\nds_read_b64_tr_b16 a[72:73], v248 offset:8192\nv_mul_f32 v143, v143, v175\n"
"v_mfma_f32_16x16x32_bf16 v[180:183], a[120:123], v[36:39], v[180:183]\ns_add_u32 s78, s78, 0x40000\nds_read_b64_tr_b16 a[74:75], v248 offset:8704\nv_cvt_pk_bf16_f32 v74, v212, v213\n"
"v_mfma_f32_16x16x32_bf16 v[180:183], a[116:119], v[20:23], v[180:183]\ns_add_u32 m0, s73, 0x1a000\nds_read_b64_tr_b16 a[76:77], v248 offset:12288\nv_cvt_pk_bf16_f32 v75, v214, v215\n"
"v_mfma_f32_16x16x32_bf16 v[180:183], a[112:115], v[4:7], v[180:183]\nbuffer_load_dwordx4 v251, s[44:47], s78 offen lds\nv_mfma_f32_16x16x32_bf16 v[180:183], a[108:111], a[244:247], v[180:183]\nds_read_b64_tr_b16 a[78:79], v248 offset:12800\n"
"v_mul_f32 v216, v216, v238\nv_mfma_f32_16x16x32_bf16 v[180:183], a[104:107], a[228:231], v[180:183]\ns_add_u32 s78, s78, 0x40000\nds_read_b64_tr_b16 a[80:81], v248 offset:16384\n"
"v_mul_f32 v217, v217, v238\nv_mfma_f32_16x16x32_bf16 v[180:183], a[100:103], a[212:215], v[180:183]\ns_add_u32 m0, s73, 0x1b000\nds_read_b64_tr_b16 a[82:83], v248 offset:16896\n"
"v_mul_f32 v218, v218, v238\nv_mfma_f32_16x16x32_bf16 v[180:183], a[96:99], a[196:199], v[180:183]\nbuffer_load_dwordx4 v251, s[44:47], s78 offen lds\nv_mfma_f32_16x16x32_bf16 v[184:187], a[96:99], a[200:203], v[184:187]\n"
"s_add_u32 s78, s78, 0x40000\nds_read_b64_tr_b16 a[84:85], v248 offset:20480\nv_mul_f32 v219, v219, v238\nv_mfma_f32_16x16x32_bf16 v[184:187], a[100:103], a[216:219], v[184:187]\n"
"s_add_u32 m0, s73, 0x1c000\nds_read_b64_tr_b16 a[86:87], v248 offset:20992\nv_cvt_pk_bf16_f32 v76, v216, v217\nv_mfma_f32_16x16x32_bf16 v[184:187], a[104:107], a[232:235], v[184:187]\n"
"buffer_load_dwordx4 v251, s[44:47], s78 offen lds\nv_mfma_f32_16x16x32_bf16 v[184:187], a[108:111], a[248:251], v[184:187]\nds_read_b64_tr_b16 a[88:89], v248 offset:24576\nv_cvt_pk_bf16_f32 v77, v218, v219\n"
"v_mfma_f32_16x16x32_bf16 v[184:187], a[112:115], v[8:11], v[184:187]\ns_add_u32 s78, s78, 0x40000\nds_read_b64_tr_b16 a[90:91], v248 offset:25088\nv_mul_f32 v220, v220, v239\n"
"v_mfma_f32_16x16x32_bf16 v[184:187], a[116:119], v[24:27], v[184:187]\ns_add_u32 m0, s73, 0x1d000\nv_mul_f32 v221, v221, v239\nv_mul_f32 v222, v222, v239\n"
"v_mfma_f32_16x16x32_bf16 v[184:187], a[120:123], v[40:43], v[184:187]\nbuffer_load_dwordx4 v251, s[44:47], s78 offen lds\nv_mfma_f32_16x16x32_bf16 v[188:191], a[120:123], v[44:47], v[188:191]\nv_mul_f32 v223, v223, v239\n"
"v_cvt_pk_bf16_f32 v78, v220, v221\nv_mfma_f32_16x16x32_bf16 v[188:191], a[116:119], v[28:31], v[188:191]\ns_add_u32 s78, s78, 0x40000\nv_cvt_pk_bf16_f32 v79, v222, v223\n"
"v_mul_f32 v192, v192, v236\nv_mfma_f32_16x16x32_bf16 v[188:191], a[112:115], v[12:15], v[188:191]\ns_add_u32 m0, s73, 0x1e000\nv_mul_f32 v193, v193, v236\n"
"v_mul_f32 v194, v194, v236\nv_mfma_f32_16x16x32_bf16 v[188:191], a[108:111], a[252:255], v[188:191]\nbuffer_load_dwordx4 v251, s[44:47], s78 offen lds\nv_mfma_f32_16x16x32_bf16 v[188:191], a[104:107], a[236:239], v[188:191]\n"
"v_mul_f32 v195, v195, v236\nv_mul_f32 v196, v196, v237\nv_mfma_f32_16x16x32_bf16 v[188:191], a[100:103], a[220:223], v[188:191]\nv_mul_f32 v197, v197, v237\n"
"v_mul_f32 v198, v198, v237\nv_mfma_f32_16x16x32_bf16 v[188:191], a[96:99], a[204:207], v[188:191]\nv_cvt_pk_bf16_f32 v80, v192, v193\nv_mul_f32 v199, v199, v237\n"
"s_waitcnt vmcnt(14) lgkmcnt(0)\ns_barrier\nv_mfma_f32_16x16x32_bf16 v[160:163], a[64:67], a[128:131], 0\ns_add_u32 s78, s78, 0xffcbfe80\n"
"ds_read_b64_tr_b16 a[96:97], v249 offset:0\nv_cvt_pk_bf16_f32 v81, v194, v195\nv_mfma_f32_16x16x32_bf16 v[160:163], a[68:71], a[144:147], v[160:163]\ns_add_u32 m0, s73, 0x0\n"
"ds_read_b64_tr_b16 a[98:99], v249 offset:512\nv_cvt_pk_bf16_f32 v82, v196, v197\nv_mfma_f32_16x16x32_bf16 v[160:163], a[72:75], a[160:163], v[160:163]\nbuffer_load_dwordx4 v251, s[44:47], s78 offen lds\n"
"v_mfma_f32_16x16x32_bf16 v[160:163], a[76:79], a[176:179], v[160:163]\ns_add_u32 s78, s78, 0x40000\nds_read_b64_tr_b16 a[100:101], v249 offset:4096\nv_cvt_pk_bf16_f32 v83, v198, v199\n"
"v_mfma_f32_16x16x32_bf16 v[160:163], a[80:83], a[0:3], v[160:163]\ns_add_u32 m0, s73, 0x1000\nds_read_b64_tr_b16 a[102:103], v249 offset:4608\nv_mul_f32 v200, v200, v238\n"
"v_mfma_f32_16x16x32_bf16 v[160:163], a[84:87], a[16:19], v[160:163]\nbuffer_load_dwordx4 v251, s[44:47], s78 offen lds\nv_mfma_f32_16x16x32_bf16 v[160:163], a[88:91], a[32:35], v[160:163]\nds_read_b64_tr_b16 a[104:105], v249 offset:8192\n"
"v_mul_f32 v201, v201, v238\nv_mfma_f32_16x16x32_bf16 v[164:167], a[88:91], a[36:39], 0\ns_add_u32 s78, s78, 0x40000\nds_read_b64_tr_b16 a[106:107], v249 offset:8704\n"
"v_mul_f32 v202, v202, v238\nv_mfma_f32_16x16x32_bf16 v[164:167], a[84:87], a[20:23], v[164:167]\ns_add_u32 m0, s73, 0x2000\nds_read_b64_tr_b16 a[108:109], v249 offset:12288\n"
"v_mul_f32 v203, v203, v238\nv_mfma_f32_16x16x32_bf16 v[164:167], a[80:83], a[4:7], v[164:167]\nbuffer_load_dwordx4 v251, s[44:47], s78 offen lds\nv_mfma_f32_16x16x32_bf16 v[164:167], a[76:79], a[180:183], v[164:167]\n"
"ds_read_b64_tr_b16 a[110:111], v249 offset:12800\nv_mul_f32 v204, v204, v239\nv_mfma_f32_16x16x32_bf16 v[164:167], a[72:75], a[164:167], v[164:167]\ns_add_u32 s78, s78, 0x40000\n"
"ds_read_b64_tr_b16 a[112:113], v249 offset:16384\nv_mul_f32 v205, v205, v239\nv_mfma_f32_16x16x32_bf16 v[164:167], a[68:71], a[148:151], v[164:167]\ns_add_u32 m0, s73, 0x3000\n"
"ds_read_b64_tr_b16 a[114:115], v249 offset:16896\nv_cvt_pk_bf16_f32 v84, v200, v201\nv_mfma_f32_16x16x32_bf16 v[164:167], a[64:67], a[132:135], v[164:167]\nbuffer_load_dwordx4 v251, s[44:47], s78 offen lds\n"
"v_mfma_f32_16x16x32_bf16 v[168:171], a[64:67], a[136:139], 0\ns_add_u32 s78, s78, 0x40000\nds_read_b64_tr_b16 a[116:117], v249 offset:20480\nv_cvt_pk_bf16_f32 v85, v202, v203\n"
"v_mfma_f32_16x16x32_bf16 v[168:171], a[68:71], a[152:155], v[168:171]\ns_add_u32 m0, s73, 0x4000\nds_read_b64_tr_b16 a[118:119], v249 offset:20992\nv_mul_f32 v206, v206, v239\n"
"v_mfma_f32_16x16x32_bf16 v[168:171], a[72:75], a[168:171], v[168:171]\nbuffer_load_dwordx4 v251, s[44:47], s78 offen lds\nv_mfma_f32_16x16x32_bf16 v[168:171], a[76:79], a[184:187], v[168:171]\nds_read_b64_tr_b16 a[120:121], v249 offset:24576\n"
"v_mul_f32 v207, v207, v239\nv_mfma_f32_16x16x32_bf16 v[168:171], a[80:83], a[8:11], v[168:171]\ns_add_u32 s78, s78, 0x40000\nds_read_b64_tr_b16 a[122:123], v249 offset:25088\n"
"v_cvt_pk_bf16_f32 v86, v204, v205\nv_mfma_f32_16x16x32_bf16 v[168:171], a[84:87], a[24:27], v[168:171]\ns_add_u32 m0, s73, 0x5000\nv_cvt_pk_bf16_f32 v112, v128, v129\n"
"v_cvt_pk_bf16_f32 v87, v206, v207\nv_mfma_f32_16x16x32_bf16 v[168:171], a[88:91], a[40:43], v[168:171]\nbuffer_load_dwordx4 v251, s[44:47], s78 offen lds\nv_mfma_f32_16x16x32_bf16 v[172:175], a[88:91], a[44:47], 0\n"
"v_cvt_pk_bf16_f32 v113, v130, v131\nv_cvt_pk_bf16_f32 v116, v132, v133\nv_mfma_f32_16x16x32_bf16 v[172:175], a[84:87], a[28:31], v[172:175]\ns_add_u32 s78, s78, 0x40000\n"
"v_cvt_pk_bf16_f32 v117, v134, v135\nv_mul_f32 v128, v176, v176\nv_mfma_f32_16x16x32_bf16 v[172:175], a[80:83], a[12:15], v[172:175]\ns_add_u32 m0, s73, 0x6000\n"
"v_mul_f32 v129, v177, v177\nv_permlane16_swap_b32 v48, v50\nv_mfma_f32_16x16x32_bf16 v[172:175], a[76:79], a[188:191], v[172:175]\nbuffer_load_dwordx4 v251, s[44:47], s78 offen lds\n"
"v_mfma_f32_16x16x32_bf16 v[172:175], a[72:75], a[172:175], v[172:175]\nv_permlane16_swap_b32 v49, v51\nv_permlane16_swap_b32 v52, v54\nv_mfma_f32_16x16x32_bf16 v[172:175], a[68:71], a[156:159], v[172:175]\n"
"v_permlane16_swap_b32 v53, v55\nv_permlane16_swap_b32 v56, v58\nv_mfma_f32_16x16x32_bf16 v[172:175], a[64:67], a[140:143], v[172:175]\nv_permlane16_swap_b32 v57, v59\n"
"v_permlane16_swap_b32 v60, v62\ns_waitcnt vmcnt(14) lgkmcnt(0)\ns_barrier\nv_mfma_f32_16x16x32_bf16 v[160:163], a[96:99], a[192:195], v[160:163]\n"
"s_add_u32 s78, s78, 0x40000\nds_read_b64_tr_b16 a[64:65], v248 offset:32768\nv_permlane16_swap_b32 v61, v63\nv_mfma_f32_16x16x32_bf16 v[160:163], a[100:103], a[208:211], v[160:163]\n"
"s_add_u32 m0, s73, 0x10000\nds_read_b64_tr_b16 a[66:67], v248 offset:33280\nv_permlane16_swap_b32 v64, v66\nv_mfma_f32_16x16x32_bf16 v[160:163], a[104:107], a[224:227], v[160:163]\n"
"buffer_load_dwordx4 v251, s[44:47], s78 offen lds\nv_mfma_f32_16x16x32_bf16 v[160:163], a[108:111], a[240:243], v[160:163]\ns_add_u32 s78, s78, 0x40000\nds_read_b64_tr_b16 a[68:69], v248 offset:36864\n"
"v_permlane16_swap_b32 v65, v67\nv_mfma_f32_16x16x32_bf16 v[160:163], a[112:115], v[0:3], v[160:163]\ns_add_u32 m0, s73, 0x11000\nds_read_b64_tr_b16 a[70:71], v248 offset:37376\n"
"v_permlane16_swap_b32 v68, v70\nv_mfma_f32_16x16x32_bf16 v[160:163], a[116:119], v[16:19], v[160:163]\nbuffer_load_dwordx4 v251, s[44:47], s78 offen lds\nv_mfma_f32_16x16x32_bf16 v[160:163], a[120:123], v[32:35], v[160:163]\n"
"ds_read_b64_tr_b16 a[72:73], v248 offset:40960\nv_permlane16_swap_b32 v69, v71\nv_mfma_f32_16x16x32_bf16 v[164:167], a[120:123], v[36:39], v[164:167]\ns_add_u32 s78, s78, 0x40000\n"
"ds_read_b64_tr_b16 a[74:75], v248 offset:41472\nv_permlane16_swap_b32 v72, v74\nv_mfma_f32_16x16x32_bf16 v[164:167], a[116:119], v[20:23], v[164:167]\ns_add_u32 m0, s73, 0x12000\n"
"ds_read_b64_tr_b16 a[76:77], v248 offset:45056\nv_permlane16_swap_b32 v73, v75\nv_mfma_f32_16x16x32_bf16 v[164:167], a[112:115], v[4:7], v[164:167]\nbuffer_load_dwordx4 v251, s[44:47], s78 offen lds\n"
"v_mfma_f32_16x16x32_bf16 v[164:167], a[108:111], a[244:247], v[164:167]\nds_read_b64_tr_b16 a[78:79], v248 offset:45568\nv_permlane16_swap_b32 v76, v78\nv_mfma_f32_16x16x32_bf16 v[164:167], a[104:107], a[228:231], v[164:167]\n"
"s_add_u32 s78, s78, 0x40000\nds_read_b64_tr_b16 a[80:81], v248 offset:49152\nv_permlane16_swap_b32 v77, v79\nv_mfma_f32_16x16x32_bf16 v[164:167], a[100:103], a[212:215], v[164:167]\n"
"s_add_u32 m0, s73, 0x13000\nds_read_b64_tr_b16 a[82:83], v248 offset:49664\nv_permlane16_swap_b32 v80, v82\nv_mfma_f32_16x16x32_bf16 v[164:167], a[96:99], a[196:199], v[164:167]\n"
"buffer_load_dwordx4 v251, s[44:47], s78 offen lds\nv_mfma_f32_16x16x32_bf16 v[168:171], a[96:99], a[200:203], v[168:171]\ns_add_u32 s78, s78, 0x40000\nds_read_b64_tr_b16 a[84:85], v248 offset:53248\n"
"v_permlane16_swap_b32 v81, v83\nv_mfma_f32_16x16x32_bf16 v[168:171], a[100:103], a[216:219], v[168:171]\ns_add_u32 m0, s73, 0x14000\nds_read_b64_tr_b16 a[86:87], v248 offset:53760\n"
"v_mul_f32 v130, v178, v178\nv_mfma_f32_16x16x32_bf16 v[168:171], a[104:107], a[232:235], v[168:171]\nbuffer_load_dwordx4 v251, s[44:47], s78 offen lds\nv_mfma_f32_16x16x32_bf16 v[168:171], a[108:111], a[248:251], v[168:171]\n"
"ds_read_b64_tr_b16 a[88:89], v248 offset:57344\nv_mul_f32 v131, v179, v179\nv_mfma_f32_16x16x32_bf16 v[168:171], a[112:115], v[8:11], v[168:171]\ns_add_u32 s78, s78, 0x40000\n"
"ds_read_b64_tr_b16 a[90:91], v248 offset:57856\nv_mul_f32 v132, v180, v180\nv_mfma_f32_16x16x32_bf16 v[168:171], a[116:119], v[24:27], v[168:171]\ns_add_u32 m0, s73, 0x15000\n"
"v_mul_f32 v133, v181, v181\nv_fma_f32 v128, v128, v148, v152\nv_mfma_f32_16x16x32_bf16 v[168:171], a[120:123], v[40:43], v[168:171]\nbuffer_load_dwordx4 v251, s[44:47], s78 offen lds\n"
"v_mfma_f32_16x16x32_bf16 v[172:175], a[120:123], v[44:47], v[172:175]\nv_mul_f32 v134, v182, v182\nv_permlane16_swap_b32 v84, v86\nv_mfma_f32_16x16x32_bf16 v[172:175], a[116:119], v[28:31], v[172:175]\n"
"s_add_u32 s78, s78, 0x40000\nv_permlane16_swap_b32 v85, v87\nv_cvt_pk_bf16_f32 v120, v136, v137\nv_mfma_f32_16x16x32_bf16 v[172:175], a[112:115], v[12:15], v[172:175]\n"
"s_add_u32 m0, s73, 0x16000\nv_mul_f32 v135, v183, v183\nv_cvt_pk_bf16_f32 v121, v138, v139\nv_mfma_f32_16x16x32_bf16 v[172:175], a[108:111], a[252:255], v[172:175]\n"
"buffer_load_dwordx4 v251, s[44:47], s78 offen lds\nv_mfma_f32_16x16x32_bf16 v[172:175], a[104:107], a[236:239], v[172:175]\nv_fma_f32 v129, v129, v148, v152\nv_fma_f32 v130, v130, v148, v152\n"
"v_mfma_f32_16x16x32_bf16 v[172:175], a[100:103], a[220:223], v[172:175]\nv_fma_f32 v131, v131, v148, v152\nv_fma_f32 v132, v132, v149, v153\nv_mfma_f32_16x16x32_bf16 v[172:175], a[96:99], a[204:207], v[172:175]\n"
"v_fma_f32 v133, v133, v149, v153\nv_mul_f32 v128, v128, v176\ns_waitcnt vmcnt(14) lgkmcnt(0)\ns_barrier\n"
"v_mfma_f32_16x16x32_bf16 v[208:211], a[64:67], a[128:131], 0\ns_add_u32 s78, s78, 0xffcc0200\nds_read_b64_tr_b16 a[96:97], v249 offset:32768\nv_fma_f32 v134, v134, v149, v153\n"
"v_mfma_f32_16x16x32_bf16 v[208:211], a[68:71], a[144:147], v[208:211]\ns_add_u32 m0, s73, 0x8000\nds_read_b64_tr_b16 a[98:99], v249 offset:33280\nv_fma_f32 v135, v135, v149, v153\n"
"v_mfma_f32_16x16x32_bf16 v[208:211], a[72:75], a[160:163], v[208:211]\nbuffer_load_dwordx4 v251, s[44:47], s78 offen lds\nv_mfma_f32_16x16x32_bf16 v[208:211], a[76:79], a[176:179], v[208:211]\ns_add_u32 s78, s78, 0x40000\n"
"ds_read_b64_tr_b16 a[100:101], v249 offset:36864\nv_mul_f32 v136, v184, v184\nv_mfma_f32_16x16x32_bf16 v[208:211], a[80:83], a[0:3], v[208:211]\ns_add_u32 m0, s73, 0x9000\n"
"ds_read_b64_tr_b16 a[102:103], v249 offset:37376\nv_mul_f32 v137, v185, v185\nv_mfma_f32_16x16x32_bf16 v[208:211], a[84:87], a[16:19], v[208:211]\nbuffer_load_dwordx4 v251, s[44:47], s78 offen lds\n"
"v_mfma_f32_16x16x32_bf16 v[208:211], a[88:91], a[32:35], v[208:211]\nds_read_b64_tr_b16 a[104:105], v249 offset:40960\nv_mul_f32 v138, v186, v186\nv_mfma_f32_16x16x32_bf16 v[212:215], a[88:91], a[36:39], 0\n"
"s_add_u32 s78, s78, 0x40000\nds_read_b64_tr_b16 a[106:107], v249 offset:41472\nv_mul_f32 v129, v129, v177\nv_mfma_f32_16x16x32_bf16 v[212:215], a[84:87], a[20:23], v[212:215]\n"
"s_add_u32 m0, s73, 0xa000\nds_read_b64_tr_b16 a[108:109], v249 offset:45056\nv_mul_f32 v130, v130, v178\nv_mfma_f32_16x16x32_bf16 v[212:215], a[80:83], a[4:7], v[212:215]\n"
"buffer_load_dwordx4 v251, s[44:47], s78 offen lds\nv_mfma_f32_16x16x32_bf16 v[212:215], a[76:79], a[180:183], v[212:215]\nds_read_b64_tr_b16 a[110:111], v249 offset:45568\nv_mul_f32 v131, v131, v179\n"
"v_mfma_f32_16x16x32_bf16 v[212:215], a[72:75], a[164:167], v[212:215]\ns_add_u32 s78, s78, 0x40000\nds_read_b64_tr_b16 a[112:113], v249 offset:49152\nv_mul_f32 v132, v132, v180\n"
"v_mfma_f32_16x16x32_bf16 v[212:215], a[68:71], a[148:151], v[212:215]\ns_add_u32 m0, s73, 0xb000\nds_read_b64_tr_b16 a[114:115], v249 offset:49664\nv_mul_f32 v139, v187, v187\n"
"v_mfma_f32_16x16x32_bf16 v[212:215], a[64:67], a[132:135], v[212:215]\nbuffer_load_dwordx4 v251, s[44:47], s78 offen lds\nv_mfma_f32_16x16x32_bf16 v[216:219], a[64:67], a[136:139], 0\ns_add_u32 s78, s78, 0x40000\n"
"ds_read_b64_tr_b16 a[116:117], v249 offset:53248\nv_cvt_pk_bf16_f32 v124, v140, v141\nv_mfma_f32_16x16x32_bf16 v[216:219], a[68:71], a[152:155], v[216:219]\ns_add_u32 m0, s73, 0xc000\n"
"ds_read_b64_tr_b16 a[118:119], v249 offset:53760\nv_mul_f32 v133, v133, v181\nv_mfma_f32_16x16x32_bf16 v[216:219], a[72:75], a[168:171], v[216:219]\nbuffer_load_dwordx4 v251, s[44:47], s78 offen lds\n"
"v_mfma_f32_16x16x32_bf16 v[216:219], a[76:79], a[184:187], v[216:219]\nds_read_b64_tr_b16 a[120:121], v249 offset:57344\nv_mul_f32 v134, v134, v182\nv_mfma_f32_16x16x32_bf16 v[216:219], a[80:83], a[8:11], v[216:219]\n"
"s_add_u32 s78, s78, 0x40000\nds_read_b64_tr_b16 a[122:123], v249 offset:57856\nv_mul_f32 v135, v135, v183\nv_mfma_f32_16x16x32_bf16 v[216:219], a[84:87], a[24:27], v[216:219]\n"
"s_add_u32 m0, s73, 0xd000\nv_exp_f32 v128, v128\nv_mfma_f32_16x16x32_bf16 v[216:219], a[88:91], a[40:43], v[216:219]\nbuffer_load_dwordx4 v251, s[44:47], s78 offen lds\n"
"v_mfma_f32_16x16x32_bf16 v[220:223], a[88:91], a[44:47], 0\nv_fma_f32 v136, v136, v150, v154\nv_fma_f32 v137, v137, v150, v154\nv_mfma_f32_16x16x32_bf16 v[220:223], a[84:87], a[28:31], v[220:223]\n"
"s_add_u32 s78, s78, 0x40000\nv_fma_f32 v138, v138, v150, v154\nv_fma_f32 v139, v139, v150, v154\nv_mfma_f32_16x16x32_bf16 v[220:223], a[80:83], a[12:15], v[220:223]\n"
"s_add_u32 m0, s73, 0xe000\nv_exp_f32 v129, v129\nv_mfma_f32_16x16x32_bf16 v[220:223], a[76:79], a[188:191], v[220:223]\nbuffer_load_dwordx4 v251, s[44:47], s78 offen lds\n"
"v_mfma_f32_16x16x32_bf16 v[220:223], a[72:75], a[172:175], v[220:223]\nv_exp_f32 v130, v130\nv_mfma_f32_16x16x32_bf16 v[220:223], a[68:71], a[156:159], v[220:223]\nv_exp_f32 v131, v131\n"
"v_mfma_f32_16x16x32_bf16 v[220:223], a[64:67], a[140:143], v[220:223]\nv_exp_f32 v132, v132\ns_waitcnt vmcnt(14) lgkmcnt(0)\ns_barrier\n"
"v_mfma_f32_16x16x32_bf16 v[208:211], a[96:99], a[192:195], v[208:211]\ns_add_u32 s78, s78, 0x40000\nds_read_b64_tr_b16 a[64:65], v248 offset:0\nv_cvt_pk_bf16_f32 v125, v142, v143\n"
"v_mfma_f32_16x16x32_bf16 v[208:211], a[100:103], a[208:211], v[208:211]\ns_add_u32 m0, s73, 0x18000\nds_read_b64_tr_b16 a[66:67], v248 offset:512\nv_mul_f32 v140, v188, v188\n"
"v_mfma_f32_16x16x32_bf16 v[208:211], a[104:107], a[224:227], v[208:211]\nbuffer_load_dwordx4 v251, s[44:47], s78 offen lds\nv_mfma_f32_16x16x32_bf16 v[208:211], a[108:111], a[240:243], v[208:211]\ns_add_u32 s78, s78, 0x40000\n"
"ds_read_b64_tr_b16 a[68:69], v248 offset:4096\nv_fma_f32 v128, v128, v156, v156\nv_mfma_f32_16x16x32_bf16 v[208:211], a[112:115], v[0:3], v[208:211]\ns_add_u32 m0, s73, 0x19000\n"
"ds_read_b64_tr_b16 a[70:71], v248 offset:4608\nv_mul_f32 v136, v136, v184\nv_mfma_f32_16x16x32_bf16 v[208:211], a[116:119], v[16:19], v[208:211]\nbuffer_load_dwordx4 v251, s[44:47], s78 offen lds\n"
"v_mfma_f32_16x16x32_bf16 v[208:211], a[120:123], v[32:35], v[208:211]\nds_read_b64_tr_b16 a[72:73], v248 offset:8192\nv_mul_f32 v137, v137, v185\nv_mfma_f32_16x16x32_bf16 v[212:215], a[120:123], v[36:39], v[212:215]\n"
"s_add_u32 s78, s78, 0x40000\nds_read_b64_tr_b16 a[74:75], v248 offset:8704\nv_mul_f32 v138, v138, v186\nv_mfma_f32_16x16x32_bf16 v[212:215], a[116:119], v[20:23], v[212:215]\n"
"s_add_u32 m0, s73, 0x1a000\nds_read_b64_tr_b16 a[76:77], v248 offset:12288\nv_fma_f32 v129, v129, v156, v156\nv_mfma_f32_16x16x32_bf16 v[212:215], a[112:115], v[4:7], v[212:215]\n"
"buffer_load_dwordx4 v251, s[44:47], s78 offen lds\nv_mfma_f32_16x16x32_bf16 v[212:215], a[108:111], a[244:247], v[212:215]\nds_read_b64_tr_b16 a[78:79], v248 offset:12800\nv_mul_f32 v208, v208, v236\n"
"v_mfma_f32_16x16x32_bf16 v[212:215], a[104:107], a[228:231], v[212:215]\ns_add_u32 s78, s78, 0x40000\nds_read_b64_tr_b16 a[80:81], v248 offset:16384\nv_mul_f32 v209, v209, v236\n"
"v_mfma_f32_16x16x32_bf16 v[212:215], a[100:103], a[212:215], v[212:215]\ns_add_u32 m0, s73, 0x1b000\nds_read_b64_tr_b16 a[82:83], v248 offset:16896\nv_mul_f32 v210, v210, v236\n"
"v_mfma_f32_16x16x32_bf16 v[212:215], a[96:99], a[196:199], v[212:215]\nbuffer_load_dwordx4 v251, s[44:47], s78 offen lds\nv_mfma_f32_16x16x32_bf16 v[216:219], a[96:99], a[200:203], v[216:219]\ns_add_u32 s78, s78, 0x40000\n"
"ds_read_b64_tr_b16 a[84:85], v248 offset:20480\nv_mul_f32 v211, v211, v236\nv_mfma_f32_16x16x32_bf16 v[216:219], a[100:103], a[216:219], v[216:219]\ns_add_u32 m0, s73, 0x1c000\n"
"ds_read_b64_tr_b16 a[86:87], v248 offset:20992\nv_cvt_pk_bf16_f32 v88, v208, v209\nv_mfma_f32_16x16x32_bf16 v[216:219], a[104:107], a[232:235], v[216:219]\nbuffer_load_dwordx4 v251, s[44:47], s78 offen lds\n"
"v_mfma_f32_16x16x32_bf16 v[216:219], a[108:111], a[248:251], v[216:219]\nds_read_b64_tr_b16 a[88:89], v248 offset:24576\nv_mul_f32 v212, v212, v237\nv_mfma_f32_16x16x32_bf16 v[216:219], a[112:115], v[8:11], v[216:219]\n"
"s_add_u32 s78, s78, 0x40000\nds_read_b64_tr_b16 a[90:91], v248 offset:25088\nv_mul_f32 v213, v213, v237\nv_mfma_f32_16x16x32_bf16 v[216:219], a[116:119], v[24:27], v[216:219]\n"
"s_add_u32 m0, s73, 0x1d000\nv_mul_f32 v214, v214, v237\nv_mul_f32 v215, v215, v237\nv_mfma_f32_16x16x32_bf16 v[216:219], a[120:123], v[40:43], v[216:219]\n"
"buffer_load_dwordx4 v251, s[44:47], s78 offen lds\nv_mfma_f32_16x16x32_bf16 v[220:223], a[120:123], v[44:47], v[220:223]\nv_cvt_pk_bf16_f32 v89, v210, v211\nv_cvt_pk_bf16_f32 v90, v212, v213\n"
"v_mfma_f32_16x16x32_bf16 v[220:223], a[116:119], v[28:31], v[220:223]\ns_add_u32 s78, s78, 0x40000\nv_cvt_pk_bf16_f32 v91, v214, v215\nv_permlane16_swap_b32 v88, v90\n"
"v_mfma_f32_16x16x32_bf16 v[220:223], a[112:115], v[12:15], v[220:223]\ns_add_u32 m0, s73, 0x1e000\nv_permlane16_swap_b32 v89, v91\nv_fma_f32 v130, v130, v156, v156\n"
"v_mfma_f32_16x16x32_bf16 v[220:223], a[108:111], a[252:255], v[220:223]\nbuffer_load_dwordx4 v251, s[44:47], s78 offen lds\nv_mfma_f32_16x16x32_bf16 v[220:223], a[104:107], a[236:239], v[220:223]\nv_mul_f32 v216, v216, v238\n"
"v_mul_f32 v217, v217, v238\nv_mfma_f32_16x16x32_bf16 v[220:223], a[100:103], a[220:223], v[220:223]\nv_mul_f32 v218, v218, v238\nv_mul_f32 v219, v219, v238\n"
"v_mfma_f32_16x16x32_bf16 v[220:223], a[96:99], a[204:207], v[220:223]\nv_cvt_pk_bf16_f32 v92, v216, v217\nv_cvt_pk_bf16_f32 v93, v218, v219\ns_waitcnt vmcnt(14) lgkmcnt(0)\n"
"s_barrier\nv_mfma_f32_16x16x32_bf16 v[192:195], a[64:67], a[128:131], 0\ns_add_u32 s79, s79, 0x0\nds_read_b64_tr_b16 a[96:97], v249 offset:0\n"
"v_fma_f32 v131, v131, v156, v156\nv_mfma_f32_16x16x32_bf16 v[192:195], a[68:71], a[144:147], v[192:195]\ns_add_u32 m0, s73, 0x0\nds_read_b64_tr_b16 a[98:99], v249 offset:512\n"
"v_mul_f32 v139, v139, v187\nv_mfma_f32_16x16x32_bf16 v[192:195], a[72:75], a[160:163], v[192:195]\nbuffer_load_dwordx4 v231, s[48:51], s79 offen lds\nv_mfma_f32_16x16x32_bf16 v[192:195], a[76:79], a[176:179], v[192:195]\n"
"s_add_u32 s79, s79, 0x80\nds_read_b64_tr_b16 a[100:101], v249 offset:4096\nv_mul_f32 v220, v220, v239\nv_mfma_f32_16x16x32_bf16 v[192:195], a[80:83], a[0:3], v[192:195]\n"
"s_add_u32 m0, s73, 0x2000\nds_read_b64_tr_b16 a[102:103], v249 offset:4608\nv_mul_f32 v221, v221, v239\nv_mfma_f32_16x16x32_bf16 v[192:195], a[84:87], a[16:19], v[192:195]\n"
"buffer_load_dwordx4 v231, s[48:51], s79 offen lds\nv_mfma_f32_16x16x32_bf16 v[192:195], a[88:91], a[32:35], v[192:195]\ns_add_u32 s79, s79, 0x80\nds_read_b64_tr_b16 a[104:105], v249 offset:8192\n"
"v_mul_f32 v222, v222, v239\nv_mfma_f32_16x16x32_bf16 v[196:199], a[88:91], a[36:39], 0\ns_add_u32 m0, s73, 0x4000\nds_read_b64_tr_b16 a[106:107], v249 offset:8704\n"
"v_mul_f32 v223, v223, v239\nv_mfma_f32_16x16x32_bf16 v[196:199], a[84:87], a[20:23], v[196:199]\nbuffer_load_dwordx4 v231, s[48:51], s79 offen lds\nv_mfma_f32_16x16x32_bf16 v[196:199], a[80:83], a[4:7], v[196:199]\n"
"s_add_u32 s79, s79, 0x80\nds_read_b64_tr_b16 a[108:109], v249 offset:12288\nv_cvt_pk_bf16_f32 v94, v220, v221\nv_mfma_f32_16x16x32_bf16 v[196:199], a[76:79], a[180:183], v[196:199]\n"
"s_add_u32 m0, s73, 0x6000\nds_read_b64_tr_b16 a[110:111], v249 offset:12800\nv_cvt_pk_bf16_f32 v95, v222, v223\nv_mfma_f32_16x16x32_bf16 v[196:199], a[72:75], a[164:167], v[196:199]\n"
"buffer_load_dwordx4 v231, s[48:51], s79 offen lds\nv_mfma_f32_16x16x32_bf16 v[196:199], a[68:71], a[148:151], v[196:199]\nds_read_b64_tr_b16 a[112:113], v249 offset:16384\nv_permlane16_swap_b32 v92, v94\n"
"v_mfma_f32_16x16x32_bf16 v[196:199], a[64:67], a[132:135], v[196:199]\ns_add_u32 s79, s79, 0x3e80\nds_read_b64_tr_b16 a[114:115], v249 offset:16896\nv_permlane16_swap_b32 v93, v95\n"
"v_mfma_f32_16x16x32_bf16 v[200:203], a[64:67], a[136:139], 0\ns_add_u32 m0, s73, 0x1000\nds_read_b64_tr_b16 a[116:117], v249 offset:20480\nv_mul_f32 v141, v189, v189\n"
"v_mfma_f32_16x16x32_bf16 v[200:203], a[68:71], a[152:155], v[200:203]\nbuffer_load_dwordx4 v231, s[48:51], s79 offen lds\nv_mfma_f32_16x16x32_bf16 v[200:203], a[72:75], a[168:171], v[200:203]\ns_add_u32 s79, s79, 0x80\n"
"ds_read_b64_tr_b16 a[118:119], v249 offset:20992\nv_mul_f32 v142, v190, v190\nv_mfma_f32_16x16x32_bf16 v[200:203], a[76:79], a[184:187], v[200:203]\ns_add_u32 m0, s73, 0x3000\n"
"ds_read_b64_tr_b16 a[120:121], v249 offset:24576\nv_fma_f32 v132, v132, v157, v157\nv_mfma_f32_16x16x32_bf16 v[200:203], a[80:83], a[8:11], v[200:203]\nbuffer_load_dwordx4 v231, s[48:51], s79 offen lds\n"
"v_mfma_f32_16x16x32_bf16 v[200:203], a[84:87], a[24:27], v[200:203]\ns_add_u32 s79, s79, 0x80\nds_read_b64_tr_b16 a[122:123], v249 offset:25088\nv_fma_f32 v140, v140, v151, v155\n"
"v_mfma_f32_16x16x32_bf16 v[200:203], a[88:91], a[40:43], v[200:203]\ns_add_u32 m0, s73, 0x5000\nv_exp_f32 v133, v133\nv_mfma_f32_16x16x32_bf16 v[204:207], a[88:91], a[44:47], 0\n"
"buffer_load_dwordx4 v231, s[48:51], s79 offen lds\nv_mfma_f32_16x16x32_bf16 v[204:207], a[84:87], a[28:31], v[204:207]\ns_add_u32 s79, s79, 0x80\nv_exp_f32 v134, v134\n"
"v_mfma_f32_16x16x32_bf16 v[204:207], a[80:83], a[12:15], v[204:207]\ns_add_u32 m0, s73, 0x7000\nv_exp_f32 v135, v135\nv_mfma_f32_16x16x32_bf16 v[204:207], a[76:79], a[188:191], v[204:207]\n"
"buffer_load_dwordx4 v231, s[48:51], s79 offen lds\nv_mfma_f32_16x16x32_bf16 v[204:207], a[72:75], a[172:175], v[204:207]\nv_fma_f32 v133, v133, v157, v157\nv_mul_f32 v143, v191, v191\n"
"v_mfma_f32_16x16x32_bf16 v[204:207], a[68:71], a[156:159], v[204:207]\nv_fma_f32 v134, v134, v157, v157\nv_fma_f32 v135, v135, v157, v157\nv_mfma_f32_16x16x32_bf16 v[204:207], a[64:67], a[140:143], v[204:207]\n"
"v_exp_f32 v136, v136\ns_waitcnt vmcnt(15) lgkmcnt(0)\ns_barrier\nv_mfma_f32_16x16x32_bf16 v[192:195], a[96:99], a[192:195], v[192:195]\n"
"s_add_u32 m0, s73, 0x10000\nds_read_b64_tr_b16 a[64:65], v248 offset:32768\nv_fma_f32 v141, v141, v151, v155\nv_mfma_f32_16x16x32_bf16 v[192:195], a[100:103], a[208:211], v[192:195]\n"
"s_add_u32 s79, s79, 0x3e80\nbuffer_load_dwordx4 v231, s[48:51], s79 offen lds\nv_mfma_f32_16x16x32_bf16 v[192:195], a[104:107], a[224:227], v[192:195]\ns_add_u32 m0, s73, 0x12000\n"
"ds_read_b64_tr_b16 a[66:67], v248 offset:33280\nv_fma_f32 v142, v142, v151, v155\nv_mfma_f32_16x16x32_bf16 v[192:195], a[108:111], a[240:243], v[192:195]\ns_add_u32 s79, s79, 0x80\n"
"buffer_load_dwordx4 v231, s[48:51], s79 offen lds\nv_mfma_f32_16x16x32_bf16 v[192:195], a[112:115], v[0:3], v[192:195]\ns_add_u32 m0, s73, 0x14000\nds_read_b64_tr_b16 a[68:69], v248 offset:36864\n"
"v_mul_f32 v140, v140, v188\nv_mfma_f32_16x16x32_bf16 v[192:195], a[116:119], v[16:19], v[192:195]\ns_add_u32 s79, s79, 0x80\nbuffer_load_dwordx4 v231, s[48:51], s79 offen lds\n"
"v_mfma_f32_16x16x32_bf16 v[192:195], a[120:123], v[32:35], v[192:195]\ns_add_u32 m0, s73, 0x16000\nds_read_b64_tr_b16 a[70:71], v248 offset:37376\nv_fma_f32 v143, v143, v151, v155\n"
"v_mfma_f32_16x16x32_bf16 v[196:199], a[120:123], v[36:39], v[196:199]\ns_add_u32 s79, s79, 0x80\nbuffer_load_dwordx4 v231, s[48:51], s79 offen lds\nv_mfma_f32_16x16x32_bf16 v[196:199], a[116:119], v[20:23], v[196:199]\n"
"s_add_u32 m0, s73, 0x11000\nds_read_b64_tr_b16 a[72:73], v248 offset:40960\nv_fma_f32 v136, v136, v158, v158\nv_mfma_f32_16x16x32_bf16 v[196:199], a[112:115], v[4:7], v[196:199]\n"
"s_add_u32 s79, s79, 0x3e80\nbuffer_load_dwordx4 v231, s[48:51], s79 offen lds\nv_mfma_f32_16x16x32_bf16 v[196:199], a[108:111], a[244:247], v[196:199]\ns_add_u32 m0, s73, 0x13000\n"
"ds_read_b64_tr_b16 a[74:75], v248 offset:41472\nv_mul_f32 v192, v192, v236\nv_mfma_f32_16x16x32_bf16 v[196:199], a[104:107], a[228:231], v[196:199]\ns_add_u32 s79, s79, 0x80\n"
"buffer_load_dwordx4 v231, s[48:51], s79 offen lds\nv_mfma_f32_16x16x32_bf16 v[196:199], a[100:103], a[212:215], v[196:199]\ns_add_u32 m0, s73, 0x15000\nds_read_b64_tr_b16 a[76:77], v248 offset:45056\n"
"v_mul_f32 v193, v193, v236\nv_mfma_f32_16x16x32_bf16 v[196:199], a[96:99], a[196:199], v[196:199]\ns_add_u32 s79, s79, 0x80\nbuffer_load_dwordx4 v231, s[48:51], s79 offen lds\n"
"v_mfma_f32_16x16x32_bf16 v[200:203], a[96:99], a[200:203], v[200:203]\ns_add_u32 m0, s73, 0x17000\nds_read_b64_tr_b16 a[78:79], v248 offset:45568\nv_mul_f32 v194, v194, v236\n"
"v_mfma_f32_16x16x32_bf16 v[200:203], a[100:103], a[216:219], v[200:203]\ns_add_u32 s79, s79, 0x80\nbuffer_load_dwordx4 v231, s[48:51], s79 offen lds\nv_mfma_f32_16x16x32_bf16 v[200:203], a[104:107], a[232:235], v[200:203]\n"
"ds_read_b64_tr_b16 a[80:81], v248 offset:49152\nv_mul_f32 v195, v195, v236\nv_mfma_f32_16x16x32_bf16 v[200:203], a[108:111], a[248:251], v[200:203]\nds_read_b64_tr_b16 a[82:83], v248 offset:49664\n"
"v_mul_f32 v196, v196, v237\nv_mfma_f32_16x16x32_bf16 v[200:203], a[112:115], v[8:11], v[200:203]\nds_read_b64_tr_b16 a[84:85], v248 offset:53248\nv_mul_f32 v197, v197, v237\n"
"v_mfma_f32_16x16x32_bf16 v[200:203], a[116:119], v[24:27], v[200:203]\nds_read_b64_tr_b16 a[86:87], v248 offset:53760\nv_mul_f32 v198, v198, v237\nv_mfma_f32_16x16x32_bf16 v[200:203], a[120:123], v[40:43], v[200:203]\n"
"ds_read_b64_tr_b16 a[88:89], v248 offset:57344\nv_mul_f32 v199, v199, v237\nv_mfma_f32_16x16x32_bf16 v[204:207], a[120:123], v[44:47], v[204:207]\nds_read_b64_tr_b16 a[90:91], v248 offset:57856\n"
"v_cvt_pk_bf16_f32 v96, v192, v193\nv_mfma_f32_16x16x32_bf16 v[204:207], a[116:119], v[28:31], v[204:207]\nv_cvt_pk_bf16_f32 v97, v194, v195\nv_cvt_pk_bf16_f32 v98, v196, v197\n"
"v_mfma_f32_16x16x32_bf16 v[204:207], a[112:115], v[12:15], v[204:207]\nv_cvt_pk_bf16_f32 v99, v198, v199\nv_permlane16_swap_b32 v96, v98\nv_mfma_f32_16x16x32_bf16 v[204:207], a[108:111], a[252:255], v[204:207]\n"
"v_mul_f32 v200, v200, v238\nv_mul_f32 v201, v201, v238\nv_mfma_f32_16x16x32_bf16 v[204:207], a[104:107], a[236:239], v[204:207]\nv_mul_f32 v202, v202, v238\n"
"v_mul_f32 v203, v203, v238\nv_mfma_f32_16x16x32_bf16 v[204:207], a[100:103], a[220:223], v[204:207]\nv_cvt_pk_bf16_f32 v100, v200, v201\nv_cvt_pk_bf16_f32 v101, v202, v203\n"
"v_mfma_f32_16x16x32_bf16 v[204:207], a[96:99], a[204:207], v[204:207]\nv_permlane16_swap_b32 v97, v99\nv_mul_f32 v141, v141, v189\ns_waitcnt vmcnt(16) lgkmcnt(0)\n"
"s_barrier\nv_mfma_f32_16x16x32_bf16 v[208:211], a[64:67], a[128:131], 0\nds_read_b64_tr_b16 a[96:97], v249 offset:32768\nds_read_b64_tr_b16 a[98:99], v249 offset:33280\n"
"v_mfma_f32_16x16x32_bf16 v[208:211], a[68:71], a[144:147], v[208:211]\nds_read_b64_tr_b16 a[100:101], v249 offset:36864\nds_read_b64_tr_b16 a[102:103], v249 offset:37376\nv_mfma_f32_16x16x32_bf16 v[208:211], a[72:75], a[160:163], v[208:211]\n"
"ds_write_b128 v252, v[48:51] offset:0\nds_read_b64_tr_b16 a[104:105], v249 offset:40960\nv_mfma_f32_16x16x32_bf16 v[208:211], a[76:79], a[176:179], v[208:211]\nds_read_b64_tr_b16 a[106:107], v249 offset:41472\n"
"ds_read_b64_tr_b16 a[108:109], v249 offset:45056\nv_mfma_f32_16x16x32_bf16 v[208:211], a[80:83], a[0:3], v[208:211]\nds_write_b128 v252, v[52:55] offset:2048\nds_read_b64_tr_b16 a[110:111], v249 offset:45568\n"
"v_mfma_f32_16x16x32_bf16 v[208:211], a[84:87], a[16:19], v[208:211]\nds_read_b64_tr_b16 a[112:113], v249 offset:49152\nds_read_b64_tr_b16 a[114:115], v249 offset:49664\nv_mfma_f32_16x16x32_bf16 v[208:211], a[88:91], a[32:35], v[208:211]\n"
"ds_write_b128 v252, v[56:59] offset:8192\nds_read_b64_tr_b16 a[116:117], v249 offset:53248\nv_mfma_f32_16x16x32_bf16 v[212:215], a[88:91], a[36:39], 0\nds_read_b64_tr_b16 a[118:119], v249 offset:53760\n"
"ds_read_b64_tr_b16 a[120:121], v249 offset:57344\nv_mfma_f32_16x16x32_bf16 v[212:215], a[84:87], a[20:23], v[212:215]\nds_write_b128 v252, v[60:63] offset:10240\nds_read_b64_tr_b16 a[122:123], v249 offset:57856\n"
"v_mfma_f32_16x16x32_bf16 v[212:215], a[80:83], a[4:7], v[212:215]\nv_mul_f32 v204, v204, v239\nv_mul_f32 v205, v205, v239\nv_mfma_f32_16x16x32_bf16 v[212:215], a[76:79], a[180:183], v[212:215]\n"
"ds_write_b128 v104, v[64:67] offset:0\nv_mfma_f32_16x16x32_bf16 v[212:215], a[72:75], a[164:167], v[212:215]\nv_mul_f32 v206, v206, v239\nv_mul_f32 v207, v207, v239\n"
"v_mfma_f32_16x16x32_bf16 v[212:215], a[68:71], a[148:151], v[212:215]\nds_write_b128 v104, v[68:71] offset:2048\nv_mfma_f32_16x16x32_bf16 v[212:215], a[64:67], a[132:135], v[212:215]\nv_cvt_pk_bf16_f32 v102, v204, v205\n"
"v_cvt_pk_bf16_f32 v103, v206, v207\nv_mfma_f32_16x16x32_bf16 v[216:219], a[64:67], a[136:139], 0\nds_write_b128 v104, v[72:75] offset:8192\nv_mfma_f32_16x16x32_bf16 v[216:219], a[68:71], a[152:155], v[216:219]\n"
"v_permlane16_swap_b32 v100, v102\nv_permlane16_swap_b32 v101, v103\nv_mfma_f32_16x16x32_bf16 v[216:219], a[72:75], a[168:171], v[216:219]\nds_write_b128 v104, v[76:79] offset:10240\n"
"v_mfma_f32_16x16x32_bf16 v[216:219], a[76:79], a[184:187], v[216:219]\nv_rcp_f32 v128, v128\nv_mfma_f32_16x16x32_bf16 v[216:219], a[80:83], a[8:11], v[216:219]\nds_write_b128 v104, v[80:83] offset:16384\n"
"v_mfma_f32_16x16x32_bf16 v[216:219], a[84:87], a[24:27], v[216:219]\nv_exp_f32 v137, v137\nv_mfma_f32_16x16x32_bf16 v[216:219], a[88:91], a[40:43], v[216:219]\nds_write_b128 v104, v[84:87] offset:18432\n"
"v_mfma_f32_16x16x32_bf16 v[220:223], a[88:91], a[44:47], 0\nv_exp_f32 v138, v138\nv_mfma_f32_16x16x32_bf16 v[220:223], a[84:87], a[28:31], v[220:223]\nds_write_b128 v104, v[88:91] offset:24576\n"
"v_mfma_f32_16x16x32_bf16 v[220:223], a[80:83], a[12:15], v[220:223]\nv_rcp_f32 v129, v129\nv_mfma_f32_16x16x32_bf16 v[220:223], a[76:79], a[188:191], v[220:223]\nds_write_b128 v104, v[92:95] offset:26624\n"
"v_mfma_f32_16x16x32_bf16 v[220:223], a[72:75], a[172:175], v[220:223]\nv_rcp_f32 v130, v130\nv_mfma_f32_16x16x32_bf16 v[220:223], a[68:71], a[156:159], v[220:223]\nds_write_b128 v252, v[96:99] offset:16384\n"
"v_mfma_f32_16x16x32_bf16 v[220:223], a[64:67], a[140:143], v[220:223]\nv_rcp_f32 v131, v131\ns_waitcnt vmcnt(8) lgkmcnt(0)\ns_barrier\n"
"v_mfma_f32_16x16x32_bf16 v[208:211], a[96:99], a[192:195], v[208:211]\ns_add_u32 s79, s79, 0x3e80\nds_read_b128 a[128:131], v225 offset:0\nds_read_b128 a[132:135], v226 offset:0\n"
"v_mfma_f32_16x16x32_bf16 v[208:211], a[100:103], a[208:211], v[208:211]\ns_add_u32 m0, s73, 0x18000\nds_read_b128 a[136:139], v225 offset:8192\nds_read_b128 a[140:143], v226 offset:8192\n"
"v_mfma_f32_16x16x32_bf16 v[208:211], a[104:107], a[224:227], v[208:211]\nbuffer_load_dwordx4 v231, s[48:51], s79 offen lds\nv_mfma_f32_16x16x32_bf16 v[208:211], a[108:111], a[240:243], v[208:211]\ns_add_u32 s79, s79, 0x80\n"
"ds_read_b128 a[144:147], v225 offset:16384\nds_read_b128 a[148:151], v226 offset:16384\nv_mfma_f32_16x16x32_bf16 v[208:211], a[112:115], v[0:3], v[208:211]\ns_add_u32 m0, s73, 0x1a000\n"
"ds_read_b128 a[152:155], v225 offset:24576\nds_read_b128 a[156:159], v226 offset:24576\nv_mfma_f32_16x16x32_bf16 v[208:211], a[116:119], v[16:19], v[208:211]\nbuffer_load_dwordx4 v231, s[48:51], s79 offen lds\n"
"v_mfma_f32_16x16x32_bf16 v[208:211], a[120:123], v[32:35], v[208:211]\ns_add_u32 s79, s79, 0x80\nds_read_b128 a[160:163], v225 offset:2048\nds_read_b128 a[164:167], v226 offset:2048\n"
"v_mfma_f32_16x16x32_bf16 v[212:215], a[120:123], v[36:39], v[212:215]\ns_add_u32 m0, s73, 0x1c000\nds_read_b128 a[168:171], v225 offset:10240\nds_read_b128 a[172:175], v226 offset:10240\n"
"v_mfma_f32_16x16x32_bf16 v[212:215], a[116:119], v[20:23], v[212:215]\nbuffer_load_dwordx4 v231, s[48:51], s79 offen lds\nv_mfma_f32_16x16x32_bf16 v[212:215], a[112:115], v[4:7], v[212:215]\ns_add_u32 s79, s79, 0x80\n"
"ds_read_b128 a[176:179], v225 offset:18432\nds_read_b128 a[180:183], v226 offset:18432\nv_mfma_f32_16x16x32_bf16 v[212:215], a[108:111], a[244:247], v[212:215]\ns_add_u32 m0, s73, 0x1e000\n"
"ds_read_b128 a[184:187], v225 offset:26624\nds_read_b128 a[188:191], v226 offset:26624\nv_mul_f32 v208, v208, v236\nv_mul_f32 v209, v209, v236\n"
"v_mfma_f32_16x16x32_bf16 v[212:215], a[104:107], a[228:231], v[212:215]\nbuffer_load_dwordx4 v231, s[48:51], s79 offen lds\nv_mfma_f32_16x16x32_bf16 v[212:215], a[100:103], a[212:215], v[212:215]\nds_read_b128 a[0:3], v253 offset:0\n"
"ds_read_b128 a[4:7], v253 offset:1024\nv_mul_f32 v210, v210, v236\nv_mul_f32 v211, v211, v236\nv_mfma_f32_16x16x32_bf16 v[212:215], a[96:99], a[196:199], v[212:215]\n"
"s_add_u32 s79, s79, 0x3e80\nds_read_b128 a[8:11], v253 offset:2048\nds_read_b128 a[12:15], v253 offset:3072\nv_cvt_pk_bf16_f32 v208, v208, v209\n"
"v_cvt_pk_bf16_f32 v209, v210, v211\nv_mfma_f32_16x16x32_bf16 v[216:219], a[96:99], a[200:203], v[216:219]\ns_add_u32 m0, s73, 0x19000\nds_read_b128 a[16:19], v253 offset:4096\n"
"ds_read_b128 a[20:23], v253 offset:5120\nv_mfma_f32_16x16x32_bf16 v[216:219], a[100:103], a[216:219], v[216:219]\nbuffer_load_dwordx4 v231, s[48:51], s79 offen lds\nv_mfma_f32_16x16x32_bf16 v[216:219], a[104:107], a[232:235], v[216:219]\n"
"s_add_u32 s79, s79, 0x80\nds_read_b128 a[24:27], v253 offset:6144\nds_read_b128 a[28:31], v253 offset:7168\nv_mfma_f32_16x16x32_bf16 v[216:219], a[108:111], a[248:251], v[216:219]\n"
"s_add_u32 m0, s73, 0x1b000\nds_read_b128 a[32:35], v105 offset:0\nds_read_b128 a[36:39], v105 offset:1024\nv_mul_f32 v212, v212, v237\n"
"v_mul_f32 v213, v213, v237\nv_mfma_f32_16x16x32_bf16 v[216:219], a[112:115], v[8:11], v[216:219]\nbuffer_load_dwordx4 v231, s[48:51], s79 offen lds\nv_mfma_f32_16x16x32_bf16 v[216:219], a[116:119], v[24:27], v[216:219]\n"
"s_add_u32 s79, s79, 0x80\nds_read_b128 a[40:43], v105 offset:2048\nds_read_b128 a[44:47], v105 offset:3072\nv_mul_f32 v214, v214, v237\n"
"v_mul_f32 v215, v215, v237\nv_mfma_f32_16x16x32_bf16 v[216:219], a[120:123], v[40:43], v[216:219]\ns_add_u32 m0, s73, 0x1d000\nds_read_b128 a[48:51], v105 offset:4096\n"
"ds_read_b128 a[52:55], v105 offset:5120\nv_cvt_pk_bf16_f32 v210, v212, v213\nv_cvt_pk_bf16_f32 v211, v214, v215\nv_mfma_f32_16x16x32_bf16 v[220:223], a[120:123], v[44:47], v[220:223]\n"
"buffer_load_dwordx4 v231, s[48:51], s79 offen lds\nv_mfma_f32_16x16x32_bf16 v[220:223], a[116:119], v[28:31], v[220:223]\ns_add_u32 s79, s79, 0x80\nds_read_b128 a[56:59], v105 offset:6144\n"
"ds_read_b128 a[60:63], v105 offset:7168\nv_permlane16_swap_b32 v208, v210\nv_permlane16_swap_b32 v209, v211\nv_mfma_f32_16x16x32_bf16 v[220:223], a[112:115], v[12:15], v[220:223]\n"
"s_add_u32 m0, s73, 0x1f000\nds_read_b128 a[64:67], v105 offset:16384\nds_read_b128 a[68:71], v105 offset:17408\nv_exp_f32 v139, v139\n"
"v_mfma_f32_16x16x32_bf16 v[220:223], a[108:111], a[252:255], v[220:223]\nbuffer_load_dwordx4 v231, s[48:51], s79 offen lds\nv_mfma_f32_16x16x32_bf16 v[220:223], a[104:107], a[236:239], v[220:223]\nds_read_b128 a[72:75], v105 offset:18432\n"
"ds_read_b128 a[76:79], v105 offset:19456\nv_mul_f32 v216, v216, v238\nv_mul_f32 v217, v217, v238\nv_mfma_f32_16x16x32_bf16 v[220:223], a[100:103], a[220:223], v[220:223]\n"
"ds_read_b128 a[80:83], v105 offset:20480\nds_read_b128 a[84:87], v105 offset:21504\nv_mul_f32 v218, v218, v238\nv_mul_f32 v219, v219, v238\n"
"v_mfma_f32_16x16x32_bf16 v[220:223], a[96:99], a[204:207], v[220:223]\nds_read_b128 a[88:91], v105 offset:22528\nds_read_b128 a[92:95], v105 offset:23552\nv_cvt_pk_bf16_f32 v216, v216, v217\n"
"v_cvt_pk_bf16_f32 v217, v218, v219\nds_write_b128 v252, v[100:103] offset:18432\nds_write_b128 v252, v[208:211] offset:24576\ns_nop 1\n"
"v_mul_f32 v220, v220, v239\nv_mul_f32 v221, v221, v239\nv_cvt_pk_bf16_f32 v218, v220, v221\ns_nop 1\n"
"v_permlane16_swap_b32 v216, v218\nv_mul_f32 v222, v222, v239\nv_mul_f32 v223, v223, v239\nv_cvt_pk_bf16_f32 v219, v222, v223\n"
"s_nop 1\nv_permlane16_swap_b32 v217, v219\nds_write_b128 v252, v[216:219] offset:26624\ns_waitcnt vmcnt(8) lgkmcnt(0)\n"
"s_barrier\nds_read_b128 a[96:99], v253 offset:16384\nds_read_b128 a[100:103], v253 offset:17408\nds_read_b128 a[104:107], v253 offset:18432\n"
"ds_read_b128 a[108:111], v253 offset:19456\nds_read_b128 a[112:115], v253 offset:20480\nds_read_b128 a[116:119], v253 offset:21504\nds_read_b128 a[120:123], v253 offset:22528\n"
"ds_read_b128 a[124:127], v253 offset:23552\nv_mfma_f32_16x16x32_bf16 v[76:79], a[128:131], a[0:3], 0\ns_add_u32 s80, s80, 0x0\ns_add_u32 s86, s73, 0x20000\n"
"v_add_u32 v245, s86, v224\ns_and_b32 s76, s96, 0xc2cc0000\nv_mfma_f32_16x16x32_bf16 v[80:83], a[128:131], a[4:7], 0\ns_add_u32 m0, s73, 0x8000\n"
"v_rcp_f32 v132, v132\nv_add_u32 v225, 0x10000, v225\nv_mfma_f32_16x16x32_bf16 v[84:87], a[128:131], a[8:11], 0\nbuffer_load_dwordx4 v232, s[52:55], s80 offen lds\n"
"v_mfma_f32_16x16x32_bf16 v[88:91], a[128:131], a[12:15], 0\ns_add_u32 s80, s80, 0x80\nds_read_b128 a[192:195], v225 offset:0\nv_rcp_f32 v133, v133\n"
"v_mfma_f32_16x16x32_bf16 v[76:79], a[132:135], a[16:19], v[76:79]\ns_add_u32 m0, s73, 0xa000\nds_read_b128 a[200:203], v225 offset:8192\nv_rcp_f32 v134, v134\n"
"v_mfma_f32_16x16x32_bf16 v[80:83], a[132:135], a[20:23], v[80:83]\nbuffer_load_dwordx4 v232, s[52:55], s80 offen lds\nv_mfma_f32_16x16x32_bf16 v[84:87], a[132:135], a[24:27], v[84:87]\ns_add_u32 s80, s80, 0x4080\n"
"ds_read_b128 a[208:211], v225 offset:16384\nv_rcp_f32 v135, v135\nv_mfma_f32_16x16x32_bf16 v[88:91], a[132:135], a[28:31], v[88:91]\ns_add_u32 m0, s73, 0xc000\n"
"ds_read_b128 a[216:219], v225 offset:24576\nv_add_u32 v226, 0x10000, v226\nv_mul_f32 v128, v128, v176\nv_mfma_f32_16x16x32_bf16 v[76:79], a[136:139], a[32:35], v[76:79]\n"
"buffer_load_dwordx4 v232, s[52:55], s80 offen lds\nv_mfma_f32_16x16x32_bf16 v[80:83], a[136:139], a[36:39], v[80:83]\ns_add_u32 s80, s80, 0x80\nds_read_b128 a[196:199], v226 offset:0\n"
"v_fma_f32 v137, v137, v158, v158\nv_fma_f32 v138, v138, v158, v158\nv_mfma_f32_16x16x32_bf16 v[84:87], a[136:139], a[40:43], v[84:87]\ns_add_u32 m0, s73, 0xe000\n"
"ds_read_b128 a[204:207], v226 offset:8192\nv_mul_f32 v129, v129, v177\nv_mul_f32 v130, v130, v178\nv_mfma_f32_16x16x32_bf16 v[88:91], a[136:139], a[44:47], v[88:91]\n"
"buffer_load_dwordx4 v232, s[52:55], s80 offen lds\nv_mfma_f32_16x16x32_bf16 v[76:79], a[140:143], a[48:51], v[76:79]\ns_add_u32 s80, s80, 0xfffffe80\nds_read_b128 a[212:215], v226 offset:16384\n"
"v_mul_f32 v142, v142, v190\nv_mul_f32 v131, v131, v179\nv_mfma_f32_16x16x32_bf16 v[80:83], a[140:143], a[52:55], v[80:83]\ns_add_u32 m0, s73, 0x9000\n"
"ds_read_b128 a[220:223], v226 offset:24576\nv_fma_f32 v139, v139, v158, v158\nv_mul_f32 v143, v143, v191\nv_mfma_f32_16x16x32_bf16 v[84:87], a[140:143], a[56:59], v[84:87]\n"
"buffer_load_dwordx4 v232, s[52:55], s80 offen lds\nv_mfma_f32_16x16x32_bf16 v[88:91], a[140:143], a[60:63], v[88:91]\ns_add_u32 s80, s80, 0x80\nds_read_b128 a[224:227], v225 offset:2048\n"
"v_mul_f32 v132, v132, v180\nv_mul_f32 v133, v133, v181\nv_mfma_f32_16x16x32_bf16 v[76:79], a[144:147], a[64:67], v[76:79]\ns_add_u32 m0, s73, 0xb000\n"
"ds_read_b128 a[228:231], v226 offset:2048\nv_exp_f32 v140, v140\nv_mfma_f32_16x16x32_bf16 v[80:83], a[144:147], a[68:71], v[80:83]\nbuffer_load_dwordx4 v232, s[52:55], s80 offen lds\n"
"v_mfma_f32_16x16x32_bf16 v[84:87], a[144:147], a[72:75], v[84:87]\ns_add_u32 s80, s80, 0xffffc080\nds_read_b128 a[232:235], v225 offset:10240\nv_rcp_f32 v136, v136\n"
"v_mfma_f32_16x16x32_bf16 v[88:91], a[144:147], a[76:79], v[88:91]\ns_add_u32 m0, s73, 0xd000\nds_read_b128 a[236:239], v226 offset:10240\nv_mul_f32 v134, v134, v182\n"
"v_mul_f32 v135, v135, v183\nv_mfma_f32_16x16x32_bf16 v[76:79], a[148:151], a[80:83], v[76:79]\nbuffer_load_dwordx4 v232, s[52:55], s80 offen lds\nv_mfma_f32_16x16x32_bf16 v[80:83], a[148:151], a[84:87], v[80:83]\n"
"s_add_u32 s80, s80, 0x80\nds_read_b128 a[240:243], v225 offset:18432\nv_exp_f32 v141, v141\nv_mfma_f32_16x16x32_bf16 v[84:87], a[148:151], a[88:91], v[84:87]\n"
"s_add_u32 m0, s73, 0xf000\nds_read_b128 a[244:247], v226 offset:18432\nv_rcp_f32 v137, v137\nv_mfma_f32_16x16x32_bf16 v[88:91], a[148:151], a[92:95], v[88:91]\n"
"buffer_load_dwordx4 v232, s[52:55], s80 offen lds\ns_waitcnt lgkmcnt(4)\nv_mfma_f32_16x16x32_bf16 v[76:79], a[152:155], a[96:99], v[76:79]\ns_add_u32 s80, s80, 0x7e80\n"
"ds_read_b128 a[248:251], v225 offset:26624\nv_rcp_f32 v138, v138\nv_mfma_f32_16x16x32_bf16 v[80:83], a[152:155], a[100:103], v[80:83]\ns_add_u32 m0, s73, 0x0\n"
"ds_read_b128 a[252:255], v226 offset:26624\nv_mul_f32 v128, v128, v160\nv_mul_f32 v129, v129, v161\nv_mfma_f32_16x16x32_bf16 v[84:87], a[152:155], a[104:107], v[84:87]\n"
"buffer_load_dwordx4 v232, s[52:55], s80 offen lds\nv_mfma_f32_16x16x32_bf16 v[88:91], a[152:155], a[108:111], v[88:91]\ns_mul_i32 s34, s75, 37\nv_exp_f32 v142, v142\n"
"v_mul_f32 v130, v130, v162\nv_mfma_f32_16x16x32_bf16 v[76:79], a[156:159], a[112:115], v[76:79]\ns_lshr_b32 s34, s34, 8\nv_rcp_f32 v139, v139\n"
"v_mul_f32 v131, v131, v163\nv_mfma_f32_16x16x32_bf16 v[80:83], a[156:159], a[116:119], v[80:83]\ns_mul_i32 s35, s34, 7\nv_exp_f32 v143, v143\n"
"v_fma_f32 v140, v140, v159, v159\nv_mfma_f32_16x16x32_bf16 v[84:87], a[156:159], a[120:123], v[84:87]\ns_lshl_b32 s34, s34, 3\nv_mul_f32 v132, v132, v164\n"
"v_mul_f32 v133, v133, v165\nv_mul_f32 v136, v136, v184\nv_mfma_f32_16x16x32_bf16 v[88:91], a[156:159], a[124:127], v[88:91]\ns_add_u32 s34, s34, s74\n"
"v_mul_f32 v134, v134, v166\nv_mul_f32 v135, v135, v167\nv_fma_f32 v141, v141, v159, v159\nv_mfma_f32_16x16x32_bf16 v[92:95], a[160:163], a[0:3], 0\n"
"s_lshl_b32 s34, s34, 3\nv_max3_f32 v240, v240, v76, v77\nv_cvt_pk_bf16_f32 v114, v128, v129\nv_cvt_pk_bf16_f32 v115, v130, v131\n"
"v_mfma_f32_16x16x32_bf16 v[92:95], a[164:167], a[16:19], v[92:95]\ns_lshr_b32 s33, s75, 3\nv_max3_f32 v241, v241, v80, v81\nv_max3_f32 v240, v240, v78, v79\n"
"v_mul_f32 v137, v137, v185\nv_mfma_f32_16x16x32_bf16 v[92:95], a[168:171], a[32:35], v[92:95]\ns_mul_i32 s33, s33, 56\nv_max3_f32 v242, v242, v84, v85\n"
"v_max3_f32 v241, v241, v82, v83\nv_mul_f32 v138, v138, v186\nv_mfma_f32_16x16x32_bf16 v[92:95], a[172:175], a[48:51], v[92:95]\ns_and_b32 s87, s75, 7\n"
"v_max3_f32 v243, v243, v88, v89\nv_max3_f32 v242, v242, v86, v87\nv_fma_f32 v142, v142, v159, v159\nv_mfma_f32_16x16x32_bf16 v[92:95], a[176:179], a[64:67], v[92:95]\n"
"s_add_u32 s33, s33, s87\nv_max3_f32 v243, v243, v90, v91\nv_mul_f32 v139, v139, v187\nv_fma_f32 v143, v143, v159, v159\n"
"v_mfma_f32_16x16x32_bf16 v[92:95], a[180:183], a[80:83], v[92:95]\ns_sub_u32 s33, s33, 64\nds_write_b128 v245, v[240:243] offset:0\nv_cvt_pk_bf16_f32 v118, v132, v133\n"
"v_mfma_f32_16x16x32_bf16 v[92:95], a[184:187], a[96:99], v[92:95]\ns_sub_u32 s35, s75, s35\nv_rcp_f32 v140, v140\nv_cvt_pk_bf16_f32 v119, v134, v135\n"
"v_mfma_f32_16x16x32_bf16 v[92:95], a[188:191], a[112:115], v[92:95]\ns_add_u32 s35, s35, 8\nv_rcp_f32 v141, v141\nv_mul_f32 v136, v136, v168\n"
"v_mfma_f32_16x16x32_bf16 v[96:99], a[188:191], a[116:119], 0\ns_lshl_b32 s87, %1, 8\nbuffer_store_dwordx4 v[112:115], v224, s[60:63], s82 offen offset:0 sc1\nv_mul_f32 v137, v137, v169\n"
"v_mfma_f32_16x16x32_bf16 v[96:99], a[184:187], a[100:103], v[96:99]\ns_cmp_lt_u32 s72, 2\ns_cselect_b64 s[92:93], s[48:49], s[52:53]\nv_rcp_f32 v142, v142\nv_mul_f32 v138, v138, v170\n"
"v_mfma_f32_16x16x32_bf16 v[96:99], a[180:183], a[84:87], v[96:99]\ns_waitcnt lgkmcnt(0)\ns_barrier\nds_read_b128 v[0:3], v244 offset:0\nv_rcp_f32 v143, v143\n"
"v_mfma_f32_16x16x32_bf16 v[96:99], a[176:179], a[68:71], v[96:99]\ns_add_u32 s80, s80, 0x80\nds_read_b128 v[4:7], v244 offset:256\nbuffer_store_dwordx4 v[116:119], v224, s[60:63], s82 offen offset:1024 sc1\n"
"v_mfma_f32_16x16x32_bf16 v[96:99], a[172:175], a[52:55], v[96:99]\ns_add_u32 m0, s73, 0x2000\nds_read_b128 v[8:11], v244 offset:512\nv_mul_f32 v139, v139, v171\n"
"v_mul_f32 v140, v140, v188\nv_mfma_f32_16x16x32_bf16 v[96:99], a[168:171], a[36:39], v[96:99]\nbuffer_load_dwordx4 v232, s[52:55], s80 offen lds\nv_mfma_f32_16x16x32_bf16 v[96:99], a[164:167], a[20:23], v[96:99]\n"
"s_add_u32 s80, s80, 0x4080\nds_read_b128 v[12:15], v244 offset:768\nv_cvt_pk_bf16_f32 v122, v136, v137\nv_cvt_pk_bf16_f32 v123, v138, v139\n"
"v_mfma_f32_16x16x32_bf16 v[96:99], a[160:163], a[4:7], v[96:99]\ns_add_u32 m0, s73, 0x4000\nds_read_b128 v[16:19], v244 offset:1024\nv_mul_f32 v141, v141, v189\n"
"v_mul_f32 v142, v142, v190\nv_mfma_f32_16x16x32_bf16 v[100:103], a[160:163], a[8:11], 0\nbuffer_load_dwordx4 v232, s[52:55], s80 offen lds\nv_mfma_f32_16x16x32_bf16 v[100:103], a[164:167], a[24:27], v[100:103]\n"
"s_add_u32 s80, s80, 0x80\nds_read_b128 v[20:23], v244 offset:1280\nv_mul_f32 v143, v143, v191\nv_mul_f32 v140, v140, v172\n"
"v_mfma_f32_16x16x32_bf16 v[100:103], a[168:171], a[40:43], v[100:103]\ns_add_u32 m0, s73, 0x6000\nds_read_b128 v[24:27], v244 offset:1536\nbuffer_store_dwordx4 v[120:123], v224, s[60:63], s82 offen offset:2048 sc1\n"
"v_mfma_f32_16x16x32_bf16 v[100:103], a[172:175], a[56:59], v[100:103]\nbuffer_load_dwordx4 v232, s[52:55], s80 offen lds\nv_mfma_f32_16x16x32_bf16 v[100:103], a[176:179], a[72:75], v[100:103]\ns_add_u32 s80, s80, 0xfffffe80\n"
"ds_read_b128 v[28:31], v244 offset:1792\nv_mul_f32 v141, v141, v173\nv_mul_f32 v142, v142, v174\nv_mfma_f32_16x16x32_bf16 v[100:103], a[180:183], a[88:91], v[100:103]\n"
"s_add_u32 m0, s73, 0x1000\ns_waitcnt lgkmcnt(4)\nv_max3_f32 v0, v0, v4, v8\nv_max3_f32 v1, v1, v5, v9\n"
"v_max3_f32 v2, v2, v6, v10\nv_mfma_f32_16x16x32_bf16 v[100:103], a[184:187], a[104:107], v[100:103]\nbuffer_load_dwordx4 v232, s[52:55], s80 offen lds\nv_mfma_f32_16x16x32_bf16 v[100:103], a[188:191], a[120:123], v[100:103]\n"
"s_add_u32 s80, s80, 0x80\nv_max3_f32 v3, v3, v7, v11\nv_mul_f32 v143, v143, v175\nv_cvt_pk_bf16_f32 v126, v140, v141\n"
"v_mfma_f32_16x16x32_bf16 v[104:107], a[188:191], a[124:127], 0\ns_add_u32 m0, s73, 0x3000\ns_waitcnt lgkmcnt(2)\nv_max3_f32 v0, v0, v12, v16\n"
"v_max3_f32 v1, v1, v13, v17\nv_max3_f32 v2, v2, v14, v18\nv_mfma_f32_16x16x32_bf16 v[104:107], a[184:187], a[108:111], v[104:107]\nbuffer_load_dwordx4 v232, s[52:55], s80 offen lds\n"
"v_mfma_f32_16x16x32_bf16 v[104:107], a[180:183], a[92:95], v[104:107]\ns_add_u32 s80, s80, 0xffffc080\nv_max3_f32 v3, v3, v15, v19\nv_cvt_pk_bf16_f32 v127, v142, v143\n"
"v_mfma_f32_16x16x32_bf16 v[104:107], a[176:179], a[76:79], v[104:107]\ns_add_u32 m0, s73, 0x5000\ns_waitcnt lgkmcnt(0)\nv_max3_f32 v0, v0, v20, v24\n"
"v_max3_f32 v1, v1, v21, v25\nv_max3_f32 v2, v2, v22, v26\nv_mfma_f32_16x16x32_bf16 v[104:107], a[172:175], a[60:63], v[104:107]\nbuffer_load_dwordx4 v232, s[52:55], s80 offen lds\n"
"v_mfma_f32_16x16x32_bf16 v[104:107], a[168:171], a[44:47], v[104:107]\ns_add_u32 s80, s80, 0x80\nv_max3_f32 v3, v3, v23, v27\nv_max_f32 v0, v0, v28\n"
"v_max_f32 v1, v1, v29\nv_mfma_f32_16x16x32_bf16 v[104:107], a[164:167], a[28:31], v[104:107]\ns_add_u32 m0, s73, 0x7000\nv_max_f32 v2, v2, v30\n"
"v_max_f32 v3, v3, v31\nv_sub_f32 v208, s76, v0\nv_mfma_f32_16x16x32_bf16 v[104:107], a[160:163], a[12:15], v[104:107]\nbuffer_load_dwordx4 v232, s[52:55], s80 offen lds\n"
"s_waitcnt vmcnt(11)\ns_barrier\nv_sub_f32 v209, s76, v0\nv_sub_f32 v210, s76, v0\n"
"v_sub_f32 v211, s76, v0\ns_nop 1\nv_mfma_f32_16x16x32_bf16 v[128:131], a[192:195], a[0:3], v[208:211]\ns_add_u32 s79, s79, 0x3e80\n"
"ds_read_b64_tr_b16 a[128:129], v227 offset:32768\nv_sub_f32 v212, s76, v1\nv_sub_f32 v213, s76, v1\nv_mfma_f32_16x16x32_bf16 v[128:131], a[196:199], a[16:19], v[128:131]\n"
"s_add_u32 m0, s73, 0x10000\nds_read_b64_tr_b16 a[130:131], v227 offset:34816\nv_sub_f32 v214, s76, v1\nv_sub_f32 v215, s76, v1\n"
"v_mfma_f32_16x16x32_bf16 v[128:131], a[200:203], a[32:35], v[128:131]\nbuffer_load_dwordx4 v231, s[48:51], s79 offen lds\nv_mfma_f32_16x16x32_bf16 v[128:131], a[204:207], a[48:51], v[128:131]\ns_add_u32 s79, s79, 0x80\n"
"ds_read_b64_tr_b16 a[132:133], v227 offset:36864\nv_sub_f32 v216, s76, v2\nv_sub_f32 v217, s76, v2\nv_mfma_f32_16x16x32_bf16 v[128:131], a[208:211], a[64:67], v[128:131]\n"
"s_add_u32 m0, s73, 0x12000\nds_read_b64_tr_b16 a[134:135], v227 offset:38912\nv_sub_f32 v218, s76, v2\nv_sub_f32 v219, s76, v2\n"
"v_mfma_f32_16x16x32_bf16 v[128:131], a[212:215], a[80:83], v[128:131]\nbuffer_load_dwordx4 v231, s[48:51], s79 offen lds\nv_mfma_f32_16x16x32_bf16 v[128:131], a[216:219], a[96:99], v[128:131]\ns_add_u32 s79, s79, 0x80\n"
"ds_read_b64_tr_b16 a[136:137], v228 offset:32768\nv_sub_f32 v220, s76, v3\nv_sub_f32 v221, s76, v3\nv_mfma_f32_16x16x32_bf16 v[128:131], a[220:223], a[112:115], v[128:131]\n"
"s_add_u32 m0, s73, 0x14000\nds_read_b64_tr_b16 a[138:139], v228 offset:34816\nv_sub_f32 v222, s76, v3\nv_sub_f32 v223, s76, v3\n"
"v_mfma_f32_16x16x32_bf16 v[132:135], a[220:223], a[116:119], v[212:215]\nbuffer_load_dwordx4 v231, s[48:51], s79 offen lds\nv_mfma_f32_16x16x32_bf16 v[132:135], a[216:219], a[100:103], v[132:135]\ns_add_u32 s79, s79, 0x80\n"
"ds_read_b64_tr_b16 a[140:141], v228 offset:36864\nbuffer_store_dwordx4 v[124:127], v224, s[60:63], s82 offen offset:3072 sc1\nv_mfma_f32_16x16x32_bf16 v[132:135], a[212:215], a[84:87], v[132:135]\ns_add_u32 m0, s73, 0x16000\n"
"ds_read_b64_tr_b16 a[142:143], v228 offset:38912\nv_add_f32 v76, v76, v208\nv_add_f32 v77, v77, v208\nv_mfma_f32_16x16x32_bf16 v[132:135], a[208:211], a[68:71], v[132:135]\n"
"buffer_load_dwordx4 v231, s[48:51], s79 offen lds\nv_mfma_f32_16x16x32_bf16 v[132:135], a[204:207], a[52:55], v[132:135]\ns_add_u32 s79, s79, 0x3e80\nds_read_b64_tr_b16 a[144:145], v229 offset:32768\n"
"v_add_f32 v78, v78, v208\nv_add_f32 v79, v79, v208\nv_mfma_f32_16x16x32_bf16 v[132:135], a[200:203], a[36:39], v[132:135]\ns_add_u32 m0, s73, 0x11000\n"
"ds_read_b64_tr_b16 a[146:147], v229 offset:34816\nv_add_f32 v80, v80, v212\nv_add_f32 v81, v81, v212\nv_mfma_f32_16x16x32_bf16 v[132:135], a[196:199], a[20:23], v[132:135]\n"
"buffer_load_dwordx4 v231, s[48:51], s79 offen lds\nv_mfma_f32_16x16x32_bf16 v[132:135], a[192:195], a[4:7], v[132:135]\ns_add_u32 s79, s79, 0x80\nds_read_b64_tr_b16 a[148:149], v229 offset:36864\n"
"v_add_f32 v82, v82, v212\nv_add_f32 v83, v83, v212\nv_mfma_f32_16x16x32_bf16 v[136:139], a[192:195], a[8:11], v[216:219]\ns_add_u32 m0, s73, 0x13000\n"
"ds_read_b64_tr_b16 a[150:151], v229 offset:38912\nv_add_f32 v84, v84, v216\nv_add_f32 v85, v85, v216\nv_mfma_f32_16x16x32_bf16 v[136:139], a[196:199], a[24:27], v[136:139]\n"
"buffer_load_dwordx4 v231, s[48:51], s79 offen lds\nv_mfma_f32_16x16x32_bf16 v[136:139], a[200:203], a[40:43], v[136:139]\ns_add_u32 s79, s79, 0x80\nds_read_b64_tr_b16 a[152:153], v230 offset:32768\n"
"v_add_f32 v92, v92, v208\nv_add_f32 v93, v93, v208\nv_mfma_f32_16x16x32_bf16 v[136:139], a[204:207], a[56:59], v[136:139]\ns_add_u32 m0, s73, 0x15000\n"
"ds_read_b64_tr_b16 a[154:155], v230 offset:34816\nv_add_f32 v94, v94, v208\nv_add_f32 v95, v95, v208\nv_mfma_f32_16x16x32_bf16 v[136:139], a[208:211], a[72:75], v[136:139]\n"
"buffer_load_dwordx4 v231, s[48:51], s79 offen lds\nv_mfma_f32_16x16x32_bf16 v[136:139], a[212:215], a[88:91], v[136:139]\ns_add_u32 s79, s79, 0x80\nds_read_b64_tr_b16 a[156:157], v230 offset:36864\n"
"v_add_f32 v96, v96, v212\nv_add_f32 v97, v97, v212\nv_mfma_f32_16x16x32_bf16 v[136:139], a[216:219], a[104:107], v[136:139]\ns_add_u32 m0, s73, 0x17000\n"
"ds_read_b64_tr_b16 a[158:159], v230 offset:38912\nv_add_f32 v98, v98, v212\nv_add_f32 v99, v99, v212\nv_mfma_f32_16x16x32_bf16 v[136:139], a[220:223], a[120:123], v[136:139]\n"
"buffer_load_dwordx4 v231, s[48:51], s79 offen lds\nv_mfma_f32_16x16x32_bf16 v[140:143], a[220:223], a[124:127], v[220:223]\ns_add_u32 s94, s88, 0x100000\nds_read_b64_tr_b16 a[160:161], v227 offset:40960\n"
"v_exp_f32 v76, v76\nv_mfma_f32_16x16x32_bf16 v[140:143], a[216:219], a[108:111], v[140:143]\ns_and_b32 s91, s85, 63\nds_read_b64_tr_b16 a[162:163], v227 offset:43008\n"
"v_exp_f32 v77, v77\nv_mfma_f32_16x16x32_bf16 v[140:143], a[212:215], a[92:95], v[140:143]\ns_mul_i32 s91, s91, 1024\nds_read_b64_tr_b16 a[164:165], v227 offset:45056\n"
"v_exp_f32 v78, v78\nv_mfma_f32_16x16x32_bf16 v[140:143], a[208:211], a[76:79], v[140:143]\ns_add_u32 s91, s91, s88\nds_read_b64_tr_b16 a[166:167], v227 offset:47104\n"
"v_exp_f32 v79, v79\nv_mfma_f32_16x16x32_bf16 v[140:143], a[204:207], a[60:63], v[140:143]\ns_add_u32 s91, s91, 0x30000\nds_read_b64_tr_b16 a[168:169], v228 offset:40960\n"
"v_exp_f32 v80, v80\nv_mfma_f32_16x16x32_bf16 v[140:143], a[200:203], a[44:47], v[140:143]\nds_read_b64_tr_b16 a[170:171], v228 offset:43008\nv_exp_f32 v81, v81\n"
"v_mfma_f32_16x16x32_bf16 v[140:143], a[196:199], a[28:31], v[140:143]\nds_read_b64_tr_b16 a[172:173], v228 offset:45056\nv_exp_f32 v82, v82\nv_mfma_f32_16x16x32_bf16 v[140:143], a[192:195], a[12:15], v[140:143]\n"
"ds_read_b64_tr_b16 a[174:175], v228 offset:47104\nv_exp_f32 v83, v83\nv_mfma_f32_16x16x32_bf16 v[144:147], a[224:227], a[0:3], v[208:211]\nds_read_b64_tr_b16 a[176:177], v229 offset:40960\n"
"v_exp_f32 v84, v84\nv_mfma_f32_16x16x32_bf16 v[144:147], a[228:231], a[16:19], v[144:147]\nds_read_b64_tr_b16 a[178:179], v229 offset:43008\nv_exp_f32 v85, v85\n"
"v_mfma_f32_16x16x32_bf16 v[144:147], a[232:235], a[32:35], v[144:147]\nds_read_b64_tr_b16 a[180:181], v229 offset:45056\nv_add_f32 v86, v86, v216\nv_add_f32 v87, v87, v216\n"
"v_mfma_f32_16x16x32_bf16 v[144:147], a[236:239], a[48:51], v[144:147]\nds_read_b64_tr_b16 a[182:183], v229 offset:47104\nv_exp_f32 v92, v92\nv_mfma_f32_16x16x32_bf16 v[144:147], a[240:243], a[64:67], v[144:147]\n"
"ds_read_b64_tr_b16 a[184:185], v230 offset:40960\nv_exp_f32 v93, v93\nv_mfma_f32_16x16x32_bf16 v[144:147], a[244:247], a[80:83], v[144:147]\nds_read_b64_tr_b16 a[186:187], v230 offset:43008\n"
"v_exp_f32 v94, v94\nv_mfma_f32_16x16x32_bf16 v[144:147], a[248:251], a[96:99], v[144:147]\nds_read_b64_tr_b16 a[188:189], v230 offset:45056\nv_exp_f32 v95, v95\n"
"v_mfma_f32_16x16x32_bf16 v[144:147], a[252:255], a[112:115], v[144:147]\nds_read_b64_tr_b16 a[190:191], v230 offset:47104\nv_exp_f32 v96, v96\nv_mfma_f32_16x16x32_bf16 v[148:151], a[252:255], a[116:119], v[212:215]\n"
"v_exp_f32 v97, v97\nv_add_f32 v100, v100, v216\nv_mfma_f32_16x16x32_bf16 v[148:151], a[248:251], a[100:103], v[148:151]\nv_exp_f32 v98, v98\n"
"v_add_f32 v101, v101, v216\nv_mfma_f32_16x16x32_bf16 v[148:151], a[244:247], a[84:87], v[148:151]\nv_exp_f32 v99, v99\nv_add_f32 v102, v102, v216\n"
"v_mfma_f32_16x16x32_bf16 v[148:151], a[240:243], a[68:71], v[148:151]\nv_exp_f32 v86, v86\nv_add_f32 v103, v103, v216\nv_mfma_f32_16x16x32_bf16 v[148:151], a[236:239], a[52:55], v[148:151]\n"
"v_exp_f32 v87, v87\nv_add_f32 v88, v88, v220\nv_mfma_f32_16x16x32_bf16 v[148:151], a[232:235], a[36:39], v[148:151]\nv_exp_f32 v100, v100\n"
"v_add_f32 v89, v89, v220\nv_mfma_f32_16x16x32_bf16 v[148:151], a[228:231], a[20:23], v[148:151]\nv_exp_f32 v101, v101\nv_add_f32 v90, v90, v220\n"
"v_mfma_f32_16x16x32_bf16 v[148:151], a[224:227], a[4:7], v[148:151]\nv_exp_f32 v102, v102\nv_add_f32 v91, v91, v220\nv_mfma_f32_16x16x32_bf16 v[152:155], a[224:227], a[8:11], v[216:219]\n"
"v_exp_f32 v103, v103\nv_add_f32 v104, v104, v220\nv_mfma_f32_16x16x32_bf16 v[152:155], a[228:231], a[24:27], v[152:155]\nv_exp_f32 v88, v88\n"
"v_add_f32 v105, v105, v220\nv_mfma_f32_16x16x32_bf16 v[152:155], a[232:235], a[40:43], v[152:155]\nv_exp_f32 v89, v89\nv_cvt_pk_bf16_f32 v160, v76, v77\n"
"v_mfma_f32_16x16x32_bf16 v[152:155], a[236:239], a[56:59], v[152:155]\nv_add_f32 v106, v106, v220\nv_add_f32 v107, v107, v220\nv_cvt_pk_bf16_f32 v161, v78, v79\n"
"v_mfma_f32_16x16x32_bf16 v[152:155], a[240:243], a[72:75], v[152:155]\nv_exp_f32 v90, v90\nv_cvt_pk_bf16_f32 v162, v92, v93\nv_mfma_f32_16x16x32_bf16 v[152:155], a[244:247], a[88:91], v[152:155]\n"
"v_exp_f32 v91, v91\nv_cvt_pk_bf16_f32 v164, v80, v81\nv_mfma_f32_16x16x32_bf16 v[152:155], a[248:251], a[104:107], v[152:155]\nv_cvt_pk_bf16_f32 v165, v82, v83\n"
"v_cvt_pk_bf16_f32 v163, v94, v95\nv_cvt_pk_bf16_f32 v166, v96, v97\nv_mfma_f32_16x16x32_bf16 v[152:155], a[252:255], a[120:123], v[152:155]\nds_write_b128 v233, v[160:163] offset:16384\n"
"v_cvt_pk_bf16_f32 v167, v98, v99\nv_mfma_f32_16x16x32_bf16 v[156:159], a[252:255], a[124:127], v[220:223]\nv_exp_f32 v104, v104\nv_cvt_pk_bf16_f32 v168, v84, v85\n"
"v_mfma_f32_16x16x32_bf16 v[156:159], a[248:251], a[108:111], v[156:159]\nds_write_b128 v233, v[164:167] offset:17408\nv_cvt_pk_bf16_f32 v169, v86, v87\nv_mfma_f32_16x16x32_bf16 v[156:159], a[244:247], a[92:95], v[156:159]\n"
"v_exp_f32 v105, v105\nv_cvt_pk_bf16_f32 v170, v100, v101\nv_mfma_f32_16x16x32_bf16 v[156:159], a[240:243], a[76:79], v[156:159]\nv_cvt_pk_bf16_f32 v171, v102, v103\n"
"v_exp_f32 v106, v106\nv_mfma_f32_16x16x32_bf16 v[156:159], a[236:239], a[60:63], v[156:159]\nds_write_b128 v233, v[168:171] offset:18432\nv_cvt_pk_bf16_f32 v172, v88, v89\n"
"v_mfma_f32_16x16x32_bf16 v[156:159], a[232:235], a[44:47], v[156:159]\nv_exp_f32 v107, v107\nv_cvt_pk_bf16_f32 v173, v90, v91\nv_mfma_f32_16x16x32_bf16 v[156:159], a[228:231], a[28:31], v[156:159]\n"
"v_cvt_pk_bf16_f32 v174, v104, v105\nv_cvt_pk_bf16_f32 v175, v106, v107\nv_add_u32 v247, s34, v254\nv_mfma_f32_16x16x32_bf16 v[156:159], a[224:227], a[12:15], v[156:159]\n"
"ds_write_b128 v233, v[172:175] offset:19456\nv_lshl_add_u32 v251, v254, 3, s33\nv_mov_b32 v250, s35\nv_mov_b32 v252, s74\n"
"v_cmp_gt_u32 vcc, 8, v254\ns_nop 1\nv_cndmask_b32 v247, v251, v247, vcc\nv_cndmask_b32 v250, v252, v250, vcc\n"
"s_cmp_lt_u32 s75, 28\ns_cselect_b32 s34, -1, 0\ns_and_b32 s34, s34, 0xff\ns_or_b32 s34, s34, 0x7f00\ns_mov_b32 s35, 0\nv_mov_b32 v251, 0x7f0000\nv_cndmask_b32 v247, v251, v247, s[34:35]\nv_lshlrev_b32 v247, 8, v247\n"
"v_lshl_add_u32 v250, v250, 2, v247\nv_lshl_add_u32 v246, v254, 2, s87\ns_cmp_eq_u32 s72, 0\ns_cselect_b32 s35, 0, 0x7f000000\ns_mov_b32 s33, 0x7f000000\ns_waitcnt lgkmcnt(0)\n"
"s_barrier\nL_att%=:\nv_mfma_f32_16x16x32_bf16 v[0:3], a[128:131], v[160:163], 0\ns_add_u32 s80, s80, 0x7e80\n"
"ds_read_b128 v[192:195], v234 offset:16384\nv_exp_f32 v128, v128\nv_mfma_f32_16x16x32_bf16 v[4:7], a[128:131], v[164:167], 0\ns_add_u32 m0, s73, 0x8000\n"
"ds_read_b128 v[196:199], v234 offset:17408\nv_exp_f32 v129, v129\nv_mfma_f32_16x16x32_bf16 v[8:11], a[128:131], v[168:171], 0\nbuffer_load_dwordx4 v232, s[52:55], s80 offen lds\n"
"v_mfma_f32_16x16x32_bf16 v[12:15], a[128:131], v[172:175], 0\ns_add_u32 s80, s80, 0x80\nds_read_b128 v[200:203], v234 offset:18432\nv_exp_f32 v130, v130\n"
"v_mfma_f32_16x16x32_bf16 v[16:19], a[136:139], v[160:163], 0\ns_add_u32 m0, s73, 0xa000\nds_read_b128 v[204:207], v234 offset:19456\nv_exp_f32 v131, v131\n"
"v_mfma_f32_16x16x32_bf16 v[20:23], a[136:139], v[164:167], 0\nbuffer_load_dwordx4 v232, s[52:55], s80 offen lds\nv_mfma_f32_16x16x32_bf16 v[24:27], a[136:139], v[168:171], 0\ns_add_u32 s80, s80, 0x4080\n"
"ds_read_b128 a[192:195], v225 offset:32768\nv_exp_f32 v144, v144\nv_mfma_f32_16x16x32_bf16 v[28:31], a[136:139], v[172:175], 0\ns_add_u32 m0, s73, 0xc000\n"
"ds_read_b128 a[196:199], v226 offset:32768\nv_exp_f32 v145, v145\nv_mfma_f32_16x16x32_bf16 v[32:35], a[144:147], v[160:163], 0\nbuffer_load_dwordx4 v232, s[52:55], s80 offen lds\n"
"v_mfma_f32_16x16x32_bf16 v[36:39], a[144:147], v[164:167], 0\ns_add_u32 s80, s80, 0x80\nds_read_b128 a[200:203], v225 offset:40960\nv_exp_f32 v146, v146\n"
"v_mfma_f32_16x16x32_bf16 v[40:43], a[144:147], v[168:171], 0\ns_add_u32 m0, s73, 0xe000\nds_read_b128 a[204:207], v226 offset:40960\nv_exp_f32 v147, v147\n"
"v_mfma_f32_16x16x32_bf16 v[44:47], a[144:147], v[172:175], 0\nbuffer_load_dwordx4 v232, s[52:55], s80 offen lds\ns_waitcnt lgkmcnt(4)\nv_mfma_f32_16x16x32_bf16 v[0:3], a[132:135], v[192:195], v[0:3]\n"
"s_add_u32 s80, s80, 0xfffffe80\nds_read_b128 a[208:211], v225 offset:49152\nv_exp_f32 v132, v132\nv_mfma_f32_16x16x32_bf16 v[4:7], a[132:135], v[196:199], v[4:7]\n"
"s_add_u32 m0, s73, 0x9000\nds_read_b128 a[212:215], v226 offset:49152\nv_exp_f32 v133, v133\nv_mfma_f32_16x16x32_bf16 v[8:11], a[132:135], v[200:203], v[8:11]\n"
"buffer_load_dwordx4 v232, s[52:55], s80 offen lds\nv_mfma_f32_16x16x32_bf16 v[12:15], a[132:135], v[204:207], v[12:15]\ns_add_u32 s80, s80, 0x80\nds_read_b128 a[216:219], v225 offset:57344\n"
"v_exp_f32 v134, v134\nv_mfma_f32_16x16x32_bf16 v[16:19], a[140:143], v[192:195], v[16:19]\ns_add_u32 m0, s73, 0xb000\nds_read_b128 a[220:223], v226 offset:57344\n"
"v_exp_f32 v135, v135\nv_mfma_f32_16x16x32_bf16 v[20:23], a[140:143], v[196:199], v[20:23]\nbuffer_load_dwordx4 v232, s[52:55], s80 offen lds\nv_mfma_f32_16x16x32_bf16 v[24:27], a[140:143], v[200:203], v[24:27]\n"
"s_add_u32 s80, s80, 0xffffc080\nds_read_b128 a[224:227], v225 offset:34816\nv_exp_f32 v148, v148\nv_mfma_f32_16x16x32_bf16 v[28:31], a[140:143], v[204:207], v[28:31]\n"
"s_add_u32 m0, s73, 0xd000\nds_read_b128 a[228:231], v226 offset:34816\nv_exp_f32 v149, v149\nv_mfma_f32_16x16x32_bf16 v[32:35], a[148:151], v[192:195], v[32:35]\n"
"buffer_load_dwordx4 v232, s[52:55], s80 offen lds\nv_mfma_f32_16x16x32_bf16 v[36:39], a[148:151], v[196:199], v[36:39]\ns_add_u32 s80, s80, 0x80\nds_read_b128 a[232:235], v225 offset:43008\n"
"v_exp_f32 v150, v150\nv_mfma_f32_16x16x32_bf16 v[40:43], a[148:151], v[200:203], v[40:43]\ns_add_u32 m0, s73, 0xf000\nds_read_b128 a[236:239], v226 offset:43008\n"
"v_exp_f32 v151, v151\nv_mfma_f32_16x16x32_bf16 v[44:47], a[148:151], v[204:207], v[44:47]\nbuffer_load_dwordx4 v232, s[52:55], s80 offen lds\nv_mfma_f32_16x16x32_bf16 v[48:51], a[152:155], v[160:163], 0\n"
"ds_read_b128 a[240:243], v225 offset:51200\nv_exp_f32 v136, v136\nv_mfma_f32_16x16x32_bf16 v[48:51], a[156:159], v[192:195], v[48:51]\nds_read_b128 a[244:247], v226 offset:51200\n"
"v_exp_f32 v137, v137\nv_mfma_f32_16x16x32_bf16 v[52:55], a[152:155], v[164:167], 0\nds_read_b128 a[248:251], v225 offset:59392\nv_exp_f32 v138, v138\n"
"v_mfma_f32_16x16x32_bf16 v[52:55], a[156:159], v[196:199], v[52:55]\nds_read_b128 a[252:255], v226 offset:59392\nv_exp_f32 v139, v139\nv_mfma_f32_16x16x32_bf16 v[56:59], a[152:155], v[168:171], 0\n"
"v_exp_f32 v152, v152\nv_mov_b32 v236, 0\nv_mfma_f32_16x16x32_bf16 v[56:59], a[156:159], v[200:203], v[56:59]\nv_exp_f32 v153, v153\n"
"v_mov_b32 v237, 0\nv_mfma_f32_16x16x32_bf16 v[60:63], a[152:155], v[172:175], 0\nv_exp_f32 v154, v154\nv_add_f32 v236, v236, v76\n"
"v_mfma_f32_16x16x32_bf16 v[60:63], a[156:159], v[204:207], v[60:63]\nv_exp_f32 v155, v155\nv_add_f32 v237, v237, v80\nv_mfma_f32_16x16x32_bf16 v[64:67], a[160:163], v[160:163], 0\n"
"v_exp_f32 v140, v140\nv_add_f32 v236, v236, v77\nv_mfma_f32_16x16x32_bf16 v[64:67], a[164:167], v[192:195], v[64:67]\nv_exp_f32 v141, v141\n"
"v_add_f32 v237, v237, v81\nv_mfma_f32_16x16x32_bf16 v[68:71], a[160:163], v[164:167], 0\nv_exp_f32 v142, v142\nv_add_f32 v236, v236, v78\n"
"v_mfma_f32_16x16x32_bf16 v[68:71], a[164:167], v[196:199], v[68:71]\nv_exp_f32 v143, v143\nv_mov_b32 v238, 0\nv_mfma_f32_16x16x32_bf16 v[72:75], a[160:163], v[168:171], 0\n"
"v_exp_f32 v156, v156\nv_cvt_pk_bf16_f32 v176, v128, v129\nv_mfma_f32_16x16x32_bf16 v[72:75], a[164:167], v[200:203], v[72:75]\nv_exp_f32 v157, v157\n"
"v_cvt_pk_bf16_f32 v177, v130, v131\nv_add_f32 v236, v236, v79\nv_mfma_f32_16x16x32_bf16 v[76:79], a[160:163], v[172:175], 0\nv_cvt_pk_bf16_f32 v178, v144, v145\n"
"v_exp_f32 v158, v158\nv_mfma_f32_16x16x32_bf16 v[76:79], a[164:167], v[204:207], v[76:79]\nv_cvt_pk_bf16_f32 v179, v146, v147\nv_exp_f32 v159, v159\n"
"v_add_f32 v237, v237, v82\nv_add_f32 v237, v237, v83\nv_mfma_f32_16x16x32_bf16 v[80:83], a[168:171], v[160:163], 0\nds_write_b128 v233, v[176:179] offset:0\n"
"v_cvt_pk_bf16_f32 v180, v132, v133\nv_mfma_f32_16x16x32_bf16 v[80:83], a[172:175], v[192:195], v[80:83]\nv_cvt_pk_bf16_f32 v181, v134, v135\nv_cvt_pk_bf16_f32 v182, v148, v149\n"
"v_cvt_pk_bf16_f32 v183, v150, v151\nv_add_f32 v238, v238, v84\nv_add_f32 v238, v238, v85\nv_add_f32 v238, v238, v86\n"
"v_add_f32 v238, v238, v87\nv_mfma_f32_16x16x32_bf16 v[84:87], a[168:171], v[164:167], 0\nds_write_b128 v233, v[180:183] offset:1024\nv_cvt_pk_bf16_f32 v184, v136, v137\n"
"v_mfma_f32_16x16x32_bf16 v[84:87], a[172:175], v[196:199], v[84:87]\nv_cvt_pk_bf16_f32 v185, v138, v139\nv_mov_b32 v239, 0\nv_cvt_pk_bf16_f32 v186, v152, v153\n"
"v_add_f32 v239, v239, v88\nv_add_f32 v239, v239, v89\nv_add_f32 v239, v239, v90\nv_add_f32 v239, v239, v91\n"
"v_mfma_f32_16x16x32_bf16 v[88:91], a[168:171], v[168:171], 0\nv_cvt_pk_bf16_f32 v187, v154, v155\nv_cvt_pk_bf16_f32 v188, v140, v141\nv_cvt_pk_bf16_f32 v189, v142, v143\n"
"v_mfma_f32_16x16x32_bf16 v[88:91], a[172:175], v[200:203], v[88:91]\nds_write_b128 v233, v[184:187] offset:2048\nv_cvt_pk_bf16_f32 v190, v156, v157\nv_add_f32 v236, v236, v92\n"
"v_add_f32 v236, v236, v93\nv_add_f32 v236, v236, v94\nv_add_f32 v236, v236, v95\nv_mfma_f32_16x16x32_bf16 v[92:95], a[168:171], v[172:175], 0\n"
"v_cvt_pk_bf16_f32 v191, v158, v159\nv_add_f32 v237, v237, v96\nv_add_f32 v238, v238, v100\nv_mfma_f32_16x16x32_bf16 v[92:95], a[172:175], v[204:207], v[92:95]\n"
"ds_write_b128 v233, v[188:191] offset:3072\nv_add_f32 v237, v237, v97\nv_add_f32 v237, v237, v98\nv_add_f32 v237, v237, v99\n"
"v_mfma_f32_16x16x32_bf16 v[96:99], a[176:179], v[160:163], 0\nv_add_f32 v238, v238, v101\nv_add_f32 v239, v239, v104\nv_add_f32 v236, v236, v128\n"
"v_mfma_f32_16x16x32_bf16 v[96:99], a[180:183], v[192:195], v[96:99]\nv_add_f32 v238, v238, v102\nv_add_f32 v239, v239, v105\nv_add_f32 v236, v236, v129\n"
"v_add_f32 v238, v238, v103\nv_mfma_f32_16x16x32_bf16 v[100:103], a[176:179], v[164:167], 0\nv_add_f32 v239, v239, v106\nv_add_f32 v237, v237, v132\n"
"v_add_f32 v238, v238, v136\nv_mfma_f32_16x16x32_bf16 v[100:103], a[180:183], v[196:199], v[100:103]\nv_add_f32 v239, v239, v107\nv_add_f32 v237, v237, v133\n"
"v_add_f32 v236, v236, v130\nv_mfma_f32_16x16x32_bf16 v[104:107], a[176:179], v[168:171], 0\nv_add_f32 v238, v238, v137\nv_add_f32 v237, v237, v134\n"
"v_mfma_f32_16x16x32_bf16 v[104:107], a[180:183], v[200:203], v[104:107]\nv_add_f32 v238, v238, v138\nv_add_f32 v236, v236, v131\nv_add_f32 v239, v239, v140\n"
"v_mfma_f32_16x16x32_bf16 v[108:111], a[176:179], v[172:175], 0\nv_add_f32 v237, v237, v135\nv_add_f32 v238, v238, v139\nv_add_f32 v239, v239, v141\n"
"v_mfma_f32_16x16x32_bf16 v[108:111], a[180:183], v[204:207], v[108:111]\nv_add_f32 v239, v239, v142\nv_add_f32 v236, v236, v144\nv_add_f32 v237, v237, v148\n"
"v_mfma_f32_16x16x32_bf16 v[112:115], a[184:187], v[160:163], 0\nv_lshrrev_b32 v249, 3, v254\nv_add_f32 v239, v239, v143\nv_add_f32 v238, v238, v152\n"
"v_mfma_f32_16x16x32_bf16 v[112:115], a[188:191], v[192:195], v[112:115]\nv_add_f32 v236, v236, v145\nv_add_f32 v237, v237, v149\nv_add_f32 v238, v238, v153\n"
"v_mfma_f32_16x16x32_bf16 v[116:119], a[184:187], v[164:167], 0\nv_lshlrev_b32 v249, 28, v249\nv_add_f32 v236, v236, v146\nv_add_f32 v239, v239, v156\n"
"v_mfma_f32_16x16x32_bf16 v[116:119], a[188:191], v[196:199], v[116:119]\nv_add_f32 v237, v237, v150\nv_add_f32 v238, v238, v154\nv_add_f32 v239, v239, v157\n"
"v_mfma_f32_16x16x32_bf16 v[120:123], a[184:187], v[168:171], 0\nv_lshl_add_u32 v249, v254, 7, v249\nv_add_f32 v236, v236, v147\nv_add_f32 v239, v239, v158\n"
"v_mfma_f32_16x16x32_bf16 v[120:123], a[188:191], v[200:203], v[120:123]\nv_add_f32 v237, v237, v151\nv_add_f32 v238, v238, v155\nv_add_f32 v239, v239, v159\n"
"v_mfma_f32_16x16x32_bf16 v[124:127], a[184:187], v[172:175], 0\nv_mfma_f32_16x16x32_bf16 v[124:127], a[188:191], v[204:207], v[124:127]\ns_waitcnt vmcnt(17) lgkmcnt(0)\ns_barrier\n"
"L_join%=:\ns_mov_b32 s84, 10\nL_rb%=:\nv_mfma_f32_16x16x32_bf16 v[128:131], a[192:195], a[0:3], v[208:211]\n"
"s_add_u32 s79, s79, 0x3e80\nv_mov_b32 v240, v236\nds_read_b128 v[192:195], v234 offset:0\nv_mfma_f32_16x16x32_bf16 v[128:131], a[196:199], a[16:19], v[128:131]\n"
"s_add_u32 m0, s73, 0x18000\nv_mov_b32 v241, v237\nds_read_b128 v[196:199], v234 offset:1024\nv_mfma_f32_16x16x32_bf16 v[128:131], a[200:203], a[32:35], v[128:131]\n"
"buffer_load_dwordx4 v231, s[48:51], s79 offen lds\nv_mfma_f32_16x16x32_bf16 v[128:131], a[204:207], a[48:51], v[128:131]\nv_mov_b32 v242, v238\nds_read_b128 v[200:203], v234 offset:2048\n"
"v_mfma_f32_16x16x32_bf16 v[128:131], a[208:211], a[64:67], v[128:131]\nv_mov_b32 v243, v239\nds_read_b128 v[204:207], v234 offset:3072\nv_mfma_f32_16x16x32_bf16 v[128:131], a[212:215], a[80:83], v[128:131]\n"
"ds_read_b64_tr_b16 a[128:129], v227 offset:0\nv_mov_b32 v244, v240\nv_mfma_f32_16x16x32_bf16 v[128:131], a[216:219], a[96:99], v[128:131]\nds_read_b64_tr_b16 a[130:131], v227 offset:2048\n"
"v_mov_b32 v253, v241\nv_mfma_f32_16x16x32_bf16 v[128:131], a[220:223], a[112:115], v[128:131]\ns_add_u32 s79, s79, 0x80\nds_read_b64_tr_b16 a[132:133], v227 offset:4096\n"
"v_permlane32_swap_b32 v240, v244\nv_mfma_f32_16x16x32_bf16 v[132:135], a[220:223], a[116:119], v[212:215]\ns_add_u32 m0, s73, 0x1a000\nds_read_b64_tr_b16 a[134:135], v227 offset:6144\n"
"v_permlane32_swap_b32 v241, v253\nv_mfma_f32_16x16x32_bf16 v[132:135], a[216:219], a[100:103], v[132:135]\nbuffer_load_dwordx4 v231, s[48:51], s79 offen lds\nv_mfma_f32_16x16x32_bf16 v[132:135], a[212:215], a[84:87], v[132:135]\n"
"ds_read_b64_tr_b16 a[136:137], v228 offset:0\nv_add_f32 v240, v240, v244\nv_mfma_f32_16x16x32_bf16 v[132:135], a[208:211], a[68:71], v[132:135]\nds_read_b64_tr_b16 a[138:139], v228 offset:2048\n"
"v_add_f32 v241, v241, v253\nv_mfma_f32_16x16x32_bf16 v[132:135], a[204:207], a[52:55], v[132:135]\nds_read_b64_tr_b16 a[140:141], v228 offset:4096\nv_mov_b32 v244, v240\n"
"v_mfma_f32_16x16x32_bf16 v[132:135], a[200:203], a[36:39], v[132:135]\nds_read_b64_tr_b16 a[142:143], v228 offset:6144\nv_mov_b32 v253, v241\nv_mfma_f32_16x16x32_bf16 v[132:135], a[196:199], a[20:23], v[132:135]\n"
"ds_read_b64_tr_b16 a[144:145], v229 offset:0\nv_permlane16_swap_b32 v240, v244\nv_mfma_f32_16x16x32_bf16 v[132:135], a[192:195], a[4:7], v[132:135]\ns_add_u32 s79, s79, 0x80\n"
"ds_read_b64_tr_b16 a[146:147], v229 offset:2048\nv_permlane16_swap_b32 v241, v253\nv_mfma_f32_16x16x32_bf16 v[136:139], a[192:195], a[8:11], v[216:219]\ns_add_u32 m0, s73, 0x1c000\n"
"ds_read_b64_tr_b16 a[148:149], v229 offset:4096\nv_add_f32 v240, v240, v244\nv_mfma_f32_16x16x32_bf16 v[136:139], a[196:199], a[24:27], v[136:139]\nbuffer_load_dwordx4 v231, s[48:51], s79 offen lds\n"
"v_mfma_f32_16x16x32_bf16 v[136:139], a[200:203], a[40:43], v[136:139]\nds_read_b64_tr_b16 a[150:151], v229 offset:6144\nv_add_f32 v241, v241, v253\nv_mfma_f32_16x16x32_bf16 v[136:139], a[204:207], a[56:59], v[136:139]\n"
"ds_read_b64_tr_b16 a[152:153], v230 offset:0\nv_mov_b32 v244, v242\nv_mfma_f32_16x16x32_bf16 v[136:139], a[208:211], a[72:75], v[136:139]\nds_read_b64_tr_b16 a[154:155], v230 offset:2048\n"
"v_mov_b32 v253, v243\nv_mfma_f32_16x16x32_bf16 v[136:139], a[212:215], a[88:91], v[136:139]\nds_read_b64_tr_b16 a[156:157], v230 offset:4096\nv_permlane32_swap_b32 v242, v244\n"
"v_mfma_f32_16x16x32_bf16 v[136:139], a[216:219], a[104:107], v[136:139]\nds_read_b64_tr_b16 a[158:159], v230 offset:6144\nv_permlane32_swap_b32 v243, v253\nv_mfma_f32_16x16x32_bf16 v[136:139], a[220:223], a[120:123], v[136:139]\n"
"s_add_u32 s79, s79, 0x80\nds_read_b64_tr_b16 a[160:161], v227 offset:8192\nv_add_f32 v242, v242, v244\nv_mfma_f32_16x16x32_bf16 v[140:143], a[220:223], a[124:127], v[220:223]\n"
"s_add_u32 m0, s73, 0x1e000\nds_read_b64_tr_b16 a[162:163], v227 offset:10240\nv_add_f32 v243, v243, v253\nv_mfma_f32_16x16x32_bf16 v[140:143], a[216:219], a[108:111], v[140:143]\n"
"buffer_load_dwordx4 v231, s[48:51], s79 offen lds\nv_mfma_f32_16x16x32_bf16 v[140:143], a[212:215], a[92:95], v[140:143]\nds_read_b64_tr_b16 a[164:165], v227 offset:12288\nv_mov_b32 v244, v242\n"
"v_mfma_f32_16x16x32_bf16 v[140:143], a[208:211], a[76:79], v[140:143]\nds_read_b64_tr_b16 a[166:167], v227 offset:14336\nv_mov_b32 v253, v243\nv_mfma_f32_16x16x32_bf16 v[140:143], a[204:207], a[60:63], v[140:143]\n"
"ds_read_b64_tr_b16 a[168:169], v228 offset:8192\nv_permlane16_swap_b32 v242, v244\nv_mfma_f32_16x16x32_bf16 v[140:143], a[200:203], a[44:47], v[140:143]\nds_read_b64_tr_b16 a[170:171], v228 offset:10240\n"
"v_permlane16_swap_b32 v243, v253\nv_mfma_f32_16x16x32_bf16 v[140:143], a[196:199], a[28:31], v[140:143]\nds_read_b64_tr_b16 a[172:173], v228 offset:12288\nv_add_f32 v242, v242, v244\n"
"v_mfma_f32_16x16x32_bf16 v[140:143], a[192:195], a[12:15], v[140:143]\ns_add_u32 s79, s79, 0x3e80\nds_read_b64_tr_b16 a[174:175], v228 offset:14336\nv_add_f32 v243, v243, v253\n"
"v_mfma_f32_16x16x32_bf16 v[144:147], a[224:227], a[0:3], v[208:211]\ns_add_u32 m0, s73, 0x19000\nv_exp_f32 v128, v128\nv_mfma_f32_16x16x32_bf16 v[144:147], a[228:231], a[16:19], v[144:147]\n"
"buffer_load_dwordx4 v231, s[48:51], s79 offen lds\nv_mfma_f32_16x16x32_bf16 v[144:147], a[232:235], a[32:35], v[144:147]\nds_read_b64_tr_b16 a[176:177], v229 offset:8192\nv_add_f32 v236, v236, v128\n"
"v_mfma_f32_16x16x32_bf16 v[144:147], a[236:239], a[48:51], v[144:147]\nv_exp_f32 v132, v132\nv_mfma_f32_16x16x32_bf16 v[144:147], a[240:243], a[64:67], v[144:147]\nds_read_b64_tr_b16 a[178:179], v229 offset:10240\n"
"v_add_f32 v237, v237, v132\nv_mfma_f32_16x16x32_bf16 v[144:147], a[244:247], a[80:83], v[144:147]\nv_exp_f32 v136, v136\nv_mfma_f32_16x16x32_bf16 v[144:147], a[248:251], a[96:99], v[144:147]\n"
"s_add_u32 s79, s79, 0x80\nds_read_b64_tr_b16 a[180:181], v229 offset:12288\nv_add_f32 v238, v238, v136\nv_mfma_f32_16x16x32_bf16 v[144:147], a[252:255], a[112:115], v[144:147]\n"
"s_add_u32 m0, s73, 0x1b000\nv_exp_f32 v140, v140\nv_mfma_f32_16x16x32_bf16 v[148:151], a[252:255], a[116:119], v[212:215]\nbuffer_load_dwordx4 v231, s[48:51], s79 offen lds\n"
"v_mfma_f32_16x16x32_bf16 v[148:151], a[248:251], a[100:103], v[148:151]\nds_read_b64_tr_b16 a[182:183], v229 offset:14336\nv_add_f32 v239, v239, v140\nv_mfma_f32_16x16x32_bf16 v[148:151], a[244:247], a[84:87], v[148:151]\n"
"v_exp_f32 v129, v129\nv_mfma_f32_16x16x32_bf16 v[148:151], a[240:243], a[68:71], v[148:151]\nds_read_b64_tr_b16 a[184:185], v230 offset:8192\nv_cvt_pk_bf16_f32 v160, v128, v129\n"
"v_mfma_f32_16x16x32_bf16 v[148:151], a[236:239], a[52:55], v[148:151]\nds_read_b64_tr_b16 a[186:187], v230 offset:10240\nv_add_f32 v236, v236, v129\nv_mfma_f32_16x16x32_bf16 v[148:151], a[232:235], a[36:39], v[148:151]\n"
"v_exp_f32 v130, v130\nv_mfma_f32_16x16x32_bf16 v[148:151], a[228:231], a[20:23], v[148:151]\ns_add_u32 s79, s79, 0x80\nds_read_b64_tr_b16 a[188:189], v230 offset:12288\n"
"v_add_f32 v236, v236, v130\nv_mfma_f32_16x16x32_bf16 v[148:151], a[224:227], a[4:7], v[148:151]\ns_add_u32 m0, s73, 0x1d000\nv_exp_f32 v131, v131\n"
"v_mfma_f32_16x16x32_bf16 v[152:155], a[224:227], a[8:11], v[216:219]\nbuffer_load_dwordx4 v231, s[48:51], s79 offen lds\nv_mfma_f32_16x16x32_bf16 v[152:155], a[228:231], a[24:27], v[152:155]\nds_read_b64_tr_b16 a[190:191], v230 offset:14336\n"
"v_cvt_pk_bf16_f32 v161, v130, v131\nv_mfma_f32_16x16x32_bf16 v[152:155], a[232:235], a[40:43], v[152:155]\nv_exp_f32 v133, v133\nv_mfma_f32_16x16x32_bf16 v[152:155], a[236:239], a[56:59], v[152:155]\n"
"v_exp_f32 v134, v134\nv_mfma_f32_16x16x32_bf16 v[152:155], a[240:243], a[72:75], v[152:155]\nv_exp_f32 v135, v135\nv_mfma_f32_16x16x32_bf16 v[152:155], a[244:247], a[88:91], v[152:155]\n"
"v_exp_f32 v144, v144\nv_mfma_f32_16x16x32_bf16 v[152:155], a[248:251], a[104:107], v[152:155]\ns_add_u32 s79, s79, 0x80\nv_exp_f32 v145, v145\n"
"v_mfma_f32_16x16x32_bf16 v[152:155], a[252:255], a[120:123], v[152:155]\ns_add_u32 m0, s73, 0x1f000\nv_exp_f32 v146, v146\nv_mfma_f32_16x16x32_bf16 v[156:159], a[252:255], a[124:127], v[220:223]\n"
"buffer_load_dwordx4 v231, s[48:51], s79 offen lds\nv_mfma_f32_16x16x32_bf16 v[156:159], a[248:251], a[108:111], v[156:159]\nv_exp_f32 v147, v147\nv_mfma_f32_16x16x32_bf16 v[156:159], a[244:247], a[92:95], v[156:159]\n"
"v_exp_f32 v148, v148\nv_mfma_f32_16x16x32_bf16 v[156:159], a[240:243], a[76:79], v[156:159]\nv_exp_f32 v149, v149\nv_mfma_f32_16x16x32_bf16 v[156:159], a[236:239], a[60:63], v[156:159]\n"
"v_exp_f32 v150, v150\nv_mfma_f32_16x16x32_bf16 v[156:159], a[232:235], a[44:47], v[156:159]\nv_exp_f32 v151, v151\nv_mfma_f32_16x16x32_bf16 v[156:159], a[228:231], a[28:31], v[156:159]\n"
"v_exp_f32 v137, v137\nv_mfma_f32_16x16x32_bf16 v[156:159], a[224:227], a[12:15], v[156:159]\nv_exp_f32 v141, v141\ns_waitcnt vmcnt(16) lgkmcnt(0)\n"
"s_barrier\nv_mfma_f32_16x16x32_bf16 v[0:3], a[128:131], v[176:179], v[0:3]\ns_add_u32 s80, s80, 0x7e80\nds_read_b128 a[192:195], v225 offset:0\n"
"v_cvt_pk_bf16_f32 v164, v132, v133\nv_mfma_f32_16x16x32_bf16 v[0:3], a[132:135], v[192:195], v[0:3]\ns_add_u32 m0, s73, 0x0\nds_read_b128 a[196:199], v226 offset:0\n"
"v_cvt_pk_bf16_f32 v165, v134, v135\nv_mfma_f32_16x16x32_bf16 v[4:7], a[132:135], v[196:199], v[4:7]\nbuffer_load_dwordx4 v232, s[52:55], s80 offen lds\nv_mfma_f32_16x16x32_bf16 v[4:7], a[128:131], v[180:183], v[4:7]\n"
"s_add_u32 s80, s80, 0x80\nds_read_b128 a[200:203], v225 offset:8192\nv_cvt_pk_bf16_f32 v162, v144, v145\nv_mfma_f32_16x16x32_bf16 v[8:11], a[128:131], v[184:187], v[8:11]\n"
"s_add_u32 m0, s73, 0x2000\nds_read_b128 a[204:207], v226 offset:8192\nv_cvt_pk_bf16_f32 v163, v146, v147\nv_mfma_f32_16x16x32_bf16 v[8:11], a[132:135], v[200:203], v[8:11]\n"
"buffer_load_dwordx4 v232, s[52:55], s80 offen lds\nv_mfma_f32_16x16x32_bf16 v[12:15], a[132:135], v[204:207], v[12:15]\ns_add_u32 s80, s80, 0x4080\nds_read_b128 a[208:211], v225 offset:16384\n"
"v_cvt_pk_bf16_f32 v166, v148, v149\nv_mfma_f32_16x16x32_bf16 v[12:15], a[128:131], v[188:191], v[12:15]\ns_add_u32 m0, s73, 0x4000\nds_read_b128 a[212:215], v226 offset:16384\n"
"v_cvt_pk_bf16_f32 v167, v150, v151\nv_mfma_f32_16x16x32_bf16 v[16:19], a[136:139], v[176:179], v[16:19]\nbuffer_load_dwordx4 v232, s[52:55], s80 offen lds\nv_mfma_f32_16x16x32_bf16 v[16:19], a[140:143], v[192:195], v[16:19]\n"
"s_add_u32 s80, s80, 0x80\nds_read_b128 a[216:219], v225 offset:24576\nv_add_f32 v237, v237, v133\nv_mfma_f32_16x16x32_bf16 v[20:23], a[140:143], v[196:199], v[20:23]\n"
"s_add_u32 m0, s73, 0x6000\nds_read_b128 a[220:223], v226 offset:24576\nv_add_f32 v238, v238, v137\nv_mfma_f32_16x16x32_bf16 v[20:23], a[136:139], v[180:183], v[20:23]\n"
"buffer_load_dwordx4 v232, s[52:55], s80 offen lds\nv_mfma_f32_16x16x32_bf16 v[24:27], a[136:139], v[184:187], v[24:27]\ns_add_u32 s80, s80, 0xfffffe80\nds_read_b128 a[224:227], v225 offset:2048\n"
"v_add_f32 v239, v239, v141\nv_mfma_f32_16x16x32_bf16 v[24:27], a[140:143], v[200:203], v[24:27]\ns_add_u32 m0, s73, 0x1000\nds_read_b128 a[228:231], v226 offset:2048\n"
"v_cvt_pk_bf16_f32 v168, v136, v137\nv_mfma_f32_16x16x32_bf16 v[28:31], a[140:143], v[204:207], v[28:31]\nbuffer_load_dwordx4 v232, s[52:55], s80 offen lds\nv_mfma_f32_16x16x32_bf16 v[28:31], a[136:139], v[188:191], v[28:31]\n"
"s_add_u32 s80, s80, 0x80\nds_read_b128 a[232:235], v225 offset:10240\nv_add_f32 v237, v237, v134\nv_mfma_f32_16x16x32_bf16 v[32:35], a[144:147], v[176:179], v[32:35]\n"
"s_add_u32 m0, s73, 0x3000\nds_read_b128 a[236:239], v226 offset:10240\nv_cvt_pk_bf16_f32 v172, v140, v141\nv_mfma_f32_16x16x32_bf16 v[32:35], a[148:151], v[192:195], v[32:35]\n"
"buffer_load_dwordx4 v232, s[52:55], s80 offen lds\nv_mfma_f32_16x16x32_bf16 v[36:39], a[148:151], v[196:199], v[36:39]\ns_add_u32 s80, s80, 0xffffc080\nds_read_b128 a[240:243], v225 offset:18432\n"
"v_add_f32 v236, v236, v131\nv_mfma_f32_16x16x32_bf16 v[36:39], a[144:147], v[180:183], v[36:39]\ns_add_u32 m0, s73, 0x5000\nds_read_b128 a[244:247], v226 offset:18432\n"
"v_add_f32 v237, v237, v135\nv_mfma_f32_16x16x32_bf16 v[40:43], a[144:147], v[184:187], v[40:43]\nbuffer_load_dwordx4 v232, s[52:55], s80 offen lds\nv_mfma_f32_16x16x32_bf16 v[40:43], a[148:151], v[200:203], v[40:43]\n"
"s_add_u32 s80, s80, 0x80\nds_read_b128 a[248:251], v225 offset:26624\nv_add_f32 v236, v236, v144\nv_mfma_f32_16x16x32_bf16 v[44:47], a[148:151], v[204:207], v[44:47]\n"
"s_add_u32 m0, s73, 0x7000\nv_exp_f32 v138, v138\nv_mfma_f32_16x16x32_bf16 v[44:47], a[144:147], v[188:191], v[44:47]\nbuffer_load_dwordx4 v232, s[52:55], s80 offen lds\n"
"v_mfma_f32_16x16x32_bf16 v[48:51], a[152:155], v[176:179], v[48:51]\nv_exp_f32 v139, v139\nv_mfma_f32_16x16x32_bf16 v[48:51], a[156:159], v[192:195], v[48:51]\nv_exp_f32 v152, v152\n"
"v_mfma_f32_16x16x32_bf16 v[52:55], a[156:159], v[196:199], v[52:55]\nv_exp_f32 v153, v153\nv_mfma_f32_16x16x32_bf16 v[52:55], a[152:155], v[180:183], v[52:55]\nv_exp_f32 v154, v154\n"
"v_mfma_f32_16x16x32_bf16 v[56:59], a[152:155], v[184:187], v[56:59]\nv_exp_f32 v155, v155\nv_mfma_f32_16x16x32_bf16 v[56:59], a[156:159], v[200:203], v[56:59]\nds_read_b128 a[252:255], v226 offset:26624\n"
"v_cvt_pk_bf16_f32 v169, v138, v139\nv_mfma_f32_16x16x32_bf16 v[60:63], a[156:159], v[204:207], v[60:63]\nv_exp_f32 v142, v142\nv_mfma_f32_16x16x32_bf16 v[60:63], a[152:155], v[188:191], v[60:63]\n"
"v_cvt_pk_bf16_f32 v170, v152, v153\nv_cvt_pk_bf16_f32 v171, v154, v155\nv_mfma_f32_16x16x32_bf16 v[64:67], a[160:163], v[176:179], v[64:67]\nv_exp_f32 v143, v143\n"
"v_mfma_f32_16x16x32_bf16 v[64:67], a[164:167], v[192:195], v[64:67]\nv_exp_f32 v156, v156\nv_mfma_f32_16x16x32_bf16 v[68:71], a[164:167], v[196:199], v[68:71]\nv_exp_f32 v157, v157\n"
"v_mfma_f32_16x16x32_bf16 v[68:71], a[160:163], v[180:183], v[68:71]\nv_exp_f32 v158, v158\nv_mfma_f32_16x16x32_bf16 v[72:75], a[160:163], v[184:187], v[72:75]\nv_exp_f32 v159, v159\n"
"v_mfma_f32_16x16x32_bf16 v[72:75], a[164:167], v[200:203], v[72:75]\nv_add_f32 v238, v238, v138\nv_add_f32 v239, v239, v142\nv_mfma_f32_16x16x32_bf16 v[76:79], a[164:167], v[204:207], v[76:79]\n"
"v_cvt_pk_bf16_f32 v173, v142, v143\nv_cvt_pk_bf16_f32 v174, v156, v157\nv_mfma_f32_16x16x32_bf16 v[76:79], a[160:163], v[188:191], v[76:79]\nv_cvt_pk_bf16_f32 v175, v158, v159\n"
"v_add_f32 v238, v238, v139\nv_mfma_f32_16x16x32_bf16 v[80:83], a[168:171], v[176:179], v[80:83]\nds_write_b128 v233, v[240:243] offset:0\nv_mfma_f32_16x16x32_bf16 v[80:83], a[172:175], v[192:195], v[80:83]\n"
"v_add_f32 v239, v239, v143\nv_add_f32 v237, v237, v148\nv_mfma_f32_16x16x32_bf16 v[84:87], a[172:175], v[196:199], v[84:87]\nv_add_f32 v238, v238, v152\n"
"v_add_f32 v239, v239, v156\nv_mfma_f32_16x16x32_bf16 v[84:87], a[168:171], v[180:183], v[84:87]\nv_add_f32 v236, v236, v145\nv_add_f32 v237, v237, v149\n"
"v_mfma_f32_16x16x32_bf16 v[88:91], a[168:171], v[184:187], v[88:91]\nds_write_b128 v233, v[160:163] offset:16384\nv_mfma_f32_16x16x32_bf16 v[88:91], a[172:175], v[200:203], v[88:91]\nv_add_f32 v238, v238, v153\n"
"v_add_f32 v239, v239, v157\nv_mfma_f32_16x16x32_bf16 v[92:95], a[172:175], v[204:207], v[92:95]\nv_add_f32 v236, v236, v146\nv_add_f32 v237, v237, v150\n"
"v_mfma_f32_16x16x32_bf16 v[92:95], a[168:171], v[188:191], v[92:95]\nv_add_f32 v238, v238, v154\nv_add_f32 v239, v239, v158\nv_mfma_f32_16x16x32_bf16 v[96:99], a[176:179], v[176:179], v[96:99]\n"
"ds_write_b128 v233, v[164:167] offset:17408\nv_mfma_f32_16x16x32_bf16 v[96:99], a[180:183], v[192:195], v[96:99]\nbuffer_load_dword v248, v249, s[92:95], s91 offen offset:0\nv_mfma_f32_16x16x32_bf16 v[100:103], a[180:183], v[196:199], v[100:103]\n"
"s_add_u32 s91, s91, 0x10000\nv_add_f32 v236, v236, v147\nv_add_f32 v237, v237, v151\nv_mfma_f32_16x16x32_bf16 v[100:103], a[176:179], v[180:183], v[100:103]\n"
"v_add_f32 v238, v238, v155\nv_add_f32 v239, v239, v159\nv_mfma_f32_16x16x32_bf16 v[104:107], a[176:179], v[184:187], v[104:107]\nds_write_b128 v233, v[168:171] offset:18432\n"
"v_mfma_f32_16x16x32_bf16 v[104:107], a[180:183], v[200:203], v[104:107]\nv_mfma_f32_16x16x32_bf16 v[108:111], a[180:183], v[204:207], v[108:111]\nv_mfma_f32_16x16x32_bf16 v[108:111], a[176:179], v[188:191], v[108:111]\nv_mfma_f32_16x16x32_bf16 v[112:115], a[184:187], v[176:179], v[112:115]\n"
"ds_write_b128 v233, v[172:175] offset:19456\nv_mfma_f32_16x16x32_bf16 v[112:115], a[188:191], v[192:195], v[112:115]\nv_mfma_f32_16x16x32_bf16 v[116:119], a[188:191], v[196:199], v[116:119]\nv_mfma_f32_16x16x32_bf16 v[116:119], a[184:187], v[180:183], v[116:119]\n"
"v_mfma_f32_16x16x32_bf16 v[120:123], a[184:187], v[184:187], v[120:123]\nv_mfma_f32_16x16x32_bf16 v[120:123], a[188:191], v[200:203], v[120:123]\nv_mfma_f32_16x16x32_bf16 v[124:127], a[188:191], v[204:207], v[124:127]\nv_mfma_f32_16x16x32_bf16 v[124:127], a[184:187], v[188:191], v[124:127]\n"
"s_waitcnt vmcnt(17) lgkmcnt(0)\ns_barrier\nv_mfma_f32_16x16x32_bf16 v[128:131], a[192:195], a[0:3], v[208:211]\ns_add_u32 s79, s79, 0x3e80\n"
"ds_read_b128 v[156:159], v234 offset:0\nv_cvt_i32_f32 v253, s76\nv_mfma_f32_16x16x32_bf16 v[128:131], a[196:199], a[16:19], v[128:131]\ns_add_u32 m0, s73, 0x10000\n"
"ds_read_b128 v[192:195], v234 offset:16384\nv_add_u32 v253, 1, v253\nv_mfma_f32_16x16x32_bf16 v[128:131], a[200:203], a[32:35], v[128:131]\nbuffer_load_dwordx4 v231, s[48:51], s79 offen lds\n"
"v_mfma_f32_16x16x32_bf16 v[128:131], a[204:207], a[48:51], v[128:131]\nds_read_b128 v[196:199], v234 offset:17408\nv_mfma_f32_16x16x32_bf16 v[128:131], a[208:211], a[64:67], v[128:131]\nds_read_b128 v[200:203], v234 offset:18432\n"
"v_mfma_f32_16x16x32_bf16 v[128:131], a[212:215], a[80:83], v[128:131]\nds_read_b128 v[204:207], v234 offset:19456\nv_mfma_f32_16x16x32_bf16 v[128:131], a[216:219], a[96:99], v[128:131]\nds_read_b64_tr_b16 a[128:129], v227 offset:32768\n"
"v_mfma_f32_16x16x32_bf16 v[128:131], a[220:223], a[112:115], v[128:131]\ns_add_u32 s79, s79, 0x80\nds_read_b64_tr_b16 a[130:131], v227 offset:34816\nv_mfma_f32_16x16x32_bf16 v[132:135], a[220:223], a[116:119], v[212:215]\n"
"s_add_u32 m0, s73, 0x12000\nds_read_b64_tr_b16 a[132:133], v227 offset:36864\nv_mfma_f32_16x16x32_bf16 v[132:135], a[216:219], a[100:103], v[132:135]\nbuffer_load_dwordx4 v231, s[48:51], s79 offen lds\n"
"v_mfma_f32_16x16x32_bf16 v[132:135], a[212:215], a[84:87], v[132:135]\nds_read_b64_tr_b16 a[134:135], v227 offset:38912\ns_waitcnt lgkmcnt(6)\nv_add_f32 v240, v240, v156\n"
"v_mfma_f32_16x16x32_bf16 v[132:135], a[208:211], a[68:71], v[132:135]\nv_exp_f32 v128, v128\nv_mfma_f32_16x16x32_bf16 v[132:135], a[204:207], a[52:55], v[132:135]\nv_exp_f32 v129, v129\n"
"v_mfma_f32_16x16x32_bf16 v[132:135], a[200:203], a[36:39], v[132:135]\nds_read_b64_tr_b16 a[136:137], v228 offset:32768\nv_add_f32 v236, v236, v128\nv_mfma_f32_16x16x32_bf16 v[132:135], a[196:199], a[20:23], v[132:135]\n"
"ds_read_b64_tr_b16 a[138:139], v228 offset:34816\nv_add_f32 v241, v241, v157\nv_mfma_f32_16x16x32_bf16 v[132:135], a[192:195], a[4:7], v[132:135]\ns_add_u32 s79, s79, 0x80\n"
"ds_read_b64_tr_b16 a[140:141], v228 offset:36864\nv_add_f32 v242, v242, v158\nv_mfma_f32_16x16x32_bf16 v[136:139], a[192:195], a[8:11], v[216:219]\ns_add_u32 m0, s73, 0x14000\n"
"ds_read_b64_tr_b16 a[142:143], v228 offset:38912\nv_add_f32 v243, v243, v159\nv_mfma_f32_16x16x32_bf16 v[136:139], a[196:199], a[24:27], v[136:139]\nbuffer_load_dwordx4 v231, s[48:51], s79 offen lds\n"
"v_mfma_f32_16x16x32_bf16 v[136:139], a[200:203], a[40:43], v[136:139]\nds_read_b64_tr_b16 a[144:145], v229 offset:32768\nv_add_f32 v236, v236, v129\nv_mfma_f32_16x16x32_bf16 v[136:139], a[204:207], a[56:59], v[136:139]\n"
"v_exp_f32 v132, v132\nv_mfma_f32_16x16x32_bf16 v[136:139], a[208:211], a[72:75], v[136:139]\nv_exp_f32 v133, v133\nv_mfma_f32_16x16x32_bf16 v[136:139], a[212:215], a[88:91], v[136:139]\n"
"ds_read_b64_tr_b16 a[146:147], v229 offset:34816\nv_add_f32 v237, v237, v132\nv_mfma_f32_16x16x32_bf16 v[136:139], a[216:219], a[104:107], v[136:139]\nds_read_b64_tr_b16 a[148:149], v229 offset:36864\n"
"v_frexp_exp_i32_f32 v240, v240\nv_mfma_f32_16x16x32_bf16 v[136:139], a[220:223], a[120:123], v[136:139]\ns_add_u32 s79, s79, 0x80\nds_read_b64_tr_b16 a[150:151], v229 offset:38912\n"
"v_add_f32 v237, v237, v133\nv_mfma_f32_16x16x32_bf16 v[140:143], a[220:223], a[124:127], v[220:223]\ns_add_u32 m0, s73, 0x16000\nds_read_b64_tr_b16 a[152:153], v230 offset:32768\n"
"v_frexp_exp_i32_f32 v241, v241\nv_mfma_f32_16x16x32_bf16 v[140:143], a[216:219], a[108:111], v[140:143]\nbuffer_load_dwordx4 v231, s[48:51], s79 offen lds\nv_mfma_f32_16x16x32_bf16 v[140:143], a[212:215], a[92:95], v[140:143]\n"
"ds_read_b64_tr_b16 a[154:155], v230 offset:34816\nv_sub_u32 v240, v253, v240\nv_mfma_f32_16x16x32_bf16 v[140:143], a[208:211], a[76:79], v[140:143]\nv_exp_f32 v136, v136\n"
"v_mfma_f32_16x16x32_bf16 v[140:143], a[204:207], a[60:63], v[140:143]\nv_exp_f32 v137, v137\nv_mfma_f32_16x16x32_bf16 v[140:143], a[200:203], a[44:47], v[140:143]\nds_read_b64_tr_b16 a[156:157], v230 offset:36864\n"
"v_add_f32 v238, v238, v136\nv_mfma_f32_16x16x32_bf16 v[140:143], a[196:199], a[28:31], v[140:143]\nds_read_b64_tr_b16 a[158:159], v230 offset:38912\nv_add_f32 v238, v238, v137\n"
"v_mfma_f32_16x16x32_bf16 v[140:143], a[192:195], a[12:15], v[140:143]\ns_add_u32 s79, s79, 0x3e80\nds_read_b64_tr_b16 a[160:161], v227 offset:40960\nv_sub_u32 v241, v253, v241\n"
"v_mfma_f32_16x16x32_bf16 v[144:147], a[224:227], a[0:3], v[208:211]\ns_add_u32 m0, s73, 0x11000\nds_read_b64_tr_b16 a[162:163], v227 offset:43008\nv_frexp_exp_i32_f32 v242, v242\n"
"v_mfma_f32_16x16x32_bf16 v[144:147], a[228:231], a[16:19], v[144:147]\nbuffer_load_dwordx4 v231, s[48:51], s79 offen lds\nv_mfma_f32_16x16x32_bf16 v[144:147], a[232:235], a[32:35], v[144:147]\nds_read_b64_tr_b16 a[164:165], v227 offset:45056\n"
"v_frexp_exp_i32_f32 v243, v243\nv_mfma_f32_16x16x32_bf16 v[144:147], a[236:239], a[48:51], v[144:147]\nds_read_b64_tr_b16 a[166:167], v227 offset:47104\nv_min_i32 v240, 0, v240\n"
"v_mfma_f32_16x16x32_bf16 v[144:147], a[240:243], a[64:67], v[144:147]\nv_exp_f32 v140, v140\nv_mfma_f32_16x16x32_bf16 v[144:147], a[244:247], a[80:83], v[144:147]\nds_read_b64_tr_b16 a[168:169], v228 offset:40960\n"
"v_add_f32 v239, v239, v140\nv_mfma_f32_16x16x32_bf16 v[144:147], a[248:251], a[96:99], v[144:147]\ns_add_u32 s79, s79, 0x80\nds_read_b64_tr_b16 a[170:171], v228 offset:43008\n"
"v_min_i32 v241, 0, v241\nv_mfma_f32_16x16x32_bf16 v[144:147], a[252:255], a[112:115], v[144:147]\ns_add_u32 m0, s73, 0x13000\nds_read_b64_tr_b16 a[172:173], v228 offset:45056\n"
"v_cvt_pk_bf16_f32 v176, v128, v129\nv_mfma_f32_16x16x32_bf16 v[148:151], a[252:255], a[116:119], v[212:215]\nbuffer_load_dwordx4 v231, s[48:51], s79 offen lds\nv_mfma_f32_16x16x32_bf16 v[148:151], a[248:251], a[100:103], v[148:151]\n"
"ds_read_b64_tr_b16 a[174:175], v228 offset:47104\nv_cvt_pk_bf16_f32 v180, v132, v133\nv_mfma_f32_16x16x32_bf16 v[148:151], a[244:247], a[84:87], v[148:151]\nds_read_b64_tr_b16 a[176:177], v229 offset:40960\n"
"v_sub_u32 v242, v253, v242\nv_mfma_f32_16x16x32_bf16 v[148:151], a[240:243], a[68:71], v[148:151]\nds_read_b64_tr_b16 a[178:179], v229 offset:43008\nv_sub_u32 v243, v253, v243\n"
"v_mfma_f32_16x16x32_bf16 v[148:151], a[236:239], a[52:55], v[148:151]\nds_read_b64_tr_b16 a[180:181], v229 offset:45056\nv_cvt_pk_bf16_f32 v184, v136, v137\nv_mfma_f32_16x16x32_bf16 v[148:151], a[232:235], a[36:39], v[148:151]\n"
"ds_read_b64_tr_b16 a[182:183], v229 offset:47104\nv_max_i32 v240, 0xffffff88, v240\nv_mfma_f32_16x16x32_bf16 v[148:151], a[228:231], a[20:23], v[148:151]\ns_add_u32 s79, s79, 0x80\n"
"ds_read_b64_tr_b16 a[184:185], v230 offset:40960\nv_max_i32 v241, 0xffffff88, v241\nv_mfma_f32_16x16x32_bf16 v[148:151], a[224:227], a[4:7], v[148:151]\ns_add_u32 m0, s73, 0x15000\n"
"ds_read_b64_tr_b16 a[186:187], v230 offset:43008\nv_min_i32 v242, 0, v242\nv_mfma_f32_16x16x32_bf16 v[152:155], a[224:227], a[8:11], v[216:219]\nbuffer_load_dwordx4 v231, s[48:51], s79 offen lds\n"
"v_mfma_f32_16x16x32_bf16 v[152:155], a[228:231], a[24:27], v[152:155]\nds_read_b64_tr_b16 a[188:189], v230 offset:45056\nv_min_i32 v243, 0, v243\nv_mfma_f32_16x16x32_bf16 v[152:155], a[232:235], a[40:43], v[152:155]\n"
"ds_read_b64_tr_b16 a[190:191], v230 offset:47104\nv_cvt_f32_i32 v244, v240\nv_mfma_f32_16x16x32_bf16 v[152:155], a[236:239], a[56:59], v[152:155]\nv_exp_f32 v130, v130\n"
"v_mfma_f32_16x16x32_bf16 v[152:155], a[240:243], a[72:75], v[152:155]\nv_exp_f32 v141, v141\nv_mfma_f32_16x16x32_bf16 v[152:155], a[244:247], a[88:91], v[152:155]\nv_exp_f32 v134, v134\n"
"v_mfma_f32_16x16x32_bf16 v[152:155], a[248:251], a[104:107], v[152:155]\ns_add_u32 s79, s79, 0x80\nv_exp_f32 v138, v138\nv_mfma_f32_16x16x32_bf16 v[152:155], a[252:255], a[120:123], v[152:155]\n"
"s_add_u32 m0, s73, 0x17000\nv_exp_f32 v142, v142\nv_mfma_f32_16x16x32_bf16 v[156:159], a[252:255], a[124:127], v[220:223]\nbuffer_load_dwordx4 v231, s[48:51], s79 offen lds\n"
"v_mfma_f32_16x16x32_bf16 v[156:159], a[248:251], a[108:111], v[156:159]\nv_add_f32 v236, v236, v130\nv_add_f32 v239, v239, v141\nv_mfma_f32_16x16x32_bf16 v[156:159], a[244:247], a[92:95], v[156:159]\n"
"v_exp_f32 v131, v131\nv_mfma_f32_16x16x32_bf16 v[156:159], a[240:243], a[76:79], v[156:159]\nv_exp_f32 v135, v135\nv_mfma_f32_16x16x32_bf16 v[156:159], a[236:239], a[60:63], v[156:159]\n"
"v_add_f32 v237, v237, v134\nv_add_f32 v238, v238, v138\nv_mfma_f32_16x16x32_bf16 v[156:159], a[232:235], a[44:47], v[156:159]\nv_exp_f32 v139, v139\n"
"v_mfma_f32_16x16x32_bf16 v[156:159], a[228:231], a[28:31], v[156:159]\nv_exp_f32 v143, v143\nv_mfma_f32_16x16x32_bf16 v[156:159], a[224:227], a[12:15], v[156:159]\nv_exp_f32 v144, v144\n"
"s_waitcnt vmcnt(17) lgkmcnt(0)\ns_barrier\nv_mfma_f32_16x16x32_bf16 v[0:3], a[128:131], v[160:163], v[0:3]\ns_add_u32 s80, s80, 0x7e80\n"
"ds_read_b128 a[192:195], v225 offset:32768\nv_add_f32 v239, v239, v142\nv_mfma_f32_16x16x32_bf16 v[0:3], a[132:135], v[192:195], v[0:3]\ns_add_u32 m0, s73, 0x8000\n"
"ds_read_b128 a[196:199], v226 offset:32768\nv_add_f32 v236, v236, v131\nv_mfma_f32_16x16x32_bf16 v[4:7], a[132:135], v[196:199], v[4:7]\nbuffer_load_dwordx4 v232, s[52:55], s80 offen lds\n"
"v_mfma_f32_16x16x32_bf16 v[4:7], a[128:131], v[164:167], v[4:7]\ns_add_u32 s80, s80, 0x80\nds_read_b128 a[200:203], v225 offset:40960\nv_add_f32 v237, v237, v135\n"
"v_mfma_f32_16x16x32_bf16 v[8:11], a[128:131], v[168:171], v[8:11]\ns_add_u32 m0, s73, 0xa000\nds_read_b128 a[204:207], v226 offset:40960\nv_add_f32 v238, v238, v139\n"
"v_mfma_f32_16x16x32_bf16 v[8:11], a[132:135], v[200:203], v[8:11]\nbuffer_load_dwordx4 v232, s[52:55], s80 offen lds\nv_mfma_f32_16x16x32_bf16 v[12:15], a[132:135], v[204:207], v[12:15]\ns_add_u32 s80, s80, 0x4080\n"
"ds_read_b128 a[208:211], v225 offset:49152\nv_add_f32 v239, v239, v143\nv_mfma_f32_16x16x32_bf16 v[12:15], a[128:131], v[172:175], v[12:15]\ns_add_u32 m0, s73, 0xc000\n"
"ds_read_b128 a[212:215], v226 offset:49152\nv_cvt_pk_bf16_f32 v177, v130, v131\nv_mfma_f32_16x16x32_bf16 v[16:19], a[136:139], v[160:163], v[16:19]\nbuffer_load_dwordx4 v232, s[52:55], s80 offen lds\n"
"v_mfma_f32_16x16x32_bf16 v[16:19], a[140:143], v[192:195], v[16:19]\ns_add_u32 s80, s80, 0x80\nds_read_b128 a[216:219], v225 offset:57344\nv_cvt_pk_bf16_f32 v181, v134, v135\n"
"v_mfma_f32_16x16x32_bf16 v[20:23], a[140:143], v[196:199], v[20:23]\ns_add_u32 m0, s73, 0xe000\nds_read_b128 a[220:223], v226 offset:57344\nv_add_f32 v236, v236, v144\n"
"v_mfma_f32_16x16x32_bf16 v[20:23], a[136:139], v[164:167], v[20:23]\nbuffer_load_dwordx4 v232, s[52:55], s80 offen lds\nv_mfma_f32_16x16x32_bf16 v[24:27], a[136:139], v[168:171], v[24:27]\ns_add_u32 s80, s80, 0xfffffe80\n"
"ds_read_b128 a[224:227], v225 offset:34816\nv_cvt_pk_bf16_f32 v185, v138, v139\nv_mfma_f32_16x16x32_bf16 v[24:27], a[140:143], v[200:203], v[24:27]\ns_add_u32 m0, s73, 0x9000\n"
"ds_read_b128 a[228:231], v226 offset:34816\nv_cvt_pk_bf16_f32 v188, v140, v141\nv_mfma_f32_16x16x32_bf16 v[28:31], a[140:143], v[204:207], v[28:31]\nbuffer_load_dwordx4 v232, s[52:55], s80 offen lds\n"
"v_mfma_f32_16x16x32_bf16 v[28:31], a[136:139], v[172:175], v[28:31]\ns_add_u32 s80, s80, 0x80\nds_read_b128 a[232:235], v225 offset:43008\nv_cvt_pk_bf16_f32 v189, v142, v143\n"
"v_mfma_f32_16x16x32_bf16 v[32:35], a[144:147], v[160:163], v[32:35]\ns_add_u32 m0, s73, 0xb000\nds_read_b128 a[236:239], v226 offset:43008\nv_cvt_f32_i32 v253, v241\n"
"v_mfma_f32_16x16x32_bf16 v[32:35], a[148:151], v[192:195], v[32:35]\nbuffer_load_dwordx4 v232, s[52:55], s80 offen lds\nv_mfma_f32_16x16x32_bf16 v[36:39], a[148:151], v[196:199], v[36:39]\ns_add_u32 s80, s80, 0xffffc080\n"
"v_exp_f32 v145, v145\nv_mfma_f32_16x16x32_bf16 v[36:39], a[144:147], v[164:167], v[36:39]\ns_add_u32 m0, s73, 0xd000\nv_exp_f32 v146, v146\n"
"v_mfma_f32_16x16x32_bf16 v[40:43], a[144:147], v[168:171], v[40:43]\nbuffer_load_dwordx4 v232, s[52:55], s80 offen lds\nv_mfma_f32_16x16x32_bf16 v[40:43], a[148:151], v[200:203], v[40:43]\ns_add_u32 s80, s80, 0x80\n"
"v_exp_f32 v147, v147\nv_mfma_f32_16x16x32_bf16 v[44:47], a[148:151], v[204:207], v[44:47]\ns_add_u32 m0, s73, 0xf000\nds_read_b128 a[240:243], v225 offset:51200\n"
"v_cvt_pk_bf16_f32 v178, v144, v145\nv_mfma_f32_16x16x32_bf16 v[44:47], a[144:147], v[172:175], v[44:47]\nbuffer_load_dwordx4 v232, s[52:55], s80 offen lds\nv_mfma_f32_16x16x32_bf16 v[48:51], a[152:155], v[160:163], v[48:51]\n"
"v_exp_f32 v148, v148\nv_mfma_f32_16x16x32_bf16 v[48:51], a[156:159], v[192:195], v[48:51]\nv_exp_f32 v149, v149\nv_mfma_f32_16x16x32_bf16 v[52:55], a[156:159], v[196:199], v[52:55]\n"
"v_exp_f32 v150, v150\nv_mfma_f32_16x16x32_bf16 v[52:55], a[152:155], v[164:167], v[52:55]\nv_exp_f32 v151, v151\nv_mfma_f32_16x16x32_bf16 v[56:59], a[152:155], v[168:171], v[56:59]\n"
"ds_read_b128 a[244:247], v226 offset:51200\nv_cvt_pk_bf16_f32 v179, v146, v147\nv_mfma_f32_16x16x32_bf16 v[56:59], a[156:159], v[200:203], v[56:59]\nds_read_b128 a[248:251], v225 offset:59392\n"
"v_cvt_pk_bf16_f32 v182, v148, v149\nv_mfma_f32_16x16x32_bf16 v[60:63], a[156:159], v[204:207], v[60:63]\nv_exp_f32 v152, v152\nv_mfma_f32_16x16x32_bf16 v[60:63], a[152:155], v[172:175], v[60:63]\n"
"v_exp_f32 v153, v153\nv_mfma_f32_16x16x32_bf16 v[64:67], a[160:163], v[160:163], v[64:67]\nv_exp_f32 v154, v154\nv_mfma_f32_16x16x32_bf16 v[64:67], a[164:167], v[192:195], v[64:67]\n"
"v_exp_f32 v155, v155\nv_mfma_f32_16x16x32_bf16 v[68:71], a[164:167], v[196:199], v[68:71]\nds_read_b128 a[252:255], v226 offset:59392\nv_cvt_pk_bf16_f32 v183, v150, v151\n"
"v_mfma_f32_16x16x32_bf16 v[68:71], a[160:163], v[164:167], v[68:71]\nv_exp_f32 v156, v156\nv_mfma_f32_16x16x32_bf16 v[72:75], a[160:163], v[168:171], v[72:75]\nv_exp_f32 v157, v157\n"
"v_mfma_f32_16x16x32_bf16 v[72:75], a[164:167], v[200:203], v[72:75]\nv_exp_f32 v158, v158\nv_mfma_f32_16x16x32_bf16 v[76:79], a[164:167], v[204:207], v[76:79]\nv_exp_f32 v159, v159\n"
"v_mfma_f32_16x16x32_bf16 v[76:79], a[160:163], v[172:175], v[76:79]\nv_add_f32 v237, v237, v148\nv_cvt_pk_bf16_f32 v186, v152, v153\nv_mfma_f32_16x16x32_bf16 v[80:83], a[168:171], v[160:163], v[80:83]\n"
"v_cvt_pk_bf16_f32 v187, v154, v155\nv_add_f32 v238, v238, v152\nv_mfma_f32_16x16x32_bf16 v[80:83], a[172:175], v[192:195], v[80:83]\nv_add_f32 v239, v239, v156\n"
"v_cvt_pk_bf16_f32 v190, v156, v157\nv_mfma_f32_16x16x32_bf16 v[84:87], a[172:175], v[196:199], v[84:87]\nv_cvt_pk_bf16_f32 v191, v158, v159\nv_add_f32 v236, v236, v145\n"
"v_mfma_f32_16x16x32_bf16 v[84:87], a[168:171], v[164:167], v[84:87]\nv_add_f32 v237, v237, v149\nv_add_f32 v238, v238, v153\nv_mfma_f32_16x16x32_bf16 v[88:91], a[168:171], v[168:171], v[88:91]\n"
"ds_write_b128 v233, v[176:179] offset:0\nv_mfma_f32_16x16x32_bf16 v[88:91], a[172:175], v[200:203], v[88:91]\nv_add_f32 v239, v239, v157\nv_max_i32 v242, 0xffffff88, v242\n"
"v_mfma_f32_16x16x32_bf16 v[92:95], a[172:175], v[204:207], v[92:95]\nv_max_i32 v243, 0xffffff88, v243\nv_add_f32 v236, v236, v146\nv_mfma_f32_16x16x32_bf16 v[92:95], a[168:171], v[172:175], v[92:95]\n"
"v_add_f32 v237, v237, v150\nv_add_f32 v208, v208, v244\nv_mfma_f32_16x16x32_bf16 v[96:99], a[176:179], v[160:163], v[96:99]\nds_write_b128 v233, v[180:183] offset:1024\n"
"v_mfma_f32_16x16x32_bf16 v[96:99], a[180:183], v[192:195], v[96:99]\nv_add_f32 v209, v209, v244\nv_add_f32 v210, v210, v244\nv_mfma_f32_16x16x32_bf16 v[100:103], a[180:183], v[196:199], v[100:103]\n"
"v_add_f32 v211, v211, v244\nv_add_f32 v212, v212, v253\nv_mfma_f32_16x16x32_bf16 v[100:103], a[176:179], v[164:167], v[100:103]\nv_add_f32 v213, v213, v253\n"
"v_add_f32 v214, v214, v253\nv_mfma_f32_16x16x32_bf16 v[104:107], a[176:179], v[168:171], v[104:107]\nds_write_b128 v233, v[184:187] offset:2048\nv_mfma_f32_16x16x32_bf16 v[104:107], a[180:183], v[200:203], v[104:107]\n"
"v_add_f32 v215, v215, v253\nv_add_f32 v238, v238, v154\nv_mfma_f32_16x16x32_bf16 v[108:111], a[180:183], v[204:207], v[108:111]\nv_add_f32 v239, v239, v158\n"
"v_cvt_f32_i32 v244, v242\nv_mfma_f32_16x16x32_bf16 v[108:111], a[176:179], v[172:175], v[108:111]\nv_cvt_f32_i32 v253, v243\nv_add_f32 v236, v236, v147\n"
"v_mfma_f32_16x16x32_bf16 v[112:115], a[184:187], v[160:163], v[112:115]\nds_write_b128 v233, v[188:191] offset:3072\nv_mfma_f32_16x16x32_bf16 v[112:115], a[188:191], v[192:195], v[112:115]\nv_add_f32 v237, v237, v151\n"
"v_add_f32 v238, v238, v155\nv_mfma_f32_16x16x32_bf16 v[116:119], a[188:191], v[196:199], v[116:119]\nv_add_f32 v239, v239, v159\nv_add_f32 v216, v216, v244\n"
"v_mfma_f32_16x16x32_bf16 v[116:119], a[184:187], v[164:167], v[116:119]\nv_add_f32 v217, v217, v244\nv_add_f32 v218, v218, v244\nv_mfma_f32_16x16x32_bf16 v[120:123], a[184:187], v[168:171], v[120:123]\n"
"v_add_f32 v219, v219, v244\nv_add_f32 v220, v220, v253\nv_mfma_f32_16x16x32_bf16 v[120:123], a[188:191], v[200:203], v[120:123]\nv_add_f32 v221, v221, v253\n"
"v_add_f32 v222, v222, v253\nv_mfma_f32_16x16x32_bf16 v[124:127], a[188:191], v[204:207], v[124:127]\nv_add_f32 v223, v223, v253\nv_mfma_f32_16x16x32_bf16 v[124:127], a[184:187], v[172:175], v[124:127]\n"
"s_waitcnt vmcnt(17) lgkmcnt(0)\ns_barrier\nv_mfma_f32_16x16x32_bf16 v[128:131], a[192:195], a[0:3], v[208:211]\ns_add_u32 s79, s79, 0x3e80\n"
"v_ldexp_f32 v236, v236, 32\nds_read_b128 v[192:195], v234 offset:0\nv_mfma_f32_16x16x32_bf16 v[128:131], a[196:199], a[16:19], v[128:131]\ns_add_u32 m0, s73, 0x18000\n"
"v_ldexp_f32 v237, v237, 32\nds_read_b128 v[196:199], v234 offset:1024\nv_mfma_f32_16x16x32_bf16 v[128:131], a[200:203], a[32:35], v[128:131]\nbuffer_load_dwordx4 v231, s[48:51], s79 offen lds\n"
"v_mfma_f32_16x16x32_bf16 v[128:131], a[204:207], a[48:51], v[128:131]\nv_ldexp_f32 v238, v238, 32\nds_read_b128 v[200:203], v234 offset:2048\nv_mfma_f32_16x16x32_bf16 v[128:131], a[208:211], a[64:67], v[128:131]\n"
"v_ldexp_f32 v239, v239, 32\nds_read_b128 v[204:207], v234 offset:3072\nv_mfma_f32_16x16x32_bf16 v[128:131], a[212:215], a[80:83], v[128:131]\nv_mul_f32 v236, 0x2f800000, v236\n"
"v_mul_f32 v237, 0x2f800000, v237\nv_mfma_f32_16x16x32_bf16 v[128:131], a[216:219], a[96:99], v[128:131]\nv_mul_f32 v238, 0x2f800000, v238\nv_mul_f32 v239, 0x2f800000, v239\n"
"v_mfma_f32_16x16x32_bf16 v[128:131], a[220:223], a[112:115], v[128:131]\ns_add_u32 s79, s79, 0x80\nv_ldexp_f32 v236, v236, v240\nv_ldexp_f32 v237, v237, v241\n"
"v_mfma_f32_16x16x32_bf16 v[132:135], a[220:223], a[116:119], v[212:215]\ns_add_u32 m0, s73, 0x1a000\nv_ldexp_f32 v238, v238, v242\nv_ldexp_f32 v239, v239, v243\n"
"v_mfma_f32_16x16x32_bf16 v[132:135], a[216:219], a[100:103], v[132:135]\nbuffer_load_dwordx4 v231, s[48:51], s79 offen lds\nv_mfma_f32_16x16x32_bf16 v[132:135], a[212:215], a[84:87], v[132:135]\nds_read_b64_tr_b16 a[128:129], v227 offset:0\n"
"v_mfma_f32_16x16x32_bf16 v[132:135], a[208:211], a[68:71], v[132:135]\nv_exp_f32 v128, v128\nv_mfma_f32_16x16x32_bf16 v[132:135], a[204:207], a[52:55], v[132:135]\nds_read_b64_tr_b16 a[130:131], v227 offset:2048\n"
"v_add_f32 v236, v236, v128\nv_mfma_f32_16x16x32_bf16 v[132:135], a[200:203], a[36:39], v[132:135]\nv_exp_f32 v129, v129\nv_mfma_f32_16x16x32_bf16 v[132:135], a[196:199], a[20:23], v[132:135]\n"
"ds_read_b64_tr_b16 a[132:133], v227 offset:4096\nv_cvt_pk_bf16_f32 v160, v128, v129\nv_mfma_f32_16x16x32_bf16 v[132:135], a[192:195], a[4:7], v[132:135]\ns_add_u32 s79, s79, 0x80\n"
"ds_read_b64_tr_b16 a[134:135], v227 offset:6144\nv_add_f32 v236, v236, v129\nv_mfma_f32_16x16x32_bf16 v[136:139], a[192:195], a[8:11], v[216:219]\ns_add_u32 m0, s73, 0x1c000\n"
"v_exp_f32 v130, v130\nv_mfma_f32_16x16x32_bf16 v[136:139], a[196:199], a[24:27], v[136:139]\nbuffer_load_dwordx4 v231, s[48:51], s79 offen lds\nv_mfma_f32_16x16x32_bf16 v[136:139], a[200:203], a[40:43], v[136:139]\n"
"ds_read_b64_tr_b16 a[136:137], v228 offset:0\nv_add_f32 v236, v236, v130\nv_mfma_f32_16x16x32_bf16 v[136:139], a[204:207], a[56:59], v[136:139]\nv_exp_f32 v131, v131\n"
"v_mfma_f32_16x16x32_bf16 v[136:139], a[208:211], a[72:75], v[136:139]\nds_read_b64_tr_b16 a[138:139], v228 offset:2048\nv_cvt_pk_bf16_f32 v161, v130, v131\nv_mfma_f32_16x16x32_bf16 v[136:139], a[212:215], a[88:91], v[136:139]\n"
"ds_read_b64_tr_b16 a[140:141], v228 offset:4096\nv_add_f32 v236, v236, v131\nv_mfma_f32_16x16x32_bf16 v[136:139], a[216:219], a[104:107], v[136:139]\nv_exp_f32 v132, v132\n"
"v_mfma_f32_16x16x32_bf16 v[136:139], a[220:223], a[120:123], v[136:139]\ns_add_u32 s79, s79, 0x80\nds_read_b64_tr_b16 a[142:143], v228 offset:6144\nv_add_f32 v237, v237, v132\n"
"v_mfma_f32_16x16x32_bf16 v[140:143], a[220:223], a[124:127], v[220:223]\ns_add_u32 m0, s73, 0x1e000\nv_exp_f32 v133, v133\nv_mfma_f32_16x16x32_bf16 v[140:143], a[216:219], a[108:111], v[140:143]\n"
"buffer_load_dwordx4 v231, s[48:51], s79 offen lds\nv_mfma_f32_16x16x32_bf16 v[140:143], a[212:215], a[92:95], v[140:143]\nds_read_b64_tr_b16 a[144:145], v229 offset:0\nv_cvt_pk_bf16_f32 v164, v132, v133\n"
"v_mfma_f32_16x16x32_bf16 v[140:143], a[208:211], a[76:79], v[140:143]\nds_read_b64_tr_b16 a[146:147], v229 offset:2048\nv_add_f32 v237, v237, v133\nv_mfma_f32_16x16x32_bf16 v[140:143], a[204:207], a[60:63], v[140:143]\n"
"v_exp_f32 v134, v134\nv_mfma_f32_16x16x32_bf16 v[140:143], a[200:203], a[44:47], v[140:143]\nds_read_b64_tr_b16 a[148:149], v229 offset:4096\nv_add_f32 v237, v237, v134\n"
"v_mfma_f32_16x16x32_bf16 v[140:143], a[196:199], a[28:31], v[140:143]\nv_exp_f32 v135, v135\nv_mfma_f32_16x16x32_bf16 v[140:143], a[192:195], a[12:15], v[140:143]\ns_add_u32 s79, s79, 0x3e80\n"
"ds_read_b64_tr_b16 a[150:151], v229 offset:6144\nv_cvt_pk_bf16_f32 v165, v134, v135\nv_mfma_f32_16x16x32_bf16 v[144:147], a[224:227], a[0:3], v[208:211]\ns_add_u32 m0, s73, 0x19000\n"
"ds_read_b64_tr_b16 a[152:153], v230 offset:0\nv_add_f32 v237, v237, v135\nv_mfma_f32_16x16x32_bf16 v[144:147], a[228:231], a[16:19], v[144:147]\nbuffer_load_dwordx4 v231, s[48:51], s79 offen lds\n"
"v_mfma_f32_16x16x32_bf16 v[144:147], a[232:235], a[32:35], v[144:147]\nds_read_b64_tr_b16 a[154:155], v230 offset:2048\nv_mfma_f32_16x16x32_bf16 v[144:147], a[236:239], a[48:51], v[144:147]\nds_read_b64_tr_b16 a[156:157], v230 offset:4096\n"
"v_mfma_f32_16x16x32_bf16 v[144:147], a[240:243], a[64:67], v[144:147]\nds_read_b64_tr_b16 a[158:159], v230 offset:6144\nv_mfma_f32_16x16x32_bf16 v[144:147], a[244:247], a[80:83], v[144:147]\nds_read_b64_tr_b16 a[160:161], v227 offset:8192\n"
"v_mfma_f32_16x16x32_bf16 v[144:147], a[248:251], a[96:99], v[144:147]\ns_add_u32 s79, s79, 0x80\nds_read_b64_tr_b16 a[162:163], v227 offset:10240\nv_mfma_f32_16x16x32_bf16 v[144:147], a[252:255], a[112:115], v[144:147]\n"
"s_add_u32 m0, s73, 0x1b000\nv_exp_f32 v136, v136\nv_mfma_f32_16x16x32_bf16 v[148:151], a[252:255], a[116:119], v[212:215]\nbuffer_load_dwordx4 v231, s[48:51], s79 offen lds\n"
"v_mfma_f32_16x16x32_bf16 v[148:151], a[248:251], a[100:103], v[148:151]\nds_read_b64_tr_b16 a[164:165], v227 offset:12288\nv_add_f32 v238, v238, v136\nv_mfma_f32_16x16x32_bf16 v[148:151], a[244:247], a[84:87], v[148:151]\n"
"ds_read_b64_tr_b16 a[166:167], v227 offset:14336\nv_mfma_f32_16x16x32_bf16 v[148:151], a[240:243], a[68:71], v[148:151]\nds_read_b64_tr_b16 a[168:169], v228 offset:8192\nv_mfma_f32_16x16x32_bf16 v[148:151], a[236:239], a[52:55], v[148:151]\n"
"ds_read_b64_tr_b16 a[170:171], v228 offset:10240\nv_mfma_f32_16x16x32_bf16 v[148:151], a[232:235], a[36:39], v[148:151]\nds_read_b64_tr_b16 a[172:173], v228 offset:12288\nv_mfma_f32_16x16x32_bf16 v[148:151], a[228:231], a[20:23], v[148:151]\n"
"s_add_u32 s79, s79, 0x80\nds_read_b64_tr_b16 a[174:175], v228 offset:14336\nv_mfma_f32_16x16x32_bf16 v[148:151], a[224:227], a[4:7], v[148:151]\ns_add_u32 m0, s73, 0x1d000\n"
"ds_read_b64_tr_b16 a[176:177], v229 offset:8192\nv_mfma_f32_16x16x32_bf16 v[152:155], a[224:227], a[8:11], v[216:219]\nbuffer_load_dwordx4 v231, s[48:51], s79 offen lds\nv_mfma_f32_16x16x32_bf16 v[152:155], a[228:231], a[24:27], v[152:155]\n"
"ds_read_b64_tr_b16 a[178:179], v229 offset:10240\nv_mfma_f32_16x16x32_bf16 v[152:155], a[232:235], a[40:43], v[152:155]\nds_read_b64_tr_b16 a[180:181], v229 offset:12288\nv_mfma_f32_16x16x32_bf16 v[152:155], a[236:239], a[56:59], v[152:155]\n"
"ds_read_b64_tr_b16 a[182:183], v229 offset:14336\nv_mfma_f32_16x16x32_bf16 v[152:155], a[240:243], a[72:75], v[152:155]\nds_read_b64_tr_b16 a[184:185], v230 offset:8192\nv_mfma_f32_16x16x32_bf16 v[152:155], a[244:247], a[88:91], v[152:155]\n"
"ds_read_b64_tr_b16 a[186:187], v230 offset:10240\nv_mfma_f32_16x16x32_bf16 v[152:155], a[248:251], a[104:107], v[152:155]\ns_add_u32 s79, s79, 0x80\nds_read_b64_tr_b16 a[188:189], v230 offset:12288\n"
"v_mfma_f32_16x16x32_bf16 v[152:155], a[252:255], a[120:123], v[152:155]\ns_add_u32 m0, s73, 0x1f000\nds_read_b64_tr_b16 a[190:191], v230 offset:14336\nv_mfma_f32_16x16x32_bf16 v[156:159], a[252:255], a[124:127], v[220:223]\n"
"buffer_load_dwordx4 v231, s[48:51], s79 offen lds\nv_mfma_f32_16x16x32_bf16 v[156:159], a[248:251], a[108:111], v[156:159]\nv_exp_f32 v144, v144\nv_mfma_f32_16x16x32_bf16 v[156:159], a[244:247], a[92:95], v[156:159]\n"
"v_exp_f32 v145, v145\nv_mfma_f32_16x16x32_bf16 v[156:159], a[240:243], a[76:79], v[156:159]\nv_exp_f32 v146, v146\nv_mfma_f32_16x16x32_bf16 v[156:159], a[236:239], a[60:63], v[156:159]\n"
"v_exp_f32 v147, v147\nv_mfma_f32_16x16x32_bf16 v[156:159], a[232:235], a[44:47], v[156:159]\nv_exp_f32 v148, v148\nv_mfma_f32_16x16x32_bf16 v[156:159], a[228:231], a[28:31], v[156:159]\n"
"v_exp_f32 v149, v149\nv_mfma_f32_16x16x32_bf16 v[156:159], a[224:227], a[12:15], v[156:159]\nv_exp_f32 v150, v150\ns_waitcnt vmcnt(16) lgkmcnt(0)\n"
"s_barrier\nv_mfma_f32_16x16x32_bf16 v[0:3], a[128:131], v[176:179], v[0:3]\ns_add_u32 s80, s80, 0x7e80\nds_read_b128 a[192:195], v225 offset:0\n"
"v_cvt_pk_bf16_f32 v162, v144, v145\nv_mfma_f32_16x16x32_bf16 v[0:3], a[132:135], v[192:195], v[0:3]\ns_add_u32 m0, s73, 0x0\nds_read_b128 a[196:199], v226 offset:0\n"
"v_cvt_pk_bf16_f32 v163, v146, v147\nv_mfma_f32_16x16x32_bf16 v[4:7], a[132:135], v[196:199], v[4:7]\nbuffer_load_dwordx4 v232, s[52:55], s80 offen lds\nv_mfma_f32_16x16x32_bf16 v[4:7], a[128:131], v[180:183], v[4:7]\n"
"s_add_u32 s80, s80, 0x80\nds_read_b128 a[200:203], v225 offset:8192\nv_cvt_pk_bf16_f32 v166, v148, v149\nv_mfma_f32_16x16x32_bf16 v[8:11], a[128:131], v[184:187], v[8:11]\n"
"s_add_u32 m0, s73, 0x2000\nds_read_b128 a[204:207], v226 offset:8192\nv_add_f32 v236, v236, v144\nv_mfma_f32_16x16x32_bf16 v[8:11], a[132:135], v[200:203], v[8:11]\n"
"buffer_load_dwordx4 v232, s[52:55], s80 offen lds\nv_mfma_f32_16x16x32_bf16 v[12:15], a[132:135], v[204:207], v[12:15]\ns_add_u32 s80, s80, 0x4080\nds_read_b128 a[208:211], v225 offset:16384\n"
"v_add_f32 v236, v236, v145\nv_mfma_f32_16x16x32_bf16 v[12:15], a[128:131], v[188:191], v[12:15]\ns_add_u32 m0, s73, 0x4000\nds_read_b128 a[212:215], v226 offset:16384\n"
"v_add_f32 v237, v237, v148\nv_mfma_f32_16x16x32_bf16 v[16:19], a[136:139], v[176:179], v[16:19]\nbuffer_load_dwordx4 v232, s[52:55], s80 offen lds\nv_mfma_f32_16x16x32_bf16 v[16:19], a[140:143], v[192:195], v[16:19]\n"
"s_add_u32 s80, s80, 0x80\nds_read_b128 a[216:219], v225 offset:24576\nv_add_f32 v237, v237, v149\nv_mfma_f32_16x16x32_bf16 v[20:23], a[140:143], v[196:199], v[20:23]\n"
"s_add_u32 m0, s73, 0x6000\nds_read_b128 a[220:223], v226 offset:24576\nv_add_f32 v236, v236, v146\nv_mfma_f32_16x16x32_bf16 v[20:23], a[136:139], v[180:183], v[20:23]\n"
"buffer_load_dwordx4 v232, s[52:55], s80 offen lds\nv_mfma_f32_16x16x32_bf16 v[24:27], a[136:139], v[184:187], v[24:27]\ns_add_u32 s80, s80, 0xfffffe80\nds_read_b128 a[224:227], v225 offset:2048\n"
"v_add_f32 v237, v237, v150\nv_mfma_f32_16x16x32_bf16 v[24:27], a[140:143], v[200:203], v[24:27]\ns_add_u32 m0, s73, 0x1000\nds_read_b128 a[228:231], v226 offset:2048\n"
"v_add_f32 v236, v236, v147\nv_mfma_f32_16x16x32_bf16 v[28:31], a[140:143], v[204:207], v[28:31]\nbuffer_load_dwordx4 v232, s[52:55], s80 offen lds\nv_mfma_f32_16x16x32_bf16 v[28:31], a[136:139], v[188:191], v[28:31]\n"
"s_add_u32 s80, s80, 0x80\nds_read_b128 a[232:235], v225 offset:10240\nv_ldexp_f32 v0, v0, v240\nv_mfma_f32_16x16x32_bf16 v[32:35], a[144:147], v[176:179], v[32:35]\n"
"s_add_u32 m0, s73, 0x3000\nds_read_b128 a[236:239], v226 offset:10240\nv_ldexp_f32 v1, v1, v240\nv_mfma_f32_16x16x32_bf16 v[32:35], a[148:151], v[192:195], v[32:35]\n"
"buffer_load_dwordx4 v232, s[52:55], s80 offen lds\nv_mfma_f32_16x16x32_bf16 v[36:39], a[148:151], v[196:199], v[36:39]\ns_add_u32 s80, s80, 0xffffc080\nds_read_b128 a[240:243], v225 offset:18432\n"
"v_ldexp_f32 v2, v2, v240\nv_mfma_f32_16x16x32_bf16 v[36:39], a[144:147], v[180:183], v[36:39]\ns_add_u32 m0, s73, 0x5000\nds_read_b128 a[244:247], v226 offset:18432\n"
"v_ldexp_f32 v3, v3, v240\nv_mfma_f32_16x16x32_bf16 v[40:43], a[144:147], v[184:187], v[40:43]\nbuffer_load_dwordx4 v232, s[52:55], s80 offen lds\nv_mfma_f32_16x16x32_bf16 v[40:43], a[148:151], v[200:203], v[40:43]\n"
"s_add_u32 s80, s80, 0x80\nds_read_b128 a[248:251], v225 offset:26624\nv_ldexp_f32 v4, v4, v241\nv_mfma_f32_16x16x32_bf16 v[44:47], a[148:151], v[204:207], v[44:47]\n"
"s_add_u32 m0, s73, 0x7000\nv_exp_f32 v151, v151\nv_mfma_f32_16x16x32_bf16 v[44:47], a[144:147], v[188:191], v[44:47]\nbuffer_load_dwordx4 v232, s[52:55], s80 offen lds\n"
"v_mfma_f32_16x16x32_bf16 v[48:51], a[152:155], v[176:179], v[48:51]\nds_read_b128 a[252:255], v226 offset:26624\nv_cvt_pk_bf16_f32 v167, v150, v151\nv_mfma_f32_16x16x32_bf16 v[48:51], a[156:159], v[192:195], v[48:51]\n"
"v_exp_f32 v137, v137\nv_mfma_f32_16x16x32_bf16 v[52:55], a[156:159], v[196:199], v[52:55]\nv_exp_f32 v138, v138\nv_mfma_f32_16x16x32_bf16 v[52:55], a[152:155], v[180:183], v[52:55]\n"
"v_exp_f32 v139, v139\nv_mfma_f32_16x16x32_bf16 v[56:59], a[152:155], v[184:187], v[56:59]\nv_exp_f32 v152, v152\nv_mfma_f32_16x16x32_bf16 v[56:59], a[156:159], v[200:203], v[56:59]\n"
"v_exp_f32 v153, v153\nv_mfma_f32_16x16x32_bf16 v[60:63], a[156:159], v[204:207], v[60:63]\nv_exp_f32 v154, v154\nv_mfma_f32_16x16x32_bf16 v[60:63], a[152:155], v[188:191], v[60:63]\n"
"v_exp_f32 v155, v155\nv_mfma_f32_16x16x32_bf16 v[64:67], a[160:163], v[176:179], v[64:67]\nv_cvt_pk_bf16_f32 v168, v136, v137\nv_cvt_pk_bf16_f32 v169, v138, v139\n"
"v_mfma_f32_16x16x32_bf16 v[64:67], a[164:167], v[192:195], v[64:67]\nv_cvt_pk_bf16_f32 v170, v152, v153\nv_cvt_pk_bf16_f32 v171, v154, v155\nv_mfma_f32_16x16x32_bf16 v[68:71], a[164:167], v[196:199], v[68:71]\n"
"v_exp_f32 v140, v140\nv_mfma_f32_16x16x32_bf16 v[68:71], a[160:163], v[180:183], v[68:71]\nv_exp_f32 v141, v141\nv_mfma_f32_16x16x32_bf16 v[72:75], a[160:163], v[184:187], v[72:75]\n"
"v_exp_f32 v142, v142\nv_mfma_f32_16x16x32_bf16 v[72:75], a[164:167], v[200:203], v[72:75]\nv_exp_f32 v143, v143\nv_mfma_f32_16x16x32_bf16 v[76:79], a[164:167], v[204:207], v[76:79]\n"
"v_exp_f32 v156, v156\nv_mfma_f32_16x16x32_bf16 v[76:79], a[160:163], v[188:191], v[76:79]\nv_exp_f32 v157, v157\nv_mfma_f32_16x16x32_bf16 v[80:83], a[168:171], v[176:179], v[80:83]\n"
"v_exp_f32 v158, v158\nv_mfma_f32_16x16x32_bf16 v[80:83], a[172:175], v[192:195], v[80:83]\nv_exp_f32 v159, v159\nv_mfma_f32_16x16x32_bf16 v[84:87], a[172:175], v[196:199], v[84:87]\n"
"v_cvt_pk_bf16_f32 v172, v140, v141\nv_cvt_pk_bf16_f32 v173, v142, v143\nv_mfma_f32_16x16x32_bf16 v[84:87], a[168:171], v[180:183], v[84:87]\nv_cvt_pk_bf16_f32 v174, v156, v157\n"
"v_cvt_pk_bf16_f32 v175, v158, v159\nv_mfma_f32_16x16x32_bf16 v[88:91], a[168:171], v[184:187], v[88:91]\nds_write_b128 v233, v[160:163] offset:16384\nv_mfma_f32_16x16x32_bf16 v[88:91], a[172:175], v[200:203], v[88:91]\n"
"buffer_load_dword v248, v249, s[92:95], s91 offen offset:0\nv_mfma_f32_16x16x32_bf16 v[92:95], a[172:175], v[204:207], v[92:95]\ns_add_u32 s91, s91, 0x10000\nv_add_f32 v238, v238, v137\n"
"v_add_f32 v239, v239, v140\nv_mfma_f32_16x16x32_bf16 v[92:95], a[168:171], v[188:191], v[92:95]\nv_add_f32 v238, v238, v138\nv_add_f32 v239, v239, v141\n"
"v_mfma_f32_16x16x32_bf16 v[96:99], a[176:179], v[176:179], v[96:99]\nds_write_b128 v233, v[164:167] offset:17408\nv_mfma_f32_16x16x32_bf16 v[96:99], a[180:183], v[192:195], v[96:99]\nv_add_f32 v238, v238, v139\n"
"v_add_f32 v239, v239, v142\nv_mfma_f32_16x16x32_bf16 v[100:103], a[180:183], v[196:199], v[100:103]\nv_add_f32 v239, v239, v143\nv_add_f32 v238, v238, v152\n"
"v_mfma_f32_16x16x32_bf16 v[100:103], a[176:179], v[180:183], v[100:103]\nv_add_f32 v239, v239, v156\nv_add_f32 v238, v238, v153\nv_mfma_f32_16x16x32_bf16 v[104:107], a[176:179], v[184:187], v[104:107]\n"
"ds_write_b128 v233, v[168:171] offset:18432\nv_mfma_f32_16x16x32_bf16 v[104:107], a[180:183], v[200:203], v[104:107]\nv_add_f32 v239, v239, v157\nv_add_f32 v238, v238, v154\n"
"v_mfma_f32_16x16x32_bf16 v[108:111], a[180:183], v[204:207], v[108:111]\nv_add_f32 v239, v239, v158\nv_add_f32 v237, v237, v151\nv_mfma_f32_16x16x32_bf16 v[108:111], a[176:179], v[188:191], v[108:111]\n"
"v_add_f32 v238, v238, v155\nv_add_f32 v239, v239, v159\nv_mfma_f32_16x16x32_bf16 v[112:115], a[184:187], v[176:179], v[112:115]\nds_write_b128 v233, v[172:175] offset:19456\n"
"v_mfma_f32_16x16x32_bf16 v[112:115], a[188:191], v[192:195], v[112:115]\nv_ldexp_f32 v5, v5, v241\nv_ldexp_f32 v6, v6, v241\nv_mfma_f32_16x16x32_bf16 v[116:119], a[188:191], v[196:199], v[116:119]\n"
"v_ldexp_f32 v7, v7, v241\nv_ldexp_f32 v8, v8, v242\nv_mfma_f32_16x16x32_bf16 v[116:119], a[184:187], v[180:183], v[116:119]\nv_ldexp_f32 v9, v9, v242\n"
"v_ldexp_f32 v10, v10, v242\nv_mfma_f32_16x16x32_bf16 v[120:123], a[184:187], v[184:187], v[120:123]\nv_ldexp_f32 v11, v11, v242\nv_ldexp_f32 v12, v12, v243\n"
"v_mfma_f32_16x16x32_bf16 v[120:123], a[188:191], v[200:203], v[120:123]\nv_ldexp_f32 v13, v13, v243\nv_ldexp_f32 v14, v14, v243\nv_mfma_f32_16x16x32_bf16 v[124:127], a[188:191], v[204:207], v[124:127]\n"
"v_ldexp_f32 v15, v15, v243\nv_ldexp_f32 v16, v16, v240\nv_mfma_f32_16x16x32_bf16 v[124:127], a[184:187], v[188:191], v[124:127]\nv_ldexp_f32 v17, v17, v240\n"
"v_ldexp_f32 v18, v18, v240\ns_waitcnt vmcnt(17) lgkmcnt(0)\ns_barrier\nv_mfma_f32_16x16x32_bf16 v[128:131], a[192:195], a[0:3], v[208:211]\n"
"s_add_u32 s79, s79, 0x3e80\nds_read_b128 v[192:195], v234 offset:16384\nv_ldexp_f32 v19, v19, v240\nv_mfma_f32_16x16x32_bf16 v[128:131], a[196:199], a[16:19], v[128:131]\n"
"s_add_u32 m0, s73, 0x10000\nds_read_b128 v[196:199], v234 offset:17408\nv_ldexp_f32 v20, v20, v241\nv_mfma_f32_16x16x32_bf16 v[128:131], a[200:203], a[32:35], v[128:131]\n"
"buffer_load_dwordx4 v231, s[48:51], s79 offen lds\nv_mfma_f32_16x16x32_bf16 v[128:131], a[204:207], a[48:51], v[128:131]\nds_read_b128 v[200:203], v234 offset:18432\nv_ldexp_f32 v21, v21, v241\n"
"v_mfma_f32_16x16x32_bf16 v[128:131], a[208:211], a[64:67], v[128:131]\nds_read_b128 v[204:207], v234 offset:19456\nv_ldexp_f32 v22, v22, v241\nv_mfma_f32_16x16x32_bf16 v[128:131], a[212:215], a[80:83], v[128:131]\n"
"ds_read_b64_tr_b16 a[128:129], v227 offset:32768\nv_ldexp_f32 v23, v23, v241\nv_mfma_f32_16x16x32_bf16 v[128:131], a[216:219], a[96:99], v[128:131]\nds_read_b64_tr_b16 a[130:131], v227 offset:34816\n"
"v_ldexp_f32 v24, v24, v242\nv_mfma_f32_16x16x32_bf16 v[128:131], a[220:223], a[112:115], v[128:131]\ns_add_u32 s79, s79, 0x80\nds_read_b64_tr_b16 a[132:133], v227 offset:36864\n"
"v_ldexp_f32 v25, v25, v242\nv_mfma_f32_16x16x32_bf16 v[132:135], a[220:223], a[116:119], v[212:215]\ns_add_u32 m0, s73, 0x12000\nds_read_b64_tr_b16 a[134:135], v227 offset:38912\n"
"v_ldexp_f32 v26, v26, v242\nv_mfma_f32_16x16x32_bf16 v[132:135], a[216:219], a[100:103], v[132:135]\nbuffer_load_dwordx4 v231, s[48:51], s79 offen lds\nv_mfma_f32_16x16x32_bf16 v[132:135], a[212:215], a[84:87], v[132:135]\n"
"ds_read_b64_tr_b16 a[136:137], v228 offset:32768\nv_ldexp_f32 v27, v27, v242\nv_mfma_f32_16x16x32_bf16 v[132:135], a[208:211], a[68:71], v[132:135]\nds_read_b64_tr_b16 a[138:139], v228 offset:34816\n"
"v_ldexp_f32 v28, v28, v243\nv_mfma_f32_16x16x32_bf16 v[132:135], a[204:207], a[52:55], v[132:135]\nds_read_b64_tr_b16 a[140:141], v228 offset:36864\nv_ldexp_f32 v29, v29, v243\n"
"v_mfma_f32_16x16x32_bf16 v[132:135], a[200:203], a[36:39], v[132:135]\nds_read_b64_tr_b16 a[142:143], v228 offset:38912\nv_ldexp_f32 v30, v30, v243\nv_mfma_f32_16x16x32_bf16 v[132:135], a[196:199], a[20:23], v[132:135]\n"
"ds_read_b64_tr_b16 a[144:145], v229 offset:32768\nv_ldexp_f32 v31, v31, v243\nv_mfma_f32_16x16x32_bf16 v[132:135], a[192:195], a[4:7], v[132:135]\ns_add_u32 s79, s79, 0x80\n"
"ds_read_b64_tr_b16 a[146:147], v229 offset:34816\nv_ldexp_f32 v32, v32, v240\nv_mfma_f32_16x16x32_bf16 v[136:139], a[192:195], a[8:11], v[216:219]\ns_add_u32 m0, s73, 0x14000\n"
"ds_read_b64_tr_b16 a[148:149], v229 offset:36864\nv_ldexp_f32 v33, v33, v240\nv_mfma_f32_16x16x32_bf16 v[136:139], a[196:199], a[24:27], v[136:139]\nbuffer_load_dwordx4 v231, s[48:51], s79 offen lds\n"
"v_mfma_f32_16x16x32_bf16 v[136:139], a[200:203], a[40:43], v[136:139]\nds_read_b64_tr_b16 a[150:151], v229 offset:38912\nv_ldexp_f32 v34, v34, v240\nv_mfma_f32_16x16x32_bf16 v[136:139], a[204:207], a[56:59], v[136:139]\n"
"ds_read_b64_tr_b16 a[152:153], v230 offset:32768\nv_ldexp_f32 v35, v35, v240\nv_mfma_f32_16x16x32_bf16 v[136:139], a[208:211], a[72:75], v[136:139]\nds_read_b64_tr_b16 a[154:155], v230 offset:34816\n"
"v_ldexp_f32 v36, v36, v241\nv_mfma_f32_16x16x32_bf16 v[136:139], a[212:215], a[88:91], v[136:139]\nds_read_b64_tr_b16 a[156:157], v230 offset:36864\nv_ldexp_f32 v37, v37, v241\n"
"v_mfma_f32_16x16x32_bf16 v[136:139], a[216:219], a[104:107], v[136:139]\nds_read_b64_tr_b16 a[158:159], v230 offset:38912\nv_exp_f32 v128, v128\nv_mfma_f32_16x16x32_bf16 v[136:139], a[220:223], a[120:123], v[136:139]\n"
"s_add_u32 s79, s79, 0x80\nds_read_b64_tr_b16 a[160:161], v227 offset:40960\nv_add_f32 v236, v236, v128\nv_mfma_f32_16x16x32_bf16 v[140:143], a[220:223], a[124:127], v[220:223]\n"
"s_add_u32 m0, s73, 0x16000\nds_read_b64_tr_b16 a[162:163], v227 offset:43008\nv_exp_f32 v132, v132\nv_mfma_f32_16x16x32_bf16 v[140:143], a[216:219], a[108:111], v[140:143]\n"
"buffer_load_dwordx4 v231, s[48:51], s79 offen lds\nv_mfma_f32_16x16x32_bf16 v[140:143], a[212:215], a[92:95], v[140:143]\nds_read_b64_tr_b16 a[164:165], v227 offset:45056\nv_exp_f32 v129, v129\n"
"v_mfma_f32_16x16x32_bf16 v[140:143], a[208:211], a[76:79], v[140:143]\nds_read_b64_tr_b16 a[166:167], v227 offset:47104\nv_exp_f32 v136, v136\nv_mfma_f32_16x16x32_bf16 v[140:143], a[204:207], a[60:63], v[140:143]\n"
"ds_read_b64_tr_b16 a[168:169], v228 offset:40960\nv_exp_f32 v133, v133\nv_add_f32 v237, v237, v132\nv_mfma_f32_16x16x32_bf16 v[140:143], a[200:203], a[44:47], v[140:143]\n"
"ds_read_b64_tr_b16 a[170:171], v228 offset:43008\nv_exp_f32 v137, v137\nv_add_f32 v238, v238, v136\nv_mfma_f32_16x16x32_bf16 v[140:143], a[196:199], a[28:31], v[140:143]\n"
"ds_read_b64_tr_b16 a[172:173], v228 offset:45056\nv_add_f32 v236, v236, v129\nv_mfma_f32_16x16x32_bf16 v[140:143], a[192:195], a[12:15], v[140:143]\ns_add_u32 s79, s79, 0x3e80\n"
"ds_read_b64_tr_b16 a[174:175], v228 offset:47104\nv_add_f32 v237, v237, v133\nv_mfma_f32_16x16x32_bf16 v[144:147], a[224:227], a[0:3], v[208:211]\ns_add_u32 m0, s73, 0x11000\n"
"ds_read_b64_tr_b16 a[176:177], v229 offset:40960\nv_exp_f32 v130, v130\nv_mfma_f32_16x16x32_bf16 v[144:147], a[228:231], a[16:19], v[144:147]\nbuffer_load_dwordx4 v231, s[48:51], s79 offen lds\n"
"v_mfma_f32_16x16x32_bf16 v[144:147], a[232:235], a[32:35], v[144:147]\nds_read_b64_tr_b16 a[178:179], v229 offset:43008\nv_exp_f32 v134, v134\nv_mfma_f32_16x16x32_bf16 v[144:147], a[236:239], a[48:51], v[144:147]\n"
"ds_read_b64_tr_b16 a[180:181], v229 offset:45056\nv_exp_f32 v140, v140\nv_mfma_f32_16x16x32_bf16 v[144:147], a[240:243], a[64:67], v[144:147]\nds_read_b64_tr_b16 a[182:183], v229 offset:47104\n"
"v_exp_f32 v141, v141\nv_add_f32 v239, v239, v140\nv_mfma_f32_16x16x32_bf16 v[144:147], a[244:247], a[80:83], v[144:147]\nds_read_b64_tr_b16 a[184:185], v230 offset:40960\n"
"v_ldexp_f32 v38, v38, v241\nv_mfma_f32_16x16x32_bf16 v[144:147], a[248:251], a[96:99], v[144:147]\ns_add_u32 s79, s79, 0x80\nds_read_b64_tr_b16 a[186:187], v230 offset:43008\n"
"v_ldexp_f32 v39, v39, v241\nv_mfma_f32_16x16x32_bf16 v[144:147], a[252:255], a[112:115], v[144:147]\ns_add_u32 m0, s73, 0x13000\nds_read_b64_tr_b16 a[188:189], v230 offset:45056\n"
"v_add_f32 v238, v238, v137\nv_mfma_f32_16x16x32_bf16 v[148:151], a[252:255], a[116:119], v[212:215]\nbuffer_load_dwordx4 v231, s[48:51], s79 offen lds\nv_mfma_f32_16x16x32_bf16 v[148:151], a[248:251], a[100:103], v[148:151]\n"
"ds_read_b64_tr_b16 a[190:191], v230 offset:47104\nv_add_f32 v239, v239, v141\nv_mfma_f32_16x16x32_bf16 v[148:151], a[244:247], a[84:87], v[148:151]\nv_exp_f32 v138, v138\n"
"v_mfma_f32_16x16x32_bf16 v[148:151], a[240:243], a[68:71], v[148:151]\nv_ldexp_f32 v40, v40, v242\nv_ldexp_f32 v41, v41, v242\nv_mfma_f32_16x16x32_bf16 v[148:151], a[236:239], a[52:55], v[148:151]\n"
"v_exp_f32 v142, v142\nv_mfma_f32_16x16x32_bf16 v[148:151], a[232:235], a[36:39], v[148:151]\nv_ldexp_f32 v42, v42, v242\nv_ldexp_f32 v43, v43, v242\n"
"v_ldexp_f32 v44, v44, v243\nv_ldexp_f32 v45, v45, v243\nv_mfma_f32_16x16x32_bf16 v[148:151], a[228:231], a[20:23], v[148:151]\ns_add_u32 s79, s79, 0x80\n"
"v_ldexp_f32 v46, v46, v243\nv_ldexp_f32 v47, v47, v243\nv_mfma_f32_16x16x32_bf16 v[148:151], a[224:227], a[4:7], v[148:151]\ns_add_u32 m0, s73, 0x15000\n"
"v_ldexp_f32 v48, v48, v240\nv_ldexp_f32 v49, v49, v240\nv_mfma_f32_16x16x32_bf16 v[152:155], a[224:227], a[8:11], v[216:219]\nbuffer_load_dwordx4 v231, s[48:51], s79 offen lds\n"
"v_mfma_f32_16x16x32_bf16 v[152:155], a[228:231], a[24:27], v[152:155]\nv_ldexp_f32 v50, v50, v240\nv_ldexp_f32 v51, v51, v240\nv_exp_f32 v131, v131\n"
"v_mfma_f32_16x16x32_bf16 v[152:155], a[232:235], a[40:43], v[152:155]\nv_ldexp_f32 v52, v52, v241\nv_ldexp_f32 v53, v53, v241\nv_exp_f32 v135, v135\n"
"v_mfma_f32_16x16x32_bf16 v[152:155], a[236:239], a[56:59], v[152:155]\nv_ldexp_f32 v54, v54, v241\nv_ldexp_f32 v55, v55, v241\nv_exp_f32 v144, v144\n"
"v_mfma_f32_16x16x32_bf16 v[152:155], a[240:243], a[72:75], v[152:155]\nv_exp_f32 v145, v145\nv_exp_f32 v146, v146\nv_mfma_f32_16x16x32_bf16 v[152:155], a[244:247], a[88:91], v[152:155]\n"
"v_exp_f32 v147, v147\nv_exp_f32 v148, v148\nv_mfma_f32_16x16x32_bf16 v[152:155], a[248:251], a[104:107], v[152:155]\ns_add_u32 s79, s79, 0x80\n"
"v_exp_f32 v149, v149\nv_exp_f32 v150, v150\nv_mfma_f32_16x16x32_bf16 v[152:155], a[252:255], a[120:123], v[152:155]\ns_add_u32 m0, s73, 0x17000\n"
"v_ldexp_f32 v56, v56, v242\nv_ldexp_f32 v57, v57, v242\nv_exp_f32 v151, v151\nv_mfma_f32_16x16x32_bf16 v[156:159], a[252:255], a[124:127], v[220:223]\n"
"buffer_load_dwordx4 v231, s[48:51], s79 offen lds\nv_mfma_f32_16x16x32_bf16 v[156:159], a[248:251], a[108:111], v[156:159]\nv_ldexp_f32 v58, v58, v242\nv_ldexp_f32 v59, v59, v242\n"
"v_ldexp_f32 v60, v60, v243\nv_ldexp_f32 v61, v61, v243\nv_mfma_f32_16x16x32_bf16 v[156:159], a[244:247], a[92:95], v[156:159]\nv_ldexp_f32 v62, v62, v243\n"
"v_ldexp_f32 v63, v63, v243\nv_exp_f32 v139, v139\nv_mfma_f32_16x16x32_bf16 v[156:159], a[240:243], a[76:79], v[156:159]\nv_exp_f32 v152, v152\n"
"v_exp_f32 v153, v153\nv_mfma_f32_16x16x32_bf16 v[156:159], a[236:239], a[60:63], v[156:159]\nv_exp_f32 v154, v154\nv_exp_f32 v155, v155\n"
"v_mfma_f32_16x16x32_bf16 v[156:159], a[232:235], a[44:47], v[156:159]\nv_ldexp_f32 v64, v64, v240\nv_ldexp_f32 v65, v65, v240\nv_ldexp_f32 v66, v66, v240\n"
"v_ldexp_f32 v67, v67, v240\nv_mfma_f32_16x16x32_bf16 v[156:159], a[228:231], a[28:31], v[156:159]\nv_ldexp_f32 v68, v68, v241\nv_ldexp_f32 v69, v69, v241\n"
"v_ldexp_f32 v70, v70, v241\nv_ldexp_f32 v71, v71, v241\nv_mfma_f32_16x16x32_bf16 v[156:159], a[224:227], a[12:15], v[156:159]\nv_ldexp_f32 v72, v72, v242\n"
"v_ldexp_f32 v73, v73, v242\nv_ldexp_f32 v74, v74, v242\nv_ldexp_f32 v75, v75, v242\ns_waitcnt vmcnt(17) lgkmcnt(0)\n"
"s_barrier\nv_mfma_f32_16x16x32_bf16 v[0:3], a[128:131], v[160:163], v[0:3]\ns_add_u32 s80, s80, 0x7e80\nds_read_b128 a[192:195], v225 offset:32768\n"
"v_add_f32 v236, v236, v130\nv_mfma_f32_16x16x32_bf16 v[0:3], a[132:135], v[192:195], v[0:3]\ns_add_u32 m0, s73, 0x8000\nds_read_b128 a[196:199], v226 offset:32768\n"
"v_add_f32 v237, v237, v134\nv_mfma_f32_16x16x32_bf16 v[4:7], a[132:135], v[196:199], v[4:7]\nbuffer_load_dwordx4 v232, s[52:55], s80 offen lds\nv_mfma_f32_16x16x32_bf16 v[4:7], a[128:131], v[164:167], v[4:7]\n"
"s_add_u32 s80, s80, 0x80\nds_read_b128 a[200:203], v225 offset:40960\nv_ldexp_f32 v76, v76, v243\nv_mfma_f32_16x16x32_bf16 v[8:11], a[128:131], v[168:171], v[8:11]\n"
"s_add_u32 m0, s73, 0xa000\nds_read_b128 a[204:207], v226 offset:40960\nv_ldexp_f32 v77, v77, v243\nv_mfma_f32_16x16x32_bf16 v[8:11], a[132:135], v[200:203], v[8:11]\n"
"buffer_load_dwordx4 v232, s[52:55], s80 offen lds\nv_mfma_f32_16x16x32_bf16 v[12:15], a[132:135], v[204:207], v[12:15]\ns_add_u32 s80, s80, 0x4080\nds_read_b128 a[208:211], v225 offset:49152\n"
"v_ldexp_f32 v78, v78, v243\nv_mfma_f32_16x16x32_bf16 v[12:15], a[128:131], v[172:175], v[12:15]\ns_add_u32 m0, s73, 0xc000\nds_read_b128 a[212:215], v226 offset:49152\n"
"v_ldexp_f32 v79, v79, v243\nv_mfma_f32_16x16x32_bf16 v[16:19], a[136:139], v[160:163], v[16:19]\nbuffer_load_dwordx4 v232, s[52:55], s80 offen lds\nv_mfma_f32_16x16x32_bf16 v[16:19], a[140:143], v[192:195], v[16:19]\n"
"s_add_u32 s80, s80, 0x80\nds_read_b128 a[216:219], v225 offset:57344\nv_ldexp_f32 v80, v80, v240\nv_mfma_f32_16x16x32_bf16 v[20:23], a[140:143], v[196:199], v[20:23]\n"
"s_add_u32 m0, s73, 0xe000\nds_read_b128 a[220:223], v226 offset:57344\nv_ldexp_f32 v81, v81, v240\nv_mfma_f32_16x16x32_bf16 v[20:23], a[136:139], v[164:167], v[20:23]\n"
"buffer_load_dwordx4 v232, s[52:55], s80 offen lds\nv_mfma_f32_16x16x32_bf16 v[24:27], a[136:139], v[168:171], v[24:27]\ns_add_u32 s80, s80, 0xfffffe80\nds_read_b128 a[224:227], v225 offset:34816\n"
"v_ldexp_f32 v82, v82, v240\nv_mfma_f32_16x16x32_bf16 v[24:27], a[140:143], v[200:203], v[24:27]\ns_add_u32 m0, s73, 0x9000\nds_read_b128 a[228:231], v226 offset:34816\n"
"v_ldexp_f32 v83, v83, v240\nv_mfma_f32_16x16x32_bf16 v[28:31], a[140:143], v[204:207], v[28:31]\nbuffer_load_dwordx4 v232, s[52:55], s80 offen lds\nv_mfma_f32_16x16x32_bf16 v[28:31], a[136:139], v[172:175], v[28:31]\n"
"s_add_u32 s80, s80, 0x80\nds_read_b128 a[232:235], v225 offset:43008\nv_add_f32 v238, v238, v138\nv_mfma_f32_16x16x32_bf16 v[32:35], a[144:147], v[160:163], v[32:35]\n"
"s_add_u32 m0, s73, 0xb000\nds_read_b128 a[236:239], v226 offset:43008\nv_add_f32 v239, v239, v142\nv_mfma_f32_16x16x32_bf16 v[32:35], a[148:151], v[192:195], v[32:35]\n"
"buffer_load_dwordx4 v232, s[52:55], s80 offen lds\nv_mfma_f32_16x16x32_bf16 v[36:39], a[148:151], v[196:199], v[36:39]\ns_add_u32 s80, s80, 0xffffc080\nds_read_b128 a[240:243], v225 offset:51200\n"
"v_ldexp_f32 v84, v84, v241\nv_mfma_f32_16x16x32_bf16 v[36:39], a[144:147], v[164:167], v[36:39]\ns_add_u32 m0, s73, 0xd000\nds_read_b128 a[244:247], v226 offset:51200\n"
"v_ldexp_f32 v85, v85, v241\nv_mfma_f32_16x16x32_bf16 v[40:43], a[144:147], v[168:171], v[40:43]\nbuffer_load_dwordx4 v232, s[52:55], s80 offen lds\nv_mfma_f32_16x16x32_bf16 v[40:43], a[148:151], v[200:203], v[40:43]\n"
"s_add_u32 s80, s80, 0x80\nds_read_b128 a[248:251], v225 offset:59392\nv_ldexp_f32 v86, v86, v241\nv_mfma_f32_16x16x32_bf16 v[44:47], a[148:151], v[204:207], v[44:47]\n"
"s_add_u32 m0, s73, 0xf000\nv_exp_f32 v143, v143\nv_mfma_f32_16x16x32_bf16 v[44:47], a[144:147], v[172:175], v[44:47]\nbuffer_load_dwordx4 v232, s[52:55], s80 offen lds\n"
"v_mfma_f32_16x16x32_bf16 v[48:51], a[152:155], v[160:163], v[48:51]\nds_read_b128 a[252:255], v226 offset:59392\nv_ldexp_f32 v87, v87, v241\nv_mfma_f32_16x16x32_bf16 v[48:51], a[156:159], v[192:195], v[48:51]\n"
"v_exp_f32 v156, v156\nv_mfma_f32_16x16x32_bf16 v[52:55], a[156:159], v[196:199], v[52:55]\nv_exp_f32 v157, v157\nv_mfma_f32_16x16x32_bf16 v[52:55], a[152:155], v[164:167], v[52:55]\n"
"v_exp_f32 v158, v158\nv_mfma_f32_16x16x32_bf16 v[56:59], a[152:155], v[168:171], v[56:59]\nv_exp_f32 v159, v159\nv_mfma_f32_16x16x32_bf16 v[56:59], a[156:159], v[200:203], v[56:59]\n"
"v_cvt_pk_bf16_f32 v176, v128, v129\nv_cvt_pk_bf16_f32 v177, v130, v131\nv_mfma_f32_16x16x32_bf16 v[60:63], a[156:159], v[204:207], v[60:63]\nv_cvt_pk_bf16_f32 v180, v132, v133\n"
"v_cvt_pk_bf16_f32 v181, v134, v135\nv_mfma_f32_16x16x32_bf16 v[60:63], a[152:155], v[172:175], v[60:63]\nv_cvt_pk_bf16_f32 v178, v144, v145\nv_cvt_pk_bf16_f32 v179, v146, v147\n"
"v_mfma_f32_16x16x32_bf16 v[64:67], a[160:163], v[160:163], v[64:67]\nv_cvt_pk_bf16_f32 v182, v148, v149\nv_cvt_pk_bf16_f32 v183, v150, v151\nv_mfma_f32_16x16x32_bf16 v[64:67], a[164:167], v[192:195], v[64:67]\n"
"v_ldexp_f32 v88, v88, v242\nv_ldexp_f32 v89, v89, v242\nv_mfma_f32_16x16x32_bf16 v[68:71], a[164:167], v[196:199], v[68:71]\nv_ldexp_f32 v90, v90, v242\n"
"v_ldexp_f32 v91, v91, v242\nv_mfma_f32_16x16x32_bf16 v[68:71], a[160:163], v[164:167], v[68:71]\nv_cvt_pk_bf16_f32 v184, v136, v137\nv_cvt_pk_bf16_f32 v185, v138, v139\n"
"v_mfma_f32_16x16x32_bf16 v[72:75], a[160:163], v[168:171], v[72:75]\nv_cvt_pk_bf16_f32 v186, v152, v153\nv_cvt_pk_bf16_f32 v187, v154, v155\nv_mfma_f32_16x16x32_bf16 v[72:75], a[164:167], v[200:203], v[72:75]\n"
"v_ldexp_f32 v92, v92, v243\nv_ldexp_f32 v93, v93, v243\nv_mfma_f32_16x16x32_bf16 v[76:79], a[164:167], v[204:207], v[76:79]\nv_ldexp_f32 v94, v94, v243\n"
"v_ldexp_f32 v95, v95, v243\nv_mfma_f32_16x16x32_bf16 v[76:79], a[160:163], v[172:175], v[76:79]\nv_ldexp_f32 v96, v96, v240\nv_ldexp_f32 v97, v97, v240\n"
"v_mfma_f32_16x16x32_bf16 v[80:83], a[168:171], v[160:163], v[80:83]\nv_ldexp_f32 v98, v98, v240\nv_ldexp_f32 v99, v99, v240\nv_mfma_f32_16x16x32_bf16 v[80:83], a[172:175], v[192:195], v[80:83]\n"
"v_add_f32 v236, v236, v131\nv_add_f32 v237, v237, v135\nv_mfma_f32_16x16x32_bf16 v[84:87], a[172:175], v[196:199], v[84:87]\nv_ldexp_f32 v100, v100, v241\n"
"v_ldexp_f32 v101, v101, v241\nv_mfma_f32_16x16x32_bf16 v[84:87], a[168:171], v[164:167], v[84:87]\nv_ldexp_f32 v102, v102, v241\nv_ldexp_f32 v103, v103, v241\n"
"v_mfma_f32_16x16x32_bf16 v[88:91], a[168:171], v[168:171], v[88:91]\nv_cvt_pk_bf16_f32 v188, v140, v141\nv_cvt_pk_bf16_f32 v189, v142, v143\nv_mfma_f32_16x16x32_bf16 v[88:91], a[172:175], v[200:203], v[88:91]\n"
"v_cvt_pk_bf16_f32 v190, v156, v157\nv_cvt_pk_bf16_f32 v191, v158, v159\nv_mfma_f32_16x16x32_bf16 v[92:95], a[172:175], v[204:207], v[92:95]\nv_add_f32 v238, v238, v139\n"
"v_add_f32 v239, v239, v143\nv_mfma_f32_16x16x32_bf16 v[92:95], a[168:171], v[172:175], v[92:95]\nv_ldexp_f32 v104, v104, v242\nv_ldexp_f32 v105, v105, v242\n"
"v_mfma_f32_16x16x32_bf16 v[96:99], a[176:179], v[160:163], v[96:99]\nv_ldexp_f32 v106, v106, v242\nv_ldexp_f32 v107, v107, v242\nv_mfma_f32_16x16x32_bf16 v[96:99], a[180:183], v[192:195], v[96:99]\n"
"ds_write_b128 v233, v[176:179] offset:0\nv_mfma_f32_16x16x32_bf16 v[100:103], a[180:183], v[196:199], v[100:103]\nv_ldexp_f32 v108, v108, v243\nv_ldexp_f32 v109, v109, v243\n"
"v_mfma_f32_16x16x32_bf16 v[100:103], a[176:179], v[164:167], v[100:103]\nv_ldexp_f32 v110, v110, v243\nv_ldexp_f32 v111, v111, v243\nv_mfma_f32_16x16x32_bf16 v[104:107], a[176:179], v[168:171], v[104:107]\n"
"v_ldexp_f32 v112, v112, v240\nv_ldexp_f32 v113, v113, v240\nv_mfma_f32_16x16x32_bf16 v[104:107], a[180:183], v[200:203], v[104:107]\nds_write_b128 v233, v[180:183] offset:1024\n"
"v_mfma_f32_16x16x32_bf16 v[108:111], a[180:183], v[204:207], v[108:111]\nv_ldexp_f32 v114, v114, v240\nv_ldexp_f32 v115, v115, v240\nv_mfma_f32_16x16x32_bf16 v[108:111], a[176:179], v[172:175], v[108:111]\n"
"v_add_f32 v236, v236, v144\nv_add_f32 v237, v237, v148\nv_mfma_f32_16x16x32_bf16 v[112:115], a[184:187], v[160:163], v[112:115]\nv_add_f32 v238, v238, v152\n"
"v_add_f32 v239, v239, v156\nv_mfma_f32_16x16x32_bf16 v[112:115], a[188:191], v[192:195], v[112:115]\nds_write_b128 v233, v[184:187] offset:2048\nv_ldexp_f32 v116, v116, v241\n"
"v_ldexp_f32 v117, v117, v241\nv_ldexp_f32 v118, v118, v241\nv_ldexp_f32 v119, v119, v241\ns_nop 1\n"
"v_mfma_f32_16x16x32_bf16 v[116:119], a[188:191], v[196:199], v[116:119]\nv_ldexp_f32 v120, v120, v242\nv_ldexp_f32 v121, v121, v242\nv_mfma_f32_16x16x32_bf16 v[116:119], a[184:187], v[164:167], v[116:119]\n"
"v_ldexp_f32 v122, v122, v242\nv_ldexp_f32 v123, v123, v242\ns_nop 1\nv_mfma_f32_16x16x32_bf16 v[120:123], a[184:187], v[168:171], v[120:123]\n"
"v_add_f32 v236, v236, v145\nv_add_f32 v237, v237, v149\nv_mfma_f32_16x16x32_bf16 v[120:123], a[188:191], v[200:203], v[120:123]\nds_write_b128 v233, v[188:191] offset:3072\n"
"v_ldexp_f32 v124, v124, v243\nv_ldexp_f32 v125, v125, v243\nv_ldexp_f32 v126, v126, v243\nv_ldexp_f32 v127, v127, v243\n"
"s_nop 1\nv_mfma_f32_16x16x32_bf16 v[124:127], a[188:191], v[204:207], v[124:127]\nv_add_f32 v238, v238, v153\nv_add_f32 v239, v239, v157\n"
"v_mfma_f32_16x16x32_bf16 v[124:127], a[184:187], v[172:175], v[124:127]\nv_add_f32 v236, v236, v146\nv_add_f32 v237, v237, v150\nv_add_f32 v236, v236, v147\n"
"v_add_f32 v237, v237, v151\nv_add_f32 v238, v238, v154\nv_add_f32 v238, v238, v155\nv_add_f32 v239, v239, v158\n"
"v_add_f32 v239, v239, v159\ns_waitcnt vmcnt(17) lgkmcnt(0)\ns_barrier\ns_branch L_lp%=\n"
"L_h1%=:\nbuffer_load_dword v251, v246, s[4:7], 0 offen sc1\nbuffer_load_dword v252, v247, s[4:7], 0 offen offset:128 sc1\ns_branch L_h1r%=\n"
"L_h2%=:\ns_waitcnt vmcnt(0)\nv_cmp_ne_u32 vcc, s30, v251\nbuffer_store_dword v252, v250, s[4:7], s33 offen sc1\n"
"s_nop 3\ns_and_b32 vcc_lo, vcc_lo, s34\ns_cselect_b32 vcc_lo, 0, 1\ns_cmp_lg_u32 s31, 0\n"
"s_cselect_b32 s31, 2, vcc_lo\ns_cselect_b32 s33, 0x7f000000, s35\ns_branch L_h2r%=\nL_lp%=:\n"
"L_loop%=:\nv_mfma_f32_16x16x32_bf16 v[128:131], a[192:195], a[0:3], v[208:211]\ns_add_u32 s79, s79, 0x3e80\nds_read_b128 v[192:195], v234 offset:0\n"
"v_mfma_f32_16x16x32_bf16 v[128:131], a[196:199], a[16:19], v[128:131]\ns_add_u32 m0, s73, 0x18000\nds_read_b128 v[196:199], v234 offset:1024\nv_mfma_f32_16x16x32_bf16 v[128:131], a[200:203], a[32:35], v[128:131]\n"
"buffer_load_dwordx4 v231, s[48:51], s79 offen lds\nv_mfma_f32_16x16x32_bf16 v[128:131], a[204:207], a[48:51], v[128:131]\ns_cmp_lg_u32 s31, 2\ns_cbranch_scc1 L_h1%=\nL_h1r%=:\nds_read_b128 v[200:203], v234 offset:2048\n"
"v_mfma_f32_16x16x32_bf16 v[128:131], a[208:211], a[64:67], v[128:131]\nds_read_b128 v[204:207], v234 offset:3072\nv_mfma_f32_16x16x32_bf16 v[128:131], a[212:215], a[80:83], v[128:131]\nds_read_b64_tr_b16 a[128:129], v227 offset:0\n"
"v_mfma_f32_16x16x32_bf16 v[128:131], a[216:219], a[96:99], v[128:131]\nds_read_b64_tr_b16 a[130:131], v227 offset:2048\nv_mfma_f32_16x16x32_bf16 v[128:131], a[220:223], a[112:115], v[128:131]\ns_add_u32 s79, s79, 0x80\n"
"ds_read_b64_tr_b16 a[132:133], v227 offset:4096\nv_mfma_f32_16x16x32_bf16 v[132:135], a[220:223], a[116:119], v[212:215]\ns_add_u32 m0, s73, 0x1a000\nds_read_b64_tr_b16 a[134:135], v227 offset:6144\n"
"v_mfma_f32_16x16x32_bf16 v[132:135], a[216:219], a[100:103], v[132:135]\nbuffer_load_dwordx4 v231, s[48:51], s79 offen lds\nv_mfma_f32_16x16x32_bf16 v[132:135], a[212:215], a[84:87], v[132:135]\nds_read_b64_tr_b16 a[136:137], v228 offset:0\n"
"v_mfma_f32_16x16x32_bf16 v[132:135], a[208:211], a[68:71], v[132:135]\nv_exp_f32 v128, v128\nv_mfma_f32_16x16x32_bf16 v[132:135], a[204:207], a[52:55], v[132:135]\nds_read_b64_tr_b16 a[138:139], v228 offset:2048\n"
"v_add_f32 v236, v236, v128\nv_mfma_f32_16x16x32_bf16 v[132:135], a[200:203], a[36:39], v[132:135]\nv_exp_f32 v129, v129\nv_mfma_f32_16x16x32_bf16 v[132:135], a[196:199], a[20:23], v[132:135]\n"
"ds_read_b64_tr_b16 a[140:141], v228 offset:4096\nv_cvt_pk_bf16_f32 v160, v128, v129\nv_mfma_f32_16x16x32_bf16 v[132:135], a[192:195], a[4:7], v[132:135]\ns_add_u32 s79, s79, 0x80\n"
"ds_read_b64_tr_b16 a[142:143], v228 offset:6144\nv_add_f32 v236, v236, v129\nv_mfma_f32_16x16x32_bf16 v[136:139], a[192:195], a[8:11], v[216:219]\ns_add_u32 m0, s73, 0x1c000\n"
"v_exp_f32 v130, v130\nv_mfma_f32_16x16x32_bf16 v[136:139], a[196:199], a[24:27], v[136:139]\nbuffer_load_dwordx4 v231, s[48:51], s79 offen lds\nv_mfma_f32_16x16x32_bf16 v[136:139], a[200:203], a[40:43], v[136:139]\n"
"ds_read_b64_tr_b16 a[144:145], v229 offset:0\nv_add_f32 v236, v236, v130\nv_mfma_f32_16x16x32_bf16 v[136:139], a[204:207], a[56:59], v[136:139]\nv_exp_f32 v132, v132\n"
"v_mfma_f32_16x16x32_bf16 v[136:139], a[208:211], a[72:75], v[136:139]\nds_read_b64_tr_b16 a[146:147], v229 offset:2048\nv_add_f32 v237, v237, v132\nv_mfma_f32_16x16x32_bf16 v[136:139], a[212:215], a[88:91], v[136:139]\n"
"v_exp_f32 v131, v131\nv_mfma_f32_16x16x32_bf16 v[136:139], a[216:219], a[104:107], v[136:139]\nds_read_b64_tr_b16 a[148:149], v229 offset:4096\nv_cvt_pk_bf16_f32 v161, v130, v131\n"
"v_mfma_f32_16x16x32_bf16 v[136:139], a[220:223], a[120:123], v[136:139]\ns_add_u32 s79, s79, 0x80\nds_read_b64_tr_b16 a[150:151], v229 offset:6144\nv_add_f32 v236, v236, v131\n"
"v_mfma_f32_16x16x32_bf16 v[140:143], a[220:223], a[124:127], v[220:223]\ns_add_u32 m0, s73, 0x1e000\nv_exp_f32 v133, v133\nv_mfma_f32_16x16x32_bf16 v[140:143], a[216:219], a[108:111], v[140:143]\n"
"buffer_load_dwordx4 v231, s[48:51], s79 offen lds\nv_mfma_f32_16x16x32_bf16 v[140:143], a[212:215], a[92:95], v[140:143]\nds_read_b64_tr_b16 a[152:153], v230 offset:0\nv_cvt_pk_bf16_f32 v164, v132, v133\n"
"v_mfma_f32_16x16x32_bf16 v[140:143], a[208:211], a[76:79], v[140:143]\nds_read_b64_tr_b16 a[154:155], v230 offset:2048\nv_add_f32 v237, v237, v133\nv_mfma_f32_16x16x32_bf16 v[140:143], a[204:207], a[60:63], v[140:143]\n"
"v_exp_f32 v136, v136\nv_mfma_f32_16x16x32_bf16 v[140:143], a[200:203], a[44:47], v[140:143]\nds_read_b64_tr_b16 a[156:157], v230 offset:4096\nv_add_f32 v238, v238, v136\n"
"v_mfma_f32_16x16x32_bf16 v[140:143], a[196:199], a[28:31], v[140:143]\nv_exp_f32 v134, v134\nv_mfma_f32_16x16x32_bf16 v[140:143], a[192:195], a[12:15], v[140:143]\ns_add_u32 s79, s79, 0x3e80\n"
"ds_read_b64_tr_b16 a[158:159], v230 offset:6144\nv_add_f32 v237, v237, v134\nv_mfma_f32_16x16x32_bf16 v[144:147], a[224:227], a[0:3], v[208:211]\ns_add_u32 m0, s73, 0x19000\n"
"v_exp_f32 v135, v135\nv_mfma_f32_16x16x32_bf16 v[144:147], a[228:231], a[16:19], v[144:147]\nbuffer_load_dwordx4 v231, s[48:51], s79 offen lds\nv_mfma_f32_16x16x32_bf16 v[144:147], a[232:235], a[32:35], v[144:147]\n"
"ds_read_b64_tr_b16 a[160:161], v227 offset:8192\nv_cvt_pk_bf16_f32 v165, v134, v135\nv_mfma_f32_16x16x32_bf16 v[144:147], a[236:239], a[48:51], v[144:147]\nds_read_b64_tr_b16 a[162:163], v227 offset:10240\n"
"v_add_f32 v237, v237, v135\nv_mfma_f32_16x16x32_bf16 v[144:147], a[240:243], a[64:67], v[144:147]\nv_exp_f32 v140, v140\nv_mfma_f32_16x16x32_bf16 v[144:147], a[244:247], a[80:83], v[144:147]\n"
"ds_read_b64_tr_b16 a[164:165], v227 offset:12288\nv_add_f32 v239, v239, v140\nv_mfma_f32_16x16x32_bf16 v[144:147], a[248:251], a[96:99], v[144:147]\ns_add_u32 s79, s79, 0x80\n"
"v_exp_f32 v137, v137\nv_mfma_f32_16x16x32_bf16 v[144:147], a[252:255], a[112:115], v[144:147]\ns_add_u32 m0, s73, 0x1b000\nds_read_b64_tr_b16 a[166:167], v227 offset:14336\n"
"v_add_f32 v238, v238, v137\nv_mfma_f32_16x16x32_bf16 v[148:151], a[252:255], a[116:119], v[212:215]\nbuffer_load_dwordx4 v231, s[48:51], s79 offen lds\nv_mfma_f32_16x16x32_bf16 v[148:151], a[248:251], a[100:103], v[148:151]\n"
"ds_read_b64_tr_b16 a[168:169], v228 offset:8192\nv_cvt_pk_bf16_f32 v168, v136, v137\nv_mfma_f32_16x16x32_bf16 v[148:151], a[244:247], a[84:87], v[148:151]\nv_exp_f32 v141, v141\n"
"v_mfma_f32_16x16x32_bf16 v[148:151], a[240:243], a[68:71], v[148:151]\nds_read_b64_tr_b16 a[170:171], v228 offset:10240\nv_add_f32 v239, v239, v141\nv_mfma_f32_16x16x32_bf16 v[148:151], a[236:239], a[52:55], v[148:151]\n"
"ds_read_b64_tr_b16 a[172:173], v228 offset:12288\nv_cvt_pk_bf16_f32 v172, v140, v141\nv_mfma_f32_16x16x32_bf16 v[148:151], a[232:235], a[36:39], v[148:151]\nds_read_b64_tr_b16 a[174:175], v228 offset:14336\n"
"v_mfma_f32_16x16x32_bf16 v[148:151], a[228:231], a[20:23], v[148:151]\ns_add_u32 s79, s79, 0x80\nds_read_b64_tr_b16 a[176:177], v229 offset:8192\nv_mfma_f32_16x16x32_bf16 v[148:151], a[224:227], a[4:7], v[148:151]\n"
"s_add_u32 m0, s73, 0x1d000\nv_exp_f32 v144, v144\nv_mfma_f32_16x16x32_bf16 v[152:155], a[224:227], a[8:11], v[216:219]\nbuffer_load_dwordx4 v231, s[48:51], s79 offen lds\n"
"v_mfma_f32_16x16x32_bf16 v[152:155], a[228:231], a[24:27], v[152:155]\nds_read_b64_tr_b16 a[178:179], v229 offset:10240\nv_add_f32 v236, v236, v144\nv_mfma_f32_16x16x32_bf16 v[152:155], a[232:235], a[40:43], v[152:155]\n"
"ds_read_b64_tr_b16 a[180:181], v229 offset:12288\nv_mfma_f32_16x16x32_bf16 v[152:155], a[236:239], a[56:59], v[152:155]\nds_read_b64_tr_b16 a[182:183], v229 offset:14336\nv_mfma_f32_16x16x32_bf16 v[152:155], a[240:243], a[72:75], v[152:155]\n"
"ds_read_b64_tr_b16 a[184:185], v230 offset:8192\nv_mfma_f32_16x16x32_bf16 v[152:155], a[244:247], a[88:91], v[152:155]\nds_read_b64_tr_b16 a[186:187], v230 offset:10240\nv_mfma_f32_16x16x32_bf16 v[152:155], a[248:251], a[104:107], v[152:155]\n"
"s_add_u32 s79, s79, 0x80\nds_read_b64_tr_b16 a[188:189], v230 offset:12288\nv_mfma_f32_16x16x32_bf16 v[152:155], a[252:255], a[120:123], v[152:155]\ns_add_u32 m0, s73, 0x1f000\n"
"ds_read_b64_tr_b16 a[190:191], v230 offset:14336\nv_mfma_f32_16x16x32_bf16 v[156:159], a[252:255], a[124:127], v[220:223]\nbuffer_load_dwordx4 v231, s[48:51], s79 offen lds\nv_mfma_f32_16x16x32_bf16 v[156:159], a[248:251], a[108:111], v[156:159]\n"
"v_exp_f32 v145, v145\nv_mfma_f32_16x16x32_bf16 v[156:159], a[244:247], a[92:95], v[156:159]\nv_exp_f32 v146, v146\nv_mfma_f32_16x16x32_bf16 v[156:159], a[240:243], a[76:79], v[156:159]\n"
"v_exp_f32 v147, v147\nv_mfma_f32_16x16x32_bf16 v[156:159], a[236:239], a[60:63], v[156:159]\nv_exp_f32 v148, v148\nv_mfma_f32_16x16x32_bf16 v[156:159], a[232:235], a[44:47], v[156:159]\n"
"v_exp_f32 v149, v149\nv_mfma_f32_16x16x32_bf16 v[156:159], a[228:231], a[28:31], v[156:159]\nv_exp_f32 v150, v150\nv_mfma_f32_16x16x32_bf16 v[156:159], a[224:227], a[12:15], v[156:159]\n"
"v_exp_f32 v151, v151\ns_waitcnt vmcnt(16) lgkmcnt(0)\ns_barrier\nv_mfma_f32_16x16x32_bf16 v[0:3], a[128:131], v[176:179], v[0:3]\n"
"s_add_u32 s80, s80, 0x7e80\nds_read_b128 a[192:195], v225 offset:0\nv_cvt_pk_bf16_f32 v162, v144, v145\nv_mfma_f32_16x16x32_bf16 v[0:3], a[132:135], v[192:195], v[0:3]\n"
"s_add_u32 m0, s73, 0x0\nds_read_b128 a[196:199], v226 offset:0\nv_cvt_pk_bf16_f32 v163, v146, v147\nv_mfma_f32_16x16x32_bf16 v[4:7], a[132:135], v[196:199], v[4:7]\n"
"buffer_load_dwordx4 v232, s[52:55], s80 offen lds\nv_mfma_f32_16x16x32_bf16 v[4:7], a[128:131], v[180:183], v[4:7]\ns_add_u32 s80, s80, 0x80\nds_read_b128 a[200:203], v225 offset:8192\n"
"v_cvt_pk_bf16_f32 v166, v148, v149\nv_mfma_f32_16x16x32_bf16 v[8:11], a[128:131], v[184:187], v[8:11]\ns_add_u32 m0, s73, 0x2000\nds_read_b128 a[204:207], v226 offset:8192\n"
"v_cvt_pk_bf16_f32 v167, v150, v151\nv_mfma_f32_16x16x32_bf16 v[8:11], a[132:135], v[200:203], v[8:11]\nbuffer_load_dwordx4 v232, s[52:55], s80 offen lds\nv_mfma_f32_16x16x32_bf16 v[12:15], a[132:135], v[204:207], v[12:15]\n"
"s_add_u32 s80, s80, 0x4080\nds_read_b128 a[208:211], v225 offset:16384\nv_add_f32 v237, v237, v148\nv_mfma_f32_16x16x32_bf16 v[12:15], a[128:131], v[188:191], v[12:15]\n"
"s_add_u32 m0, s73, 0x4000\nds_read_b128 a[212:215], v226 offset:16384\nv_add_f32 v236, v236, v145\nv_mfma_f32_16x16x32_bf16 v[16:19], a[136:139], v[176:179], v[16:19]\n"
"buffer_load_dwordx4 v232, s[52:55], s80 offen lds\nv_mfma_f32_16x16x32_bf16 v[16:19], a[140:143], v[192:195], v[16:19]\ns_add_u32 s80, s80, 0x80\nds_read_b128 a[216:219], v225 offset:24576\n"
"v_add_f32 v237, v237, v149\nv_mfma_f32_16x16x32_bf16 v[20:23], a[140:143], v[196:199], v[20:23]\ns_add_u32 m0, s73, 0x6000\nds_read_b128 a[220:223], v226 offset:24576\n"
"v_add_f32 v236, v236, v146\nv_mfma_f32_16x16x32_bf16 v[20:23], a[136:139], v[180:183], v[20:23]\nbuffer_load_dwordx4 v232, s[52:55], s80 offen lds\nv_mfma_f32_16x16x32_bf16 v[24:27], a[136:139], v[184:187], v[24:27]\n"
"s_add_u32 s80, s80, 0xfffffe80\nds_read_b128 a[224:227], v225 offset:2048\nv_add_f32 v237, v237, v150\nv_mfma_f32_16x16x32_bf16 v[24:27], a[140:143], v[200:203], v[24:27]\n"
"s_add_u32 m0, s73, 0x1000\nds_read_b128 a[228:231], v226 offset:2048\nv_add_f32 v236, v236, v147\nv_mfma_f32_16x16x32_bf16 v[28:31], a[140:143], v[204:207], v[28:31]\n"
"buffer_load_dwordx4 v232, s[52:55], s80 offen lds\nv_mfma_f32_16x16x32_bf16 v[28:31], a[136:139], v[188:191], v[28:31]\ns_add_u32 s80, s80, 0x80\nds_read_b128 a[232:235], v225 offset:10240\n"
"v_add_f32 v237, v237, v151\nv_mfma_f32_16x16x32_bf16 v[32:35], a[144:147], v[176:179], v[32:35]\ns_add_u32 m0, s73, 0x3000\nds_read_b128 a[236:239], v226 offset:10240\n"
"v_mfma_f32_16x16x32_bf16 v[32:35], a[148:151], v[192:195], v[32:35]\nbuffer_load_dwordx4 v232, s[52:55], s80 offen lds\nv_mfma_f32_16x16x32_bf16 v[36:39], a[148:151], v[196:199], v[36:39]\ns_add_u32 s80, s80, 0xffffc080\n"
"ds_read_b128 a[240:243], v225 offset:18432\nv_mfma_f32_16x16x32_bf16 v[36:39], a[144:147], v[180:183], v[36:39]\ns_add_u32 m0, s73, 0x5000\nds_read_b128 a[244:247], v226 offset:18432\n"
"v_mfma_f32_16x16x32_bf16 v[40:43], a[144:147], v[184:187], v[40:43]\nbuffer_load_dwordx4 v232, s[52:55], s80 offen lds\nv_mfma_f32_16x16x32_bf16 v[40:43], a[148:151], v[200:203], v[40:43]\ns_add_u32 s80, s80, 0x80\n"
"ds_read_b128 a[248:251], v225 offset:26624\nv_mfma_f32_16x16x32_bf16 v[44:47], a[148:151], v[204:207], v[44:47]\ns_add_u32 m0, s73, 0x7000\nv_exp_f32 v138, v138\n"
"v_mfma_f32_16x16x32_bf16 v[44:47], a[144:147], v[188:191], v[44:47]\nbuffer_load_dwordx4 v232, s[52:55], s80 offen lds\nv_mfma_f32_16x16x32_bf16 v[48:51], a[152:155], v[176:179], v[48:51]\nds_read_b128 a[252:255], v226 offset:26624\n"
"v_add_f32 v238, v238, v138\nv_mfma_f32_16x16x32_bf16 v[48:51], a[156:159], v[192:195], v[48:51]\nv_exp_f32 v139, v139\nv_mfma_f32_16x16x32_bf16 v[52:55], a[156:159], v[196:199], v[52:55]\n"
"v_exp_f32 v152, v152\nv_mfma_f32_16x16x32_bf16 v[52:55], a[152:155], v[180:183], v[52:55]\nv_exp_f32 v153, v153\nv_mfma_f32_16x16x32_bf16 v[56:59], a[152:155], v[184:187], v[56:59]\n"
"v_exp_f32 v154, v154\nv_mfma_f32_16x16x32_bf16 v[56:59], a[156:159], v[200:203], v[56:59]\nv_exp_f32 v155, v155\nv_mfma_f32_16x16x32_bf16 v[60:63], a[156:159], v[204:207], v[60:63]\n"
"v_exp_f32 v142, v142\nv_mfma_f32_16x16x32_bf16 v[60:63], a[152:155], v[188:191], v[60:63]\nv_cvt_pk_bf16_f32 v169, v138, v139\nv_cvt_pk_bf16_f32 v170, v152, v153\n"
"v_mfma_f32_16x16x32_bf16 v[64:67], a[160:163], v[176:179], v[64:67]\nv_cvt_pk_bf16_f32 v171, v154, v155\nv_add_f32 v239, v239, v142\nv_mfma_f32_16x16x32_bf16 v[64:67], a[164:167], v[192:195], v[64:67]\n"
"v_exp_f32 v143, v143\nv_mfma_f32_16x16x32_bf16 v[68:71], a[164:167], v[196:199], v[68:71]\nv_exp_f32 v156, v156\nv_mfma_f32_16x16x32_bf16 v[68:71], a[160:163], v[180:183], v[68:71]\n"
"v_exp_f32 v157, v157\nv_mfma_f32_16x16x32_bf16 v[72:75], a[160:163], v[184:187], v[72:75]\nv_exp_f32 v158, v158\nv_mfma_f32_16x16x32_bf16 v[72:75], a[164:167], v[200:203], v[72:75]\n"
"v_exp_f32 v159, v159\nv_mfma_f32_16x16x32_bf16 v[76:79], a[164:167], v[204:207], v[76:79]\nv_cvt_pk_bf16_f32 v173, v142, v143\nv_cvt_pk_bf16_f32 v174, v156, v157\n"
"v_mfma_f32_16x16x32_bf16 v[76:79], a[160:163], v[188:191], v[76:79]\nv_cvt_pk_bf16_f32 v175, v158, v159\nv_add_f32 v238, v238, v139\nv_mfma_f32_16x16x32_bf16 v[80:83], a[168:171], v[176:179], v[80:83]\n"
"v_add_f32 v239, v239, v143\nv_add_f32 v238, v238, v152\nv_mfma_f32_16x16x32_bf16 v[80:83], a[172:175], v[192:195], v[80:83]\nv_add_f32 v239, v239, v156\n"
"v_add_f32 v238, v238, v153\nv_mfma_f32_16x16x32_bf16 v[84:87], a[172:175], v[196:199], v[84:87]\nv_add_f32 v239, v239, v157\nv_add_f32 v238, v238, v154\n"
"v_mfma_f32_16x16x32_bf16 v[84:87], a[168:171], v[180:183], v[84:87]\nv_add_f32 v239, v239, v158\nv_add_f32 v238, v238, v155\nv_mfma_f32_16x16x32_bf16 v[88:91], a[168:171], v[184:187], v[88:91]\n"
"ds_write_b128 v233, v[160:163] offset:16384\nv_mfma_f32_16x16x32_bf16 v[88:91], a[172:175], v[200:203], v[88:91]\nbuffer_load_dword v248, v249, s[92:95], s91 offen offset:0\nv_mfma_f32_16x16x32_bf16 v[92:95], a[172:175], v[204:207], v[92:95]\n"
"s_add_u32 s91, s91, 0x10000\nv_add_f32 v239, v239, v159\nv_mfma_f32_16x16x32_bf16 v[92:95], a[168:171], v[188:191], v[92:95]\nv_mfma_f32_16x16x32_bf16 v[96:99], a[176:179], v[176:179], v[96:99]\n"
"ds_write_b128 v233, v[164:167] offset:17408\nv_mfma_f32_16x16x32_bf16 v[96:99], a[180:183], v[192:195], v[96:99]\nv_mfma_f32_16x16x32_bf16 v[100:103], a[180:183], v[196:199], v[100:103]\nv_mfma_f32_16x16x32_bf16 v[100:103], a[176:179], v[180:183], v[100:103]\n"
"v_mfma_f32_16x16x32_bf16 v[104:107], a[176:179], v[184:187], v[104:107]\nds_write_b128 v233, v[168:171] offset:18432\nv_mfma_f32_16x16x32_bf16 v[104:107], a[180:183], v[200:203], v[104:107]\nv_mfma_f32_16x16x32_bf16 v[108:111], a[180:183], v[204:207], v[108:111]\n"
"v_mfma_f32_16x16x32_bf16 v[108:111], a[176:179], v[188:191], v[108:111]\nv_mfma_f32_16x16x32_bf16 v[112:115], a[184:187], v[176:179], v[112:115]\nds_write_b128 v233, v[172:175] offset:19456\nv_mfma_f32_16x16x32_bf16 v[112:115], a[188:191], v[192:195], v[112:115]\n"
"v_mfma_f32_16x16x32_bf16 v[116:119], a[188:191], v[196:199], v[116:119]\nv_mfma_f32_16x16x32_bf16 v[116:119], a[184:187], v[180:183], v[116:119]\nv_mfma_f32_16x16x32_bf16 v[120:123], a[184:187], v[184:187], v[120:123]\nv_mfma_f32_16x16x32_bf16 v[120:123], a[188:191], v[200:203], v[120:123]\n"
"v_mfma_f32_16x16x32_bf16 v[124:127], a[188:191], v[204:207], v[124:127]\nv_mfma_f32_16x16x32_bf16 v[124:127], a[184:187], v[188:191], v[124:127]\ns_waitcnt vmcnt(17) lgkmcnt(0)\ns_barrier\n"
"v_mfma_f32_16x16x32_bf16 v[128:131], a[192:195], a[0:3], v[208:211]\ns_add_u32 s79, s79, 0x3e80\nds_read_b128 v[192:195], v234 offset:16384\nv_mfma_f32_16x16x32_bf16 v[128:131], a[196:199], a[16:19], v[128:131]\n"
"s_add_u32 m0, s73, 0x10000\nds_read_b128 v[196:199], v234 offset:17408\nv_mfma_f32_16x16x32_bf16 v[128:131], a[200:203], a[32:35], v[128:131]\nbuffer_load_dwordx4 v231, s[48:51], s79 offen lds\n"
"v_mfma_f32_16x16x32_bf16 v[128:131], a[204:207], a[48:51], v[128:131]\nds_read_b128 v[200:203], v234 offset:18432\nv_mfma_f32_16x16x32_bf16 v[128:131], a[208:211], a[64:67], v[128:131]\nds_read_b128 v[204:207], v234 offset:19456\n"
"v_mfma_f32_16x16x32_bf16 v[128:131], a[212:215], a[80:83], v[128:131]\nds_read_b64_tr_b16 a[128:129], v227 offset:32768\nv_mfma_f32_16x16x32_bf16 v[128:131], a[216:219], a[96:99], v[128:131]\nds_read_b64_tr_b16 a[130:131], v227 offset:34816\n"
"v_mfma_f32_16x16x32_bf16 v[128:131], a[220:223], a[112:115], v[128:131]\ns_add_u32 s79, s79, 0x80\nds_read_b64_tr_b16 a[132:133], v227 offset:36864\nv_mfma_f32_16x16x32_bf16 v[132:135], a[220:223], a[116:119], v[212:215]\n"
"s_add_u32 m0, s73, 0x12000\nds_read_b64_tr_b16 a[134:135], v227 offset:38912\nv_mfma_f32_16x16x32_bf16 v[132:135], a[216:219], a[100:103], v[132:135]\nbuffer_load_dwordx4 v231, s[48:51], s79 offen lds\n"
"v_mfma_f32_16x16x32_bf16 v[132:135], a[212:215], a[84:87], v[132:135]\nds_read_b64_tr_b16 a[136:137], v228 offset:32768\nv_mfma_f32_16x16x32_bf16 v[132:135], a[208:211], a[68:71], v[132:135]\nv_exp_f32 v128, v128\n"
"v_mfma_f32_16x16x32_bf16 v[132:135], a[204:207], a[52:55], v[132:135]\nds_read_b64_tr_b16 a[138:139], v228 offset:34816\nv_add_f32 v236, v236, v128\nv_mfma_f32_16x16x32_bf16 v[132:135], a[200:203], a[36:39], v[132:135]\n"
"v_exp_f32 v129, v129\nv_mfma_f32_16x16x32_bf16 v[132:135], a[196:199], a[20:23], v[132:135]\nds_read_b64_tr_b16 a[140:141], v228 offset:36864\nv_cvt_pk_bf16_f32 v176, v128, v129\n"
"v_mfma_f32_16x16x32_bf16 v[132:135], a[192:195], a[4:7], v[132:135]\ns_add_u32 s79, s79, 0x80\nds_read_b64_tr_b16 a[142:143], v228 offset:38912\nv_add_f32 v236, v236, v129\n"
"v_mfma_f32_16x16x32_bf16 v[136:139], a[192:195], a[8:11], v[216:219]\ns_add_u32 m0, s73, 0x14000\nv_exp_f32 v130, v130\nv_mfma_f32_16x16x32_bf16 v[136:139], a[196:199], a[24:27], v[136:139]\n"
"buffer_load_dwordx4 v231, s[48:51], s79 offen lds\nv_mfma_f32_16x16x32_bf16 v[136:139], a[200:203], a[40:43], v[136:139]\nds_read_b64_tr_b16 a[144:145], v229 offset:32768\nv_add_f32 v236, v236, v130\n"
"v_mfma_f32_16x16x32_bf16 v[136:139], a[204:207], a[56:59], v[136:139]\nv_exp_f32 v132, v132\nv_mfma_f32_16x16x32_bf16 v[136:139], a[208:211], a[72:75], v[136:139]\nds_read_b64_tr_b16 a[146:147], v229 offset:34816\n"
"v_add_f32 v237, v237, v132\nv_mfma_f32_16x16x32_bf16 v[136:139], a[212:215], a[88:91], v[136:139]\nv_exp_f32 v131, v131\nv_mfma_f32_16x16x32_bf16 v[136:139], a[216:219], a[104:107], v[136:139]\n"
"ds_read_b64_tr_b16 a[148:149], v229 offset:36864\nv_cvt_pk_bf16_f32 v177, v130, v131\nv_mfma_f32_16x16x32_bf16 v[136:139], a[220:223], a[120:123], v[136:139]\ns_add_u32 s79, s79, 0x80\n"
"ds_read_b64_tr_b16 a[150:151], v229 offset:38912\nv_add_f32 v236, v236, v131\nv_mfma_f32_16x16x32_bf16 v[140:143], a[220:223], a[124:127], v[220:223]\ns_add_u32 m0, s73, 0x16000\n"
"v_exp_f32 v133, v133\nv_mfma_f32_16x16x32_bf16 v[140:143], a[216:219], a[108:111], v[140:143]\nbuffer_load_dwordx4 v231, s[48:51], s79 offen lds\nv_mfma_f32_16x16x32_bf16 v[140:143], a[212:215], a[92:95], v[140:143]\n"
"ds_read_b64_tr_b16 a[152:153], v230 offset:32768\nv_cvt_pk_bf16_f32 v180, v132, v133\nv_mfma_f32_16x16x32_bf16 v[140:143], a[208:211], a[76:79], v[140:143]\nds_read_b64_tr_b16 a[154:155], v230 offset:34816\n"
"v_add_f32 v237, v237, v133\nv_mfma_f32_16x16x32_bf16 v[140:143], a[204:207], a[60:63], v[140:143]\nv_exp_f32 v134, v134\nv_mfma_f32_16x16x32_bf16 v[140:143], a[200:203], a[44:47], v[140:143]\n"
"ds_read_b64_tr_b16 a[156:157], v230 offset:36864\nv_add_f32 v237, v237, v134\nv_mfma_f32_16x16x32_bf16 v[140:143], a[196:199], a[28:31], v[140:143]\nv_exp_f32 v135, v135\n"
"v_mfma_f32_16x16x32_bf16 v[140:143], a[192:195], a[12:15], v[140:143]\ns_add_u32 s79, s79, 0x3e80\nds_read_b64_tr_b16 a[158:159], v230 offset:38912\nv_cvt_pk_bf16_f32 v181, v134, v135\n"
"v_mfma_f32_16x16x32_bf16 v[144:147], a[224:227], a[0:3], v[208:211]\ns_add_u32 m0, s73, 0x11000\nds_read_b64_tr_b16 a[160:161], v227 offset:40960\nv_add_f32 v237, v237, v135\n"
"v_mfma_f32_16x16x32_bf16 v[144:147], a[228:231], a[16:19], v[144:147]\nbuffer_load_dwordx4 v231, s[48:51], s79 offen lds\nv_mfma_f32_16x16x32_bf16 v[144:147], a[232:235], a[32:35], v[144:147]\nv_exp_f32 v136, v136\n"
"v_mfma_f32_16x16x32_bf16 v[144:147], a[236:239], a[48:51], v[144:147]\nds_read_b64_tr_b16 a[162:163], v227 offset:43008\nv_add_f32 v238, v238, v136\nv_mfma_f32_16x16x32_bf16 v[144:147], a[240:243], a[64:67], v[144:147]\n"
"v_exp_f32 v140, v140\nv_mfma_f32_16x16x32_bf16 v[144:147], a[244:247], a[80:83], v[144:147]\nds_read_b64_tr_b16 a[164:165], v227 offset:45056\nv_add_f32 v239, v239, v140\n"
"v_mfma_f32_16x16x32_bf16 v[144:147], a[248:251], a[96:99], v[144:147]\ns_add_u32 s79, s79, 0x80\nv_exp_f32 v137, v137\nv_mfma_f32_16x16x32_bf16 v[144:147], a[252:255], a[112:115], v[144:147]\n"
"s_add_u32 m0, s73, 0x13000\nds_read_b64_tr_b16 a[166:167], v227 offset:47104\nv_cvt_pk_bf16_f32 v184, v136, v137\nv_mfma_f32_16x16x32_bf16 v[148:151], a[252:255], a[116:119], v[212:215]\n"
"buffer_load_dwordx4 v231, s[48:51], s79 offen lds\nv_mfma_f32_16x16x32_bf16 v[148:151], a[248:251], a[100:103], v[148:151]\nds_read_b64_tr_b16 a[168:169], v228 offset:40960\nv_add_f32 v238, v238, v137\n"
"v_mfma_f32_16x16x32_bf16 v[148:151], a[244:247], a[84:87], v[148:151]\nv_exp_f32 v138, v138\nv_mfma_f32_16x16x32_bf16 v[148:151], a[240:243], a[68:71], v[148:151]\nds_read_b64_tr_b16 a[170:171], v228 offset:43008\n"
"v_add_f32 v238, v238, v138\nv_mfma_f32_16x16x32_bf16 v[148:151], a[236:239], a[52:55], v[148:151]\nds_read_b64_tr_b16 a[172:173], v228 offset:45056\nv_mfma_f32_16x16x32_bf16 v[148:151], a[232:235], a[36:39], v[148:151]\n"
"ds_read_b64_tr_b16 a[174:175], v228 offset:47104\nv_mfma_f32_16x16x32_bf16 v[148:151], a[228:231], a[20:23], v[148:151]\ns_add_u32 s79, s79, 0x80\nds_read_b64_tr_b16 a[176:177], v229 offset:40960\n"
"v_mfma_f32_16x16x32_bf16 v[148:151], a[224:227], a[4:7], v[148:151]\ns_add_u32 m0, s73, 0x15000\nv_exp_f32 v144, v144\nv_mfma_f32_16x16x32_bf16 v[152:155], a[224:227], a[8:11], v[216:219]\n"
"buffer_load_dwordx4 v231, s[48:51], s79 offen lds\nv_mfma_f32_16x16x32_bf16 v[152:155], a[228:231], a[24:27], v[152:155]\nds_read_b64_tr_b16 a[178:179], v229 offset:43008\nv_add_f32 v236, v236, v144\n"
"v_mfma_f32_16x16x32_bf16 v[152:155], a[232:235], a[40:43], v[152:155]\nds_read_b64_tr_b16 a[180:181], v229 offset:45056\nv_mfma_f32_16x16x32_bf16 v[152:155], a[236:239], a[56:59], v[152:155]\nds_read_b64_tr_b16 a[182:183], v229 offset:47104\n"
"v_mfma_f32_16x16x32_bf16 v[152:155], a[240:243], a[72:75], v[152:155]\nds_read_b64_tr_b16 a[184:185], v230 offset:40960\nv_mfma_f32_16x16x32_bf16 v[152:155], a[244:247], a[88:91], v[152:155]\nds_read_b64_tr_b16 a[186:187], v230 offset:43008\n"
"v_mfma_f32_16x16x32_bf16 v[152:155], a[248:251], a[104:107], v[152:155]\ns_add_u32 s79, s79, 0x80\nds_read_b64_tr_b16 a[188:189], v230 offset:45056\nv_mfma_f32_16x16x32_bf16 v[152:155], a[252:255], a[120:123], v[152:155]\n"
"s_add_u32 m0, s73, 0x17000\nds_read_b64_tr_b16 a[190:191], v230 offset:47104\nv_mfma_f32_16x16x32_bf16 v[156:159], a[252:255], a[124:127], v[220:223]\nbuffer_load_dwordx4 v231, s[48:51], s79 offen lds\n"
"v_mfma_f32_16x16x32_bf16 v[156:159], a[248:251], a[108:111], v[156:159]\nv_exp_f32 v145, v145\nv_mfma_f32_16x16x32_bf16 v[156:159], a[244:247], a[92:95], v[156:159]\nv_exp_f32 v146, v146\n"
"v_mfma_f32_16x16x32_bf16 v[156:159], a[240:243], a[76:79], v[156:159]\nv_exp_f32 v147, v147\nv_mfma_f32_16x16x32_bf16 v[156:159], a[236:239], a[60:63], v[156:159]\nv_exp_f32 v148, v148\n"
"v_mfma_f32_16x16x32_bf16 v[156:159], a[232:235], a[44:47], v[156:159]\nv_exp_f32 v149, v149\nv_mfma_f32_16x16x32_bf16 v[156:159], a[228:231], a[28:31], v[156:159]\nv_exp_f32 v150, v150\n"
"v_mfma_f32_16x16x32_bf16 v[156:159], a[224:227], a[12:15], v[156:159]\nv_exp_f32 v151, v151\ns_waitcnt vmcnt(17) lgkmcnt(0)\ns_barrier\n"
"v_mfma_f32_16x16x32_bf16 v[0:3], a[128:131], v[160:163], v[0:3]\ns_add_u32 s80, s80, 0x7e80\nds_read_b128 a[192:195], v225 offset:32768\nv_cvt_pk_bf16_f32 v178, v144, v145\n"
"v_mfma_f32_16x16x32_bf16 v[0:3], a[132:135], v[192:195], v[0:3]\ns_add_u32 m0, s73, 0x8000\nds_read_b128 a[196:199], v226 offset:32768\nv_cvt_pk_bf16_f32 v179, v146, v147\n"
"v_mfma_f32_16x16x32_bf16 v[4:7], a[132:135], v[196:199], v[4:7]\nbuffer_load_dwordx4 v232, s[52:55], s80 offen lds\nv_mfma_f32_16x16x32_bf16 v[4:7], a[128:131], v[164:167], v[4:7]\ns_add_u32 s80, s80, 0x80\n"
"ds_read_b128 a[200:203], v225 offset:40960\nv_cvt_pk_bf16_f32 v182, v148, v149\nv_mfma_f32_16x16x32_bf16 v[8:11], a[128:131], v[168:171], v[8:11]\ns_add_u32 m0, s73, 0xa000\n"
"ds_read_b128 a[204:207], v226 offset:40960\nv_cvt_pk_bf16_f32 v183, v150, v151\nv_mfma_f32_16x16x32_bf16 v[8:11], a[132:135], v[200:203], v[8:11]\nbuffer_load_dwordx4 v232, s[52:55], s80 offen lds\n"
"v_mfma_f32_16x16x32_bf16 v[12:15], a[132:135], v[204:207], v[12:15]\ns_add_u32 s80, s80, 0x4080\nds_read_b128 a[208:211], v225 offset:49152\nv_add_f32 v237, v237, v148\n"
"v_mfma_f32_16x16x32_bf16 v[12:15], a[128:131], v[172:175], v[12:15]\ns_add_u32 m0, s73, 0xc000\nds_read_b128 a[212:215], v226 offset:49152\nv_add_f32 v236, v236, v145\n"
"v_mfma_f32_16x16x32_bf16 v[16:19], a[136:139], v[160:163], v[16:19]\nbuffer_load_dwordx4 v232, s[52:55], s80 offen lds\nv_mfma_f32_16x16x32_bf16 v[16:19], a[140:143], v[192:195], v[16:19]\ns_add_u32 s80, s80, 0x80\n"
"ds_read_b128 a[216:219], v225 offset:57344\nv_add_f32 v237, v237, v149\nv_mfma_f32_16x16x32_bf16 v[20:23], a[140:143], v[196:199], v[20:23]\ns_add_u32 m0, s73, 0xe000\n"
"ds_read_b128 a[220:223], v226 offset:57344\nv_add_f32 v236, v236, v146\nv_mfma_f32_16x16x32_bf16 v[20:23], a[136:139], v[164:167], v[20:23]\nbuffer_load_dwordx4 v232, s[52:55], s80 offen lds\n"
"v_mfma_f32_16x16x32_bf16 v[24:27], a[136:139], v[168:171], v[24:27]\ns_add_u32 s80, s80, 0xfffffe80\nds_read_b128 a[224:227], v225 offset:34816\nv_add_f32 v237, v237, v150\n"
"v_mfma_f32_16x16x32_bf16 v[24:27], a[140:143], v[200:203], v[24:27]\ns_add_u32 m0, s73, 0x9000\nds_read_b128 a[228:231], v226 offset:34816\nv_add_f32 v236, v236, v147\n"
"v_mfma_f32_16x16x32_bf16 v[28:31], a[140:143], v[204:207], v[28:31]\nbuffer_load_dwordx4 v232, s[52:55], s80 offen lds\nv_mfma_f32_16x16x32_bf16 v[28:31], a[136:139], v[172:175], v[28:31]\ns_add_u32 s80, s80, 0x80\n"
"ds_read_b128 a[232:235], v225 offset:43008\nv_add_f32 v237, v237, v151\nv_mfma_f32_16x16x32_bf16 v[32:35], a[144:147], v[160:163], v[32:35]\ns_add_u32 m0, s73, 0xb000\n"
"ds_read_b128 a[236:239], v226 offset:43008\nv_mfma_f32_16x16x32_bf16 v[32:35], a[148:151], v[192:195], v[32:35]\nbuffer_load_dwordx4 v232, s[52:55], s80 offen lds\nv_mfma_f32_16x16x32_bf16 v[36:39], a[148:151], v[196:199], v[36:39]\n"
"s_add_u32 s80, s80, 0xffffc080\nds_read_b128 a[240:243], v225 offset:51200\nv_mfma_f32_16x16x32_bf16 v[36:39], a[144:147], v[164:167], v[36:39]\ns_add_u32 m0, s73, 0xd000\n"
"ds_read_b128 a[244:247], v226 offset:51200\nv_mfma_f32_16x16x32_bf16 v[40:43], a[144:147], v[168:171], v[40:43]\nbuffer_load_dwordx4 v232, s[52:55], s80 offen lds\nv_mfma_f32_16x16x32_bf16 v[40:43], a[148:151], v[200:203], v[40:43]\n"
"s_add_u32 s80, s80, 0x80\nds_read_b128 a[248:251], v225 offset:59392\nv_mfma_f32_16x16x32_bf16 v[44:47], a[148:151], v[204:207], v[44:47]\ns_add_u32 m0, s73, 0xf000\n"
"v_exp_f32 v139, v139\nv_mfma_f32_16x16x32_bf16 v[44:47], a[144:147], v[172:175], v[44:47]\nbuffer_load_dwordx4 v232, s[52:55], s80 offen lds\nv_mfma_f32_16x16x32_bf16 v[48:51], a[152:155], v[160:163], v[48:51]\n"
"ds_read_b128 a[252:255], v226 offset:59392\nv_cvt_pk_bf16_f32 v185, v138, v139\nv_mfma_f32_16x16x32_bf16 v[48:51], a[156:159], v[192:195], v[48:51]\nv_exp_f32 v152, v152\n"
"v_mfma_f32_16x16x32_bf16 v[52:55], a[156:159], v[196:199], v[52:55]\nv_exp_f32 v153, v153\nv_mfma_f32_16x16x32_bf16 v[52:55], a[152:155], v[164:167], v[52:55]\nv_exp_f32 v154, v154\n"
"v_mfma_f32_16x16x32_bf16 v[56:59], a[152:155], v[168:171], v[56:59]\nv_exp_f32 v155, v155\nv_mfma_f32_16x16x32_bf16 v[56:59], a[156:159], v[200:203], v[56:59]\nv_exp_f32 v141, v141\n"
"v_mfma_f32_16x16x32_bf16 v[60:63], a[156:159], v[204:207], v[60:63]\nv_cvt_pk_bf16_f32 v186, v152, v153\nv_cvt_pk_bf16_f32 v187, v154, v155\nv_mfma_f32_16x16x32_bf16 v[60:63], a[152:155], v[172:175], v[60:63]\n"
"v_exp_f32 v142, v142\nv_mfma_f32_16x16x32_bf16 v[64:67], a[160:163], v[160:163], v[64:67]\nv_exp_f32 v143, v143\nv_mfma_f32_16x16x32_bf16 v[64:67], a[164:167], v[192:195], v[64:67]\n"
"v_exp_f32 v156, v156\nv_mfma_f32_16x16x32_bf16 v[68:71], a[164:167], v[196:199], v[68:71]\nv_exp_f32 v157, v157\nv_mfma_f32_16x16x32_bf16 v[68:71], a[160:163], v[164:167], v[68:71]\n"
"v_exp_f32 v158, v158\nv_mfma_f32_16x16x32_bf16 v[72:75], a[160:163], v[168:171], v[72:75]\nv_exp_f32 v159, v159\nv_mfma_f32_16x16x32_bf16 v[72:75], a[164:167], v[200:203], v[72:75]\n"
"v_add_f32 v239, v239, v141\nv_cvt_pk_bf16_f32 v188, v140, v141\nv_mfma_f32_16x16x32_bf16 v[76:79], a[164:167], v[204:207], v[76:79]\nv_cvt_pk_bf16_f32 v189, v142, v143\n"
"v_cvt_pk_bf16_f32 v190, v156, v157\nv_mfma_f32_16x16x32_bf16 v[76:79], a[160:163], v[172:175], v[76:79]\nv_cvt_pk_bf16_f32 v191, v158, v159\nv_add_f32 v239, v239, v142\n"
"v_mfma_f32_16x16x32_bf16 v[80:83], a[168:171], v[160:163], v[80:83]\nv_add_f32 v238, v238, v139\nv_add_f32 v239, v239, v143\nv_mfma_f32_16x16x32_bf16 v[80:83], a[172:175], v[192:195], v[80:83]\n"
"v_add_f32 v238, v238, v152\nv_add_f32 v239, v239, v156\nv_mfma_f32_16x16x32_bf16 v[84:87], a[172:175], v[196:199], v[84:87]\nv_add_f32 v238, v238, v153\n"
"v_add_f32 v239, v239, v157\nv_mfma_f32_16x16x32_bf16 v[84:87], a[168:171], v[164:167], v[84:87]\nv_add_f32 v238, v238, v154\nv_add_f32 v239, v239, v158\n"
"v_mfma_f32_16x16x32_bf16 v[88:91], a[168:171], v[168:171], v[88:91]\nds_write_b128 v233, v[176:179] offset:0\nv_mfma_f32_16x16x32_bf16 v[88:91], a[172:175], v[200:203], v[88:91]\nv_add_f32 v238, v238, v155\n"
"v_add_f32 v239, v239, v159\nv_mfma_f32_16x16x32_bf16 v[92:95], a[172:175], v[204:207], v[92:95]\nv_mfma_f32_16x16x32_bf16 v[92:95], a[168:171], v[172:175], v[92:95]\nv_mfma_f32_16x16x32_bf16 v[96:99], a[176:179], v[160:163], v[96:99]\n"
"ds_write_b128 v233, v[180:183] offset:1024\nv_mfma_f32_16x16x32_bf16 v[96:99], a[180:183], v[192:195], v[96:99]\nv_mfma_f32_16x16x32_bf16 v[100:103], a[180:183], v[196:199], v[100:103]\nv_mfma_f32_16x16x32_bf16 v[100:103], a[176:179], v[164:167], v[100:103]\n"
"v_mfma_f32_16x16x32_bf16 v[104:107], a[176:179], v[168:171], v[104:107]\nds_write_b128 v233, v[184:187] offset:2048\nv_mfma_f32_16x16x32_bf16 v[104:107], a[180:183], v[200:203], v[104:107]\nv_mfma_f32_16x16x32_bf16 v[108:111], a[180:183], v[204:207], v[108:111]\n"
"v_mfma_f32_16x16x32_bf16 v[108:111], a[176:179], v[172:175], v[108:111]\nv_mfma_f32_16x16x32_bf16 v[112:115], a[184:187], v[160:163], v[112:115]\nds_write_b128 v233, v[188:191] offset:3072\nv_mfma_f32_16x16x32_bf16 v[112:115], a[188:191], v[192:195], v[112:115]\n"
"v_mfma_f32_16x16x32_bf16 v[116:119], a[188:191], v[196:199], v[116:119]\ns_cmp_lg_u32 s31, 2\ns_cbranch_scc1 L_h2%=\nL_h2r%=:\nv_mfma_f32_16x16x32_bf16 v[116:119], a[184:187], v[164:167], v[116:119]\ns_sub_u32 s84, s84, 1\n"
"v_mfma_f32_16x16x32_bf16 v[120:123], a[184:187], v[168:171], v[120:123]\ns_cmp_lg_u32 s84, 8\nv_mfma_f32_16x16x32_bf16 v[120:123], a[188:191], v[200:203], v[120:123]\ns_cselect_b32 s87, s84, 0\n"
"v_mfma_f32_16x16x32_bf16 v[124:127], a[188:191], v[204:207], v[124:127]\ns_cmp_lg_u32 s87, 0\nv_mfma_f32_16x16x32_bf16 v[124:127], a[184:187], v[172:175], v[124:127]\ns_waitcnt vmcnt(17) lgkmcnt(0)\n"
"s_barrier\ns_cbranch_scc1 L_loop%=\ns_cmp_lg_u32 s84, 0\ns_cbranch_scc1 L_rb%=\n"
"s_cmp_lg_u32 s31, 2\ns_cbranch_scc1 L_hs%=\nL_hsr%=:\nv_mfma_f32_16x16x32_bf16 v[128:131], a[192:195], a[0:3], v[208:211]\ns_add_u32 s80, s80, 0x7e80\nds_read_b128 v[192:195], v234 offset:0\n"
"v_lshlrev_b32 v249, 7, v254\nv_mfma_f32_16x16x32_bf16 v[128:131], a[196:199], a[16:19], v[128:131]\ns_add_u32 m0, s73, 0x18000\nds_read_b128 v[196:199], v234 offset:1024\n"
"s_lshl_b32 s91, s85, 13\nv_mfma_f32_16x16x32_bf16 v[128:131], a[200:203], a[32:35], v[128:131]\nbuffer_load_dwordx4 v232, s[52:55], s80 offen lds\nv_mfma_f32_16x16x32_bf16 v[128:131], a[204:207], a[48:51], v[128:131]\n"
"s_add_u32 s84, s91, s81\nds_read_b128 v[200:203], v234 offset:2048\ns_cmp_lt_u32 s85, 56\ns_cselect_b32 s84, s84, 0x7f000000\nv_mfma_f32_16x16x32_bf16 v[128:131], a[208:211], a[64:67], v[128:131]\n"
"s_lshr_b32 s91, %1, 3\nds_read_b128 v[204:207], v234 offset:3072\ns_lshl_b32 s87, s72, 5\nv_mfma_f32_16x16x32_bf16 v[128:131], a[212:215], a[80:83], v[128:131]\n"
"s_add_u32 s91, s91, s87\nds_read_b64_tr_b16 a[128:129], v227 offset:0\ns_mov_b64 s[92:93], s[28:29]\nv_mfma_f32_16x16x32_bf16 v[128:131], a[216:219], a[96:99], v[128:131]\n"
"s_and_b32 s93, s93, 0xffff\nds_read_b64_tr_b16 a[130:131], v227 offset:2048\ns_mov_b32 s94, 0x188000\nv_mfma_f32_16x16x32_bf16 v[128:131], a[220:223], a[112:115], v[128:131]\n"
"s_add_u32 s80, s80, 0x80\nbuffer_load_dword v248, v249, s[56:59], s84 offen offset:0\nv_mfma_f32_16x16x32_bf16 v[132:135], a[220:223], a[116:119], v[212:215]\ns_add_u32 m0, s73, 0x1a000\n"
"ds_read_b64_tr_b16 a[132:133], v227 offset:4096\ns_lshl_b32 s84, s91, 13\nv_mfma_f32_16x16x32_bf16 v[132:135], a[216:219], a[100:103], v[132:135]\nbuffer_load_dwordx4 v232, s[52:55], s80 offen lds\n"
"v_mfma_f32_16x16x32_bf16 v[132:135], a[212:215], a[84:87], v[132:135]\nds_read_b64_tr_b16 a[134:135], v227 offset:6144\nv_mfma_f32_16x16x32_bf16 v[132:135], a[208:211], a[68:71], v[132:135]\nv_exp_f32 v128, v128\n"
"v_mfma_f32_16x16x32_bf16 v[132:135], a[204:207], a[52:55], v[132:135]\nds_read_b64_tr_b16 a[136:137], v228 offset:0\nv_add_f32 v236, v236, v128\nv_mfma_f32_16x16x32_bf16 v[132:135], a[200:203], a[36:39], v[132:135]\n"
"v_exp_f32 v129, v129\nv_mfma_f32_16x16x32_bf16 v[132:135], a[196:199], a[20:23], v[132:135]\nds_read_b64_tr_b16 a[138:139], v228 offset:2048\nv_cvt_pk_bf16_f32 v160, v128, v129\n"
"v_mfma_f32_16x16x32_bf16 v[132:135], a[192:195], a[4:7], v[132:135]\ns_add_u32 s80, s80, 0x4080\nds_read_b64_tr_b16 a[140:141], v228 offset:4096\nv_add_f32 v236, v236, v129\n"
"v_mfma_f32_16x16x32_bf16 v[136:139], a[192:195], a[8:11], v[216:219]\ns_add_u32 m0, s73, 0x1c000\nv_exp_f32 v130, v130\nv_mfma_f32_16x16x32_bf16 v[136:139], a[196:199], a[24:27], v[136:139]\n"
"buffer_load_dwordx4 v232, s[52:55], s80 offen lds\nv_mfma_f32_16x16x32_bf16 v[136:139], a[200:203], a[40:43], v[136:139]\nds_read_b64_tr_b16 a[142:143], v228 offset:6144\nv_add_f32 v236, v236, v130\n"
"v_mfma_f32_16x16x32_bf16 v[136:139], a[204:207], a[56:59], v[136:139]\nv_exp_f32 v132, v132\nv_mfma_f32_16x16x32_bf16 v[136:139], a[208:211], a[72:75], v[136:139]\nds_read_b64_tr_b16 a[144:145], v229 offset:0\n"
"v_add_f32 v237, v237, v132\nv_mfma_f32_16x16x32_bf16 v[136:139], a[212:215], a[88:91], v[136:139]\nv_exp_f32 v131, v131\nv_mfma_f32_16x16x32_bf16 v[136:139], a[216:219], a[104:107], v[136:139]\n"
"ds_read_b64_tr_b16 a[146:147], v229 offset:2048\nv_cvt_pk_bf16_f32 v161, v130, v131\nv_mfma_f32_16x16x32_bf16 v[136:139], a[220:223], a[120:123], v[136:139]\ns_add_u32 s80, s80, 0x80\n"
"ds_read_b64_tr_b16 a[148:149], v229 offset:4096\nv_add_f32 v236, v236, v131\nv_mfma_f32_16x16x32_bf16 v[140:143], a[220:223], a[124:127], v[220:223]\ns_add_u32 m0, s73, 0x1e000\n"
"v_exp_f32 v133, v133\nv_mfma_f32_16x16x32_bf16 v[140:143], a[216:219], a[108:111], v[140:143]\nbuffer_load_dwordx4 v232, s[52:55], s80 offen lds\nv_mfma_f32_16x16x32_bf16 v[140:143], a[212:215], a[92:95], v[140:143]\n"
"ds_read_b64_tr_b16 a[150:151], v229 offset:6144\nv_cvt_pk_bf16_f32 v164, v132, v133\nv_mfma_f32_16x16x32_bf16 v[140:143], a[208:211], a[76:79], v[140:143]\nds_read_b64_tr_b16 a[152:153], v230 offset:0\n"
"v_add_f32 v237, v237, v133\nv_mfma_f32_16x16x32_bf16 v[140:143], a[204:207], a[60:63], v[140:143]\nv_exp_f32 v136, v136\nv_mfma_f32_16x16x32_bf16 v[140:143], a[200:203], a[44:47], v[140:143]\n"
"ds_read_b64_tr_b16 a[154:155], v230 offset:2048\nv_add_f32 v238, v238, v136\nv_mfma_f32_16x16x32_bf16 v[140:143], a[196:199], a[28:31], v[140:143]\nv_exp_f32 v134, v134\n"
"v_mfma_f32_16x16x32_bf16 v[140:143], a[192:195], a[12:15], v[140:143]\ns_add_u32 s80, s80, 0xfffffe80\nds_read_b64_tr_b16 a[156:157], v230 offset:4096\nv_add_f32 v237, v237, v134\n"
"v_mfma_f32_16x16x32_bf16 v[144:147], a[224:227], a[0:3], v[208:211]\ns_add_u32 m0, s73, 0x19000\nv_exp_f32 v135, v135\nv_mfma_f32_16x16x32_bf16 v[144:147], a[228:231], a[16:19], v[144:147]\n"
"buffer_load_dwordx4 v232, s[52:55], s80 offen lds\nv_mfma_f32_16x16x32_bf16 v[144:147], a[232:235], a[32:35], v[144:147]\nds_read_b64_tr_b16 a[158:159], v230 offset:6144\nv_cvt_pk_bf16_f32 v165, v134, v135\n"
"v_mfma_f32_16x16x32_bf16 v[144:147], a[236:239], a[48:51], v[144:147]\nds_read_b64_tr_b16 a[160:161], v227 offset:8192\nv_add_f32 v237, v237, v135\nv_mfma_f32_16x16x32_bf16 v[144:147], a[240:243], a[64:67], v[144:147]\n"
"v_exp_f32 v140, v140\nv_mfma_f32_16x16x32_bf16 v[144:147], a[244:247], a[80:83], v[144:147]\nds_read_b64_tr_b16 a[162:163], v227 offset:10240\nv_add_f32 v239, v239, v140\n"
"v_mfma_f32_16x16x32_bf16 v[144:147], a[248:251], a[96:99], v[144:147]\ns_add_u32 s80, s80, 0x80\nv_exp_f32 v137, v137\nv_mfma_f32_16x16x32_bf16 v[144:147], a[252:255], a[112:115], v[144:147]\n"
"s_add_u32 m0, s73, 0x1b000\nds_read_b64_tr_b16 a[164:165], v227 offset:12288\nv_add_f32 v238, v238, v137\nv_mfma_f32_16x16x32_bf16 v[148:151], a[252:255], a[116:119], v[212:215]\n"
"buffer_load_dwordx4 v232, s[52:55], s80 offen lds\nv_mfma_f32_16x16x32_bf16 v[148:151], a[248:251], a[100:103], v[148:151]\nds_read_b64_tr_b16 a[166:167], v227 offset:14336\nv_cvt_pk_bf16_f32 v168, v136, v137\n"
"v_mfma_f32_16x16x32_bf16 v[148:151], a[244:247], a[84:87], v[148:151]\nds_read_b64_tr_b16 a[168:169], v228 offset:8192\nv_mfma_f32_16x16x32_bf16 v[148:151], a[240:243], a[68:71], v[148:151]\nds_read_b64_tr_b16 a[170:171], v228 offset:10240\n"
"v_mfma_f32_16x16x32_bf16 v[148:151], a[236:239], a[52:55], v[148:151]\nds_read_b64_tr_b16 a[172:173], v228 offset:12288\nv_mfma_f32_16x16x32_bf16 v[148:151], a[232:235], a[36:39], v[148:151]\nds_read_b64_tr_b16 a[174:175], v228 offset:14336\n"
"v_mfma_f32_16x16x32_bf16 v[148:151], a[228:231], a[20:23], v[148:151]\ns_add_u32 s80, s80, 0xffffc080\nds_read_b64_tr_b16 a[176:177], v229 offset:8192\nv_mfma_f32_16x16x32_bf16 v[148:151], a[224:227], a[4:7], v[148:151]\n"
"s_add_u32 m0, s73, 0x1d000\nv_exp_f32 v144, v144\nv_mfma_f32_16x16x32_bf16 v[152:155], a[224:227], a[8:11], v[216:219]\nbuffer_load_dwordx4 v232, s[52:55], s80 offen lds\n"
"v_mfma_f32_16x16x32_bf16 v[152:155], a[228:231], a[24:27], v[152:155]\nds_read_b64_tr_b16 a[178:179], v229 offset:10240\nv_add_f32 v236, v236, v144\nv_mfma_f32_16x16x32_bf16 v[152:155], a[232:235], a[40:43], v[152:155]\n"
"ds_read_b64_tr_b16 a[180:181], v229 offset:12288\nv_mfma_f32_16x16x32_bf16 v[152:155], a[236:239], a[56:59], v[152:155]\nds_read_b64_tr_b16 a[182:183], v229 offset:14336\nv_mfma_f32_16x16x32_bf16 v[152:155], a[240:243], a[72:75], v[152:155]\n"
"ds_read_b64_tr_b16 a[184:185], v230 offset:8192\nv_mfma_f32_16x16x32_bf16 v[152:155], a[244:247], a[88:91], v[152:155]\nds_read_b64_tr_b16 a[186:187], v230 offset:10240\nv_mfma_f32_16x16x32_bf16 v[152:155], a[248:251], a[104:107], v[152:155]\n"
"s_add_u32 s80, s80, 0x80\nds_read_b64_tr_b16 a[188:189], v230 offset:12288\nv_mfma_f32_16x16x32_bf16 v[152:155], a[252:255], a[120:123], v[152:155]\ns_add_u32 m0, s73, 0x1f000\n"
"ds_read_b64_tr_b16 a[190:191], v230 offset:14336\nv_mfma_f32_16x16x32_bf16 v[156:159], a[252:255], a[124:127], v[220:223]\nbuffer_load_dwordx4 v232, s[52:55], s80 offen lds\nv_mfma_f32_16x16x32_bf16 v[156:159], a[248:251], a[108:111], v[156:159]\n"
"v_exp_f32 v145, v145\nv_mfma_f32_16x16x32_bf16 v[156:159], a[244:247], a[92:95], v[156:159]\nv_exp_f32 v146, v146\nv_mfma_f32_16x16x32_bf16 v[156:159], a[240:243], a[76:79], v[156:159]\n"
"v_exp_f32 v147, v147\nv_mfma_f32_16x16x32_bf16 v[156:159], a[236:239], a[60:63], v[156:159]\nv_exp_f32 v148, v148\nv_mfma_f32_16x16x32_bf16 v[156:159], a[232:235], a[44:47], v[156:159]\n"
"v_exp_f32 v149, v149\nv_mfma_f32_16x16x32_bf16 v[156:159], a[228:231], a[28:31], v[156:159]\nv_exp_f32 v150, v150\nv_mfma_f32_16x16x32_bf16 v[156:159], a[224:227], a[12:15], v[156:159]\n"
"v_exp_f32 v151, v151\ns_waitcnt vmcnt(17) lgkmcnt(0)\ns_barrier\nv_mfma_f32_16x16x32_bf16 v[0:3], a[128:131], v[176:179], v[0:3]\n"
"s_add_u32 s81, s81, 0x0\nds_read_b128 a[192:195], v225 offset:0\nv_cvt_pk_bf16_f32 v162, v144, v145\nv_mfma_f32_16x16x32_bf16 v[0:3], a[132:135], v[192:195], v[0:3]\n"
"s_add_u32 m0, s73, 0x0\nds_read_b128 a[196:199], v226 offset:0\nv_cvt_pk_bf16_f32 v163, v146, v147\nv_mfma_f32_16x16x32_bf16 v[4:7], a[132:135], v[196:199], v[4:7]\n"
"buffer_load_dwordx4 v235, s[56:59], s81 offen lds\nv_mfma_f32_16x16x32_bf16 v[4:7], a[128:131], v[180:183], v[4:7]\ns_add_u32 s81, s81, 0x7000\nds_read_b128 a[200:203], v225 offset:8192\n"
"v_cvt_pk_bf16_f32 v166, v148, v149\nv_mfma_f32_16x16x32_bf16 v[8:11], a[128:131], v[184:187], v[8:11]\ns_add_u32 m0, s73, 0x1000\nds_read_b128 a[204:207], v226 offset:8192\n"
"v_cvt_pk_bf16_f32 v167, v150, v151\nv_mfma_f32_16x16x32_bf16 v[8:11], a[132:135], v[200:203], v[8:11]\nbuffer_load_dwordx4 v235, s[56:59], s81 offen lds\nv_mfma_f32_16x16x32_bf16 v[12:15], a[132:135], v[204:207], v[12:15]\n"
"s_add_u32 s81, s81, 0x15000\nds_read_b128 a[208:211], v225 offset:16384\nv_add_f32 v237, v237, v148\nv_mfma_f32_16x16x32_bf16 v[12:15], a[128:131], v[188:191], v[12:15]\n"
"s_add_u32 m0, s73, 0x2000\nds_read_b128 a[212:215], v226 offset:16384\nv_add_f32 v236, v236, v145\nv_mfma_f32_16x16x32_bf16 v[16:19], a[136:139], v[176:179], v[16:19]\n"
"buffer_load_dwordx4 v235, s[56:59], s81 offen lds\nv_mfma_f32_16x16x32_bf16 v[16:19], a[140:143], v[192:195], v[16:19]\ns_add_u32 s81, s81, 0x7000\nds_read_b128 a[216:219], v225 offset:24576\n"
"v_add_f32 v237, v237, v149\nv_mfma_f32_16x16x32_bf16 v[20:23], a[140:143], v[196:199], v[20:23]\ns_add_u32 m0, s73, 0x3000\nds_read_b128 a[220:223], v226 offset:24576\n"
"v_add_f32 v236, v236, v146\nv_mfma_f32_16x16x32_bf16 v[20:23], a[136:139], v[180:183], v[20:23]\nbuffer_load_dwordx4 v235, s[56:59], s81 offen lds\nv_mfma_f32_16x16x32_bf16 v[24:27], a[136:139], v[184:187], v[24:27]\n"
"s_add_u32 s81, s81, 0x15000\nds_read_b128 a[224:227], v225 offset:2048\nv_add_f32 v237, v237, v150\nv_mfma_f32_16x16x32_bf16 v[24:27], a[140:143], v[200:203], v[24:27]\n"
"s_add_u32 m0, s73, 0x4000\nds_read_b128 a[228:231], v226 offset:2048\nv_add_f32 v236, v236, v147\nv_mfma_f32_16x16x32_bf16 v[28:31], a[140:143], v[204:207], v[28:31]\n"
"buffer_load_dwordx4 v235, s[56:59], s81 offen lds\nv_mfma_f32_16x16x32_bf16 v[28:31], a[136:139], v[188:191], v[28:31]\ns_add_u32 s81, s81, 0x7000\nds_read_b128 a[232:235], v225 offset:10240\n"
"v_add_f32 v237, v237, v151\nv_mfma_f32_16x16x32_bf16 v[32:35], a[144:147], v[176:179], v[32:35]\ns_add_u32 m0, s73, 0x5000\nds_read_b128 a[236:239], v226 offset:10240\n"
"v_mfma_f32_16x16x32_bf16 v[32:35], a[148:151], v[192:195], v[32:35]\nbuffer_load_dwordx4 v235, s[56:59], s81 offen lds\nv_mfma_f32_16x16x32_bf16 v[36:39], a[148:151], v[196:199], v[36:39]\ns_add_u32 s81, s81, 0x15000\n"
"ds_read_b128 a[240:243], v225 offset:18432\nv_mfma_f32_16x16x32_bf16 v[36:39], a[144:147], v[180:183], v[36:39]\ns_add_u32 m0, s73, 0x6000\nds_read_b128 a[244:247], v226 offset:18432\n"
"v_mfma_f32_16x16x32_bf16 v[40:43], a[144:147], v[184:187], v[40:43]\nbuffer_load_dwordx4 v235, s[56:59], s81 offen lds\nv_mfma_f32_16x16x32_bf16 v[40:43], a[148:151], v[200:203], v[40:43]\ns_add_u32 s81, s81, 0x7000\n"
"ds_read_b128 a[248:251], v225 offset:26624\nv_mfma_f32_16x16x32_bf16 v[44:47], a[148:151], v[204:207], v[44:47]\ns_add_u32 m0, s73, 0x7000\nv_exp_f32 v141, v141\n"
"v_mfma_f32_16x16x32_bf16 v[44:47], a[144:147], v[188:191], v[44:47]\nbuffer_load_dwordx4 v235, s[56:59], s81 offen lds\nv_mfma_f32_16x16x32_bf16 v[48:51], a[152:155], v[176:179], v[48:51]\nds_read_b128 a[252:255], v226 offset:26624\n"
"v_add_f32 v239, v239, v141\nv_mfma_f32_16x16x32_bf16 v[48:51], a[156:159], v[192:195], v[48:51]\nv_exp_f32 v138, v138\nv_mfma_f32_16x16x32_bf16 v[52:55], a[156:159], v[196:199], v[52:55]\n"
"v_exp_f32 v139, v139\nv_mfma_f32_16x16x32_bf16 v[52:55], a[152:155], v[180:183], v[52:55]\nv_exp_f32 v152, v152\nv_mfma_f32_16x16x32_bf16 v[56:59], a[152:155], v[184:187], v[56:59]\n"
"v_exp_f32 v153, v153\nv_mfma_f32_16x16x32_bf16 v[56:59], a[156:159], v[200:203], v[56:59]\nv_exp_f32 v154, v154\nv_mfma_f32_16x16x32_bf16 v[60:63], a[156:159], v[204:207], v[60:63]\n"
"v_exp_f32 v155, v155\nv_mfma_f32_16x16x32_bf16 v[60:63], a[152:155], v[188:191], v[60:63]\nv_exp_f32 v142, v142\nv_mfma_f32_16x16x32_bf16 v[64:67], a[160:163], v[176:179], v[64:67]\n"
"v_cvt_pk_bf16_f32 v169, v138, v139\nv_cvt_pk_bf16_f32 v170, v152, v153\nv_mfma_f32_16x16x32_bf16 v[64:67], a[164:167], v[192:195], v[64:67]\nv_cvt_pk_bf16_f32 v171, v154, v155\n"
"v_add_f32 v238, v238, v138\nv_mfma_f32_16x16x32_bf16 v[68:71], a[164:167], v[196:199], v[68:71]\nv_exp_f32 v143, v143\nv_mfma_f32_16x16x32_bf16 v[68:71], a[160:163], v[180:183], v[68:71]\n"
"v_exp_f32 v156, v156\nv_mfma_f32_16x16x32_bf16 v[72:75], a[160:163], v[184:187], v[72:75]\nv_exp_f32 v157, v157\nv_mfma_f32_16x16x32_bf16 v[72:75], a[164:167], v[200:203], v[72:75]\n"
"v_exp_f32 v158, v158\nv_mfma_f32_16x16x32_bf16 v[76:79], a[164:167], v[204:207], v[76:79]\nv_exp_f32 v159, v159\nv_mfma_f32_16x16x32_bf16 v[76:79], a[160:163], v[188:191], v[76:79]\n"
"v_add_f32 v239, v239, v142\nv_cvt_pk_bf16_f32 v172, v140, v141\nv_mfma_f32_16x16x32_bf16 v[80:83], a[168:171], v[176:179], v[80:83]\nv_cvt_pk_bf16_f32 v173, v142, v143\n"
"v_cvt_pk_bf16_f32 v174, v156, v157\nv_mfma_f32_16x16x32_bf16 v[80:83], a[172:175], v[192:195], v[80:83]\nv_cvt_pk_bf16_f32 v175, v158, v159\nv_add_f32 v238, v238, v139\n"
"v_mfma_f32_16x16x32_bf16 v[84:87], a[172:175], v[196:199], v[84:87]\nv_add_f32 v239, v239, v143\nv_add_f32 v238, v238, v152\nv_mfma_f32_16x16x32_bf16 v[84:87], a[168:171], v[180:183], v[84:87]\n"
"v_add_f32 v239, v239, v156\nv_add_f32 v238, v238, v153\nv_mfma_f32_16x16x32_bf16 v[88:91], a[168:171], v[184:187], v[88:91]\nds_write_b128 v233, v[160:163] offset:16384\n"
"v_mfma_f32_16x16x32_bf16 v[88:91], a[172:175], v[200:203], v[88:91]\nv_add_f32 v239, v239, v157\nv_add_f32 v238, v238, v154\nv_mfma_f32_16x16x32_bf16 v[92:95], a[172:175], v[204:207], v[92:95]\n"
"v_add_f32 v239, v239, v158\nv_add_f32 v238, v238, v155\nv_mfma_f32_16x16x32_bf16 v[92:95], a[168:171], v[188:191], v[92:95]\nbuffer_load_dword v248, v249, s[92:95], s84 offen offset:0\n"
"v_mfma_f32_16x16x32_bf16 v[96:99], a[176:179], v[176:179], v[96:99]\nds_write_b128 v233, v[164:167] offset:17408\nv_mfma_f32_16x16x32_bf16 v[96:99], a[180:183], v[192:195], v[96:99]\nv_add_f32 v239, v239, v159\n"
"v_mfma_f32_16x16x32_bf16 v[100:103], a[180:183], v[196:199], v[100:103]\nv_mfma_f32_16x16x32_bf16 v[100:103], a[176:179], v[180:183], v[100:103]\nv_mfma_f32_16x16x32_bf16 v[104:107], a[176:179], v[184:187], v[104:107]\nds_write_b128 v233, v[168:171] offset:18432\n"
"v_mfma_f32_16x16x32_bf16 v[104:107], a[180:183], v[200:203], v[104:107]\nv_mfma_f32_16x16x32_bf16 v[108:111], a[180:183], v[204:207], v[108:111]\nv_mfma_f32_16x16x32_bf16 v[108:111], a[176:179], v[188:191], v[108:111]\nv_mfma_f32_16x16x32_bf16 v[112:115], a[184:187], v[176:179], v[112:115]\n"
"ds_write_b128 v233, v[172:175] offset:19456\nv_mfma_f32_16x16x32_bf16 v[112:115], a[188:191], v[192:195], v[112:115]\nv_mfma_f32_16x16x32_bf16 v[116:119], a[188:191], v[196:199], v[116:119]\nv_mfma_f32_16x16x32_bf16 v[116:119], a[184:187], v[180:183], v[116:119]\n"
"v_mfma_f32_16x16x32_bf16 v[120:123], a[184:187], v[184:187], v[120:123]\nv_mfma_f32_16x16x32_bf16 v[120:123], a[188:191], v[200:203], v[120:123]\nv_mfma_f32_16x16x32_bf16 v[124:127], a[188:191], v[204:207], v[124:127]\nv_mfma_f32_16x16x32_bf16 v[124:127], a[184:187], v[188:191], v[124:127]\n"
"s_waitcnt vmcnt(18) lgkmcnt(0)\ns_barrier\nv_mfma_f32_16x16x32_bf16 v[128:131], a[192:195], a[0:3], v[208:211]\ns_add_u32 s81, s81, 0xfffb3000\n"
"ds_read_b128 v[192:195], v234 offset:16384\ns_add_u32 s84, s84, 0x100000\nv_mfma_f32_16x16x32_bf16 v[128:131], a[196:199], a[16:19], v[128:131]\ns_add_u32 m0, s73, 0x10000\n"
"ds_read_b128 v[196:199], v234 offset:17408\ns_lshl_b32 s87, s72, 2\nv_mfma_f32_16x16x32_bf16 v[128:131], a[200:203], a[32:35], v[128:131]\nbuffer_load_dwordx4 v235, s[56:59], s81 offen lds\n"
"v_mfma_f32_16x16x32_bf16 v[128:131], a[204:207], a[48:51], v[128:131]\ns_add_u32 s87, s87, 0x20000\nbuffer_load_dword v248, v249, s[92:95], s84 offen offset:0\nv_mfma_f32_16x16x32_bf16 v[128:131], a[208:211], a[64:67], v[128:131]\n"
"s_mul_i32 s84, s75, 37\nds_read_b128 v[200:203], v234 offset:18432\ns_lshr_b32 s84, s84, 8\nv_mfma_f32_16x16x32_bf16 v[128:131], a[212:215], a[80:83], v[128:131]\n"
"s_mul_i32 s91, s84, 7\nds_read_b128 v[204:207], v234 offset:19456\ns_sub_u32 s91, s75, s91\nv_mfma_f32_16x16x32_bf16 v[128:131], a[216:219], a[96:99], v[128:131]\n"
"s_lshl_b32 s84, s84, 3\nds_read_b64_tr_b16 a[128:129], v227 offset:32768\ns_add_u32 s84, s84, s74\nv_mfma_f32_16x16x32_bf16 v[128:131], a[220:223], a[112:115], v[128:131]\n"
"s_add_u32 s81, s81, 0x7000\nds_read_b64_tr_b16 a[130:131], v227 offset:34816\ns_mul_i32 s84, s84, 7\nv_mfma_f32_16x16x32_bf16 v[132:135], a[220:223], a[116:119], v[212:215]\n"
"s_add_u32 m0, s73, 0x11000\nds_read_b64_tr_b16 a[132:133], v227 offset:36864\ns_add_u32 s84, s84, s91\nv_mfma_f32_16x16x32_bf16 v[132:135], a[216:219], a[100:103], v[132:135]\n"
"buffer_load_dwordx4 v235, s[56:59], s81 offen lds\nv_mfma_f32_16x16x32_bf16 v[132:135], a[212:215], a[84:87], v[132:135]\ns_lshl_b32 s84, s84, 2\nds_read_b64_tr_b16 a[134:135], v227 offset:38912\n"
"s_add_u32 s84, s84, s72\nv_mfma_f32_16x16x32_bf16 v[132:135], a[208:211], a[68:71], v[132:135]\ns_lshl_b32 s84, s84, 13\nv_exp_f32 v128, v128\n"
"v_mfma_f32_16x16x32_bf16 v[132:135], a[204:207], a[52:55], v[132:135]\ns_and_b32 s91, %1, 7\nv_exp_f32 v129, v129\nv_mfma_f32_16x16x32_bf16 v[132:135], a[200:203], a[36:39], v[132:135]\n"
"v_exp_f32 v130, v130\nv_mfma_f32_16x16x32_bf16 v[132:135], a[196:199], a[20:23], v[132:135]\nv_exp_f32 v131, v131\nv_mfma_f32_16x16x32_bf16 v[132:135], a[192:195], a[4:7], v[132:135]\n"
"s_add_u32 s81, s81, 0x15000\nds_read_b64_tr_b16 a[136:137], v228 offset:32768\nv_add_f32 v236, v236, v128\nv_mfma_f32_16x16x32_bf16 v[136:139], a[192:195], a[8:11], v[216:219]\n"
"s_add_u32 m0, s73, 0x12000\nds_read_b64_tr_b16 a[138:139], v228 offset:34816\nv_add_f32 v236, v236, v129\nv_mfma_f32_16x16x32_bf16 v[136:139], a[196:199], a[24:27], v[136:139]\n"
"buffer_load_dwordx4 v235, s[56:59], s81 offen lds\nv_mfma_f32_16x16x32_bf16 v[136:139], a[200:203], a[40:43], v[136:139]\nds_read_b64_tr_b16 a[140:141], v228 offset:36864\nv_add_f32 v236, v236, v130\n"
"v_mfma_f32_16x16x32_bf16 v[136:139], a[204:207], a[56:59], v[136:139]\nv_exp_f32 v132, v132\nv_mfma_f32_16x16x32_bf16 v[136:139], a[208:211], a[72:75], v[136:139]\nv_exp_f32 v133, v133\n"
"v_mfma_f32_16x16x32_bf16 v[136:139], a[212:215], a[88:91], v[136:139]\nv_exp_f32 v134, v134\nv_mfma_f32_16x16x32_bf16 v[136:139], a[216:219], a[104:107], v[136:139]\nv_exp_f32 v135, v135\n"
"v_mfma_f32_16x16x32_bf16 v[136:139], a[220:223], a[120:123], v[136:139]\ns_add_u32 s81, s81, 0x7000\nds_read_b64_tr_b16 a[142:143], v228 offset:38912\nv_add_f32 v237, v237, v132\n"
"v_mfma_f32_16x16x32_bf16 v[140:143], a[220:223], a[124:127], v[220:223]\ns_add_u32 m0, s73, 0x13000\nds_read_b64_tr_b16 a[144:145], v229 offset:32768\nv_add_f32 v236, v236, v131\n"
"v_mfma_f32_16x16x32_bf16 v[140:143], a[216:219], a[108:111], v[140:143]\nbuffer_load_dwordx4 v235, s[56:59], s81 offen lds\nv_mfma_f32_16x16x32_bf16 v[140:143], a[212:215], a[92:95], v[140:143]\nds_read_b64_tr_b16 a[146:147], v229 offset:34816\n"
"v_add_f32 v237, v237, v133\nv_mfma_f32_16x16x32_bf16 v[140:143], a[208:211], a[76:79], v[140:143]\nv_exp_f32 v136, v136\nv_mfma_f32_16x16x32_bf16 v[140:143], a[204:207], a[60:63], v[140:143]\n"
"v_exp_f32 v137, v137\nv_mfma_f32_16x16x32_bf16 v[140:143], a[200:203], a[44:47], v[140:143]\nv_exp_f32 v138, v138\nv_mfma_f32_16x16x32_bf16 v[140:143], a[196:199], a[28:31], v[140:143]\n"
"v_exp_f32 v139, v139\nv_mfma_f32_16x16x32_bf16 v[140:143], a[192:195], a[12:15], v[140:143]\ns_add_u32 s81, s81, 0x15000\nds_read_b64_tr_b16 a[148:149], v229 offset:36864\n"
"v_add_f32 v238, v238, v136\nv_mfma_f32_16x16x32_bf16 v[144:147], a[224:227], a[0:3], v[208:211]\ns_add_u32 m0, s73, 0x14000\nds_read_b64_tr_b16 a[150:151], v229 offset:38912\n"
"v_add_f32 v237, v237, v134\nv_mfma_f32_16x16x32_bf16 v[144:147], a[228:231], a[16:19], v[144:147]\nbuffer_load_dwordx4 v235, s[56:59], s81 offen lds\nv_mfma_f32_16x16x32_bf16 v[144:147], a[232:235], a[32:35], v[144:147]\n"
"ds_read_b64_tr_b16 a[152:153], v230 offset:32768\nv_add_f32 v238, v238, v137\nv_mfma_f32_16x16x32_bf16 v[144:147], a[236:239], a[48:51], v[144:147]\nds_read_b64_tr_b16 a[154:155], v230 offset:34816\n"
"v_add_f32 v237, v237, v135\nv_mfma_f32_16x16x32_bf16 v[144:147], a[240:243], a[64:67], v[144:147]\nds_read_b64_tr_b16 a[156:157], v230 offset:36864\nv_add_f32 v238, v238, v138\n"
"v_mfma_f32_16x16x32_bf16 v[144:147], a[244:247], a[80:83], v[144:147]\nds_read_b64_tr_b16 a[158:159], v230 offset:38912\nv_add_f32 v238, v238, v139\nv_mfma_f32_16x16x32_bf16 v[144:147], a[248:251], a[96:99], v[144:147]\n"
"s_add_u32 s81, s81, 0x7000\nv_exp_f32 v140, v140\nv_mfma_f32_16x16x32_bf16 v[144:147], a[252:255], a[112:115], v[144:147]\ns_add_u32 m0, s73, 0x15000\n"
"v_exp_f32 v141, v141\nv_mfma_f32_16x16x32_bf16 v[148:151], a[252:255], a[116:119], v[212:215]\nbuffer_load_dwordx4 v235, s[56:59], s81 offen lds\nv_mfma_f32_16x16x32_bf16 v[148:151], a[248:251], a[100:103], v[148:151]\n"
"ds_read_b64_tr_b16 a[160:161], v227 offset:40960\nv_add_f32 v239, v239, v140\nv_mfma_f32_16x16x32_bf16 v[148:151], a[244:247], a[84:87], v[148:151]\nds_read_b64_tr_b16 a[162:163], v227 offset:43008\n"
"v_add_f32 v239, v239, v141\nv_mfma_f32_16x16x32_bf16 v[148:151], a[240:243], a[68:71], v[148:151]\nds_read_b64_tr_b16 a[164:165], v227 offset:45056\nv_cvt_pk_bf16_f32 v176, v128, v129\n"
"v_mfma_f32_16x16x32_bf16 v[148:151], a[236:239], a[52:55], v[148:151]\nds_read_b64_tr_b16 a[166:167], v227 offset:47104\nv_add_u32 v227, 0x10000, v227\nv_mfma_f32_16x16x32_bf16 v[148:151], a[232:235], a[36:39], v[148:151]\n"
"ds_read_b64_tr_b16 a[168:169], v228 offset:40960\nv_cvt_pk_bf16_f32 v177, v130, v131\nv_mfma_f32_16x16x32_bf16 v[148:151], a[228:231], a[20:23], v[148:151]\ns_add_u32 s81, s81, 0x15000\n"
"ds_read_b64_tr_b16 a[170:171], v228 offset:43008\nv_cvt_pk_bf16_f32 v180, v132, v133\nv_mfma_f32_16x16x32_bf16 v[148:151], a[224:227], a[4:7], v[148:151]\ns_add_u32 m0, s73, 0x16000\n"
"ds_read_b64_tr_b16 a[172:173], v228 offset:45056\nv_cvt_pk_bf16_f32 v181, v134, v135\nv_mfma_f32_16x16x32_bf16 v[152:155], a[224:227], a[8:11], v[216:219]\nbuffer_load_dwordx4 v235, s[56:59], s81 offen lds\n"
"v_mfma_f32_16x16x32_bf16 v[152:155], a[228:231], a[24:27], v[152:155]\nv_exp_f32 v144, v144\nv_mfma_f32_16x16x32_bf16 v[152:155], a[232:235], a[40:43], v[152:155]\nv_exp_f32 v145, v145\n"
"v_mfma_f32_16x16x32_bf16 v[152:155], a[236:239], a[56:59], v[152:155]\nv_exp_f32 v146, v146\nv_mfma_f32_16x16x32_bf16 v[152:155], a[240:243], a[72:75], v[152:155]\nv_exp_f32 v147, v147\n"
"v_mfma_f32_16x16x32_bf16 v[152:155], a[244:247], a[88:91], v[152:155]\nds_read_b64_tr_b16 a[174:175], v228 offset:47104\nv_add_f32 v236, v236, v144\nv_mfma_f32_16x16x32_bf16 v[152:155], a[248:251], a[104:107], v[152:155]\n"
"s_add_u32 s81, s81, 0x7000\nds_read_b64_tr_b16 a[176:177], v229 offset:40960\nv_add_f32 v236, v236, v145\nv_mfma_f32_16x16x32_bf16 v[152:155], a[252:255], a[120:123], v[152:155]\n"
"s_add_u32 m0, s73, 0x17000\nds_read_b64_tr_b16 a[178:179], v229 offset:43008\nv_add_f32 v236, v236, v146\nv_mfma_f32_16x16x32_bf16 v[156:159], a[252:255], a[124:127], v[220:223]\n"
"buffer_load_dwordx4 v235, s[56:59], s81 offen lds\nv_mfma_f32_16x16x32_bf16 v[156:159], a[248:251], a[108:111], v[156:159]\nds_read_b64_tr_b16 a[180:181], v229 offset:45056\nv_add_f32 v236, v236, v147\n"
"v_mfma_f32_16x16x32_bf16 v[156:159], a[244:247], a[92:95], v[156:159]\nds_read_b64_tr_b16 a[182:183], v229 offset:47104\nv_mov_b32 v222, v236\nv_mfma_f32_16x16x32_bf16 v[156:159], a[240:243], a[76:79], v[156:159]\n"
"ds_read_b64_tr_b16 a[184:185], v230 offset:40960\nv_add_u32 v228, 0x10000, v228\nv_mfma_f32_16x16x32_bf16 v[156:159], a[236:239], a[60:63], v[156:159]\nds_read_b64_tr_b16 a[186:187], v230 offset:43008\n"
"v_add_u32 v229, 0x10000, v229\nv_mfma_f32_16x16x32_bf16 v[156:159], a[232:235], a[44:47], v[156:159]\nv_exp_f32 v148, v148\nv_mfma_f32_16x16x32_bf16 v[156:159], a[228:231], a[28:31], v[156:159]\n"
"v_exp_f32 v149, v149\nv_mfma_f32_16x16x32_bf16 v[156:159], a[224:227], a[12:15], v[156:159]\nv_exp_f32 v150, v150\nds_read_b64_tr_b16 a[188:189], v230 offset:45056\n"
"ds_read_b64_tr_b16 a[190:191], v230 offset:47104\ns_waitcnt vmcnt(18) lgkmcnt(0)\ns_barrier\nv_mfma_f32_16x16x32_bf16 v[0:3], a[128:131], v[160:163], v[0:3]\n"
"s_add_u32 s81, s81, 0xfff97080\nds_read_b64_tr_b16 a[192:193], v227 offset:32768\nv_exp_f32 v151, v151\nv_mfma_f32_16x16x32_bf16 v[0:3], a[132:135], v[192:195], v[0:3]\n"
"s_add_u32 m0, s73, 0x8000\nds_read_b64_tr_b16 a[194:195], v227 offset:34816\nv_add_f32 v237, v237, v148\nv_add_u32 v230, 0x10000, v230\n"
"v_mfma_f32_16x16x32_bf16 v[4:7], a[132:135], v[196:199], v[4:7]\nbuffer_load_dwordx4 v235, s[56:59], s81 offen lds\nv_mfma_f32_16x16x32_bf16 v[4:7], a[128:131], v[164:167], v[4:7]\ns_add_u32 s81, s81, 0x7000\n"
"ds_read_b64_tr_b16 a[196:197], v227 offset:36864\nv_add_f32 v237, v237, v149\nv_permlane32_swap_b32 v236, v222\nv_mfma_f32_16x16x32_bf16 v[8:11], a[128:131], v[168:171], v[8:11]\n"
"s_add_u32 m0, s73, 0x9000\nds_read_b64_tr_b16 a[198:199], v227 offset:38912\nv_exp_f32 v142, v142\nv_mfma_f32_16x16x32_bf16 v[8:11], a[132:135], v[200:203], v[8:11]\n"
"buffer_load_dwordx4 v235, s[56:59], s81 offen lds\nv_mfma_f32_16x16x32_bf16 v[12:15], a[132:135], v[204:207], v[12:15]\ns_add_u32 s81, s81, 0x15000\nds_read_b64_tr_b16 a[200:201], v228 offset:32768\n"
"v_add_f32 v237, v237, v150\nv_add_f32 v239, v239, v142\nv_mfma_f32_16x16x32_bf16 v[12:15], a[128:131], v[172:175], v[12:15]\ns_add_u32 m0, s73, 0xa000\n"
"ds_read_b64_tr_b16 a[202:203], v228 offset:34816\nv_add_f32 v237, v237, v151\nv_add_f32 v236, v236, v222\nv_mfma_f32_16x16x32_bf16 v[16:19], a[136:139], v[160:163], v[16:19]\n"
"buffer_load_dwordx4 v235, s[56:59], s81 offen lds\nv_mfma_f32_16x16x32_bf16 v[16:19], a[140:143], v[192:195], v[16:19]\ns_add_u32 s81, s81, 0x7000\nds_read_b64_tr_b16 a[204:205], v228 offset:36864\n"
"v_exp_f32 v143, v143\nv_mfma_f32_16x16x32_bf16 v[20:23], a[140:143], v[196:199], v[20:23]\ns_add_u32 m0, s73, 0xb000\nds_read_b64_tr_b16 a[206:207], v228 offset:38912\n"
"v_exp_f32 v152, v152\nv_mfma_f32_16x16x32_bf16 v[20:23], a[136:139], v[164:167], v[20:23]\nbuffer_load_dwordx4 v235, s[56:59], s81 offen lds\nv_mfma_f32_16x16x32_bf16 v[24:27], a[136:139], v[168:171], v[24:27]\n"
"s_add_u32 s81, s81, 0x15000\nds_read_b64_tr_b16 a[208:209], v229 offset:32768\nv_mov_b32 v223, v237\nv_add_f32 v238, v238, v152\n"
"v_mfma_f32_16x16x32_bf16 v[24:27], a[140:143], v[200:203], v[24:27]\ns_add_u32 m0, s73, 0xc000\nds_read_b64_tr_b16 a[210:211], v229 offset:34816\nv_exp_f32 v153, v153\n"
"v_mfma_f32_16x16x32_bf16 v[28:31], a[140:143], v[204:207], v[28:31]\nbuffer_load_dwordx4 v235, s[56:59], s81 offen lds\nv_mfma_f32_16x16x32_bf16 v[28:31], a[136:139], v[172:175], v[28:31]\ns_add_u32 s81, s81, 0x7000\n"
"ds_read_b64_tr_b16 a[212:213], v229 offset:36864\nv_add_f32 v239, v239, v143\nv_permlane32_swap_b32 v237, v223\nv_mfma_f32_16x16x32_bf16 v[32:35], a[144:147], v[160:163], v[32:35]\n"
"s_add_u32 m0, s73, 0xd000\nds_read_b64_tr_b16 a[214:215], v229 offset:38912\nv_add_f32 v238, v238, v153\nv_add_f32 v237, v237, v223\n"
"v_mfma_f32_16x16x32_bf16 v[32:35], a[148:151], v[192:195], v[32:35]\nbuffer_load_dwordx4 v235, s[56:59], s81 offen lds\nv_mfma_f32_16x16x32_bf16 v[36:39], a[148:151], v[196:199], v[36:39]\ns_add_u32 s81, s81, 0x15000\n"
"ds_read_b64_tr_b16 a[216:217], v230 offset:32768\nv_exp_f32 v154, v154\nv_mfma_f32_16x16x32_bf16 v[36:39], a[144:147], v[164:167], v[36:39]\ns_add_u32 m0, s73, 0xe000\n"
"ds_read_b64_tr_b16 a[218:219], v230 offset:34816\nv_exp_f32 v155, v155\nv_mfma_f32_16x16x32_bf16 v[40:43], a[144:147], v[168:171], v[40:43]\nbuffer_load_dwordx4 v235, s[56:59], s81 offen lds\n"
"v_mfma_f32_16x16x32_bf16 v[40:43], a[148:151], v[200:203], v[40:43]\ns_add_u32 s81, s81, 0x7000\nds_read_b64_tr_b16 a[220:221], v230 offset:36864\nv_exp_f32 v156, v156\n"
"v_mfma_f32_16x16x32_bf16 v[44:47], a[148:151], v[204:207], v[44:47]\ns_add_u32 m0, s73, 0xf000\nds_read_b64_tr_b16 a[222:223], v230 offset:38912\nv_exp_f32 v157, v157\n"
"v_mfma_f32_16x16x32_bf16 v[44:47], a[144:147], v[172:175], v[44:47]\nbuffer_load_dwordx4 v235, s[56:59], s81 offen lds\nv_mfma_f32_16x16x32_bf16 v[48:51], a[152:155], v[160:163], v[48:51]\nds_read_b64_tr_b16 a[224:225], v227 offset:40960\n"
"v_exp_f32 v158, v158\nv_mfma_f32_16x16x32_bf16 v[48:51], a[156:159], v[192:195], v[48:51]\nds_read_b64_tr_b16 a[226:227], v227 offset:43008\nv_add_f32 v239, v239, v156\n"
"v_add_f32 v238, v238, v154\nv_mfma_f32_16x16x32_bf16 v[52:55], a[156:159], v[196:199], v[52:55]\nds_read_b64_tr_b16 a[228:229], v227 offset:45056\nv_exp_f32 v159, v159\n"
"v_mfma_f32_16x16x32_bf16 v[52:55], a[152:155], v[164:167], v[52:55]\nds_read_b64_tr_b16 a[230:231], v227 offset:47104\nv_add_f32 v239, v239, v157\nv_mov_b32 v222, v236\n"
"v_mfma_f32_16x16x32_bf16 v[56:59], a[152:155], v[168:171], v[56:59]\nds_read_b64_tr_b16 a[232:233], v228 offset:40960\nv_mov_b32 v223, v237\nv_add_f32 v239, v239, v158\n"
"v_mfma_f32_16x16x32_bf16 v[56:59], a[156:159], v[200:203], v[56:59]\nds_read_b64_tr_b16 a[234:235], v228 offset:43008\nv_add_f32 v238, v238, v155\nv_add_f32 v239, v239, v159\n"
"v_mfma_f32_16x16x32_bf16 v[60:63], a[156:159], v[204:207], v[60:63]\nds_read_b64_tr_b16 a[236:237], v228 offset:45056\nv_permlane16_swap_b32 v236, v222\nv_permlane16_swap_b32 v237, v223\n"
"v_mfma_f32_16x16x32_bf16 v[60:63], a[152:155], v[172:175], v[60:63]\nds_read_b64_tr_b16 a[238:239], v228 offset:47104\nv_add_f32 v236, v236, v222\nv_add_f32 v237, v237, v223\n"
"v_mfma_f32_16x16x32_bf16 v[64:67], a[160:163], v[160:163], v[64:67]\nds_read_b64_tr_b16 a[240:241], v229 offset:40960\nv_mov_b32 v222, v238\nv_mov_b32 v223, v239\n"
"v_mfma_f32_16x16x32_bf16 v[64:67], a[164:167], v[192:195], v[64:67]\nds_read_b64_tr_b16 a[242:243], v229 offset:43008\nv_permlane32_swap_b32 v238, v222\nv_permlane32_swap_b32 v239, v223\n"
"v_mfma_f32_16x16x32_bf16 v[68:71], a[164:167], v[196:199], v[68:71]\nds_read_b64_tr_b16 a[244:245], v229 offset:45056\nv_add_f32 v238, v238, v222\nv_add_f32 v239, v239, v223\n"
"v_mfma_f32_16x16x32_bf16 v[68:71], a[160:163], v[164:167], v[68:71]\nds_read_b64_tr_b16 a[246:247], v229 offset:47104\nv_mov_b32 v222, v238\nv_mov_b32 v223, v239\n"
"v_mfma_f32_16x16x32_bf16 v[72:75], a[160:163], v[168:171], v[72:75]\nds_read_b64_tr_b16 a[248:249], v230 offset:40960\nv_permlane16_swap_b32 v238, v222\nv_permlane16_swap_b32 v239, v223\n"
"v_mfma_f32_16x16x32_bf16 v[72:75], a[164:167], v[200:203], v[72:75]\nds_read_b64_tr_b16 a[250:251], v230 offset:43008\nv_add_f32 v238, v238, v222\nv_add_f32 v239, v239, v223\n"
"v_mfma_f32_16x16x32_bf16 v[76:79], a[164:167], v[204:207], v[76:79]\nds_write_b128 v245, v[236:239] offset:16384\nv_add_f32 v242, v236, v237\nv_mfma_f32_16x16x32_bf16 v[76:79], a[160:163], v[172:175], v[76:79]\n"
"ds_read_b64_tr_b16 a[252:253], v230 offset:45056\nbuffer_load_dword v248, v249, s[60:63], s84 offen sc1\nv_mfma_f32_16x16x32_bf16 v[80:83], a[168:171], v[160:163], v[80:83]\ns_lshl_b32 s84, s72, 3\n"
"ds_read_b64_tr_b16 a[254:255], v230 offset:47104\nv_add_f32 v243, v238, v239\ns_add_u32 s84, s84, s91\nv_mfma_f32_16x16x32_bf16 v[80:83], a[172:175], v[192:195], v[80:83]\n"
"s_mul_i32 s84, s84, 7\ns_lshr_b32 s91, %1, 3\ns_min_u32 s91, s91, 6\ns_add_u32 s84, s84, s91\n"
"v_mfma_f32_16x16x32_bf16 v[84:87], a[172:175], v[196:199], v[84:87]\ns_lshl_b32 s84, s84, 13\nv_add_u32 v240, s84, v249\nv_add_f32 v242, v242, v243\n"
"v_add_u32 v243, s87, v224\nv_mfma_f32_16x16x32_bf16 v[84:87], a[168:171], v[164:167], v[84:87]\ns_add_u32 s87, s72, 0\nds_write_b32 v243, v242 offset:20480\n"
"v_cvt_pk_bf16_f32 v184, v136, v137\nv_mfma_f32_16x16x32_bf16 v[88:91], a[168:171], v[168:171], v[88:91]\ns_and_b32 s87, s87, 3\nglobal_load_dword v248, v240, s[8:9]\n"
"v_cvt_pk_bf16_f32 v185, v138, v139\nv_mfma_f32_16x16x32_bf16 v[88:91], a[172:175], v[200:203], v[88:91]\ns_lshl_b32 s87, s87, 13\nv_cvt_pk_bf16_f32 v188, v140, v141\n"
"v_cvt_pk_bf16_f32 v189, v142, v143\nv_cvt_pk_bf16_f32 v178, v144, v145\nv_mfma_f32_16x16x32_bf16 v[92:95], a[172:175], v[204:207], v[92:95]\nv_cvt_pk_bf16_f32 v179, v146, v147\n"
"v_cvt_pk_bf16_f32 v182, v148, v149\nv_cvt_pk_bf16_f32 v183, v150, v151\nv_mfma_f32_16x16x32_bf16 v[92:95], a[168:171], v[172:175], v[92:95]\nds_write_b128 v233, v[176:179] offset:0\n"
"v_cvt_pk_bf16_f32 v186, v152, v153\nv_mfma_f32_16x16x32_bf16 v[96:99], a[176:179], v[160:163], v[96:99]\nv_cvt_pk_bf16_f32 v187, v154, v155\nv_cvt_pk_bf16_f32 v190, v156, v157\n"
"v_cvt_pk_bf16_f32 v191, v158, v159\nv_mfma_f32_16x16x32_bf16 v[96:99], a[180:183], v[192:195], v[96:99]\nds_write_b128 v233, v[180:183] offset:1024\nv_mfma_f32_16x16x32_bf16 v[100:103], a[180:183], v[196:199], v[100:103]\n"
"v_bfe_u32 v220, v254, 3, 2\nv_and_b32 v221, 3, v254\nv_mfma_f32_16x16x32_bf16 v[100:103], a[176:179], v[164:167], v[100:103]\nds_write_b128 v233, v[184:187] offset:2048\n"
"v_mfma_f32_16x16x32_bf16 v[104:107], a[176:179], v[168:171], v[104:107]\nv_xor_b32 v220, s72, v220\nv_mfma_f32_16x16x32_bf16 v[104:107], a[180:183], v[200:203], v[104:107]\nds_write_b128 v233, v[188:191] offset:3072\n"
"v_mfma_f32_16x16x32_bf16 v[108:111], a[180:183], v[204:207], v[108:111]\nv_lshlrev_b32 v220, 5, v220\nv_mfma_f32_16x16x32_bf16 v[108:111], a[176:179], v[172:175], v[108:111]\nv_lshl_add_u32 v220, v221, 3, v220\n"
"v_lshrrev_b32 v221, 2, v254\nv_mfma_f32_16x16x32_bf16 v[112:115], a[184:187], v[160:163], v[112:115]\nv_lshl_add_u32 v220, v221, 7, v220\nv_mfma_f32_16x16x32_bf16 v[112:115], a[188:191], v[192:195], v[112:115]\n"
"v_add_u32 v208, s87, v220\ns_add_u32 s87, s87, 0x18000\nv_mfma_f32_16x16x32_bf16 v[116:119], a[188:191], v[196:199], v[116:119]\nv_add_u32 v219, s87, v224\n"
"s_add_u32 s87, s72, 1\ns_and_b32 s87, s87, 3\nv_mfma_f32_16x16x32_bf16 v[116:119], a[184:187], v[164:167], v[116:119]\ns_lshl_b32 s87, s87, 13\n"
"v_add_u32 v209, s87, v220\ns_add_u32 s87, s87, 0x18000\nv_add_u32 v216, s87, v224\nv_mfma_f32_16x16x32_bf16 v[120:123], a[184:187], v[168:171], v[120:123]\n"
"s_add_u32 s87, s72, 2\ns_and_b32 s87, s87, 3\ns_lshl_b32 s87, s87, 13\nv_add_u32 v210, s87, v220\n"
"v_mfma_f32_16x16x32_bf16 v[120:123], a[188:191], v[200:203], v[120:123]\ns_add_u32 s87, s87, 0x18000\nv_add_u32 v217, s87, v224\nv_add_u32 v212, 0x10000, v208\n"
"v_add_u32 v213, 0x10000, v209\nv_mfma_f32_16x16x32_bf16 v[124:127], a[188:191], v[204:207], v[124:127]\ns_add_u32 s87, s72, 3\ns_and_b32 s87, s87, 3\n"
"s_lshl_b32 s87, s87, 13\nv_add_u32 v211, s87, v220\nv_mfma_f32_16x16x32_bf16 v[124:127], a[184:187], v[172:175], v[124:127]\ns_add_u32 s87, s87, 0x18000\n"
"v_add_u32 v214, 0x10000, v210\nv_add_u32 v215, 0x10000, v211\nv_add_u32 v218, s87, v224\ns_waitcnt vmcnt(20) lgkmcnt(0)\n"
"s_barrier\nv_mfma_f32_16x16x32_bf16 v[0:3], a[192:195], v[176:179], v[0:3]\nds_read_b128 v[192:195], v234 offset:0\nv_xor_b32 v164, 0x400, v245\n"
"v_mfma_f32_16x16x32_bf16 v[4:7], a[192:195], v[180:183], v[4:7]\nds_read_b128 v[160:163], v164 offset:16384\nv_and_b32 v253, 0xfffff3ff, v245\nv_mfma_f32_16x16x32_bf16 v[8:11], a[192:195], v[184:187], v[8:11]\n"
"ds_read_b128 v[196:199], v234 offset:1024\nv_mfma_f32_16x16x32_bf16 v[12:15], a[192:195], v[188:191], v[12:15]\nds_read_b128 v[200:203], v234 offset:2048\nv_mfma_f32_16x16x32_bf16 v[16:19], a[200:203], v[176:179], v[16:19]\n"
"ds_read_b128 v[204:207], v234 offset:3072\nv_mfma_f32_16x16x32_bf16 v[20:23], a[200:203], v[180:183], v[20:23]\nds_read_b128 v[220:223], v253 offset:20480\nv_mfma_f32_16x16x32_bf16 v[24:27], a[200:203], v[184:187], v[24:27]\n"
"ds_read_b64_tr_b16 v[128:129], v208 offset:0\nv_mfma_f32_16x16x32_bf16 v[28:31], a[200:203], v[188:191], v[28:31]\nds_read_b64_tr_b16 v[130:131], v208 offset:2048\nv_mfma_f32_16x16x32_bf16 v[32:35], a[208:211], v[176:179], v[32:35]\n"
"ds_read_b64_tr_b16 v[132:133], v208 offset:4096\nv_mfma_f32_16x16x32_bf16 v[36:39], a[208:211], v[180:183], v[36:39]\nds_read_b64_tr_b16 v[134:135], v208 offset:6144\nv_mfma_f32_16x16x32_bf16 v[40:43], a[208:211], v[184:187], v[40:43]\n"
"ds_read_b64_tr_b16 v[136:137], v209 offset:0\ns_waitcnt lgkmcnt(7)\nv_add_f32 v240, v160, v236\nv_add_f32 v241, v161, v237\n"
"v_add_f32 v242, v162, v238\nv_mfma_f32_16x16x32_bf16 v[44:47], a[208:211], v[188:191], v[44:47]\nds_read_b64_tr_b16 v[138:139], v209 offset:2048\nv_add_f32 v243, v163, v239\n"
"v_rcp_f32 v240, v240\nv_mfma_f32_16x16x32_bf16 v[0:3], a[196:199], v[192:195], v[0:3]\nds_read_b64_tr_b16 v[140:141], v209 offset:4096\nv_rcp_f32 v241, v241\n"
"v_mfma_f32_16x16x32_bf16 v[4:7], a[196:199], v[196:199], v[4:7]\nds_read_b64_tr_b16 v[142:143], v209 offset:6144\nv_rcp_f32 v242, v242\nv_mfma_f32_16x16x32_bf16 v[8:11], a[196:199], v[200:203], v[8:11]\n"
"ds_read_b64_tr_b16 v[144:145], v210 offset:0\nv_rcp_f32 v243, v243\ns_waitcnt lgkmcnt(7)\nv_mfma_f32_16x16x32_bf16 v[12:15], a[196:199], v[204:207], v[12:15]\n"
"ds_read_b64_tr_b16 v[146:147], v210 offset:2048\nv_add_f32 v220, v220, v221\nv_mfma_f32_16x16x32_bf16 v[16:19], a[204:207], v[192:195], v[16:19]\nds_read_b64_tr_b16 v[148:149], v210 offset:4096\n"
"v_mul_f32 v0, v0, v240\nv_mul_f32 v1, v1, v240\nv_mul_f32 v2, v2, v240\nv_mfma_f32_16x16x32_bf16 v[20:23], a[204:207], v[196:199], v[20:23]\n"
"ds_read_b64_tr_b16 v[150:151], v210 offset:6144\nv_mul_f32 v3, v3, v240\nv_cvt_pk_bf16_f32 v0, v0, v1\nv_mul_f32 v4, v4, v241\n"
"v_mfma_f32_16x16x32_bf16 v[24:27], a[204:207], v[200:203], v[24:27]\nds_read_b64_tr_b16 v[152:153], v211 offset:0\nv_mul_f32 v5, v5, v241\nv_cvt_pk_bf16_f32 v1, v2, v3\n"
"v_mul_f32 v6, v6, v241\nv_mfma_f32_16x16x32_bf16 v[28:31], a[204:207], v[204:207], v[28:31]\nds_read_b64_tr_b16 v[154:155], v211 offset:2048\nv_mul_f32 v7, v7, v241\n"
"v_mul_f32 v8, v8, v242\nv_cvt_pk_bf16_f32 v4, v4, v5\nv_mfma_f32_16x16x32_bf16 v[32:35], a[212:215], v[192:195], v[32:35]\nds_read_b64_tr_b16 v[156:157], v211 offset:4096\n"
"v_mul_f32 v16, v16, v240\nv_mul_f32 v17, v17, v240\nv_mul_f32 v18, v18, v240\nv_mfma_f32_16x16x32_bf16 v[36:39], a[212:215], v[196:199], v[36:39]\n"
"ds_read_b64_tr_b16 v[158:159], v211 offset:6144\nv_mul_f32 v19, v19, v240\nv_cvt_pk_bf16_f32 v2, v16, v17\nv_mul_f32 v20, v20, v241\n"
"v_mfma_f32_16x16x32_bf16 v[40:43], a[212:215], v[200:203], v[40:43]\nv_cvt_pk_bf16_f32 v3, v18, v19\nv_mul_f32 v21, v21, v241\nv_mul_f32 v22, v22, v241\n"
"v_mul_f32 v23, v23, v241\nv_mfma_f32_16x16x32_bf16 v[44:47], a[212:215], v[204:207], v[44:47]\nds_write_b128 v219, v[0:3] offset:0\nv_mul_f32 v9, v9, v242\n"
"v_mul_f32 v10, v10, v242\nv_mfma_f32_16x16x32_bf16 v[48:51], a[216:219], v[176:179], v[48:51]\nv_mul_f32 v11, v11, v242\nv_cvt_pk_bf16_f32 v5, v6, v7\n"
"v_mul_f32 v24, v24, v242\nv_mul_f32 v25, v25, v242\nv_mfma_f32_16x16x32_bf16 v[48:51], a[220:223], v[192:195], v[48:51]\nv_cvt_pk_bf16_f32 v8, v8, v9\n"
"v_mul_f32 v12, v12, v243\nv_mul_f32 v13, v13, v243\nv_mul_f32 v26, v26, v242\nv_mfma_f32_16x16x32_bf16 v[52:55], a[220:223], v[196:199], v[52:55]\n"
"v_mul_f32 v27, v27, v242\nv_cvt_pk_bf16_f32 v6, v20, v21\nv_cvt_pk_bf16_f32 v9, v10, v11\nv_mul_f32 v14, v14, v243\n"
"v_mfma_f32_16x16x32_bf16 v[52:55], a[216:219], v[180:183], v[52:55]\nv_mul_f32 v15, v15, v243\nv_mul_f32 v32, v32, v240\nv_mul_f32 v33, v33, v240\n"
"v_cvt_pk_bf16_f32 v7, v22, v23\nv_mfma_f32_16x16x32_bf16 v[56:59], a[216:219], v[184:187], v[56:59]\nds_write_b128 v219, v[4:7] offset:1024\nv_mul_f32 v28, v28, v243\n"
"v_mul_f32 v29, v29, v243\nv_mfma_f32_16x16x32_bf16 v[56:59], a[220:223], v[200:203], v[56:59]\nv_cvt_pk_bf16_f32 v12, v12, v13\nv_mul_f32 v36, v36, v241\n"
"v_mul_f32 v37, v37, v241\nv_mul_f32 v30, v30, v243\nv_mfma_f32_16x16x32_bf16 v[60:63], a[220:223], v[204:207], v[60:63]\nv_mul_f32 v31, v31, v243\n"
"v_mul_f32 v34, v34, v240\nv_mul_f32 v35, v35, v240\nv_cvt_pk_bf16_f32 v10, v24, v25\nv_mfma_f32_16x16x32_bf16 v[60:63], a[216:219], v[188:191], v[60:63]\n"
"v_cvt_pk_bf16_f32 v11, v26, v27\nv_mul_f32 v38, v38, v241\nv_mul_f32 v39, v39, v241\nv_cvt_pk_bf16_f32 v13, v14, v15\n"
"v_mfma_f32_16x16x32_bf16 v[64:67], a[224:227], v[176:179], v[64:67]\nds_write_b128 v219, v[8:11] offset:2048\nv_cvt_pk_bf16_f32 v32, v32, v33\nv_cvt_pk_bf16_f32 v36, v36, v37\n"
"v_mfma_f32_16x16x32_bf16 v[64:67], a[228:231], v[192:195], v[64:67]\nv_mul_f32 v48, v48, v240\nv_mul_f32 v49, v49, v240\nv_mul_f32 v50, v50, v240\n"
"v_mul_f32 v51, v51, v240\nv_mfma_f32_16x16x32_bf16 v[68:71], a[228:231], v[196:199], v[68:71]\nv_mul_f32 v40, v40, v242\nv_mul_f32 v41, v41, v242\n"
"v_cvt_pk_bf16_f32 v14, v28, v29\nv_cvt_pk_bf16_f32 v15, v30, v31\nv_mfma_f32_16x16x32_bf16 v[68:71], a[224:227], v[180:183], v[68:71]\nds_write_b128 v219, v[12:15] offset:3072\n"
"v_cvt_pk_bf16_f32 v33, v34, v35\nv_cvt_pk_bf16_f32 v37, v38, v39\nv_mfma_f32_16x16x32_bf16 v[72:75], a[224:227], v[184:187], v[72:75]\nv_mul_f32 v52, v52, v241\n"
"v_mul_f32 v53, v53, v241\nv_mul_f32 v54, v54, v241\nv_mul_f32 v55, v55, v241\nv_mfma_f32_16x16x32_bf16 v[72:75], a[228:231], v[200:203], v[72:75]\n"
"v_mul_f32 v42, v42, v242\nv_mul_f32 v43, v43, v242\nv_cvt_pk_bf16_f32 v34, v48, v49\nv_cvt_pk_bf16_f32 v35, v50, v51\n"
"v_mfma_f32_16x16x32_bf16 v[76:79], a[228:231], v[204:207], v[76:79]\nds_write_b128 v219, v[32:35] offset:4096\nv_cvt_pk_bf16_f32 v38, v52, v53\nv_cvt_pk_bf16_f32 v40, v40, v41\n"
"v_mfma_f32_16x16x32_bf16 v[76:79], a[224:227], v[188:191], v[76:79]\nv_cvt_pk_bf16_f32 v39, v54, v55\nv_mul_f32 v56, v56, v242\nv_mul_f32 v57, v57, v242\n"
"v_mul_f32 v58, v58, v242\nv_mfma_f32_16x16x32_bf16 v[80:83], a[232:235], v[176:179], v[80:83]\nds_write_b128 v219, v[36:39] offset:5120\nv_mul_f32 v59, v59, v242\n"
"v_cvt_pk_bf16_f32 v41, v42, v43\nv_mfma_f32_16x16x32_bf16 v[80:83], a[236:239], v[192:195], v[80:83]\nv_cvt_pk_bf16_f32 v42, v56, v57\nv_cvt_pk_bf16_f32 v43, v58, v59\n"
"v_mul_f32 v44, v44, v243\nv_mul_f32 v45, v45, v243\nv_mfma_f32_16x16x32_bf16 v[84:87], a[236:239], v[196:199], v[84:87]\nds_write_b128 v219, v[40:43] offset:6144\n"
"v_mul_f32 v46, v46, v243\nv_mul_f32 v47, v47, v243\nv_mfma_f32_16x16x32_bf16 v[84:87], a[232:235], v[180:183], v[84:87]\nv_mul_f32 v60, v60, v243\n"
"v_mul_f32 v61, v61, v243\nv_mul_f32 v62, v62, v243\nv_mul_f32 v63, v63, v243\nv_mfma_f32_16x16x32_bf16 v[88:91], a[232:235], v[184:187], v[88:91]\n"
"v_cvt_pk_bf16_f32 v44, v44, v45\nv_cvt_pk_bf16_f32 v45, v46, v47\nv_cvt_pk_bf16_f32 v46, v60, v61\nv_cvt_pk_bf16_f32 v47, v62, v63\n"
"v_mfma_f32_16x16x32_bf16 v[88:91], a[236:239], v[200:203], v[88:91]\nds_write_b128 v219, v[44:47] offset:7168\nv_mul_f32 v64, v64, v240\nv_mul_f32 v65, v65, v240\n"
"v_mfma_f32_16x16x32_bf16 v[92:95], a[236:239], v[204:207], v[92:95]\nv_mul_f32 v68, v68, v241\nv_add_f32 v222, v222, v223\nv_mul_f32 v69, v69, v241\n"
"v_mul_f32 v66, v66, v240\nv_mfma_f32_16x16x32_bf16 v[92:95], a[232:235], v[188:191], v[92:95]\nv_mul_f32 v72, v72, v242\nv_mul_f32 v73, v73, v242\n"
"v_mul_f32 v67, v67, v240\nv_cvt_pk_bf16_f32 v64, v64, v65\nv_mfma_f32_16x16x32_bf16 v[96:99], a[240:243], v[176:179], v[96:99]\nv_mul_f32 v70, v70, v241\n"
"v_mul_f32 v71, v71, v241\nv_cvt_pk_bf16_f32 v68, v68, v69\nv_mul_f32 v74, v74, v242\nv_mfma_f32_16x16x32_bf16 v[96:99], a[244:247], v[192:195], v[96:99]\n"
"v_mul_f32 v75, v75, v242\nv_mul_f32 v76, v76, v243\nv_mul_f32 v77, v77, v243\nv_add_f32 v221, v220, v222\n"
"v_mfma_f32_16x16x32_bf16 v[100:103], a[244:247], v[196:199], v[100:103]\nv_mul_f32 v80, v80, v240\nv_cvt_pk_bf16_f32 v72, v72, v73\nv_mul_f32 v81, v81, v240\n"
"v_mul_f32 v82, v82, v240\nv_mfma_f32_16x16x32_bf16 v[100:103], a[240:243], v[180:183], v[100:103]\nv_mul_f32 v83, v83, v240\nv_mul_f32 v84, v84, v241\n"
"v_cvt_pk_bf16_f32 v65, v66, v67\nv_mul_f32 v85, v85, v241\nv_mfma_f32_16x16x32_bf16 v[104:107], a[240:243], v[184:187], v[104:107]\nv_mul_f32 v86, v86, v241\n"
"v_mul_f32 v87, v87, v241\nv_cvt_pk_bf16_f32 v69, v70, v71\nv_mul_f32 v88, v88, v242\nv_mfma_f32_16x16x32_bf16 v[104:107], a[244:247], v[200:203], v[104:107]\n"
"v_mul_f32 v89, v89, v242\nv_mul_f32 v90, v90, v242\nv_mul_f32 v91, v91, v242\nv_mul_f32 v78, v78, v243\n"
"v_mfma_f32_16x16x32_bf16 v[108:111], a[244:247], v[204:207], v[108:111]\nv_mul_f32 v79, v79, v243\nv_mul_f32 v96, v96, v240\nv_cvt_pk_bf16_f32 v73, v74, v75\n"
"v_cvt_pk_bf16_f32 v76, v76, v77\nv_mfma_f32_16x16x32_bf16 v[108:111], a[240:243], v[188:191], v[108:111]\nv_mul_f32 v97, v97, v240\nv_mul_f32 v100, v100, v241\n"
"v_mul_f32 v92, v92, v243\nv_mul_f32 v93, v93, v243\nv_mfma_f32_16x16x32_bf16 v[112:115], a[248:251], v[176:179], v[112:115]\nv_mul_f32 v94, v94, v243\n"
"v_mul_f32 v101, v101, v241\nv_cmp_ngt_f32 vcc, s90, v221\nv_mul_f32 v95, v95, v243\nv_mfma_f32_16x16x32_bf16 v[112:115], a[252:255], v[192:195], v[112:115]\n"
"s_nop 2\ns_and_b64 vcc, vcc, s[96:97]\ns_cbranch_scc1 L_fb%=\nv_mul_f32 v98, v98, v240\nv_mul_f32 v99, v99, v240\n"
"v_cvt_pk_bf16_f32 v66, v80, v81\nv_cvt_pk_bf16_f32 v67, v82, v83\nv_mfma_f32_16x16x32_bf16 v[116:119], a[252:255], v[196:199], v[116:119]\nv_cvt_pk_bf16_f32 v70, v84, v85\n"
"v_cvt_pk_bf16_f32 v71, v86, v87\nv_cvt_pk_bf16_f32 v74, v88, v89\nv_cvt_pk_bf16_f32 v75, v90, v91\nv_mfma_f32_16x16x32_bf16 v[116:119], a[248:251], v[180:183], v[116:119]\n"
"v_cvt_pk_bf16_f32 v77, v78, v79\nv_cvt_pk_bf16_f32 v96, v96, v97\nv_mul_f32 v102, v102, v241\nv_mul_f32 v103, v103, v241\n"
"v_mfma_f32_16x16x32_bf16 v[120:123], a[248:251], v[184:187], v[120:123]\nv_mul_f32 v104, v104, v242\nv_mul_f32 v105, v105, v242\nv_cvt_pk_bf16_f32 v100, v100, v101\n"
"v_cvt_pk_bf16_f32 v78, v92, v93\nv_mfma_f32_16x16x32_bf16 v[120:123], a[252:255], v[200:203], v[120:123]\nv_mul_f32 v112, v112, v240\nv_mul_f32 v113, v113, v240\n"
"v_mul_f32 v114, v114, v240\nv_cvt_pk_bf16_f32 v79, v94, v95\nv_mfma_f32_16x16x32_bf16 v[124:127], a[252:255], v[204:207], v[124:127]\nv_mul_f32 v115, v115, v240\n"
"v_cvt_pk_bf16_f32 v97, v98, v99\nv_cvt_pk_bf16_f32 v101, v102, v103\nv_mul_f32 v106, v106, v242\nv_mfma_f32_16x16x32_bf16 v[124:127], a[248:251], v[188:191], v[124:127]\n"
"v_mul_f32 v116, v116, v241\nv_mul_f32 v117, v117, v241\nv_mul_f32 v118, v118, v241\nv_mul_f32 v119, v119, v241\n"
"s_waitcnt vmcnt(10) lgkmcnt(0)\ns_barrier\nv_mfma_f32_16x16x32_bf16 v[16:19], v[128:131], v[0:3], 0\nds_write_b128 v219, v[64:67] offset:32768\n"
"ds_read_b128 a[0:3], v216 offset:0\nds_read_b128 a[4:7], v216 offset:1024\nv_mfma_f32_16x16x32_bf16 v[20:23], v[128:131], v[4:7], 0\nds_read_b128 a[8:11], v216 offset:2048\n"
"ds_read_b128 a[12:15], v216 offset:3072\nds_read_b128 a[16:19], v216 offset:4096\nv_mul_f32 v107, v107, v242\nv_mfma_f32_16x16x32_bf16 v[24:27], v[128:131], v[8:11], 0\n"
"ds_write_b128 v219, v[68:71] offset:33792\nds_read_b128 a[20:23], v216 offset:5120\nds_read_b128 a[24:27], v216 offset:6144\nv_mfma_f32_16x16x32_bf16 v[28:31], v[128:131], v[12:15], 0\n"
"ds_read_b128 a[28:31], v216 offset:7168\nv_cvt_pk_bf16_f32 v104, v104, v105\nv_cvt_pk_bf16_f32 v98, v112, v113\nv_mul_f32 v108, v108, v243\n"
"v_mfma_f32_16x16x32_bf16 v[16:19], v[132:135], v[32:35], v[16:19]\nds_write_b128 v219, v[72:75] offset:34816\nv_mul_f32 v109, v109, v243\nv_cvt_pk_bf16_f32 v99, v114, v115\n"
"v_mfma_f32_16x16x32_bf16 v[20:23], v[132:135], v[36:39], v[20:23]\nds_read_b128 a[32:35], v217 offset:0\nv_mul_f32 v120, v120, v242\nv_mul_f32 v121, v121, v242\n"
"v_mul_f32 v122, v122, v242\nv_mfma_f32_16x16x32_bf16 v[24:27], v[132:135], v[40:43], v[24:27]\nds_write_b128 v219, v[76:79] offset:35840\nv_mul_f32 v123, v123, v242\n"
"v_cvt_pk_bf16_f32 v102, v116, v117\nv_mfma_f32_16x16x32_bf16 v[28:31], v[132:135], v[44:47], v[28:31]\nds_read_b128 a[36:39], v217 offset:1024\nv_cvt_pk_bf16_f32 v103, v118, v119\n"
"v_cvt_pk_bf16_f32 v105, v106, v107\nv_mul_f32 v110, v110, v243\ns_waitcnt lgkmcnt(11)\nv_mfma_f32_16x16x32_bf16 v[16:19], v[136:139], a[0:3], v[16:19]\n"
"s_add_u32 s81, s81, 0xfffb3000\nds_write_b128 v219, v[96:99] offset:36864\nds_read_b128 a[40:43], v217 offset:2048\nds_read_b128 a[44:47], v217 offset:3072\n"
"v_mfma_f32_16x16x32_bf16 v[20:23], v[136:139], a[4:7], v[20:23]\ns_add_u32 m0, s73, 0x0\nds_read_b128 a[48:51], v217 offset:4096\nds_read_b128 a[52:55], v217 offset:5120\n"
"v_mul_f32 v111, v111, v243\nv_mul_f32 v124, v124, v243\ns_waitcnt lgkmcnt(10)\nv_mfma_f32_16x16x32_bf16 v[24:27], v[136:139], a[8:11], v[24:27]\n"
"buffer_load_dwordx4 v235, s[56:59], s81 offen lds\nv_mfma_f32_16x16x32_bf16 v[28:31], v[136:139], a[12:15], v[28:31]\ns_add_u32 s81, s81, 0x7000\nds_write_b128 v219, v[100:103] offset:37888\n"
"ds_read_b128 a[56:59], v217 offset:6144\nds_read_b128 a[60:63], v217 offset:7168\nv_mfma_f32_16x16x32_bf16 v[16:19], v[140:143], a[16:19], v[16:19]\ns_add_u32 m0, s73, 0x1000\n"
"ds_read_b128 a[64:67], v218 offset:0\nds_read_b128 a[68:71], v218 offset:1024\nds_read_b128 a[72:75], v218 offset:2048\nv_mul_f32 v125, v125, v243\n"
"v_mfma_f32_16x16x32_bf16 v[20:23], v[140:143], a[20:23], v[20:23]\nbuffer_load_dwordx4 v235, s[56:59], s81 offen lds\nv_mfma_f32_16x16x32_bf16 v[24:27], v[140:143], a[24:27], v[24:27]\ns_add_u32 s81, s81, 0x15000\n"
"ds_read_b128 a[76:79], v218 offset:3072\nds_read_b128 a[80:83], v218 offset:4096\nds_read_b128 a[84:87], v218 offset:5120\nv_mul_f32 v126, v126, v243\n"
"s_waitcnt lgkmcnt(14)\nv_mfma_f32_16x16x32_bf16 v[28:31], v[140:143], a[28:31], v[28:31]\ns_add_u32 m0, s73, 0x2000\nds_read_b128 a[88:91], v218 offset:6144\n"
"ds_read_b128 a[92:95], v218 offset:7168\nds_read_b64_tr_b16 v[160:161], v212 offset:0\nv_mul_f32 v127, v127, v243\nv_mfma_f32_16x16x32_bf16 v[16:19], v[144:147], a[32:35], v[16:19]\n"
"buffer_load_dwordx4 v235, s[56:59], s81 offen lds\nv_mfma_f32_16x16x32_bf16 v[20:23], v[144:147], a[36:39], v[20:23]\ns_add_u32 s81, s81, 0x7000\nds_read_b64_tr_b16 v[162:163], v212 offset:2048\n"
"ds_read_b64_tr_b16 v[164:165], v212 offset:4096\nds_read_b64_tr_b16 v[166:167], v212 offset:6144\nv_cvt_pk_bf16_f32 v108, v108, v109\ns_waitcnt lgkmcnt(15)\n"
"v_mfma_f32_16x16x32_bf16 v[24:27], v[144:147], a[40:43], v[24:27]\ns_add_u32 m0, s73, 0x3000\nds_read_b64_tr_b16 v[168:169], v213 offset:0\nds_read_b64_tr_b16 v[170:171], v213 offset:2048\n"
"ds_read_b64_tr_b16 v[172:173], v213 offset:4096\nv_cvt_pk_bf16_f32 v106, v120, v121\nv_mfma_f32_16x16x32_bf16 v[28:31], v[144:147], a[44:47], v[28:31]\nbuffer_load_dwordx4 v235, s[56:59], s81 offen lds\n"
"v_mfma_f32_16x16x32_bf16 v[16:19], v[148:151], a[48:51], v[16:19]\ns_add_u32 s81, s81, 0x15000\nds_read_b64_tr_b16 v[174:175], v213 offset:6144\nds_read_b64_tr_b16 v[176:177], v214 offset:0\n"
"ds_read_b64_tr_b16 v[178:179], v214 offset:2048\nv_cvt_pk_bf16_f32 v107, v122, v123\nv_mfma_f32_16x16x32_bf16 v[20:23], v[148:151], a[52:55], v[20:23]\ns_add_u32 m0, s73, 0x4000\n"
"ds_write_b128 v219, v[104:107] offset:38912\nds_read_b64_tr_b16 v[180:181], v214 offset:4096\nds_read_b64_tr_b16 v[182:183], v214 offset:6144\ns_waitcnt lgkmcnt(15)\n"
"v_mfma_f32_16x16x32_bf16 v[24:27], v[148:151], a[56:59], v[24:27]\nbuffer_load_dwordx4 v235, s[56:59], s81 offen lds\nv_mfma_f32_16x16x32_bf16 v[28:31], v[148:151], a[60:63], v[28:31]\ns_add_u32 s81, s81, 0x7000\n"
"ds_read_b64_tr_b16 v[184:185], v215 offset:0\nds_read_b64_tr_b16 v[186:187], v215 offset:2048\nds_read_b64_tr_b16 v[188:189], v215 offset:4096\nv_cvt_pk_bf16_f32 v109, v110, v111\n"
"v_mfma_f32_16x16x32_bf16 v[16:19], v[152:155], a[64:67], v[16:19]\ns_add_u32 m0, s73, 0x5000\nds_read_b64_tr_b16 v[190:191], v215 offset:6144\nv_cvt_pk_bf16_f32 v110, v124, v125\n"
"v_cvt_pk_bf16_f32 v111, v126, v127\nv_mfma_f32_16x16x32_bf16 v[20:23], v[152:155], a[68:71], v[20:23]\nbuffer_load_dwordx4 v235, s[56:59], s81 offen lds\nv_mfma_f32_16x16x32_bf16 v[24:27], v[152:155], a[72:75], v[24:27]\n"
"s_add_u32 s81, s81, 0x15000\nds_write_b128 v219, v[108:111] offset:39936\nv_mfma_f32_16x16x32_bf16 v[28:31], v[152:155], a[76:79], v[28:31]\ns_add_u32 m0, s73, 0x6000\n"
"v_mfma_f32_16x16x32_bf16 v[16:19], v[156:159], a[80:83], v[16:19]\nbuffer_load_dwordx4 v235, s[56:59], s81 offen lds\nv_mfma_f32_16x16x32_bf16 v[20:23], v[156:159], a[84:87], v[20:23]\ns_add_u32 s81, s81, 0x7000\n"
"s_waitcnt lgkmcnt(5)\nv_mfma_f32_16x16x32_bf16 v[24:27], v[156:159], a[88:91], v[24:27]\ns_add_u32 m0, s73, 0x7000\nv_mfma_f32_16x16x32_bf16 v[28:31], v[156:159], a[92:95], v[28:31]\n"
"buffer_load_dwordx4 v235, s[56:59], s81 offen lds\ns_waitcnt vmcnt(10) lgkmcnt(0)\ns_barrier\nv_mfma_f32_16x16x32_bf16 v[16:19], v[160:163], v[64:67], v[16:19]\n"
"s_add_u32 s81, s81, 0xfff97080\nds_read_b128 a[96:99], v216 offset:32768\nds_read_b128 a[100:103], v216 offset:33792\nv_mfma_f32_16x16x32_bf16 v[20:23], v[160:163], v[68:71], v[20:23]\n"
"s_add_u32 m0, s73, 0x10000\nds_read_b128 a[104:107], v216 offset:34816\nds_read_b128 a[108:111], v216 offset:35840\nv_mfma_f32_16x16x32_bf16 v[24:27], v[160:163], v[72:75], v[24:27]\n"
"buffer_load_dwordx4 v235, s[56:59], s81 offen lds\nv_mfma_f32_16x16x32_bf16 v[28:31], v[160:163], v[76:79], v[28:31]\ns_add_u32 s81, s81, 0x7000\nds_read_b128 a[112:115], v216 offset:36864\n"
"ds_read_b128 a[116:119], v216 offset:37888\nv_mfma_f32_16x16x32_bf16 v[16:19], v[164:167], v[96:99], v[16:19]\ns_add_u32 m0, s73, 0x11000\nds_read_b128 a[120:123], v216 offset:38912\n"
"ds_read_b128 a[124:127], v216 offset:39936\nv_mfma_f32_16x16x32_bf16 v[20:23], v[164:167], v[100:103], v[20:23]\nbuffer_load_dwordx4 v235, s[56:59], s81 offen lds\nv_mfma_f32_16x16x32_bf16 v[24:27], v[164:167], v[104:107], v[24:27]\n"
"ds_read_b128 a[160:163], v217 offset:32768\nds_read_b128 a[164:167], v217 offset:33792\nv_mfma_f32_16x16x32_bf16 v[28:31], v[164:167], v[108:111], v[28:31]\ns_add_u32 s81, s81, 0x15000\n"
"ds_read_b128 a[168:171], v217 offset:34816\nds_read_b128 a[172:175], v217 offset:35840\ns_waitcnt lgkmcnt(10)\nv_mfma_f32_16x16x32_bf16 v[16:19], v[168:171], a[96:99], v[16:19]\n"
"s_add_u32 m0, s73, 0x12000\nds_read_b128 a[176:179], v217 offset:36864\nds_read_b128 a[180:183], v217 offset:37888\nv_mfma_f32_16x16x32_bf16 v[20:23], v[168:171], a[100:103], v[20:23]\n"
"buffer_load_dwordx4 v235, s[56:59], s81 offen lds\ns_waitcnt lgkmcnt(10)\nv_mfma_f32_16x16x32_bf16 v[24:27], v[168:171], a[104:107], v[24:27]\nds_read_b128 a[184:187], v217 offset:38912\n"
"ds_read_b128 a[188:191], v217 offset:39936\nv_mfma_f32_16x16x32_bf16 v[28:31], v[168:171], a[108:111], v[28:31]\ns_add_u32 s81, s81, 0x7000\nds_read_b128 a[192:195], v218 offset:32768\n"
"ds_read_b128 a[196:199], v218 offset:33792\ns_waitcnt lgkmcnt(10)\nv_mfma_f32_16x16x32_bf16 v[16:19], v[172:175], a[112:115], v[16:19]\ns_add_u32 m0, s73, 0x13000\n"
"ds_read_b128 a[200:203], v218 offset:34816\nds_read_b128 a[204:207], v218 offset:35840\nv_mfma_f32_16x16x32_bf16 v[20:23], v[172:175], a[116:119], v[20:23]\nbuffer_load_dwordx4 v235, s[56:59], s81 offen lds\n"
"v_mfma_f32_16x16x32_bf16 v[24:27], v[172:175], a[120:123], v[24:27]\nds_read_b128 a[208:211], v218 offset:36864\nds_read_b128 a[212:215], v218 offset:37888\nv_mfma_f32_16x16x32_bf16 v[28:31], v[172:175], a[124:127], v[28:31]\n"
"s_add_u32 s81, s81, 0x15000\nds_read_b128 a[216:219], v218 offset:38912\nds_read_b128 a[220:223], v218 offset:39936\ns_waitcnt lgkmcnt(10)\n"
"v_mfma_f32_16x16x32_bf16 v[16:19], v[176:179], a[160:163], v[16:19]\ns_add_u32 m0, s73, 0x14000\nds_read_b64_tr_b16 v[128:129], v208 offset:32768\nds_read_b64_tr_b16 v[130:131], v208 offset:34816\n"
"v_mfma_f32_16x16x32_bf16 v[20:23], v[176:179], a[164:167], v[20:23]\nbuffer_load_dwordx4 v235, s[56:59], s81 offen lds\nv_mfma_f32_16x16x32_bf16 v[24:27], v[176:179], a[168:171], v[24:27]\ns_add_u32 s81, s81, 0x7000\n"
"ds_read_b64_tr_b16 v[132:133], v208 offset:36864\nds_read_b64_tr_b16 v[134:135], v208 offset:38912\nv_mfma_f32_16x16x32_bf16 v[28:31], v[176:179], a[172:175], v[28:31]\ns_add_u32 m0, s73, 0x15000\n"
"ds_read_b64_tr_b16 v[136:137], v209 offset:32768\nds_read_b64_tr_b16 v[138:139], v209 offset:34816\nv_mfma_f32_16x16x32_bf16 v[16:19], v[180:183], a[176:179], v[16:19]\nbuffer_load_dwordx4 v235, s[56:59], s81 offen lds\n"
"v_mfma_f32_16x16x32_bf16 v[20:23], v[180:183], a[180:183], v[20:23]\nds_read_b64_tr_b16 v[140:141], v209 offset:36864\nds_read_b64_tr_b16 v[142:143], v209 offset:38912\ns_waitcnt lgkmcnt(10)\n"
"v_mfma_f32_16x16x32_bf16 v[24:27], v[180:183], a[184:187], v[24:27]\ns_add_u32 s81, s81, 0x15000\nds_read_b64_tr_b16 v[144:145], v210 offset:32768\nds_read_b64_tr_b16 v[146:147], v210 offset:34816\n"
"v_mfma_f32_16x16x32_bf16 v[28:31], v[180:183], a[188:191], v[28:31]\ns_add_u32 m0, s73, 0x16000\nds_read_b64_tr_b16 v[148:149], v210 offset:36864\nds_read_b64_tr_b16 v[150:151], v210 offset:38912\n"
"v_mfma_f32_16x16x32_bf16 v[16:19], v[184:187], a[192:195], v[16:19]\nbuffer_load_dwordx4 v235, s[56:59], s81 offen lds\nv_mfma_f32_16x16x32_bf16 v[20:23], v[184:187], a[196:199], v[20:23]\nds_read_b64_tr_b16 v[152:153], v211 offset:32768\n"
"ds_read_b64_tr_b16 v[154:155], v211 offset:34816\nv_mfma_f32_16x16x32_bf16 v[24:27], v[184:187], a[200:203], v[24:27]\ns_add_u32 s81, s81, 0x7000\nds_read_b64_tr_b16 v[156:157], v211 offset:36864\n"
"ds_read_b64_tr_b16 v[158:159], v211 offset:38912\nv_mfma_f32_16x16x32_bf16 v[28:31], v[184:187], a[204:207], v[28:31]\ns_add_u32 m0, s73, 0x17000\nv_mfma_f32_16x16x32_bf16 v[16:19], v[188:191], a[208:211], v[16:19]\n"
"buffer_load_dwordx4 v235, s[56:59], s81 offen lds\nv_mfma_f32_16x16x32_bf16 v[20:23], v[188:191], a[212:215], v[20:23]\ns_waitcnt lgkmcnt(6)\nv_mfma_f32_16x16x32_bf16 v[24:27], v[188:191], a[216:219], v[24:27]\n"
"v_mfma_f32_16x16x32_bf16 v[28:31], v[188:191], a[220:223], v[28:31]\ns_waitcnt vmcnt(8) lgkmcnt(0)\ns_barrier\nv_mfma_f32_16x16x32_bf16 v[48:51], v[128:131], v[0:3], 0\n"
"s_add_u32 m0, s73, 0x8000\nv_cvt_pk_bf16_f32 v192, v16, v17\nv_cvt_pk_bf16_f32 v193, v18, v19\nv_mfma_f32_16x16x32_bf16 v[48:51], v[132:135], v[32:35], v[48:51]\n"
"s_add_u32 s81, s81, 0xfffb3000\nbuffer_load_dwordx4 v235, s[56:59], s81 offen lds\nv_mfma_f32_16x16x32_bf16 v[48:51], v[136:139], a[0:3], v[48:51]\ns_add_u32 m0, s73, 0x9000\n"
"v_cvt_pk_bf16_f32 v194, v20, v21\nv_cvt_pk_bf16_f32 v195, v22, v23\nv_mfma_f32_16x16x32_bf16 v[48:51], v[140:143], a[16:19], v[48:51]\ns_add_u32 s81, s81, 0x7000\n"
"buffer_load_dwordx4 v235, s[56:59], s81 offen lds\nv_mfma_f32_16x16x32_bf16 v[48:51], v[144:147], a[32:35], v[48:51]\ns_add_u32 m0, s73, 0xa000\nds_read_b64_tr_b16 v[160:161], v208 offset:0\n"
"v_cvt_pk_bf16_f32 v196, v24, v25\nv_mfma_f32_16x16x32_bf16 v[48:51], v[148:151], a[48:51], v[48:51]\ns_add_u32 s81, s81, 0x15000\nbuffer_load_dwordx4 v235, s[56:59], s81 offen lds\n"
"v_mfma_f32_16x16x32_bf16 v[48:51], v[152:155], a[64:67], v[48:51]\ns_add_u32 m0, s73, 0xb000\nds_read_b64_tr_b16 v[162:163], v208 offset:2048\nv_cvt_pk_bf16_f32 v197, v26, v27\n"
"v_mfma_f32_16x16x32_bf16 v[48:51], v[156:159], a[80:83], v[48:51]\ns_add_u32 s81, s81, 0x7000\nbuffer_load_dwordx4 v235, s[56:59], s81 offen lds\nv_mfma_f32_16x16x32_bf16 v[52:55], v[156:159], a[84:87], 0\n"
"s_add_u32 m0, s73, 0xc000\nds_read_b64_tr_b16 v[164:165], v208 offset:4096\nv_cvt_pk_bf16_f32 v198, v28, v29\nv_mfma_f32_16x16x32_bf16 v[52:55], v[152:155], a[68:71], v[52:55]\n"
"s_add_u32 s81, s81, 0x15000\nbuffer_load_dwordx4 v235, s[56:59], s81 offen lds\nv_mfma_f32_16x16x32_bf16 v[52:55], v[148:151], a[52:55], v[52:55]\ns_add_u32 m0, s73, 0xd000\n"
"ds_read_b64_tr_b16 v[166:167], v208 offset:6144\nv_cvt_pk_bf16_f32 v199, v30, v31\nv_mfma_f32_16x16x32_bf16 v[52:55], v[144:147], a[36:39], v[52:55]\ns_add_u32 s81, s81, 0x7000\n"
"buffer_load_dwordx4 v235, s[56:59], s81 offen lds\nv_mfma_f32_16x16x32_bf16 v[52:55], v[140:143], a[20:23], v[52:55]\ns_add_u32 m0, s73, 0xe000\nds_read_b64_tr_b16 v[168:169], v209 offset:0\n"
"v_mfma_f32_16x16x32_bf16 v[52:55], v[136:139], a[4:7], v[52:55]\ns_add_u32 s81, s81, 0x15000\nbuffer_load_dwordx4 v235, s[56:59], s81 offen lds\nv_mfma_f32_16x16x32_bf16 v[52:55], v[132:135], v[36:39], v[52:55]\n"
"s_add_u32 m0, s73, 0xf000\nds_read_b64_tr_b16 v[170:171], v209 offset:2048\nv_mfma_f32_16x16x32_bf16 v[52:55], v[128:131], v[4:7], v[52:55]\ns_add_u32 s81, s81, 0x7000\n"
"buffer_load_dwordx4 v235, s[56:59], s81 offen lds\nv_mfma_f32_16x16x32_bf16 v[56:59], v[128:131], v[8:11], 0\ns_add_u32 m0, s73, 0x18000\nds_read_b64_tr_b16 v[172:173], v209 offset:4096\n"
"v_mfma_f32_16x16x32_bf16 v[56:59], v[132:135], v[40:43], v[56:59]\ns_add_u32 s81, s81, 0xfff97080\nbuffer_load_dwordx4 v235, s[56:59], s81 offen lds\nv_mfma_f32_16x16x32_bf16 v[56:59], v[136:139], a[8:11], v[56:59]\n"
"s_add_u32 m0, s73, 0x19000\nds_read_b64_tr_b16 v[174:175], v209 offset:6144\nv_mfma_f32_16x16x32_bf16 v[56:59], v[140:143], a[24:27], v[56:59]\ns_add_u32 s81, s81, 0x7000\n"
"buffer_load_dwordx4 v235, s[56:59], s81 offen lds\nv_mfma_f32_16x16x32_bf16 v[56:59], v[144:147], a[40:43], v[56:59]\ns_add_u32 m0, s73, 0x1a000\nds_read_b64_tr_b16 v[176:177], v210 offset:0\n"
"v_mfma_f32_16x16x32_bf16 v[56:59], v[148:151], a[56:59], v[56:59]\ns_add_u32 s81, s81, 0x15000\nbuffer_load_dwordx4 v235, s[56:59], s81 offen lds\nv_mfma_f32_16x16x32_bf16 v[56:59], v[152:155], a[72:75], v[56:59]\n"
"s_add_u32 m0, s73, 0x1b000\nds_read_b64_tr_b16 v[178:179], v210 offset:2048\nv_mfma_f32_16x16x32_bf16 v[56:59], v[156:159], a[88:91], v[56:59]\ns_add_u32 s81, s81, 0x7000\n"
"buffer_load_dwordx4 v235, s[56:59], s81 offen lds\nv_mfma_f32_16x16x32_bf16 v[60:63], v[156:159], a[92:95], 0\ns_add_u32 m0, s73, 0x1c000\nds_read_b64_tr_b16 v[180:181], v210 offset:4096\n"
"v_mfma_f32_16x16x32_bf16 v[60:63], v[152:155], a[76:79], v[60:63]\ns_add_u32 s81, s81, 0x15000\nbuffer_load_dwordx4 v235, s[56:59], s81 offen lds\nv_mfma_f32_16x16x32_bf16 v[60:63], v[148:151], a[60:63], v[60:63]\n"
"s_add_u32 m0, s73, 0x1d000\nds_read_b64_tr_b16 v[182:183], v210 offset:6144\nv_mfma_f32_16x16x32_bf16 v[60:63], v[144:147], a[44:47], v[60:63]\ns_add_u32 s81, s81, 0x7000\n"
"buffer_load_dwordx4 v235, s[56:59], s81 offen lds\nv_mfma_f32_16x16x32_bf16 v[60:63], v[140:143], a[28:31], v[60:63]\ns_add_u32 m0, s73, 0x1e000\nds_read_b64_tr_b16 v[184:185], v211 offset:0\n"
"v_mfma_f32_16x16x32_bf16 v[60:63], v[136:139], a[12:15], v[60:63]\ns_add_u32 s81, s81, 0x15000\nbuffer_load_dwordx4 v235, s[56:59], s81 offen lds\nv_mfma_f32_16x16x32_bf16 v[60:63], v[132:135], v[44:47], v[60:63]\n"
"s_add_u32 m0, s73, 0x1f000\nds_read_b64_tr_b16 v[186:187], v211 offset:2048\nv_mfma_f32_16x16x32_bf16 v[60:63], v[128:131], v[12:15], v[60:63]\ns_add_u32 s81, s81, 0x7000\n"
"buffer_load_dwordx4 v235, s[56:59], s81 offen lds\nds_read_b64_tr_b16 v[188:189], v211 offset:4096\nds_read_b64_tr_b16 v[190:191], v211 offset:6144\ns_waitcnt vmcnt(16) lgkmcnt(0)\n"
"s_barrier\nv_mfma_f32_16x16x32_bf16 v[48:51], v[160:163], v[64:67], v[48:51]\ns_add_u32 s81, s81, 0xfffb3000\nds_read_b64_tr_b16 v[128:129], v212 offset:0\n"
"v_lshrrev_b32 v236, 3, v254\nv_mfma_f32_16x16x32_bf16 v[48:51], v[164:167], v[96:99], v[48:51]\ns_add_u32 m0, s73, 0x0\nds_read_b64_tr_b16 v[130:131], v212 offset:2048\n"
"v_and_b32 v237, 7, v254\nv_mfma_f32_16x16x32_bf16 v[48:51], v[168:171], a[96:99], v[48:51]\nbuffer_load_dwordx4 v235, s[56:59], s81 offen lds\nv_mfma_f32_16x16x32_bf16 v[48:51], v[172:175], a[112:115], v[48:51]\n"
"s_add_u32 s81, s81, 0x7000\nds_read_b64_tr_b16 v[132:133], v212 offset:4096\nv_and_b32 v238, 6, v236\nv_mfma_f32_16x16x32_bf16 v[48:51], v[176:179], a[160:163], v[48:51]\n"
"s_add_u32 m0, s73, 0x1000\nds_read_b64_tr_b16 v[134:135], v212 offset:6144\nv_mul_u32_u24 v225, 896, v236\nv_mfma_f32_16x16x32_bf16 v[48:51], v[180:183], a[176:179], v[48:51]\n"
"buffer_load_dwordx4 v235, s[56:59], s81 offen lds\nv_mfma_f32_16x16x32_bf16 v[48:51], v[184:187], a[192:195], v[48:51]\ns_mul_i32 s48, s75, 37\nds_read_b64_tr_b16 v[136:137], v213 offset:0\n"
"v_xor_b32 v238, v237, v238\nv_mfma_f32_16x16x32_bf16 v[48:51], v[188:191], a[208:211], v[48:51]\ns_add_u32 s81, s81, 0x15000\nds_read_b64_tr_b16 v[138:139], v213 offset:2048\n"
"v_and_b32 v236, 15, v254\nv_mfma_f32_16x16x32_bf16 v[52:55], v[188:191], a[212:215], v[52:55]\ns_add_u32 m0, s73, 0x2000\nds_read_b64_tr_b16 v[140:141], v213 offset:4096\n"
"v_lshl_add_u32 v239, v237, 4, v225\nv_mfma_f32_16x16x32_bf16 v[52:55], v[184:187], a[196:199], v[52:55]\nbuffer_load_dwordx4 v235, s[56:59], s81 offen lds\nv_mfma_f32_16x16x32_bf16 v[52:55], v[180:183], a[180:183], v[52:55]\n"
"s_lshr_b32 s48, s48, 8\nds_read_b64_tr_b16 v[142:143], v213 offset:6144\nv_mul_u32_u24 v234, 896, v236\nv_mfma_f32_16x16x32_bf16 v[52:55], v[176:179], a[164:167], v[52:55]\n"
"s_add_u32 s81, s81, 0x7000\nds_read_b64_tr_b16 v[144:145], v214 offset:0\nv_cvt_pk_bf16_f32 v200, v48, v49\nv_mfma_f32_16x16x32_bf16 v[52:55], v[172:175], a[116:119], v[52:55]\n"
"s_add_u32 m0, s73, 0x3000\nds_read_b64_tr_b16 v[146:147], v214 offset:2048\nv_cvt_pk_bf16_f32 v201, v50, v51\nv_mfma_f32_16x16x32_bf16 v[52:55], v[168:171], a[100:103], v[52:55]\n"
"buffer_load_dwordx4 v235, s[56:59], s81 offen lds\nv_mfma_f32_16x16x32_bf16 v[52:55], v[164:167], v[100:103], v[52:55]\ns_mul_i32 s49, s48, 7\nds_read_b64_tr_b16 v[148:149], v214 offset:4096\n"
"v_lshrrev_b32 v237, 4, v254\nv_mfma_f32_16x16x32_bf16 v[52:55], v[160:163], v[68:71], v[52:55]\ns_add_u32 s81, s81, 0x15000\nds_read_b64_tr_b16 v[150:151], v214 offset:6144\n"
"v_lshlrev_b32 v236, 7, v236\nv_mfma_f32_16x16x32_bf16 v[56:59], v[160:163], v[72:75], v[56:59]\ns_add_u32 m0, s73, 0x4000\nds_read_b64_tr_b16 v[152:153], v215 offset:0\n"
"v_lshl_add_u32 v236, v237, 3, v236\nv_mfma_f32_16x16x32_bf16 v[56:59], v[164:167], v[104:107], v[56:59]\nbuffer_load_dwordx4 v235, s[56:59], s81 offen lds\nv_mfma_f32_16x16x32_bf16 v[56:59], v[168:171], a[104:107], v[56:59]\n"
"s_add_u32 s81, s81, 0x7000\nds_read_b64_tr_b16 v[154:155], v215 offset:2048\ns_sub_u32 s49, s75, s49\nv_mfma_f32_16x16x32_bf16 v[56:59], v[172:175], a[120:123], v[56:59]\n"
"s_add_u32 m0, s73, 0x5000\nds_read_b64_tr_b16 v[156:157], v215 offset:4096\nv_cvt_pk_bf16_f32 v202, v52, v53\nv_mfma_f32_16x16x32_bf16 v[56:59], v[176:179], a[168:171], v[56:59]\n"
"buffer_load_dwordx4 v235, s[56:59], s81 offen lds\nv_mfma_f32_16x16x32_bf16 v[56:59], v[180:183], a[184:187], v[56:59]\ns_lshl_b32 s48, s48, 3\nds_read_b64_tr_b16 v[158:159], v215 offset:6144\n"
"v_cvt_pk_bf16_f32 v203, v54, v55\nv_mfma_f32_16x16x32_bf16 v[56:59], v[184:187], a[200:203], v[56:59]\ns_add_u32 s81, s81, 0x15000\ns_add_u32 s48, s48, s74\n"
"s_mul_i32 s55, s72, 14336\nv_mfma_f32_16x16x32_bf16 v[56:59], v[188:191], a[216:219], v[56:59]\ns_add_u32 m0, s73, 0x6000\ns_lshl_b32 s50, s49, 7\n"
"s_add_u32 s55, s55, s50\nv_mfma_f32_16x16x32_bf16 v[60:63], v[188:191], a[220:223], v[60:63]\nbuffer_load_dwordx4 v235, s[56:59], s81 offen lds\nv_mfma_f32_16x16x32_bf16 v[60:63], v[184:187], a[204:207], v[60:63]\n"
"s_mul_i32 s80, s48, 229376\ns_lshl_b32 s50, s72, 12\ns_add_u32 s80, s80, s50\nv_mfma_f32_16x16x32_bf16 v[60:63], v[180:183], a[188:191], v[60:63]\n"
"s_add_u32 s81, s81, 0x7000\ns_mul_i32 s54, s48, 7\ns_add_u32 s54, s54, s49\nv_mfma_f32_16x16x32_bf16 v[60:63], v[176:179], a[172:175], v[60:63]\n"
"s_add_u32 m0, s73, 0x7000\nv_cvt_pk_bf16_f32 v204, v56, v57\nv_cvt_pk_bf16_f32 v205, v58, v59\nv_mfma_f32_16x16x32_bf16 v[60:63], v[172:175], a[124:127], v[60:63]\n"
"buffer_load_dwordx4 v235, s[56:59], s81 offen lds\nv_mfma_f32_16x16x32_bf16 v[60:63], v[168:171], a[108:111], v[60:63]\ns_lshl_b32 s54, s54, 16\nbuffer_store_dwordx4 v[192:195], v224, s[64:67], s83 offen offset:0 sc1\n"
"v_mfma_f32_16x16x32_bf16 v[60:63], v[164:167], v[108:111], v[60:63]\ns_lshl_b32 s50, s72, 11\nbuffer_store_dwordx4 v[196:199], v224, s[64:67], s83 offen offset:1024 sc1\nv_mfma_f32_16x16x32_bf16 v[60:63], v[160:163], v[76:79], v[60:63]\n"
"s_nop 0\ns_add_u32 s83, s83, 0x10000\nbuffer_store_dwordx4 v[200:203], v224, s[64:67], s83 offen offset:0 sc1\ns_waitcnt vmcnt(19) lgkmcnt(0)\n"
"s_barrier\nv_mfma_f32_16x16x32_bf16 v[16:19], v[128:131], v[0:3], 0\ns_add_u32 s81, s81, 0xfff97080\nds_read_b64_tr_b16 v[160:161], v208 offset:32768\n"
"s_add_u32 s54, s54, s50\nv_mfma_f32_16x16x32_bf16 v[16:19], v[132:135], v[32:35], v[16:19]\ns_add_u32 m0, s73, 0x10000\nds_read_b64_tr_b16 v[162:163], v208 offset:34816\n"
"s_lshl_b32 s50, s72, 5\nv_mfma_f32_16x16x32_bf16 v[16:19], v[136:139], a[0:3], v[16:19]\nbuffer_load_dwordx4 v235, s[56:59], s81 offen lds\nv_mfma_f32_16x16x32_bf16 v[16:19], v[140:143], a[16:19], v[16:19]\n"
"s_add_u32 s81, s81, 0x7000\nds_read_b64_tr_b16 v[164:165], v208 offset:36864\nv_cvt_pk_bf16_f32 v206, v60, v61\nv_mfma_f32_16x16x32_bf16 v[16:19], v[144:147], a[32:35], v[16:19]\n"
"s_add_u32 m0, s73, 0x11000\nds_read_b64_tr_b16 v[166:167], v208 offset:38912\nv_cvt_pk_bf16_f32 v207, v62, v63\nv_mfma_f32_16x16x32_bf16 v[16:19], v[148:151], a[48:51], v[16:19]\n"
"buffer_load_dwordx4 v235, s[56:59], s81 offen lds\nv_mfma_f32_16x16x32_bf16 v[16:19], v[152:155], a[64:67], v[16:19]\ns_add_u32 s51, s50, 0x14000\nds_read_b64_tr_b16 v[168:169], v209 offset:32768\n"
"v_add_u32 v240, s51, v236\nv_mfma_f32_16x16x32_bf16 v[16:19], v[156:159], a[80:83], v[16:19]\ns_add_u32 s81, s81, 0x15000\nds_read_b64_tr_b16 v[170:171], v209 offset:34816\n"
"v_and_b32 v236, 3, v254\nv_mfma_f32_16x16x32_bf16 v[20:23], v[156:159], a[84:87], 0\ns_add_u32 m0, s73, 0x12000\nds_read_b64_tr_b16 v[172:173], v209 offset:36864\n"
"v_lshl_add_u32 v234, v237, 3, v234\nv_mfma_f32_16x16x32_bf16 v[20:23], v[152:155], a[68:71], v[20:23]\nbuffer_load_dwordx4 v235, s[56:59], s81 offen lds\nv_mfma_f32_16x16x32_bf16 v[20:23], v[148:151], a[52:55], v[20:23]\n"
"s_lshl_b32 s52, s72, 12\nds_read_b64_tr_b16 v[174:175], v209 offset:38912\nv_lshlrev_b32 v236, 3, v236\nv_mfma_f32_16x16x32_bf16 v[20:23], v[144:147], a[36:39], v[20:23]\n"
"s_add_u32 s81, s81, 0x7000\nds_read_b64_tr_b16 v[176:177], v210 offset:32768\nv_lshrrev_b32 v237, 2, v254\nv_mfma_f32_16x16x32_bf16 v[20:23], v[140:143], a[20:23], v[20:23]\n"
"s_add_u32 m0, s73, 0x13000\nds_read_b64_tr_b16 v[178:179], v210 offset:34816\nv_lshl_add_u32 v236, v237, 7, v236\nv_mfma_f32_16x16x32_bf16 v[20:23], v[136:139], a[4:7], v[20:23]\n"
"buffer_load_dwordx4 v235, s[56:59], s81 offen lds\nv_mfma_f32_16x16x32_bf16 v[20:23], v[132:135], v[36:39], v[20:23]\ns_add_u32 s52, s52, 0x18000\nds_read_b64_tr_b16 v[180:181], v210 offset:36864\n"
"v_bfe_u32 v237, v254, 3, 2\nv_mfma_f32_16x16x32_bf16 v[20:23], v[128:131], v[4:7], v[20:23]\ns_add_u32 s81, s81, 0x15000\nds_read_b64_tr_b16 v[182:183], v210 offset:38912\n"
"v_lshl_add_u32 v225, v238, 4, v225\nv_mfma_f32_16x16x32_bf16 v[24:27], v[128:131], v[8:11], 0\ns_add_u32 m0, s73, 0x14000\nds_read_b64_tr_b16 v[184:185], v211 offset:32768\n"
"v_add_u32 v236, s52, v236\nv_mfma_f32_16x16x32_bf16 v[24:27], v[132:135], v[40:43], v[24:27]\nbuffer_load_dwordx4 v235, s[56:59], s81 offen lds\nv_mfma_f32_16x16x32_bf16 v[24:27], v[136:139], a[8:11], v[24:27]\n"
"s_add_u32 s81, s81, 0x7000\nds_read_b64_tr_b16 v[186:187], v211 offset:34816\ns_mul_i32 s76, s48, 57344\nv_mfma_f32_16x16x32_bf16 v[24:27], v[140:143], a[24:27], v[24:27]\n"
"s_add_u32 m0, s73, 0x15000\nds_read_b64_tr_b16 v[188:189], v211 offset:36864\ns_lshl_b32 s51, s49, 7\nv_mfma_f32_16x16x32_bf16 v[24:27], v[144:147], a[40:43], v[24:27]\n"
"buffer_load_dwordx4 v235, s[56:59], s81 offen lds\nv_mfma_f32_16x16x32_bf16 v[24:27], v[148:151], a[56:59], v[24:27]\ns_add_u32 s76, s76, s51\nds_read_b64_tr_b16 v[190:191], v211 offset:38912\n"
"s_mul_i32 s84, s72, 14336\nv_mfma_f32_16x16x32_bf16 v[24:27], v[152:155], a[72:75], v[24:27]\ns_add_u32 s81, s81, 0x15000\ns_add_u32 s84, s84, s76\n"
"s_add_u32 s76, s76, s50\nv_mfma_f32_16x16x32_bf16 v[24:27], v[156:159], a[88:91], v[24:27]\ns_add_u32 m0, s73, 0x16000\ns_add_u32 s50, s72, 1\n"
"s_and_b32 s50, s50, 3\nv_mfma_f32_16x16x32_bf16 v[28:31], v[156:159], a[92:95], 0\nbuffer_load_dwordx4 v235, s[56:59], s81 offen lds\nv_mfma_f32_16x16x32_bf16 v[28:31], v[152:155], a[76:79], v[28:31]\n"
"s_mov_b64 s[36:37], s[28:29]\nv_xor_b32 v238, s50, v237\ns_mul_i32 s50, s50, 12288\nv_mfma_f32_16x16x32_bf16 v[28:31], v[148:151], a[60:63], v[28:31]\n"
"s_add_u32 s81, s81, 0x7000\nv_lshl_add_u32 v226, v238, 5, v236\ns_add_u32 s50, s50, 0x8000\nv_mfma_f32_16x16x32_bf16 v[28:31], v[144:147], a[44:47], v[28:31]\n"
"s_add_u32 m0, s73, 0x17000\nv_add_u32 v230, s50, v224\ns_add_u32 s50, s72, 2\nv_mfma_f32_16x16x32_bf16 v[28:31], v[140:143], a[28:31], v[28:31]\n"
"buffer_load_dwordx4 v235, s[56:59], s81 offen lds\nv_mfma_f32_16x16x32_bf16 v[28:31], v[136:139], a[12:15], v[28:31]\ns_and_b32 s50, s50, 3\nbuffer_store_dwordx4 v[204:207], v224, s[64:67], s83 offen offset:1024 sc1\n"
"v_mfma_f32_16x16x32_bf16 v[28:31], v[132:135], v[44:47], v[28:31]\ns_nop 0\ns_add_u32 s83, s83, 0x10000\nv_xor_b32 v238, s50, v237\n"
"s_mul_i32 s50, s50, 12288\nv_mfma_f32_16x16x32_bf16 v[28:31], v[128:131], v[12:15], v[28:31]\ns_add_u32 s50, s50, 0x9000\nv_lshl_add_u32 v227, v238, 5, v236\n"
"v_add_u32 v231, s50, v224\ns_waitcnt vmcnt(20) lgkmcnt(0)\ns_barrier\nv_mfma_f32_16x16x32_bf16 v[16:19], v[160:163], v[64:67], v[16:19]\n"
"s_add_u32 s81, s81, 0xfffb3000\nds_read_b64_tr_b16 v[128:129], v212 offset:32768\ns_add_u32 s50, s72, 3\nv_mfma_f32_16x16x32_bf16 v[16:19], v[164:167], v[96:99], v[16:19]\n"
"s_add_u32 m0, s73, 0x8000\nds_read_b64_tr_b16 v[130:131], v212 offset:34816\ns_and_b32 s50, s50, 3\nv_mfma_f32_16x16x32_bf16 v[16:19], v[168:171], a[96:99], v[16:19]\n"
"buffer_load_dwordx4 v235, s[56:59], s81 offen lds\nv_mfma_f32_16x16x32_bf16 v[16:19], v[172:175], a[112:115], v[16:19]\ns_add_u32 s81, s81, 0x7000\nds_read_b64_tr_b16 v[132:133], v212 offset:36864\n"
"v_xor_b32 v238, s50, v237\nv_mfma_f32_16x16x32_bf16 v[16:19], v[176:179], a[160:163], v[16:19]\ns_add_u32 m0, s73, 0x9000\nds_read_b64_tr_b16 v[134:135], v212 offset:38912\n"
"v_lshl_add_u32 v228, v238, 5, v236\nv_mfma_f32_16x16x32_bf16 v[16:19], v[180:183], a[176:179], v[16:19]\nbuffer_load_dwordx4 v235, s[56:59], s81 offen lds\nv_mfma_f32_16x16x32_bf16 v[16:19], v[184:187], a[192:195], v[16:19]\n"
"s_mul_i32 s50, s50, 12288\nds_read_b64_tr_b16 v[136:137], v213 offset:32768\ns_add_u32 s50, s50, 0xa000\nv_mfma_f32_16x16x32_bf16 v[16:19], v[188:191], a[208:211], v[16:19]\n"
"s_add_u32 s81, s81, 0x15000\nds_read_b64_tr_b16 v[138:139], v213 offset:34816\nv_add_u32 v232, s50, v224\nv_mfma_f32_16x16x32_bf16 v[20:23], v[188:191], a[212:215], v[20:23]\n"
"s_add_u32 m0, s73, 0xa000\nds_read_b64_tr_b16 v[140:141], v213 offset:36864\ns_add_u32 s50, s72, 4\nv_mfma_f32_16x16x32_bf16 v[20:23], v[184:187], a[196:199], v[20:23]\n"
"buffer_load_dwordx4 v235, s[56:59], s81 offen lds\nv_mfma_f32_16x16x32_bf16 v[20:23], v[180:183], a[180:183], v[20:23]\ns_and_b32 s50, s50, 3\nds_read_b64_tr_b16 v[142:143], v213 offset:38912\n"
"v_xor_b32 v238, s50, v237\nv_mfma_f32_16x16x32_bf16 v[20:23], v[176:179], a[164:167], v[20:23]\ns_add_u32 s81, s81, 0x7000\nds_read_b64_tr_b16 v[144:145], v214 offset:32768\n"
"v_cvt_pk_bf16_f32 v192, v16, v17\nv_mfma_f32_16x16x32_bf16 v[20:23], v[172:175], a[116:119], v[20:23]\ns_add_u32 m0, s73, 0xb000\nds_read_b64_tr_b16 v[146:147], v214 offset:34816\n"
"v_cvt_pk_bf16_f32 v193, v18, v19\nv_mfma_f32_16x16x32_bf16 v[20:23], v[168:171], a[100:103], v[20:23]\nbuffer_load_dwordx4 v235, s[56:59], s81 offen lds\nv_mfma_f32_16x16x32_bf16 v[20:23], v[164:167], v[100:103], v[20:23]\n"
"s_and_b32 s37, s37, 0xffff\nds_read_b64_tr_b16 v[148:149], v214 offset:36864\nv_cmp_gt_u32 vcc, 8, v254\nv_mfma_f32_16x16x32_bf16 v[20:23], v[160:163], v[68:71], v[20:23]\n"
"s_add_u32 s81, s81, 0x15000\nds_read_b64_tr_b16 v[150:151], v214 offset:38912\nv_mov_b32 v253, 0x7f000000\nv_mfma_f32_16x16x32_bf16 v[24:27], v[160:163], v[72:75], v[24:27]\n"
"s_add_u32 m0, s73, 0xc000\nds_read_b64_tr_b16 v[152:153], v215 offset:32768\nv_lshlrev_b32 v233, 3, v254\nv_mfma_f32_16x16x32_bf16 v[24:27], v[164:167], v[104:107], v[24:27]\n"
"buffer_load_dwordx4 v235, s[56:59], s81 offen lds\nv_mfma_f32_16x16x32_bf16 v[24:27], v[168:171], a[104:107], v[24:27]\ns_add_u32 s81, s81, 0x7000\nds_read_b64_tr_b16 v[154:155], v215 offset:34816\n"
"v_lshl_add_u32 v229, v238, 5, v236\nv_mfma_f32_16x16x32_bf16 v[24:27], v[172:175], a[120:123], v[24:27]\ns_add_u32 m0, s73, 0xd000\nds_read_b64_tr_b16 v[156:157], v215 offset:36864\n"
"v_cvt_pk_bf16_f32 v194, v20, v21\nv_mfma_f32_16x16x32_bf16 v[24:27], v[176:179], a[168:171], v[24:27]\nbuffer_load_dwordx4 v235, s[56:59], s81 offen lds\nv_mfma_f32_16x16x32_bf16 v[24:27], v[180:183], a[184:187], v[24:27]\n"
"s_mov_b32 s38, 0x188000\nds_read_b64_tr_b16 v[158:159], v215 offset:38912\nv_cvt_pk_bf16_f32 v195, v22, v23\nv_mfma_f32_16x16x32_bf16 v[24:27], v[184:187], a[200:203], v[24:27]\n"
"s_add_u32 s81, s81, 0x15000\ns_mov_b32 s39, 0x20000\ns_mov_b64 s[40:41], s[8:9]\nv_mfma_f32_16x16x32_bf16 v[24:27], v[188:191], a[216:219], v[24:27]\n"
"s_add_u32 m0, s73, 0xe000\ns_and_b32 s41, s41, 0xffff\ns_mov_b32 s42, 0x1c0000\nv_mfma_f32_16x16x32_bf16 v[28:31], v[188:191], a[220:223], v[28:31]\n"
"buffer_load_dwordx4 v235, s[56:59], s81 offen lds\nv_mfma_f32_16x16x32_bf16 v[28:31], v[184:187], a[204:207], v[28:31]\ns_mov_b32 s43, 0x20000\ns_mov_b64 s[44:45], s[10:11]\n"
"s_and_b32 s45, s45, 0xffff\nv_mfma_f32_16x16x32_bf16 v[28:31], v[180:183], a[188:191], v[28:31]\ns_add_u32 s81, s81, 0x7000\ns_mov_b32 s46, 0x1c0000\n"
"s_mov_b32 s47, 0x20000\nv_mfma_f32_16x16x32_bf16 v[28:31], v[176:179], a[172:175], v[28:31]\ns_add_u32 m0, s73, 0xf000\nv_cvt_pk_bf16_f32 v196, v24, v25\n"
"v_cvt_pk_bf16_f32 v197, v26, v27\nv_mfma_f32_16x16x32_bf16 v[28:31], v[172:175], a[124:127], v[28:31]\nbuffer_load_dwordx4 v235, s[56:59], s81 offen lds\nv_mfma_f32_16x16x32_bf16 v[28:31], v[168:171], a[108:111], v[28:31]\n"
"s_cmp_lt_u32 s75, 28\ns_cselect_b32 s38, s38, 0\nbuffer_store_dwordx4 v[192:195], v224, s[64:67], s83 offen offset:0 sc1\nv_mfma_f32_16x16x32_bf16 v[28:31], v[164:167], v[108:111], v[28:31]\ns_add_u32 s86, s80, 0x30000\n"
"s_add_u32 s87, s80, 0x34000\ns_add_u32 s88, s80, 0x0\nv_mfma_f32_16x16x32_bf16 v[28:31], v[160:163], v[76:79], v[28:31]\ns_add_u32 s89, s80, 0x4000\n"
"s_lshl_b32 s85, s72, 11\ns_add_u32 s77, s76, 14336\ns_waitcnt vmcnt(21) lgkmcnt(0)\ns_barrier\n"
"v_mfma_f32_16x16x32_bf16 v[48:51], v[128:131], v[0:3], 0\ns_add_u32 s81, s81, 0xfff97080\nds_read_b64_tr_b16 v[160:161], v208 offset:0\ns_add_u32 s78, s77, 14336\n"
"v_mfma_f32_16x16x32_bf16 v[48:51], v[132:135], v[32:35], v[48:51]\ns_add_u32 m0, s73, 0x18000\nds_read_b64_tr_b16 v[162:163], v208 offset:2048\ns_add_u32 s79, s78, 14336\n"
"v_mfma_f32_16x16x32_bf16 v[48:51], v[136:139], a[0:3], v[48:51]\nbuffer_load_dwordx4 v235, s[56:59], s81 offen lds\nv_mfma_f32_16x16x32_bf16 v[48:51], v[140:143], a[16:19], v[48:51]\ns_add_u32 s81, s81, 0x7000\n"
"ds_read_b64_tr_b16 v[164:165], v208 offset:4096\nv_cvt_pk_bf16_f32 v198, v28, v29\nv_mfma_f32_16x16x32_bf16 v[48:51], v[144:147], a[32:35], v[48:51]\ns_add_u32 m0, s73, 0x19000\n"
"ds_read_b64_tr_b16 v[166:167], v208 offset:6144\nv_cvt_pk_bf16_f32 v199, v30, v31\nv_mfma_f32_16x16x32_bf16 v[48:51], v[148:151], a[48:51], v[48:51]\nbuffer_load_dwordx4 v235, s[56:59], s81 offen lds\n"
"v_mfma_f32_16x16x32_bf16 v[48:51], v[152:155], a[64:67], v[48:51]\ns_mul_i32 s50, s72, 12288\nds_read_b64_tr_b16 v[168:169], v209 offset:0\ns_add_u32 s50, s50, 0x8000\n"
"v_mfma_f32_16x16x32_bf16 v[48:51], v[156:159], a[80:83], v[48:51]\ns_add_u32 s81, s81, 0x15000\nds_read_b64_tr_b16 v[170:171], v209 offset:2048\nv_add_u32 v241, s50, v224\n"
"v_mfma_f32_16x16x32_bf16 v[52:55], v[156:159], a[84:87], 0\ns_add_u32 m0, s73, 0x1a000\nds_read_b64_tr_b16 v[172:173], v209 offset:4096\nv_cndmask_b32 v253, v250, v253, vcc\n"
"v_mfma_f32_16x16x32_bf16 v[52:55], v[152:155], a[68:71], v[52:55]\nbuffer_load_dwordx4 v235, s[56:59], s81 offen lds\nv_mfma_f32_16x16x32_bf16 v[52:55], v[148:151], a[52:55], v[52:55]\ns_cmp_eq_u32 s72, 0\ns_cselect_b32 s33, 0, 0x7f000000\n"
"ds_read_b64_tr_b16 v[174:175], v209 offset:6144\nv_mfma_f32_16x16x32_bf16 v[52:55], v[144:147], a[36:39], v[52:55]\ns_add_u32 s81, s81, 0x7000\nds_read_b64_tr_b16 v[176:177], v210 offset:0\n"
"v_mfma_f32_16x16x32_bf16 v[52:55], v[140:143], a[20:23], v[52:55]\ns_add_u32 m0, s73, 0x1b000\nds_read_b64_tr_b16 v[178:179], v210 offset:2048\nv_mfma_f32_16x16x32_bf16 v[52:55], v[136:139], a[4:7], v[52:55]\n"
"buffer_load_dwordx4 v235, s[56:59], s81 offen lds\nv_mfma_f32_16x16x32_bf16 v[52:55], v[132:135], v[36:39], v[52:55]\nds_read_b64_tr_b16 v[180:181], v210 offset:4096\nv_mfma_f32_16x16x32_bf16 v[52:55], v[128:131], v[4:7], v[52:55]\n"
"s_add_u32 s81, s81, 0x15000\nds_read_b64_tr_b16 v[182:183], v210 offset:6144\nv_mfma_f32_16x16x32_bf16 v[56:59], v[128:131], v[8:11], 0\ns_add_u32 m0, s73, 0x1c000\n"
"ds_read_b64_tr_b16 v[184:185], v211 offset:0\nv_mfma_f32_16x16x32_bf16 v[56:59], v[132:135], v[40:43], v[56:59]\nbuffer_load_dwordx4 v235, s[56:59], s81 offen lds\nv_mfma_f32_16x16x32_bf16 v[56:59], v[136:139], a[8:11], v[56:59]\n"
"s_add_u32 s81, s81, 0x7000\nds_read_b64_tr_b16 v[186:187], v211 offset:2048\nv_mfma_f32_16x16x32_bf16 v[56:59], v[140:143], a[24:27], v[56:59]\ns_add_u32 m0, s73, 0x1d000\n"
"ds_read_b64_tr_b16 v[188:189], v211 offset:4096\nv_mfma_f32_16x16x32_bf16 v[56:59], v[144:147], a[40:43], v[56:59]\nbuffer_load_dwordx4 v235, s[56:59], s81 offen lds\nv_mfma_f32_16x16x32_bf16 v[56:59], v[148:151], a[56:59], v[56:59]\n"
"ds_read_b64_tr_b16 v[190:191], v211 offset:6144\nv_mfma_f32_16x16x32_bf16 v[56:59], v[152:155], a[72:75], v[56:59]\ns_add_u32 s81, s81, 0x15000\nv_mfma_f32_16x16x32_bf16 v[56:59], v[156:159], a[88:91], v[56:59]\n"
"s_add_u32 m0, s73, 0x1e000\nv_mfma_f32_16x16x32_bf16 v[60:63], v[156:159], a[92:95], 0\nbuffer_load_dwordx4 v235, s[56:59], s81 offen lds\nv_mfma_f32_16x16x32_bf16 v[60:63], v[152:155], a[76:79], v[60:63]\n"
"v_mfma_f32_16x16x32_bf16 v[60:63], v[148:151], a[60:63], v[60:63]\ns_add_u32 s81, s81, 0x7000\nv_mfma_f32_16x16x32_bf16 v[60:63], v[144:147], a[44:47], v[60:63]\ns_add_u32 m0, s73, 0x1f000\n"
"v_mfma_f32_16x16x32_bf16 v[60:63], v[140:143], a[28:31], v[60:63]\nbuffer_load_dwordx4 v235, s[56:59], s81 offen lds\nv_mfma_f32_16x16x32_bf16 v[60:63], v[136:139], a[12:15], v[60:63]\nbuffer_store_dwordx4 v[196:199], v224, s[64:67], s83 offen offset:1024 sc1\n"
"v_mfma_f32_16x16x32_bf16 v[60:63], v[132:135], v[44:47], v[60:63]\ns_nop 0\ns_add_u32 s83, s83, 0x10000\nv_mfma_f32_16x16x32_bf16 v[60:63], v[128:131], v[12:15], v[60:63]\n"
"s_waitcnt vmcnt(19) lgkmcnt(0)\ns_barrier\nv_mfma_f32_16x16x32_bf16 v[48:51], v[160:163], v[64:67], v[48:51]\ns_add_u32 s81, s81, 0xfffb3000\n"
"ds_read_b64_tr_b16 v[128:129], v212 offset:0\nv_mfma_f32_16x16x32_bf16 v[48:51], v[164:167], v[96:99], v[48:51]\ns_add_u32 m0, s73, 0x0\nds_read_b64_tr_b16 v[130:131], v212 offset:2048\n"
"v_mfma_f32_16x16x32_bf16 v[48:51], v[168:171], a[96:99], v[48:51]\nbuffer_load_dwordx4 v235, s[56:59], s81 offen lds\nv_mfma_f32_16x16x32_bf16 v[48:51], v[172:175], a[112:115], v[48:51]\ns_add_u32 s81, s81, 0x7000\n"
"ds_read_b64_tr_b16 v[132:133], v212 offset:4096\nv_mfma_f32_16x16x32_bf16 v[48:51], v[176:179], a[160:163], v[48:51]\ns_add_u32 m0, s73, 0x1000\nds_read_b64_tr_b16 v[134:135], v212 offset:6144\n"
"v_mfma_f32_16x16x32_bf16 v[48:51], v[180:183], a[176:179], v[48:51]\nbuffer_load_dwordx4 v235, s[56:59], s81 offen lds\nv_mfma_f32_16x16x32_bf16 v[48:51], v[184:187], a[192:195], v[48:51]\nds_read_b64_tr_b16 v[136:137], v213 offset:0\n"
"v_mfma_f32_16x16x32_bf16 v[48:51], v[188:191], a[208:211], v[48:51]\ns_add_u32 s81, s81, 0x15000\nds_read_b64_tr_b16 v[138:139], v213 offset:2048\nv_mfma_f32_16x16x32_bf16 v[52:55], v[188:191], a[212:215], v[52:55]\n"
"s_add_u32 m0, s73, 0x2000\nds_read_b64_tr_b16 v[140:141], v213 offset:4096\nv_mfma_f32_16x16x32_bf16 v[52:55], v[184:187], a[196:199], v[52:55]\nbuffer_load_dwordx4 v235, s[56:59], s81 offen lds\n"
"v_mfma_f32_16x16x32_bf16 v[52:55], v[180:183], a[180:183], v[52:55]\nds_read_b64_tr_b16 v[142:143], v213 offset:6144\nv_mfma_f32_16x16x32_bf16 v[52:55], v[176:179], a[164:167], v[52:55]\ns_add_u32 s81, s81, 0x7000\n"
"ds_read_b64_tr_b16 v[144:145], v214 offset:0\nv_cvt_pk_bf16_f32 v200, v48, v49\nv_mfma_f32_16x16x32_bf16 v[52:55], v[172:175], a[116:119], v[52:55]\ns_add_u32 m0, s73, 0x3000\n"
"ds_read_b64_tr_b16 v[146:147], v214 offset:2048\nv_cvt_pk_bf16_f32 v201, v50, v51\nv_mfma_f32_16x16x32_bf16 v[52:55], v[168:171], a[100:103], v[52:55]\nbuffer_load_dwordx4 v235, s[56:59], s81 offen lds\n"
"v_mfma_f32_16x16x32_bf16 v[52:55], v[164:167], v[100:103], v[52:55]\nds_read_b64_tr_b16 v[148:149], v214 offset:4096\nv_mfma_f32_16x16x32_bf16 v[52:55], v[160:163], v[68:71], v[52:55]\ns_add_u32 s81, s81, 0x15000\n"
"ds_read_b64_tr_b16 v[150:151], v214 offset:6144\nv_mfma_f32_16x16x32_bf16 v[56:59], v[160:163], v[72:75], v[56:59]\ns_add_u32 m0, s73, 0x4000\nds_read_b64_tr_b16 v[152:153], v215 offset:0\n"
"v_mfma_f32_16x16x32_bf16 v[56:59], v[164:167], v[104:107], v[56:59]\nbuffer_load_dwordx4 v235, s[56:59], s81 offen lds\nv_mfma_f32_16x16x32_bf16 v[56:59], v[168:171], a[104:107], v[56:59]\ns_add_u32 s81, s81, 0x7000\n"
"ds_read_b64_tr_b16 v[154:155], v215 offset:2048\nv_mfma_f32_16x16x32_bf16 v[56:59], v[172:175], a[120:123], v[56:59]\ns_add_u32 m0, s73, 0x5000\nds_read_b64_tr_b16 v[156:157], v215 offset:4096\n"
"v_cvt_pk_bf16_f32 v202, v52, v53\nv_mfma_f32_16x16x32_bf16 v[56:59], v[176:179], a[168:171], v[56:59]\nbuffer_load_dwordx4 v235, s[56:59], s81 offen lds\nv_mfma_f32_16x16x32_bf16 v[56:59], v[180:183], a[184:187], v[56:59]\n"
"ds_read_b64_tr_b16 v[158:159], v215 offset:6144\nv_cvt_pk_bf16_f32 v203, v54, v55\nv_mfma_f32_16x16x32_bf16 v[56:59], v[184:187], a[200:203], v[56:59]\ns_add_u32 s81, s81, 0x15000\n"
"v_mfma_f32_16x16x32_bf16 v[56:59], v[188:191], a[216:219], v[56:59]\ns_add_u32 m0, s73, 0x6000\nv_mfma_f32_16x16x32_bf16 v[60:63], v[188:191], a[220:223], v[60:63]\nbuffer_load_dwordx4 v235, s[56:59], s81 offen lds\n"
"v_mfma_f32_16x16x32_bf16 v[60:63], v[184:187], a[204:207], v[60:63]\nv_mfma_f32_16x16x32_bf16 v[60:63], v[180:183], a[188:191], v[60:63]\ns_add_u32 s81, s81, 0x7000\nv_mfma_f32_16x16x32_bf16 v[60:63], v[176:179], a[172:175], v[60:63]\n"
"s_add_u32 m0, s73, 0x7000\nv_cvt_pk_bf16_f32 v204, v56, v57\nv_cvt_pk_bf16_f32 v205, v58, v59\nv_mfma_f32_16x16x32_bf16 v[60:63], v[172:175], a[124:127], v[60:63]\n"
"buffer_load_dwordx4 v235, s[56:59], s81 offen lds\nv_mfma_f32_16x16x32_bf16 v[60:63], v[168:171], a[108:111], v[60:63]\nbuffer_store_dwordx4 v[200:203], v224, s[64:67], s83 offen offset:0 sc1\nv_mfma_f32_16x16x32_bf16 v[60:63], v[164:167], v[108:111], v[60:63]\n"
"v_mfma_f32_16x16x32_bf16 v[60:63], v[160:163], v[76:79], v[60:63]\ns_waitcnt vmcnt(19) lgkmcnt(0)\ns_barrier\nv_mfma_f32_16x16x32_bf16 v[16:19], v[128:131], v[0:3], 0\n"
"s_add_u32 s81, s81, 0xfff97080\nds_read_b64_tr_b16 v[160:161], v208 offset:32768\nv_mfma_f32_16x16x32_bf16 v[16:19], v[132:135], v[32:35], v[16:19]\ns_add_u32 m0, s73, 0x10000\n"
"ds_read_b64_tr_b16 v[162:163], v208 offset:34816\nv_mfma_f32_16x16x32_bf16 v[16:19], v[136:139], a[0:3], v[16:19]\nbuffer_load_dwordx4 v235, s[56:59], s81 offen lds\nv_mfma_f32_16x16x32_bf16 v[16:19], v[140:143], a[16:19], v[16:19]\n"
"s_add_u32 s81, s81, 0x7000\nds_read_b64_tr_b16 v[164:165], v208 offset:36864\nv_cvt_pk_bf16_f32 v206, v60, v61\nv_mfma_f32_16x16x32_bf16 v[16:19], v[144:147], a[32:35], v[16:19]\n"
"s_add_u32 m0, s73, 0x11000\nds_read_b64_tr_b16 v[166:167], v208 offset:38912\nv_cvt_pk_bf16_f32 v207, v62, v63\nv_mfma_f32_16x16x32_bf16 v[16:19], v[148:151], a[48:51], v[16:19]\n"
"buffer_load_dwordx4 v235, s[56:59], s81 offen lds\nv_mfma_f32_16x16x32_bf16 v[16:19], v[152:155], a[64:67], v[16:19]\nds_read_b64_tr_b16 v[168:169], v209 offset:32768\nv_mfma_f32_16x16x32_bf16 v[16:19], v[156:159], a[80:83], v[16:19]\n"
"s_add_u32 s81, s81, 0x15000\nds_read_b64_tr_b16 v[170:171], v209 offset:34816\nv_mfma_f32_16x16x32_bf16 v[20:23], v[156:159], a[84:87], 0\ns_add_u32 m0, s73, 0x12000\n"
"ds_read_b64_tr_b16 v[172:173], v209 offset:36864\nv_mfma_f32_16x16x32_bf16 v[20:23], v[152:155], a[68:71], v[20:23]\nbuffer_load_dwordx4 v235, s[56:59], s81 offen lds\nv_mfma_f32_16x16x32_bf16 v[20:23], v[148:151], a[52:55], v[20:23]\n"
"ds_read_b64_tr_b16 v[174:175], v209 offset:38912\nv_mfma_f32_16x16x32_bf16 v[20:23], v[144:147], a[36:39], v[20:23]\ns_add_u32 s81, s81, 0x7000\nds_read_b64_tr_b16 v[176:177], v210 offset:32768\n"
"v_mfma_f32_16x16x32_bf16 v[20:23], v[140:143], a[20:23], v[20:23]\ns_add_u32 m0, s73, 0x13000\nds_read_b64_tr_b16 v[178:179], v210 offset:34816\nv_mfma_f32_16x16x32_bf16 v[20:23], v[136:139], a[4:7], v[20:23]\n"
"buffer_load_dwordx4 v235, s[56:59], s81 offen lds\nv_mfma_f32_16x16x32_bf16 v[20:23], v[132:135], v[36:39], v[20:23]\nds_read_b64_tr_b16 v[180:181], v210 offset:36864\nv_mfma_f32_16x16x32_bf16 v[20:23], v[128:131], v[4:7], v[20:23]\n"
"s_add_u32 s81, s81, 0x15000\nds_read_b64_tr_b16 v[182:183], v210 offset:38912\nv_mfma_f32_16x16x32_bf16 v[24:27], v[128:131], v[8:11], 0\ns_add_u32 m0, s73, 0x14000\n"
"ds_read_b64_tr_b16 v[184:185], v211 offset:32768\nv_mfma_f32_16x16x32_bf16 v[24:27], v[132:135], v[40:43], v[24:27]\nbuffer_load_dwordx4 v235, s[56:59], s81 offen lds\nv_mfma_f32_16x16x32_bf16 v[24:27], v[136:139], a[8:11], v[24:27]\n"
"s_add_u32 s81, s81, 0x7000\nds_read_b64_tr_b16 v[186:187], v211 offset:34816\nv_mfma_f32_16x16x32_bf16 v[24:27], v[140:143], a[24:27], v[24:27]\ns_add_u32 m0, s73, 0x15000\n"
"ds_read_b64_tr_b16 v[188:189], v211 offset:36864\nv_mfma_f32_16x16x32_bf16 v[24:27], v[144:147], a[40:43], v[24:27]\nbuffer_load_dwordx4 v235, s[56:59], s81 offen lds\nv_mfma_f32_16x16x32_bf16 v[24:27], v[148:151], a[56:59], v[24:27]\n"
"ds_read_b64_tr_b16 v[190:191], v211 offset:38912\nv_mfma_f32_16x16x32_bf16 v[24:27], v[152:155], a[72:75], v[24:27]\ns_add_u32 s81, s81, 0x15000\nv_mfma_f32_16x16x32_bf16 v[24:27], v[156:159], a[88:91], v[24:27]\n"
"s_add_u32 m0, s73, 0x16000\nv_mfma_f32_16x16x32_bf16 v[28:31], v[156:159], a[92:95], 0\nbuffer_load_dwordx4 v235, s[56:59], s81 offen lds\nv_mfma_f32_16x16x32_bf16 v[28:31], v[152:155], a[76:79], v[28:31]\n"
"v_mfma_f32_16x16x32_bf16 v[28:31], v[148:151], a[60:63], v[28:31]\ns_add_u32 s81, s81, 0x7000\nv_mfma_f32_16x16x32_bf16 v[28:31], v[144:147], a[44:47], v[28:31]\ns_add_u32 m0, s73, 0x17000\n"
"v_mfma_f32_16x16x32_bf16 v[28:31], v[140:143], a[28:31], v[28:31]\nbuffer_load_dwordx4 v235, s[56:59], s81 offen lds\nv_mfma_f32_16x16x32_bf16 v[28:31], v[136:139], a[12:15], v[28:31]\nbuffer_store_dwordx4 v[204:207], v224, s[64:67], s83 offen offset:1024 sc1\n"
"v_mfma_f32_16x16x32_bf16 v[28:31], v[132:135], v[44:47], v[28:31]\ns_nop 0\ns_add_u32 s83, s83, 0x10000\nv_mfma_f32_16x16x32_bf16 v[28:31], v[128:131], v[12:15], v[28:31]\n"
"s_waitcnt vmcnt(19) lgkmcnt(0)\ns_barrier\nv_mfma_f32_16x16x32_bf16 v[16:19], v[160:163], v[64:67], v[16:19]\ns_add_u32 s81, s81, 0xfffb3000\n"
"ds_read_b64_tr_b16 v[128:129], v212 offset:32768\nv_mfma_f32_16x16x32_bf16 v[16:19], v[164:167], v[96:99], v[16:19]\ns_add_u32 m0, s73, 0x8000\nds_read_b64_tr_b16 v[130:131], v212 offset:34816\n"
"v_mfma_f32_16x16x32_bf16 v[16:19], v[168:171], a[96:99], v[16:19]\nbuffer_load_dwordx4 v235, s[56:59], s81 offen lds\nv_mfma_f32_16x16x32_bf16 v[16:19], v[172:175], a[112:115], v[16:19]\ns_add_u32 s81, s81, 0x7000\n"
"ds_read_b64_tr_b16 v[132:133], v212 offset:36864\nv_mfma_f32_16x16x32_bf16 v[16:19], v[176:179], a[160:163], v[16:19]\ns_add_u32 m0, s73, 0x9000\nds_read_b64_tr_b16 v[134:135], v212 offset:38912\n"
"v_mfma_f32_16x16x32_bf16 v[16:19], v[180:183], a[176:179], v[16:19]\nbuffer_load_dwordx4 v235, s[56:59], s81 offen lds\nv_mfma_f32_16x16x32_bf16 v[16:19], v[184:187], a[192:195], v[16:19]\nds_read_b64_tr_b16 v[136:137], v213 offset:32768\n"
"v_mfma_f32_16x16x32_bf16 v[16:19], v[188:191], a[208:211], v[16:19]\ns_add_u32 s81, s81, 0x15000\nds_read_b64_tr_b16 v[138:139], v213 offset:34816\nv_mfma_f32_16x16x32_bf16 v[20:23], v[188:191], a[212:215], v[20:23]\n"
"s_add_u32 m0, s73, 0xa000\nds_read_b64_tr_b16 v[140:141], v213 offset:36864\nv_mfma_f32_16x16x32_bf16 v[20:23], v[184:187], a[196:199], v[20:23]\nbuffer_load_dwordx4 v235, s[56:59], s81 offen lds\n"
"v_mfma_f32_16x16x32_bf16 v[20:23], v[180:183], a[180:183], v[20:23]\nds_read_b64_tr_b16 v[142:143], v213 offset:38912\nv_mfma_f32_16x16x32_bf16 v[20:23], v[176:179], a[164:167], v[20:23]\ns_add_u32 s81, s81, 0x7000\n"
"ds_read_b64_tr_b16 v[144:145], v214 offset:32768\nv_cvt_pk_bf16_f32 v192, v16, v17\nv_mfma_f32_16x16x32_bf16 v[20:23], v[172:175], a[116:119], v[20:23]\ns_add_u32 m0, s73, 0xb000\n"
"ds_read_b64_tr_b16 v[146:147], v214 offset:34816\nv_cvt_pk_bf16_f32 v193, v18, v19\nv_mfma_f32_16x16x32_bf16 v[20:23], v[168:171], a[100:103], v[20:23]\nbuffer_load_dwordx4 v235, s[56:59], s81 offen lds\n"
"v_mfma_f32_16x16x32_bf16 v[20:23], v[164:167], v[100:103], v[20:23]\nds_read_b64_tr_b16 v[148:149], v214 offset:36864\nv_mfma_f32_16x16x32_bf16 v[20:23], v[160:163], v[68:71], v[20:23]\ns_add_u32 s81, s81, 0x15000\n"
"ds_read_b64_tr_b16 v[150:151], v214 offset:38912\nv_mfma_f32_16x16x32_bf16 v[24:27], v[160:163], v[72:75], v[24:27]\ns_add_u32 m0, s73, 0xc000\nds_read_b64_tr_b16 v[152:153], v215 offset:32768\n"
"v_mfma_f32_16x16x32_bf16 v[24:27], v[164:167], v[104:107], v[24:27]\nbuffer_load_dwordx4 v235, s[56:59], s81 offen lds\nv_mfma_f32_16x16x32_bf16 v[24:27], v[168:171], a[104:107], v[24:27]\ns_add_u32 s81, s81, 0x7000\n"
"ds_read_b64_tr_b16 v[154:155], v215 offset:34816\nv_mfma_f32_16x16x32_bf16 v[24:27], v[172:175], a[120:123], v[24:27]\ns_add_u32 m0, s73, 0xd000\nds_read_b64_tr_b16 v[156:157], v215 offset:36864\n"
"v_cvt_pk_bf16_f32 v194, v20, v21\nv_mfma_f32_16x16x32_bf16 v[24:27], v[176:179], a[168:171], v[24:27]\nbuffer_load_dwordx4 v235, s[56:59], s81 offen lds\nv_mfma_f32_16x16x32_bf16 v[24:27], v[180:183], a[184:187], v[24:27]\n"
"ds_read_b64_tr_b16 v[158:159], v215 offset:38912\nv_cvt_pk_bf16_f32 v195, v22, v23\nv_mfma_f32_16x16x32_bf16 v[24:27], v[184:187], a[200:203], v[24:27]\ns_add_u32 s81, s81, 0x15000\n"
"v_mfma_f32_16x16x32_bf16 v[24:27], v[188:191], a[216:219], v[24:27]\ns_add_u32 m0, s73, 0xe000\nv_mfma_f32_16x16x32_bf16 v[28:31], v[188:191], a[220:223], v[28:31]\nbuffer_load_dwordx4 v235, s[56:59], s81 offen lds\n"
"v_mfma_f32_16x16x32_bf16 v[28:31], v[184:187], a[204:207], v[28:31]\nv_mfma_f32_16x16x32_bf16 v[28:31], v[180:183], a[188:191], v[28:31]\ns_add_u32 s81, s81, 0x7000\nv_mfma_f32_16x16x32_bf16 v[28:31], v[176:179], a[172:175], v[28:31]\n"
"s_add_u32 m0, s73, 0xf000\nv_cvt_pk_bf16_f32 v196, v24, v25\nv_cvt_pk_bf16_f32 v197, v26, v27\nv_mfma_f32_16x16x32_bf16 v[28:31], v[172:175], a[124:127], v[28:31]\n"
"buffer_load_dwordx4 v235, s[56:59], s81 offen lds\nv_mfma_f32_16x16x32_bf16 v[28:31], v[168:171], a[108:111], v[28:31]\nbuffer_store_dwordx4 v[192:195], v224, s[64:67], s83 offen offset:0 sc1\nv_mfma_f32_16x16x32_bf16 v[28:31], v[164:167], v[108:111], v[28:31]\n"
"v_mfma_f32_16x16x32_bf16 v[28:31], v[160:163], v[76:79], v[28:31]\ns_waitcnt vmcnt(19) lgkmcnt(0)\ns_barrier\nv_mfma_f32_16x16x32_bf16 v[48:51], v[128:131], v[0:3], 0\n"
"s_add_u32 s53, s55, 0x54000\nds_read_b64_tr_b16 v[160:161], v208 offset:0\nv_mfma_f32_16x16x32_bf16 v[48:51], v[132:135], v[32:35], v[48:51]\ns_add_u32 m0, s52, 0x8000\n"
"ds_read_b64_tr_b16 v[162:163], v208 offset:2048\nv_mfma_f32_16x16x32_bf16 v[48:51], v[136:139], a[0:3], v[48:51]\nbuffer_load_dwordx4 v225, s[36:39], s53 offen lds\nv_mfma_f32_16x16x32_bf16 v[48:51], v[140:143], a[16:19], v[48:51]\n"
"ds_read_b64_tr_b16 v[164:165], v208 offset:4096\nv_cvt_pk_bf16_f32 v198, v28, v29\nv_mfma_f32_16x16x32_bf16 v[48:51], v[144:147], a[32:35], v[48:51]\nds_read_b64_tr_b16 v[166:167], v208 offset:6144\n"
"v_cvt_pk_bf16_f32 v199, v30, v31\nv_mfma_f32_16x16x32_bf16 v[48:51], v[148:151], a[48:51], v[48:51]\nbuffer_store_dwordx4 v[196:199], v224, s[64:67], s83 offen offset:1024 sc1\nv_mfma_f32_16x16x32_bf16 v[48:51], v[152:155], a[64:67], v[48:51]\n"
"s_nop 0\ns_add_u32 s83, s83, 0x10000\nbuffer_load_dwordx2 a[224:225], v233, s[60:63], s86 offen offset:0 sc1\nv_mfma_f32_16x16x32_bf16 v[48:51], v[156:159], a[80:83], v[48:51]\n"
"s_add_u32 s53, s55, 0x55c00\nds_read_b64_tr_b16 v[168:169], v209 offset:0\nv_mfma_f32_16x16x32_bf16 v[52:55], v[156:159], a[84:87], 0\ns_add_u32 m0, s52, 0x8400\n"
"buffer_load_dwordx2 a[226:227], v233, s[60:63], s86 offen offset:512 sc1\nv_mfma_f32_16x16x32_bf16 v[52:55], v[152:155], a[68:71], v[52:55]\nbuffer_load_dwordx4 v225, s[36:39], s53 offen lds\nv_mfma_f32_16x16x32_bf16 v[52:55], v[148:151], a[52:55], v[52:55]\n"
"ds_read_b64_tr_b16 v[170:171], v209 offset:2048\nv_mfma_f32_16x16x32_bf16 v[52:55], v[144:147], a[36:39], v[52:55]\nds_read_b64_tr_b16 v[172:173], v209 offset:4096\nv_mfma_f32_16x16x32_bf16 v[52:55], v[140:143], a[20:23], v[52:55]\n"
"ds_read_b64_tr_b16 v[174:175], v209 offset:6144\nv_mfma_f32_16x16x32_bf16 v[52:55], v[136:139], a[4:7], v[52:55]\nds_read_b64_tr_b16 v[176:177], v210 offset:0\nv_mfma_f32_16x16x32_bf16 v[52:55], v[132:135], v[36:39], v[52:55]\n"
"ds_read_b64_tr_b16 v[178:179], v210 offset:2048\nv_mfma_f32_16x16x32_bf16 v[52:55], v[128:131], v[4:7], v[52:55]\ns_add_u32 s53, s55, 0xb6000\nds_read_b64_tr_b16 v[180:181], v210 offset:4096\n"
"v_mfma_f32_16x16x32_bf16 v[56:59], v[128:131], v[8:11], 0\ns_add_u32 m0, s52, 0x8800\nds_read_b64_tr_b16 v[182:183], v210 offset:6144\nv_mfma_f32_16x16x32_bf16 v[56:59], v[132:135], v[40:43], v[56:59]\n"
"buffer_load_dwordx4 v225, s[36:39], s53 offen lds\nv_mfma_f32_16x16x32_bf16 v[56:59], v[136:139], a[8:11], v[56:59]\nds_read_b64_tr_b16 v[184:185], v211 offset:0\nv_mfma_f32_16x16x32_bf16 v[56:59], v[140:143], a[24:27], v[56:59]\n"
"ds_read_b64_tr_b16 v[186:187], v211 offset:2048\nv_mfma_f32_16x16x32_bf16 v[56:59], v[144:147], a[40:43], v[56:59]\nds_read_b64_tr_b16 v[188:189], v211 offset:4096\nv_mfma_f32_16x16x32_bf16 v[56:59], v[148:151], a[56:59], v[56:59]\n"
"ds_read_b64_tr_b16 v[190:191], v211 offset:6144\nv_mfma_f32_16x16x32_bf16 v[56:59], v[152:155], a[72:75], v[56:59]\ns_add_u32 s53, s55, 0xb7c00\nbuffer_load_dwordx2 a[228:229], v233, s[60:63], s86 offen offset:1024 sc1\n"
"v_mfma_f32_16x16x32_bf16 v[56:59], v[156:159], a[88:91], v[56:59]\ns_add_u32 m0, s52, 0x8c00\nbuffer_load_dwordx2 a[230:231], v233, s[60:63], s86 offen offset:1536 sc1\nv_mfma_f32_16x16x32_bf16 v[60:63], v[156:159], a[92:95], 0\n"
"buffer_load_dwordx4 v225, s[36:39], s53 offen lds\nv_mfma_f32_16x16x32_bf16 v[60:63], v[152:155], a[76:79], v[60:63]\nbuffer_load_dwordx2 a[232:233], v233, s[60:63], s86 offen offset:2048 sc1\nv_mfma_f32_16x16x32_bf16 v[60:63], v[148:151], a[60:63], v[60:63]\n"
"buffer_load_dwordx2 a[234:235], v233, s[60:63], s86 offen offset:2560 sc1\nv_mfma_f32_16x16x32_bf16 v[60:63], v[144:147], a[44:47], v[60:63]\nbuffer_load_dwordx2 a[236:237], v233, s[60:63], s86 offen offset:3072 sc1\nv_mfma_f32_16x16x32_bf16 v[60:63], v[140:143], a[28:31], v[60:63]\n"
"buffer_load_dwordx2 a[238:239], v233, s[60:63], s86 offen offset:3584 sc1\nv_mfma_f32_16x16x32_bf16 v[60:63], v[136:139], a[12:15], v[60:63]\nv_mfma_f32_16x16x32_bf16 v[60:63], v[132:135], v[44:47], v[60:63]\nv_mfma_f32_16x16x32_bf16 v[60:63], v[128:131], v[12:15], v[60:63]\n"
"s_waitcnt vmcnt(23) lgkmcnt(0)\ns_barrier\nv_mfma_f32_16x16x32_bf16 v[48:51], v[160:163], v[64:67], v[48:51]\ns_add_u32 s53, s55, 0x118000\n"
"ds_read_b64_tr_b16 v[128:129], v212 offset:0\nv_mfma_f32_16x16x32_bf16 v[48:51], v[164:167], v[96:99], v[48:51]\ns_add_u32 m0, s52, 0xc000\nds_read_b64_tr_b16 v[130:131], v212 offset:2048\n"
"v_mfma_f32_16x16x32_bf16 v[48:51], v[168:171], a[96:99], v[48:51]\nbuffer_load_dwordx4 v225, s[36:39], s53 offen lds\nv_mfma_f32_16x16x32_bf16 v[48:51], v[172:175], a[112:115], v[48:51]\nds_read_b64_tr_b16 v[132:133], v212 offset:4096\n"
"v_mfma_f32_16x16x32_bf16 v[48:51], v[176:179], a[160:163], v[48:51]\nbuffer_load_dwordx2 a[240:241], v233, s[60:63], s87 offen offset:0 sc1\nv_mfma_f32_16x16x32_bf16 v[48:51], v[180:183], a[176:179], v[48:51]\nds_read_b64_tr_b16 v[134:135], v212 offset:6144\n"
"v_mfma_f32_16x16x32_bf16 v[48:51], v[184:187], a[192:195], v[48:51]\nds_read_b64_tr_b16 v[136:137], v213 offset:0\nv_mfma_f32_16x16x32_bf16 v[48:51], v[188:191], a[208:211], v[48:51]\ns_add_u32 s53, s55, 0x119c00\n"
"buffer_load_dwordx2 a[242:243], v233, s[60:63], s87 offen offset:512 sc1\nv_mfma_f32_16x16x32_bf16 v[52:55], v[188:191], a[212:215], v[52:55]\ns_add_u32 m0, s52, 0xc400\nds_read_b64_tr_b16 v[138:139], v213 offset:2048\n"
"v_mfma_f32_16x16x32_bf16 v[52:55], v[184:187], a[196:199], v[52:55]\nbuffer_load_dwordx4 v225, s[36:39], s53 offen lds\nv_mfma_f32_16x16x32_bf16 v[52:55], v[180:183], a[180:183], v[52:55]\nds_read_b64_tr_b16 v[140:141], v213 offset:4096\n"
"v_mfma_f32_16x16x32_bf16 v[52:55], v[176:179], a[164:167], v[52:55]\nds_read_b64_tr_b16 v[142:143], v213 offset:6144\nv_cvt_pk_bf16_f32 v200, v48, v49\nv_mfma_f32_16x16x32_bf16 v[52:55], v[172:175], a[116:119], v[52:55]\n"
"ds_read_b64_tr_b16 v[144:145], v214 offset:0\nv_cvt_pk_bf16_f32 v201, v50, v51\nv_mfma_f32_16x16x32_bf16 v[52:55], v[168:171], a[100:103], v[52:55]\nds_read_b64_tr_b16 v[146:147], v214 offset:2048\n"
"v_mfma_f32_16x16x32_bf16 v[52:55], v[164:167], v[100:103], v[52:55]\nds_read_b64_tr_b16 v[148:149], v214 offset:4096\nv_mfma_f32_16x16x32_bf16 v[52:55], v[160:163], v[68:71], v[52:55]\ns_add_u32 s53, s55, 0x17a000\n"
"ds_read_b64_tr_b16 v[150:151], v214 offset:6144\nv_mfma_f32_16x16x32_bf16 v[56:59], v[160:163], v[72:75], v[56:59]\ns_add_u32 m0, s52, 0xc800\nbuffer_load_dwordx2 a[244:245], v233, s[60:63], s87 offen offset:1024 sc1\n"
"v_mfma_f32_16x16x32_bf16 v[56:59], v[164:167], v[104:107], v[56:59]\nbuffer_load_dwordx4 v225, s[36:39], s53 offen lds\nv_mfma_f32_16x16x32_bf16 v[56:59], v[168:171], a[104:107], v[56:59]\nds_read_b64_tr_b16 v[152:153], v215 offset:0\n"
"v_mfma_f32_16x16x32_bf16 v[56:59], v[172:175], a[120:123], v[56:59]\nds_read_b64_tr_b16 v[154:155], v215 offset:2048\nv_cvt_pk_bf16_f32 v202, v52, v53\nv_mfma_f32_16x16x32_bf16 v[56:59], v[176:179], a[168:171], v[56:59]\n"
"ds_read_b64_tr_b16 v[156:157], v215 offset:4096\nv_cvt_pk_bf16_f32 v203, v54, v55\nv_mfma_f32_16x16x32_bf16 v[56:59], v[180:183], a[184:187], v[56:59]\nds_read_b64_tr_b16 v[158:159], v215 offset:6144\n"
"v_mfma_f32_16x16x32_bf16 v[56:59], v[184:187], a[200:203], v[56:59]\ns_add_u32 s53, s55, 0x17bc00\nbuffer_store_dwordx4 v[200:203], v224, s[64:67], s83 offen offset:0 sc1\nv_mfma_f32_16x16x32_bf16 v[56:59], v[188:191], a[216:219], v[56:59]\n"
"s_add_u32 m0, s52, 0xcc00\nbuffer_load_dwordx2 a[246:247], v233, s[60:63], s87 offen offset:1536 sc1\nv_mfma_f32_16x16x32_bf16 v[60:63], v[188:191], a[220:223], v[60:63]\nbuffer_load_dwordx4 v225, s[36:39], s53 offen lds\n"
"v_mfma_f32_16x16x32_bf16 v[60:63], v[184:187], a[204:207], v[60:63]\nbuffer_load_dwordx2 a[248:249], v233, s[60:63], s87 offen offset:2048 sc1\nv_mfma_f32_16x16x32_bf16 v[60:63], v[180:183], a[188:191], v[60:63]\nbuffer_load_dwordx2 a[250:251], v233, s[60:63], s87 offen offset:2560 sc1\n"
"v_mfma_f32_16x16x32_bf16 v[60:63], v[176:179], a[172:175], v[60:63]\nbuffer_load_dwordx2 a[252:253], v233, s[60:63], s87 offen offset:3072 sc1\nv_mfma_f32_16x16x32_bf16 v[60:63], v[172:175], a[124:127], v[60:63]\nbuffer_load_dwordx2 a[254:255], v233, s[60:63], s87 offen offset:3584 sc1\n"
"v_mfma_f32_16x16x32_bf16 v[60:63], v[168:171], a[108:111], v[60:63]\nv_cvt_pk_bf16_f32 v204, v56, v57\nv_cvt_pk_bf16_f32 v205, v58, v59\nv_mfma_f32_16x16x32_bf16 v[60:63], v[164:167], v[108:111], v[60:63]\n"
"v_mfma_f32_16x16x32_bf16 v[60:63], v[160:163], v[76:79], v[60:63]\ns_waitcnt vmcnt(27) lgkmcnt(0)\ns_barrier\nv_mfma_f32_16x16x32_bf16 v[16:19], v[128:131], v[0:3], 0\n"
"s_add_u32 s53, s55, 0x0\nds_read_b64_tr_b16 v[160:161], v208 offset:32768\nv_mfma_f32_16x16x32_bf16 v[16:19], v[132:135], v[32:35], v[16:19]\ns_add_u32 m0, s52, 0x0\n"
"ds_read_b64_tr_b16 v[162:163], v208 offset:34816\nv_mfma_f32_16x16x32_bf16 v[16:19], v[136:139], a[0:3], v[16:19]\nbuffer_load_dwordx4 v225, s[36:39], s53 offen lds\nv_mfma_f32_16x16x32_bf16 v[16:19], v[140:143], a[16:19], v[16:19]\n"
"ds_read_b64_tr_b16 v[164:165], v208 offset:36864\nv_cvt_pk_bf16_f32 v206, v60, v61\nv_mfma_f32_16x16x32_bf16 v[16:19], v[144:147], a[32:35], v[16:19]\nds_read_b64_tr_b16 v[166:167], v208 offset:38912\n"
"v_cvt_pk_bf16_f32 v207, v62, v63\nv_mfma_f32_16x16x32_bf16 v[16:19], v[148:151], a[48:51], v[16:19]\nbuffer_store_dwordx4 v[204:207], v224, s[64:67], s83 offen offset:1024 sc1\nv_mfma_f32_16x16x32_bf16 v[16:19], v[152:155], a[64:67], v[16:19]\n"
"s_nop 0\ns_add_u32 s83, s83, 0x10000\nbuffer_load_dwordx4 a[128:131], v224, s[60:63], s88 offen offset:0 sc1\nv_mfma_f32_16x16x32_bf16 v[16:19], v[156:159], a[80:83], v[16:19]\n"
"s_add_u32 s53, s55, 0x1c00\nds_read_b64_tr_b16 v[168:169], v209 offset:32768\nv_mfma_f32_16x16x32_bf16 v[20:23], v[156:159], a[84:87], 0\ns_add_u32 m0, s52, 0x400\n"
"ds_read_b64_tr_b16 v[170:171], v209 offset:34816\nv_mfma_f32_16x16x32_bf16 v[20:23], v[152:155], a[68:71], v[20:23]\nbuffer_load_dwordx4 v225, s[36:39], s53 offen lds\nv_mfma_f32_16x16x32_bf16 v[20:23], v[148:151], a[52:55], v[20:23]\n"
"ds_read_b64_tr_b16 v[172:173], v209 offset:36864\nv_mfma_f32_16x16x32_bf16 v[20:23], v[144:147], a[36:39], v[20:23]\nbuffer_load_dwordx4 a[132:135], v224, s[60:63], s88 offen offset:1024 sc1\nv_mfma_f32_16x16x32_bf16 v[20:23], v[140:143], a[20:23], v[20:23]\n"
"ds_read_b64_tr_b16 v[174:175], v209 offset:38912\nv_mfma_f32_16x16x32_bf16 v[20:23], v[136:139], a[4:7], v[20:23]\nds_read_b64_tr_b16 v[176:177], v210 offset:32768\nv_mfma_f32_16x16x32_bf16 v[20:23], v[132:135], v[36:39], v[20:23]\n"
"ds_read_b64_tr_b16 v[178:179], v210 offset:34816\nv_mfma_f32_16x16x32_bf16 v[20:23], v[128:131], v[4:7], v[20:23]\ns_add_u32 s53, s55, 0xe000\nds_read_b64_tr_b16 v[180:181], v210 offset:36864\n"
"v_mfma_f32_16x16x32_bf16 v[24:27], v[128:131], v[8:11], 0\ns_add_u32 m0, s52, 0x800\nds_read_b64_tr_b16 v[182:183], v210 offset:38912\nv_mfma_f32_16x16x32_bf16 v[24:27], v[132:135], v[40:43], v[24:27]\n"
"buffer_load_dwordx4 v225, s[36:39], s53 offen lds\nv_mfma_f32_16x16x32_bf16 v[24:27], v[136:139], a[8:11], v[24:27]\nbuffer_load_dwordx4 a[136:139], v224, s[60:63], s88 offen offset:2048 sc1\nv_mfma_f32_16x16x32_bf16 v[24:27], v[140:143], a[24:27], v[24:27]\n"
"ds_read_b64_tr_b16 v[184:185], v211 offset:32768\nv_mfma_f32_16x16x32_bf16 v[24:27], v[144:147], a[40:43], v[24:27]\nds_read_b64_tr_b16 v[186:187], v211 offset:34816\nv_mfma_f32_16x16x32_bf16 v[24:27], v[148:151], a[56:59], v[24:27]\n"
"ds_read_b64_tr_b16 v[188:189], v211 offset:36864\nv_mfma_f32_16x16x32_bf16 v[24:27], v[152:155], a[72:75], v[24:27]\ns_add_u32 s53, s55, 0xfc00\nds_read_b64_tr_b16 v[190:191], v211 offset:38912\n"
"v_mfma_f32_16x16x32_bf16 v[24:27], v[156:159], a[88:91], v[24:27]\ns_add_u32 m0, s52, 0xc00\nv_mfma_f32_16x16x32_bf16 v[28:31], v[156:159], a[92:95], 0\nbuffer_load_dwordx4 v225, s[36:39], s53 offen lds\n"
"v_mfma_f32_16x16x32_bf16 v[28:31], v[152:155], a[76:79], v[28:31]\nbuffer_load_dwordx4 a[140:143], v224, s[60:63], s88 offen offset:3072 sc1\nv_mfma_f32_16x16x32_bf16 v[28:31], v[148:151], a[60:63], v[28:31]\nv_mfma_f32_16x16x32_bf16 v[28:31], v[144:147], a[44:47], v[28:31]\n"
"v_mfma_f32_16x16x32_bf16 v[28:31], v[140:143], a[28:31], v[28:31]\nv_mfma_f32_16x16x32_bf16 v[28:31], v[136:139], a[12:15], v[28:31]\nv_mfma_f32_16x16x32_bf16 v[28:31], v[132:135], v[44:47], v[28:31]\nv_mfma_f32_16x16x32_bf16 v[28:31], v[128:131], v[12:15], v[28:31]\n"
"s_waitcnt lgkmcnt(0)\ns_barrier\nv_mfma_f32_16x16x32_bf16 v[16:19], v[160:163], v[64:67], v[16:19]\ns_add_u32 s53, s55, 0x1c000\n"
"v_mfma_f32_16x16x32_bf16 v[16:19], v[164:167], v[96:99], v[16:19]\ns_add_u32 m0, s52, 0x4000\nv_mfma_f32_16x16x32_bf16 v[16:19], v[168:171], a[96:99], v[16:19]\nbuffer_load_dwordx4 v225, s[36:39], s53 offen lds\n"
"v_mfma_f32_16x16x32_bf16 v[16:19], v[172:175], a[112:115], v[16:19]\nv_mfma_f32_16x16x32_bf16 v[16:19], v[176:179], a[160:163], v[16:19]\nbuffer_load_dwordx4 a[144:147], v224, s[60:63], s89 offen offset:0 sc1\nv_mfma_f32_16x16x32_bf16 v[16:19], v[180:183], a[176:179], v[16:19]\n"
"v_mfma_f32_16x16x32_bf16 v[16:19], v[184:187], a[192:195], v[16:19]\nv_mfma_f32_16x16x32_bf16 v[16:19], v[188:191], a[208:211], v[16:19]\ns_add_u32 s53, s55, 0x1dc00\nv_mfma_f32_16x16x32_bf16 v[20:23], v[188:191], a[212:215], v[20:23]\n"
"s_add_u32 m0, s52, 0x4400\nv_mfma_f32_16x16x32_bf16 v[20:23], v[184:187], a[196:199], v[20:23]\nbuffer_load_dwordx4 v225, s[36:39], s53 offen lds\nv_mfma_f32_16x16x32_bf16 v[20:23], v[180:183], a[180:183], v[20:23]\n"
"v_mfma_f32_16x16x32_bf16 v[20:23], v[176:179], a[164:167], v[20:23]\ns_nop 0\nv_cvt_pk_bf16_f32 v192, v16, v17\nv_cvt_pk_bf16_f32 v193, v18, v19\n"
"v_mfma_f32_16x16x32_bf16 v[20:23], v[172:175], a[116:119], v[20:23]\nbuffer_load_dwordx4 a[148:151], v224, s[60:63], s89 offen offset:1024 sc1\nv_mfma_f32_16x16x32_bf16 v[20:23], v[168:171], a[100:103], v[20:23]\nv_mfma_f32_16x16x32_bf16 v[20:23], v[164:167], v[100:103], v[20:23]\n"
"v_mfma_f32_16x16x32_bf16 v[20:23], v[160:163], v[68:71], v[20:23]\ns_add_u32 s53, s55, 0x2a000\nv_mfma_f32_16x16x32_bf16 v[24:27], v[160:163], v[72:75], v[24:27]\ns_add_u32 m0, s52, 0x4800\n"
"v_mfma_f32_16x16x32_bf16 v[24:27], v[164:167], v[104:107], v[24:27]\nbuffer_load_dwordx4 v225, s[36:39], s53 offen lds\nv_mfma_f32_16x16x32_bf16 v[24:27], v[168:171], a[104:107], v[24:27]\nbuffer_load_dwordx4 a[152:155], v224, s[60:63], s89 offen offset:2048 sc1\n"
"v_mfma_f32_16x16x32_bf16 v[24:27], v[172:175], a[120:123], v[24:27]\nv_cvt_pk_bf16_f32 v194, v20, v21\nv_cvt_pk_bf16_f32 v195, v22, v23\nv_mfma_f32_16x16x32_bf16 v[24:27], v[176:179], a[168:171], v[24:27]\n"
"buffer_store_dwordx4 v[192:195], v224, s[64:67], s83 offen offset:0 sc1\nv_mfma_f32_16x16x32_bf16 v[24:27], v[180:183], a[184:187], v[24:27]\nv_mfma_f32_16x16x32_bf16 v[24:27], v[184:187], a[200:203], v[24:27]\ns_add_u32 s53, s55, 0x2bc00\n"
"v_mfma_f32_16x16x32_bf16 v[24:27], v[188:191], a[216:219], v[24:27]\ns_add_u32 m0, s52, 0x4c00\nv_mfma_f32_16x16x32_bf16 v[28:31], v[188:191], a[220:223], v[28:31]\nbuffer_load_dwordx4 v225, s[36:39], s53 offen lds\n"
"v_mfma_f32_16x16x32_bf16 v[28:31], v[184:187], a[204:207], v[28:31]\nbuffer_load_dwordx4 a[156:159], v224, s[60:63], s89 offen offset:3072 sc1\nv_mfma_f32_16x16x32_bf16 v[28:31], v[180:183], a[188:191], v[28:31]\nv_mfma_f32_16x16x32_bf16 v[28:31], v[176:179], a[172:175], v[28:31]\n"
"s_nop 0\nv_cvt_pk_bf16_f32 v196, v24, v25\nv_cvt_pk_bf16_f32 v197, v26, v27\nv_mfma_f32_16x16x32_bf16 v[28:31], v[172:175], a[124:127], v[28:31]\n"
"v_mfma_f32_16x16x32_bf16 v[28:31], v[168:171], a[108:111], v[28:31]\nv_mfma_f32_16x16x32_bf16 v[28:31], v[164:167], v[108:111], v[28:31]\nv_mfma_f32_16x16x32_bf16 v[28:31], v[160:163], v[76:79], v[28:31]\ns_nop 7\n"
"v_cvt_pk_bf16_f32 v198, v28, v29\nv_cvt_pk_bf16_f32 v199, v30, v31\nbuffer_store_dwordx4 v[196:199], v224, s[64:67], s83 offen offset:1024 sc1\ns_nop 1\n"
"s_add_u32 s83, s83, 0x10000\ns_cmp_ge_u32 s75, 28\ns_cbranch_scc1 L_nt%=\ns_add_u32 m0, s85, 0x14000\ns_add_u32 s53, s84, 0\n"
"buffer_load_dwordx4 v239, s[40:43], s53 offen lds\ns_add_u32 m0, s85, 0x14400\ns_add_u32 s53, s84, 7168\nbuffer_load_dwordx4 v239, s[40:43], s53 offen lds\n"
"s_waitcnt vmcnt(42)\nds_read_b64_tr_b16 v[0:1], v226 offset:32768\ns_waitcnt vmcnt(38)\nds_read_b64_tr_b16 v[2:3], v226 offset:34816\n"
"ds_read_b64_tr_b16 v[4:5], v227 offset:32768\nds_read_b64_tr_b16 v[6:7], v227 offset:34816\nds_read_b64_tr_b16 v[8:9], v228 offset:32768\nds_read_b64_tr_b16 v[10:11], v228 offset:34816\n"
"ds_read_b64_tr_b16 v[12:13], v229 offset:32768\nds_read_b64_tr_b16 v[14:15], v229 offset:34816\ns_waitcnt lgkmcnt(6)\nv_mfma_f32_16x16x32_bf16 v[16:19], v[0:3], a[224:227], 0\n"
"v_mfma_f32_16x16x32_bf16 v[20:23], v[0:3], a[228:231], 0\ns_waitcnt vmcnt(36)\nv_mfma_f32_16x16x32_bf16 v[24:27], v[0:3], a[232:235], 0\ns_waitcnt vmcnt(34)\n"
"v_mfma_f32_16x16x32_bf16 v[28:31], v[0:3], a[236:239], 0\ns_waitcnt lgkmcnt(4)\nv_mfma_f32_16x16x32_bf16 v[32:35], v[4:7], a[224:227], 0\nv_mfma_f32_16x16x32_bf16 v[36:39], v[4:7], a[228:231], 0\n"
"v_mfma_f32_16x16x32_bf16 v[40:43], v[4:7], a[232:235], 0\nv_mfma_f32_16x16x32_bf16 v[44:47], v[4:7], a[236:239], 0\ns_waitcnt lgkmcnt(0)\nv_mfma_f32_16x16x32_bf16 v[48:51], v[8:11], a[224:227], 0\n"
"v_mfma_f32_16x16x32_bf16 v[52:55], v[8:11], a[228:231], 0\nv_mfma_f32_16x16x32_bf16 v[56:59], v[8:11], a[232:235], 0\nv_mfma_f32_16x16x32_bf16 v[60:63], v[8:11], a[236:239], 0\nv_mfma_f32_16x16x32_bf16 v[64:67], v[12:15], a[224:227], 0\n"
"v_mfma_f32_16x16x32_bf16 v[68:71], v[12:15], a[228:231], 0\nv_mfma_f32_16x16x32_bf16 v[72:75], v[12:15], a[232:235], 0\nv_mfma_f32_16x16x32_bf16 v[76:79], v[12:15], a[236:239], 0\ns_waitcnt vmcnt(0)\n"
"s_barrier\nbuffer_store_dword v252, v253, s[4:7], s33 offen offset:64 sc1\nds_read_b64 v[156:157], v240 offset:0\nds_read_b64 v[158:159], v240 offset:2048\n"
"ds_read_b64 v[160:161], v240 offset:4096\nds_read_b64 v[162:163], v240 offset:6144\nds_read_b64_tr_b16 v[0:1], v226 offset:49152\nds_read_b64_tr_b16 v[2:3], v226 offset:51200\n"
"ds_read_b64_tr_b16 v[4:5], v227 offset:49152\nds_read_b64_tr_b16 v[6:7], v227 offset:51200\nds_read_b64_tr_b16 v[8:9], v228 offset:49152\nds_read_b64_tr_b16 v[10:11], v228 offset:51200\n"
"ds_read_b64_tr_b16 v[12:13], v229 offset:49152\nds_read_b64_tr_b16 v[14:15], v229 offset:51200\ns_add_u32 m0, s52, 0x8000\ns_add_u32 s53, s55, 0x38000\n"
"buffer_load_dwordx4 v225, s[36:39], s53 offen lds\ns_add_u32 m0, s52, 0x8400\ns_add_u32 s53, s55, 0x39c00\nbuffer_load_dwordx4 v225, s[36:39], s53 offen lds\n"
"s_waitcnt lgkmcnt(6)\nv_mfma_f32_16x16x32_bf16 v[16:19], v[0:3], a[240:243], v[16:19]\nv_mfma_f32_16x16x32_bf16 v[20:23], v[0:3], a[244:247], v[20:23]\nv_mfma_f32_16x16x32_bf16 v[24:27], v[0:3], a[248:251], v[24:27]\n"
"v_mfma_f32_16x16x32_bf16 v[28:31], v[0:3], a[252:255], v[28:31]\ns_add_u32 m0, s52, 0x8800\ns_add_u32 s53, s55, 0x46000\nbuffer_load_dwordx4 v225, s[36:39], s53 offen lds\n"
"s_add_u32 m0, s52, 0x8c00\ns_add_u32 s53, s55, 0x47c00\nbuffer_load_dwordx4 v225, s[36:39], s53 offen lds\ns_waitcnt lgkmcnt(4)\n"
"v_mfma_f32_16x16x32_bf16 v[32:35], v[4:7], a[240:243], v[32:35]\nv_mfma_f32_16x16x32_bf16 v[36:39], v[4:7], a[244:247], v[36:39]\nv_mfma_f32_16x16x32_bf16 v[40:43], v[4:7], a[248:251], v[40:43]\nv_mfma_f32_16x16x32_bf16 v[44:47], v[4:7], a[252:255], v[44:47]\n"
"s_add_u32 s86, s80, 0x8000\nbuffer_load_dwordx4 a[224:227], v224, s[60:63], s86 offen offset:0 sc1\nbuffer_load_dwordx4 a[228:231], v224, s[60:63], s86 offen offset:1024 sc1\ns_waitcnt lgkmcnt(0)\n"
"v_mfma_f32_16x16x32_bf16 v[48:51], v[8:11], a[240:243], v[48:51]\nv_mfma_f32_16x16x32_bf16 v[52:55], v[8:11], a[244:247], v[52:55]\nv_mfma_f32_16x16x32_bf16 v[56:59], v[8:11], a[248:251], v[56:59]\nv_mfma_f32_16x16x32_bf16 v[60:63], v[8:11], a[252:255], v[60:63]\n"
"buffer_load_dwordx4 a[232:235], v224, s[60:63], s86 offen offset:2048 sc1\nbuffer_load_dwordx4 a[236:239], v224, s[60:63], s86 offen offset:3072 sc1\nv_mfma_f32_16x16x32_bf16 v[64:67], v[12:15], a[240:243], v[64:67]\nv_mfma_f32_16x16x32_bf16 v[68:71], v[12:15], a[244:247], v[68:71]\n"
"v_mfma_f32_16x16x32_bf16 v[72:75], v[12:15], a[248:251], v[72:75]\nv_mfma_f32_16x16x32_bf16 v[76:79], v[12:15], a[252:255], v[76:79]\nv_lshlrev_b32 v200, 16, v156\nv_and_b32 v201, 0xffff0000, v156\n"
"v_lshlrev_b32 v202, 16, v157\nv_and_b32 v203, 0xffff0000, v157\nv_lshlrev_b32 v204, 16, v158\nv_and_b32 v205, 0xffff0000, v158\n"
"v_lshlrev_b32 v206, 16, v159\nv_and_b32 v207, 0xffff0000, v159\nv_lshlrev_b32 v208, 16, v160\nv_and_b32 v209, 0xffff0000, v160\n"
"v_lshlrev_b32 v210, 16, v161\nv_and_b32 v211, 0xffff0000, v161\nv_lshlrev_b32 v212, 16, v162\nv_and_b32 v213, 0xffff0000, v162\n"
"v_lshlrev_b32 v214, 16, v163\nv_and_b32 v215, 0xffff0000, v163\nds_read_b64_tr_b16 v[0:1], v226 offset:0\nds_read_b64_tr_b16 v[2:3], v226 offset:2048\n"
"ds_read_b64_tr_b16 v[4:5], v227 offset:0\nds_read_b64_tr_b16 v[6:7], v227 offset:2048\nds_read_b64_tr_b16 v[8:9], v228 offset:0\nds_read_b64_tr_b16 v[10:11], v228 offset:2048\n"
"ds_read_b64_tr_b16 v[12:13], v229 offset:0\nds_read_b64_tr_b16 v[14:15], v229 offset:2048\ns_add_u32 m0, s52, 0xc000\ns_add_u32 s53, s55, 0x62000\n"
"buffer_load_dwordx4 v225, s[36:39], s53 offen lds\ns_add_u32 m0, s52, 0xc400\ns_add_u32 s53, s55, 0x63c00\nbuffer_load_dwordx4 v225, s[36:39], s53 offen lds\n"
"s_waitcnt lgkmcnt(6)\nv_mfma_f32_16x16x32_bf16 v[16:19], v[0:3], a[128:131], v[16:19]\nv_mfma_f32_16x16x32_bf16 v[20:23], v[0:3], a[132:135], v[20:23]\nv_mfma_f32_16x16x32_bf16 v[24:27], v[0:3], a[136:139], v[24:27]\n"
"v_mfma_f32_16x16x32_bf16 v[28:31], v[0:3], a[140:143], v[28:31]\ns_add_u32 m0, s52, 0xc800\ns_add_u32 s53, s55, 0x70000\nbuffer_load_dwordx4 v225, s[36:39], s53 offen lds\n"
"s_add_u32 m0, s52, 0xcc00\ns_add_u32 s53, s55, 0x71c00\nbuffer_load_dwordx4 v225, s[36:39], s53 offen lds\ns_waitcnt lgkmcnt(4)\n"
"v_mfma_f32_16x16x32_bf16 v[32:35], v[4:7], a[128:131], v[32:35]\nv_mfma_f32_16x16x32_bf16 v[36:39], v[4:7], a[132:135], v[36:39]\nv_mfma_f32_16x16x32_bf16 v[40:43], v[4:7], a[136:139], v[40:43]\nv_mfma_f32_16x16x32_bf16 v[44:47], v[4:7], a[140:143], v[44:47]\n"
"s_add_u32 s87, s80, 0xc000\nbuffer_load_dwordx4 a[240:243], v224, s[60:63], s87 offen offset:0 sc1\nbuffer_load_dwordx4 a[244:247], v224, s[60:63], s87 offen offset:1024 sc1\ns_waitcnt lgkmcnt(0)\n"
"v_mfma_f32_16x16x32_bf16 v[48:51], v[8:11], a[128:131], v[48:51]\nv_mfma_f32_16x16x32_bf16 v[52:55], v[8:11], a[132:135], v[52:55]\nv_mfma_f32_16x16x32_bf16 v[56:59], v[8:11], a[136:139], v[56:59]\nv_mfma_f32_16x16x32_bf16 v[60:63], v[8:11], a[140:143], v[60:63]\n"
"buffer_load_dwordx4 a[248:251], v224, s[60:63], s87 offen offset:2048 sc1\nbuffer_load_dwordx4 a[252:255], v224, s[60:63], s87 offen offset:3072 sc1\nv_mfma_f32_16x16x32_bf16 v[64:67], v[12:15], a[128:131], v[64:67]\nv_mfma_f32_16x16x32_bf16 v[68:71], v[12:15], a[132:135], v[68:71]\n"
"v_mfma_f32_16x16x32_bf16 v[72:75], v[12:15], a[136:139], v[72:75]\nv_mfma_f32_16x16x32_bf16 v[76:79], v[12:15], a[140:143], v[76:79]\nv_lshrrev_b32 v236, 4, v254\nv_bfe_u32 v237, v254, 2, 2\n"
"v_xor_b32 v236, v236, v237\nv_bfe_u32 v237, v254, 1, 1\nv_lshl_add_u32 v236, v236, 1, v237\nv_and_b32 v237, 1, v254\n"
"v_lshlrev_b32 v237, 4, v237\nv_mov_b32 v238, 0x3f80\nv_lshlrev_b32 v238, v237, v238\nv_mov_b32 v80, 0\n"
"v_mov_b32 v81, 0\nv_mov_b32 v84, 0\nv_mov_b32 v85, 0\nv_cmp_eq_u32 vcc, 0, v236\n"
"s_nop 1\nv_cndmask_b32 v82, 0, v238, vcc\nv_cmp_eq_u32 vcc, 1, v236\ns_nop 1\n"
"v_cndmask_b32 v83, 0, v238, vcc\nds_read_b64_tr_b16 v[0:1], v226 offset:16384\nds_read_b64_tr_b16 v[2:3], v226 offset:18432\nds_read_b64_tr_b16 v[4:5], v227 offset:16384\n"
"ds_read_b64_tr_b16 v[6:7], v227 offset:18432\nds_read_b64_tr_b16 v[8:9], v228 offset:16384\nds_read_b64_tr_b16 v[10:11], v228 offset:18432\nds_read_b64_tr_b16 v[12:13], v229 offset:16384\n"
"ds_read_b64_tr_b16 v[14:15], v229 offset:18432\ns_add_u32 m0, s52, 0x0\ns_add_u32 s53, s55, 0x7e000\nbuffer_load_dwordx4 v225, s[36:39], s53 offen lds\n"
"s_add_u32 m0, s52, 0x400\ns_add_u32 s53, s55, 0x7fc00\nbuffer_load_dwordx4 v225, s[36:39], s53 offen lds\ns_waitcnt lgkmcnt(6)\n"
"v_mfma_f32_16x16x32_bf16 v[16:19], v[0:3], a[144:147], v[16:19]\nv_mfma_f32_16x16x32_bf16 v[20:23], v[0:3], a[148:151], v[20:23]\nv_mfma_f32_16x16x32_bf16 v[24:27], v[0:3], a[152:155], v[24:27]\nv_mfma_f32_16x16x32_bf16 v[28:31], v[0:3], a[156:159], v[28:31]\n"
"s_add_u32 m0, s52, 0x800\ns_add_u32 s53, s55, 0x8c000\nbuffer_load_dwordx4 v225, s[36:39], s53 offen lds\ns_add_u32 m0, s52, 0xc00\n"
"s_add_u32 s53, s55, 0x8dc00\nbuffer_load_dwordx4 v225, s[36:39], s53 offen lds\ns_waitcnt lgkmcnt(4)\nv_mfma_f32_16x16x32_bf16 v[32:35], v[4:7], a[144:147], v[32:35]\n"
"v_mfma_f32_16x16x32_bf16 v[36:39], v[4:7], a[148:151], v[36:39]\nv_mfma_f32_16x16x32_bf16 v[40:43], v[4:7], a[152:155], v[40:43]\nv_mfma_f32_16x16x32_bf16 v[44:47], v[4:7], a[156:159], v[44:47]\ns_add_u32 s88, s80, 0x10000\n"
"buffer_load_dwordx4 a[128:131], v224, s[60:63], s88 offen offset:0 sc1\nbuffer_load_dwordx4 a[132:135], v224, s[60:63], s88 offen offset:1024 sc1\ns_waitcnt lgkmcnt(0)\nv_mfma_f32_16x16x32_bf16 v[48:51], v[8:11], a[144:147], v[48:51]\n"
"v_mfma_f32_16x16x32_bf16 v[52:55], v[8:11], a[148:151], v[52:55]\nv_mfma_f32_16x16x32_bf16 v[56:59], v[8:11], a[152:155], v[56:59]\nv_mfma_f32_16x16x32_bf16 v[60:63], v[8:11], a[156:159], v[60:63]\nbuffer_load_dwordx4 a[136:139], v224, s[60:63], s88 offen offset:2048 sc1\n"
"buffer_load_dwordx4 a[140:143], v224, s[60:63], s88 offen offset:3072 sc1\nv_mfma_f32_16x16x32_bf16 v[64:67], v[12:15], a[144:147], v[64:67]\nv_mfma_f32_16x16x32_bf16 v[68:71], v[12:15], a[148:151], v[68:71]\nv_mfma_f32_16x16x32_bf16 v[72:75], v[12:15], a[152:155], v[72:75]\n"
"v_mfma_f32_16x16x32_bf16 v[76:79], v[12:15], a[156:159], v[76:79]\ns_waitcnt vmcnt(22)\nds_read_b64_tr_b16 v[0:1], v226 offset:32768\ns_waitcnt vmcnt(20)\n"
"ds_read_b64_tr_b16 v[2:3], v226 offset:34816\nds_read_b64_tr_b16 v[4:5], v227 offset:32768\nds_read_b64_tr_b16 v[6:7], v227 offset:34816\nds_read_b64_tr_b16 v[8:9], v228 offset:32768\n"
"ds_read_b64_tr_b16 v[10:11], v228 offset:34816\nds_read_b64_tr_b16 v[12:13], v229 offset:32768\nds_read_b64_tr_b16 v[14:15], v229 offset:34816\ns_add_u32 m0, s52, 0x4000\n"
"s_add_u32 s53, s55, 0x9a000\nbuffer_load_dwordx4 v225, s[36:39], s53 offen lds\ns_add_u32 m0, s52, 0x4400\ns_add_u32 s53, s55, 0x9bc00\n"
"buffer_load_dwordx4 v225, s[36:39], s53 offen lds\ns_waitcnt vmcnt(21) lgkmcnt(6)\nv_mfma_f32_16x16x32_bf16 v[16:19], v[0:3], a[224:227], v[16:19]\ns_waitcnt vmcnt(20)\n"
"v_mfma_f32_16x16x32_bf16 v[20:23], v[0:3], a[228:231], v[20:23]\ns_waitcnt vmcnt(19)\nv_mfma_f32_16x16x32_bf16 v[24:27], v[0:3], a[232:235], v[24:27]\ns_waitcnt vmcnt(18)\n"
"v_mfma_f32_16x16x32_bf16 v[28:31], v[0:3], a[236:239], v[28:31]\ns_add_u32 m0, s52, 0x4800\ns_add_u32 s53, s55, 0xa8000\nbuffer_load_dwordx4 v225, s[36:39], s53 offen lds\n"
"s_add_u32 m0, s52, 0x4c00\ns_add_u32 s53, s55, 0xa9c00\nbuffer_load_dwordx4 v225, s[36:39], s53 offen lds\ns_waitcnt lgkmcnt(4)\n"
"v_mfma_f32_16x16x32_bf16 v[32:35], v[4:7], a[224:227], v[32:35]\nv_mfma_f32_16x16x32_bf16 v[36:39], v[4:7], a[228:231], v[36:39]\nv_mfma_f32_16x16x32_bf16 v[40:43], v[4:7], a[232:235], v[40:43]\nv_mfma_f32_16x16x32_bf16 v[44:47], v[4:7], a[236:239], v[44:47]\n"
"s_add_u32 s89, s80, 0x14000\nbuffer_load_dwordx4 a[144:147], v224, s[60:63], s89 offen offset:0 sc1\nbuffer_load_dwordx4 a[148:151], v224, s[60:63], s89 offen offset:1024 sc1\ns_waitcnt lgkmcnt(0)\n"
"v_mfma_f32_16x16x32_bf16 v[48:51], v[8:11], a[224:227], v[48:51]\nv_mfma_f32_16x16x32_bf16 v[52:55], v[8:11], a[228:231], v[52:55]\nv_mfma_f32_16x16x32_bf16 v[56:59], v[8:11], a[232:235], v[56:59]\nv_mfma_f32_16x16x32_bf16 v[60:63], v[8:11], a[236:239], v[60:63]\n"
"buffer_load_dwordx4 a[152:155], v224, s[60:63], s89 offen offset:2048 sc1\nbuffer_load_dwordx4 a[156:159], v224, s[60:63], s89 offen offset:3072 sc1\nv_mfma_f32_16x16x32_bf16 v[64:67], v[12:15], a[224:227], v[64:67]\nv_mfma_f32_16x16x32_bf16 v[68:71], v[12:15], a[228:231], v[68:71]\n"
"v_mfma_f32_16x16x32_bf16 v[72:75], v[12:15], a[232:235], v[72:75]\nv_mfma_f32_16x16x32_bf16 v[76:79], v[12:15], a[236:239], v[76:79]\ns_waitcnt vmcnt(22)\nds_read_b64_tr_b16 v[0:1], v226 offset:49152\n"
"s_waitcnt vmcnt(20)\nds_read_b64_tr_b16 v[2:3], v226 offset:51200\nds_read_b64_tr_b16 v[4:5], v227 offset:49152\nds_read_b64_tr_b16 v[6:7], v227 offset:51200\n"
"ds_read_b64_tr_b16 v[8:9], v228 offset:49152\nds_read_b64_tr_b16 v[10:11], v228 offset:51200\nds_read_b64_tr_b16 v[12:13], v229 offset:49152\nds_read_b64_tr_b16 v[14:15], v229 offset:51200\n"
"buffer_load_dword v251, v246, s[4:7], 0 offen offset:64 sc1\ns_add_u32 m0, s52, 0x8000\ns_add_u32 s53, s55, 0xc4000\nbuffer_load_dwordx4 v225, s[36:39], s53 offen lds\n"
"s_add_u32 m0, s52, 0x8400\ns_add_u32 s53, s55, 0xc5c00\nbuffer_load_dwordx4 v225, s[36:39], s53 offen lds\ns_waitcnt vmcnt(22) lgkmcnt(6)\n"
"v_mfma_f32_16x16x32_bf16 v[16:19], v[0:3], a[240:243], v[16:19]\ns_waitcnt vmcnt(21)\nv_mfma_f32_16x16x32_bf16 v[20:23], v[0:3], a[244:247], v[20:23]\ns_waitcnt vmcnt(20)\n"
"v_mfma_f32_16x16x32_bf16 v[24:27], v[0:3], a[248:251], v[24:27]\ns_waitcnt vmcnt(19)\nv_mfma_f32_16x16x32_bf16 v[28:31], v[0:3], a[252:255], v[28:31]\ns_add_u32 m0, s52, 0x8800\n"
"s_add_u32 s53, s55, 0xd2000\nbuffer_load_dwordx4 v225, s[36:39], s53 offen lds\ns_add_u32 m0, s52, 0x8c00\ns_add_u32 s53, s55, 0xd3c00\n"
"buffer_load_dwordx4 v225, s[36:39], s53 offen lds\ns_waitcnt lgkmcnt(4)\nv_mfma_f32_16x16x32_bf16 v[32:35], v[4:7], a[240:243], v[32:35]\nv_mfma_f32_16x16x32_bf16 v[36:39], v[4:7], a[244:247], v[36:39]\n"
"v_mfma_f32_16x16x32_bf16 v[40:43], v[4:7], a[248:251], v[40:43]\nv_mfma_f32_16x16x32_bf16 v[44:47], v[4:7], a[252:255], v[44:47]\ns_add_u32 s86, s80, 0x18000\nbuffer_load_dwordx4 a[224:227], v224, s[60:63], s86 offen offset:0 sc1\n"
"buffer_load_dwordx4 a[228:231], v224, s[60:63], s86 offen offset:1024 sc1\ns_waitcnt lgkmcnt(0)\nv_mfma_f32_16x16x32_bf16 v[48:51], v[8:11], a[240:243], v[48:51]\nv_mfma_f32_16x16x32_bf16 v[52:55], v[8:11], a[244:247], v[52:55]\n"
"v_mfma_f32_16x16x32_bf16 v[56:59], v[8:11], a[248:251], v[56:59]\nv_mfma_f32_16x16x32_bf16 v[60:63], v[8:11], a[252:255], v[60:63]\nbuffer_load_dwordx4 a[232:235], v224, s[60:63], s86 offen offset:2048 sc1\nbuffer_load_dwordx4 a[236:239], v224, s[60:63], s86 offen offset:3072 sc1\n"
"v_mfma_f32_16x16x32_bf16 v[64:67], v[12:15], a[240:243], v[64:67]\nv_mfma_f32_16x16x32_bf16 v[68:71], v[12:15], a[244:247], v[68:71]\nv_mfma_f32_16x16x32_bf16 v[72:75], v[12:15], a[248:251], v[72:75]\nv_mfma_f32_16x16x32_bf16 v[76:79], v[12:15], a[252:255], v[76:79]\n"
"s_waitcnt vmcnt(23)\nds_read_b64_tr_b16 v[0:1], v226 offset:0\ns_waitcnt vmcnt(21)\nds_read_b64_tr_b16 v[2:3], v226 offset:2048\n"
"ds_read_b64_tr_b16 v[4:5], v227 offset:0\nds_read_b64_tr_b16 v[6:7], v227 offset:2048\nds_read_b64_tr_b16 v[8:9], v228 offset:0\nds_read_b64_tr_b16 v[10:11], v228 offset:2048\n"
"ds_read_b64_tr_b16 v[12:13], v229 offset:0\nds_read_b64_tr_b16 v[14:15], v229 offset:2048\ns_add_u32 m0, s52, 0xc000\ns_add_u32 s53, s55, 0xe0000\n"
"buffer_load_dwordx4 v225, s[36:39], s53 offen lds\ns_add_u32 m0, s52, 0xc400\ns_add_u32 s53, s55, 0xe1c00\nbuffer_load_dwordx4 v225, s[36:39], s53 offen lds\n"
"s_waitcnt vmcnt(22) lgkmcnt(6)\nv_mfma_f32_16x16x32_bf16 v[16:19], v[0:3], a[128:131], v[16:19]\ns_waitcnt vmcnt(21)\nv_mfma_f32_16x16x32_bf16 v[20:23], v[0:3], a[132:135], v[20:23]\n"
"s_waitcnt vmcnt(20)\nv_mfma_f32_16x16x32_bf16 v[24:27], v[0:3], a[136:139], v[24:27]\ns_waitcnt vmcnt(19)\nv_mfma_f32_16x16x32_bf16 v[28:31], v[0:3], a[140:143], v[28:31]\n"
"s_add_u32 m0, s52, 0xc800\ns_add_u32 s53, s55, 0xee000\nbuffer_load_dwordx4 v225, s[36:39], s53 offen lds\ns_add_u32 m0, s52, 0xcc00\n"
"s_add_u32 s53, s55, 0xefc00\nbuffer_load_dwordx4 v225, s[36:39], s53 offen lds\ns_waitcnt lgkmcnt(4)\nv_mfma_f32_16x16x32_bf16 v[32:35], v[4:7], a[128:131], v[32:35]\n"
"v_mfma_f32_16x16x32_bf16 v[36:39], v[4:7], a[132:135], v[36:39]\nv_mfma_f32_16x16x32_bf16 v[40:43], v[4:7], a[136:139], v[40:43]\nv_mfma_f32_16x16x32_bf16 v[44:47], v[4:7], a[140:143], v[44:47]\ns_add_u32 s87, s80, 0x1c000\n"
"buffer_load_dwordx4 a[240:243], v224, s[60:63], s87 offen offset:0 sc1\nbuffer_load_dwordx4 a[244:247], v224, s[60:63], s87 offen offset:1024 sc1\ns_waitcnt lgkmcnt(0)\nv_mfma_f32_16x16x32_bf16 v[48:51], v[8:11], a[128:131], v[48:51]\n"
"v_mfma_f32_16x16x32_bf16 v[52:55], v[8:11], a[132:135], v[52:55]\nv_mfma_f32_16x16x32_bf16 v[56:59], v[8:11], a[136:139], v[56:59]\nv_mfma_f32_16x16x32_bf16 v[60:63], v[8:11], a[140:143], v[60:63]\nbuffer_load_dwordx4 a[248:251], v224, s[60:63], s87 offen offset:2048 sc1\n"
"buffer_load_dwordx4 a[252:255], v224, s[60:63], s87 offen offset:3072 sc1\nv_mfma_f32_16x16x32_bf16 v[64:67], v[12:15], a[128:131], v[64:67]\nv_mfma_f32_16x16x32_bf16 v[68:71], v[12:15], a[132:135], v[68:71]\nv_mfma_f32_16x16x32_bf16 v[72:75], v[12:15], a[136:139], v[72:75]\n"
"v_mfma_f32_16x16x32_bf16 v[76:79], v[12:15], a[140:143], v[76:79]\ns_waitcnt vmcnt(23)\nds_read_b64_tr_b16 v[0:1], v226 offset:16384\ns_waitcnt vmcnt(21)\n"
"ds_read_b64_tr_b16 v[2:3], v226 offset:18432\nds_read_b64_tr_b16 v[4:5], v227 offset:16384\nds_read_b64_tr_b16 v[6:7], v227 offset:18432\nds_read_b64_tr_b16 v[8:9], v228 offset:16384\n"
"ds_read_b64_tr_b16 v[10:11], v228 offset:18432\nds_read_b64_tr_b16 v[12:13], v229 offset:16384\nds_read_b64_tr_b16 v[14:15], v229 offset:18432\ns_waitcnt vmcnt(16)\n"
"v_cmp_ne_u32 vcc, s30, v251\ns_nop 4\ns_and_b32 vcc_lo, vcc_lo, 0xff\ns_cbranch_scc1 L_p2%=\nL_p2r%=:\ns_add_u32 m0, s52, 0x0\n"
"s_add_u32 s53, s55, 0xfc000\nbuffer_load_dwordx4 v225, s[36:39], s53 offen lds\ns_add_u32 m0, s52, 0x400\ns_add_u32 s53, s55, 0xfdc00\n"
"buffer_load_dwordx4 v225, s[36:39], s53 offen lds\ns_add_u32 m0, s52, 0x800\ns_add_u32 s53, s55, 0x10a000\nbuffer_load_dwordx4 v225, s[36:39], s53 offen lds\n"
"s_waitcnt lgkmcnt(6)\nv_mfma_f32_16x16x32_bf16 v[16:19], v[0:3], a[144:147], v[16:19]\nv_mfma_f32_16x16x32_bf16 v[20:23], v[0:3], a[148:151], v[20:23]\nv_mfma_f32_16x16x32_bf16 v[24:27], v[0:3], a[152:155], v[24:27]\n"
"v_mfma_f32_16x16x32_bf16 v[28:31], v[0:3], a[156:159], v[28:31]\ns_add_u32 m0, s52, 0xc00\ns_add_u32 s53, s55, 0x10bc00\nbuffer_load_dwordx4 v225, s[36:39], s53 offen lds\n"
"s_add_u32 s88, s80, 0x20000\nbuffer_load_dwordx4 a[128:131], v224, s[60:63], s88 offen offset:0 sc1\ns_waitcnt lgkmcnt(4)\nv_mfma_f32_16x16x32_bf16 v[32:35], v[4:7], a[144:147], v[32:35]\n"
"v_mfma_f32_16x16x32_bf16 v[36:39], v[4:7], a[148:151], v[36:39]\nv_mfma_f32_16x16x32_bf16 v[40:43], v[4:7], a[152:155], v[40:43]\nv_mfma_f32_16x16x32_bf16 v[44:47], v[4:7], a[156:159], v[44:47]\nbuffer_load_dwordx4 a[132:135], v224, s[60:63], s88 offen offset:1024 sc1\n"
"buffer_load_dwordx4 a[136:139], v224, s[60:63], s88 offen offset:2048 sc1\ns_waitcnt lgkmcnt(0)\nv_mfma_f32_16x16x32_bf16 v[48:51], v[8:11], a[144:147], v[48:51]\nv_mfma_f32_16x16x32_bf16 v[52:55], v[8:11], a[148:151], v[52:55]\n"
"v_mfma_f32_16x16x32_bf16 v[56:59], v[8:11], a[152:155], v[56:59]\nv_mfma_f32_16x16x32_bf16 v[60:63], v[8:11], a[156:159], v[60:63]\nbuffer_load_dwordx4 a[140:143], v224, s[60:63], s88 offen offset:3072 sc1\nbuffer_load_dwordx4 v[92:95], v224, s[64:67], s54 offen offset:0 sc1\n"
"v_mfma_f32_16x16x32_bf16 v[64:67], v[12:15], a[144:147], v[64:67]\nv_mfma_f32_16x16x32_bf16 v[68:71], v[12:15], a[148:151], v[68:71]\nv_mfma_f32_16x16x32_bf16 v[72:75], v[12:15], a[152:155], v[72:75]\nv_mfma_f32_16x16x32_bf16 v[76:79], v[12:15], a[156:159], v[76:79]\n"
"s_waitcnt vmcnt(23)\nds_read_b64_tr_b16 v[0:1], v226 offset:32768\ns_waitcnt vmcnt(21)\nds_read_b64_tr_b16 v[2:3], v226 offset:34816\n"
"ds_read_b64_tr_b16 v[4:5], v227 offset:32768\nds_read_b64_tr_b16 v[6:7], v227 offset:34816\nds_read_b64_tr_b16 v[8:9], v228 offset:32768\nds_read_b64_tr_b16 v[10:11], v228 offset:34816\n"
"ds_read_b64_tr_b16 v[12:13], v229 offset:32768\nds_read_b64_tr_b16 v[14:15], v229 offset:34816\ns_add_u32 m0, s52, 0x4000\ns_add_u32 s53, s55, 0x126000\n"
"buffer_load_dwordx4 v225, s[36:39], s53 offen lds\ns_add_u32 m0, s52, 0x4400\ns_add_u32 s53, s55, 0x127c00\nbuffer_load_dwordx4 v225, s[36:39], s53 offen lds\n"
"s_add_u32 m0, s52, 0x4800\ns_add_u32 s53, s55, 0x134000\nbuffer_load_dwordx4 v225, s[36:39], s53 offen lds\ns_waitcnt vmcnt(23) lgkmcnt(6)\n"
"v_mfma_f32_16x16x32_bf16 v[16:19], v[0:3], a[224:227], v[16:19]\ns_waitcnt vmcnt(22)\nv_mfma_f32_16x16x32_bf16 v[20:23], v[0:3], a[228:231], v[20:23]\ns_waitcnt vmcnt(21)\n"
"v_mfma_f32_16x16x32_bf16 v[24:27], v[0:3], a[232:235], v[24:27]\ns_waitcnt vmcnt(20)\nv_mfma_f32_16x16x32_bf16 v[28:31], v[0:3], a[236:239], v[28:31]\ns_add_u32 m0, s52, 0x4c00\n"
"s_add_u32 s53, s55, 0x135c00\nbuffer_load_dwordx4 v225, s[36:39], s53 offen lds\ns_add_u32 s89, s80, 0x24000\nbuffer_load_dwordx4 a[144:147], v224, s[60:63], s89 offen offset:0 sc1\n"
"s_waitcnt lgkmcnt(4)\nv_mfma_f32_16x16x32_bf16 v[32:35], v[4:7], a[224:227], v[32:35]\nv_mfma_f32_16x16x32_bf16 v[36:39], v[4:7], a[228:231], v[36:39]\nv_mfma_f32_16x16x32_bf16 v[40:43], v[4:7], a[232:235], v[40:43]\n"
"v_mfma_f32_16x16x32_bf16 v[44:47], v[4:7], a[236:239], v[44:47]\nbuffer_load_dwordx4 a[148:151], v224, s[60:63], s89 offen offset:1024 sc1\nbuffer_load_dwordx4 a[152:155], v224, s[60:63], s89 offen offset:2048 sc1\ns_waitcnt lgkmcnt(0)\n"
"v_mfma_f32_16x16x32_bf16 v[48:51], v[8:11], a[224:227], v[48:51]\nv_mfma_f32_16x16x32_bf16 v[52:55], v[8:11], a[228:231], v[52:55]\nv_mfma_f32_16x16x32_bf16 v[56:59], v[8:11], a[232:235], v[56:59]\nv_mfma_f32_16x16x32_bf16 v[60:63], v[8:11], a[236:239], v[60:63]\n"
"buffer_load_dwordx4 a[156:159], v224, s[60:63], s89 offen offset:3072 sc1\nbuffer_load_dwordx4 v[96:99], v224, s[64:67], s54 offen offset:1024 sc1\ns_add_u32 s54, s54, 0x2000\nv_mfma_f32_16x16x32_bf16 v[64:67], v[12:15], a[224:227], v[64:67]\n"
"v_mfma_f32_16x16x32_bf16 v[68:71], v[12:15], a[228:231], v[68:71]\nv_mfma_f32_16x16x32_bf16 v[72:75], v[12:15], a[232:235], v[72:75]\nv_mfma_f32_16x16x32_bf16 v[76:79], v[12:15], a[236:239], v[76:79]\ns_waitcnt vmcnt(24)\n"
"ds_read_b64_tr_b16 v[0:1], v226 offset:49152\ns_waitcnt vmcnt(22)\nds_read_b64_tr_b16 v[2:3], v226 offset:51200\nds_read_b64_tr_b16 v[4:5], v227 offset:49152\n"
"ds_read_b64_tr_b16 v[6:7], v227 offset:51200\nds_read_b64_tr_b16 v[8:9], v228 offset:49152\nds_read_b64_tr_b16 v[10:11], v228 offset:51200\nds_read_b64_tr_b16 v[12:13], v229 offset:49152\n"
"ds_read_b64_tr_b16 v[14:15], v229 offset:51200\ns_add_u32 m0, s52, 0x8000\ns_add_u32 s53, s55, 0x142000\nbuffer_load_dwordx4 v225, s[36:39], s53 offen lds\n"
"s_add_u32 m0, s52, 0x8400\ns_add_u32 s53, s55, 0x143c00\nbuffer_load_dwordx4 v225, s[36:39], s53 offen lds\ns_add_u32 m0, s52, 0x8800\n"
"s_add_u32 s53, s55, 0x150000\nbuffer_load_dwordx4 v225, s[36:39], s53 offen lds\ns_waitcnt vmcnt(24) lgkmcnt(6)\nv_mfma_f32_16x16x32_bf16 v[16:19], v[0:3], a[240:243], v[16:19]\n"
"s_waitcnt vmcnt(23)\nv_mfma_f32_16x16x32_bf16 v[20:23], v[0:3], a[244:247], v[20:23]\ns_waitcnt vmcnt(22)\nv_mfma_f32_16x16x32_bf16 v[24:27], v[0:3], a[248:251], v[24:27]\n"
"s_waitcnt vmcnt(21)\nv_mfma_f32_16x16x32_bf16 v[28:31], v[0:3], a[252:255], v[28:31]\ns_add_u32 m0, s52, 0x8c00\ns_add_u32 s53, s55, 0x151c00\n"
"buffer_load_dwordx4 v225, s[36:39], s53 offen lds\ns_add_u32 s86, s80, 0x28000\nbuffer_load_dwordx4 a[224:227], v224, s[60:63], s86 offen offset:0 sc1\ns_waitcnt lgkmcnt(4)\n"
"v_mfma_f32_16x16x32_bf16 v[32:35], v[4:7], a[240:243], v[32:35]\nv_mfma_f32_16x16x32_bf16 v[36:39], v[4:7], a[244:247], v[36:39]\nv_mfma_f32_16x16x32_bf16 v[40:43], v[4:7], a[248:251], v[40:43]\nv_mfma_f32_16x16x32_bf16 v[44:47], v[4:7], a[252:255], v[44:47]\n"
"buffer_load_dwordx4 a[228:231], v224, s[60:63], s86 offen offset:1024 sc1\nbuffer_load_dwordx4 a[232:235], v224, s[60:63], s86 offen offset:2048 sc1\ns_waitcnt lgkmcnt(0)\nv_mfma_f32_16x16x32_bf16 v[48:51], v[8:11], a[240:243], v[48:51]\n"
"v_mfma_f32_16x16x32_bf16 v[52:55], v[8:11], a[244:247], v[52:55]\nv_mfma_f32_16x16x32_bf16 v[56:59], v[8:11], a[248:251], v[56:59]\nv_mfma_f32_16x16x32_bf16 v[60:63], v[8:11], a[252:255], v[60:63]\nbuffer_load_dwordx4 a[236:239], v224, s[60:63], s86 offen offset:3072 sc1\n"
"buffer_load_dwordx4 v[100:103], v224, s[64:67], s54 offen offset:0 sc1\nv_mfma_f32_16x16x32_bf16 v[64:67], v[12:15], a[240:243], v[64:67]\nv_mfma_f32_16x16x32_bf16 v[68:71], v[12:15], a[244:247], v[68:71]\nv_mfma_f32_16x16x32_bf16 v[72:75], v[12:15], a[248:251], v[72:75]\n"
"v_mfma_f32_16x16x32_bf16 v[76:79], v[12:15], a[252:255], v[76:79]\ns_waitcnt vmcnt(25)\nds_read_b64_tr_b16 v[0:1], v226 offset:0\ns_waitcnt vmcnt(23)\n"
"ds_read_b64_tr_b16 v[2:3], v226 offset:2048\nds_read_b64_tr_b16 v[4:5], v227 offset:0\nds_read_b64_tr_b16 v[6:7], v227 offset:2048\nds_read_b64_tr_b16 v[8:9], v228 offset:0\n"
"ds_read_b64_tr_b16 v[10:11], v228 offset:2048\nds_read_b64_tr_b16 v[12:13], v229 offset:0\nds_read_b64_tr_b16 v[14:15], v229 offset:2048\ns_add_u32 m0, s52, 0xc000\n"
"s_add_u32 s53, s55, 0x15e000\nbuffer_load_dwordx4 v225, s[36:39], s53 offen lds\ns_add_u32 m0, s52, 0xc400\ns_add_u32 s53, s55, 0x15fc00\n"
"buffer_load_dwordx4 v225, s[36:39], s53 offen lds\ns_add_u32 m0, s52, 0xc800\ns_add_u32 s53, s55, 0x16c000\nbuffer_load_dwordx4 v225, s[36:39], s53 offen lds\n"
"s_waitcnt vmcnt(25) lgkmcnt(6)\nv_mfma_f32_16x16x32_bf16 v[16:19], v[0:3], a[128:131], v[16:19]\ns_waitcnt vmcnt(24)\nv_mfma_f32_16x16x32_bf16 v[20:23], v[0:3], a[132:135], v[20:23]\n"
"s_waitcnt vmcnt(23)\nv_mfma_f32_16x16x32_bf16 v[24:27], v[0:3], a[136:139], v[24:27]\ns_waitcnt vmcnt(22)\nv_mfma_f32_16x16x32_bf16 v[28:31], v[0:3], a[140:143], v[28:31]\n"
"s_add_u32 m0, s52, 0xcc00\ns_add_u32 s53, s55, 0x16dc00\nbuffer_load_dwordx4 v225, s[36:39], s53 offen lds\ns_add_u32 s87, s80, 0x2c000\n"
"buffer_load_dwordx4 a[240:243], v224, s[60:63], s87 offen offset:0 sc1\ns_waitcnt lgkmcnt(4)\nv_mfma_f32_16x16x32_bf16 v[32:35], v[4:7], a[128:131], v[32:35]\nv_mfma_f32_16x16x32_bf16 v[36:39], v[4:7], a[132:135], v[36:39]\n"
"v_mfma_f32_16x16x32_bf16 v[40:43], v[4:7], a[136:139], v[40:43]\nv_mfma_f32_16x16x32_bf16 v[44:47], v[4:7], a[140:143], v[44:47]\nbuffer_load_dwordx4 a[244:247], v224, s[60:63], s87 offen offset:1024 sc1\nbuffer_load_dwordx4 a[248:251], v224, s[60:63], s87 offen offset:2048 sc1\n"
"s_waitcnt lgkmcnt(0)\nv_mfma_f32_16x16x32_bf16 v[48:51], v[8:11], a[128:131], v[48:51]\nv_mfma_f32_16x16x32_bf16 v[52:55], v[8:11], a[132:135], v[52:55]\nv_mfma_f32_16x16x32_bf16 v[56:59], v[8:11], a[136:139], v[56:59]\n"
"v_mfma_f32_16x16x32_bf16 v[60:63], v[8:11], a[140:143], v[60:63]\nbuffer_load_dwordx4 a[252:255], v224, s[60:63], s87 offen offset:3072 sc1\nbuffer_load_dwordx4 v[104:107], v224, s[64:67], s54 offen offset:1024 sc1\ns_add_u32 s54, s54, 0x2000\n"
"v_mfma_f32_16x16x32_bf16 v[64:67], v[12:15], a[128:131], v[64:67]\nv_mfma_f32_16x16x32_bf16 v[68:71], v[12:15], a[132:135], v[68:71]\nv_mfma_f32_16x16x32_bf16 v[72:75], v[12:15], a[136:139], v[72:75]\nv_mfma_f32_16x16x32_bf16 v[76:79], v[12:15], a[140:143], v[76:79]\n"
"s_waitcnt vmcnt(25)\nds_read_b64_tr_b16 v[0:1], v226 offset:16384\ns_waitcnt vmcnt(23)\nds_read_b64_tr_b16 v[2:3], v226 offset:18432\n"
"ds_read_b64_tr_b16 v[4:5], v227 offset:16384\nds_read_b64_tr_b16 v[6:7], v227 offset:18432\nds_read_b64_tr_b16 v[8:9], v228 offset:16384\nds_read_b64_tr_b16 v[10:11], v228 offset:18432\n"
"ds_read_b64_tr_b16 v[12:13], v229 offset:16384\nds_read_b64_tr_b16 v[14:15], v229 offset:18432\nbuffer_load_dwordx4 v[108:111], v224, s[64:67], s54 offen offset:0 sc1\nbuffer_load_dwordx4 v[112:115], v224, s[64:67], s54 offen offset:1024 sc1\n"
"s_waitcnt vmcnt(24) lgkmcnt(6)\nv_mfma_f32_16x16x32_bf16 v[16:19], v[0:3], a[144:147], v[16:19]\ns_waitcnt vmcnt(23)\nv_mfma_f32_16x16x32_bf16 v[20:23], v[0:3], a[148:151], v[20:23]\n"
"s_waitcnt vmcnt(22)\nv_mfma_f32_16x16x32_bf16 v[24:27], v[0:3], a[152:155], v[24:27]\ns_waitcnt vmcnt(21)\nv_mfma_f32_16x16x32_bf16 v[28:31], v[0:3], a[156:159], v[28:31]\n"
"s_add_u32 s54, s54, 0x2000\nbuffer_load_dwordx4 v[116:119], v224, s[64:67], s54 offen offset:0 sc1\nbuffer_load_dwordx4 v[120:123], v224, s[64:67], s54 offen offset:1024 sc1\ns_waitcnt lgkmcnt(4)\n"
"v_mfma_f32_16x16x32_bf16 v[32:35], v[4:7], a[144:147], v[32:35]\nv_mfma_f32_16x16x32_bf16 v[36:39], v[4:7], a[148:151], v[36:39]\nv_mfma_f32_16x16x32_bf16 v[40:43], v[4:7], a[152:155], v[40:43]\nv_mfma_f32_16x16x32_bf16 v[44:47], v[4:7], a[156:159], v[44:47]\n"
"s_add_u32 s54, s54, 0x2000\nbuffer_load_dwordx4 v[124:127], v224, s[64:67], s54 offen offset:0 sc1\nbuffer_load_dwordx4 v[128:131], v224, s[64:67], s54 offen offset:1024 sc1\ns_waitcnt lgkmcnt(0)\n"
"v_mfma_f32_16x16x32_bf16 v[48:51], v[8:11], a[144:147], v[48:51]\nv_mfma_f32_16x16x32_bf16 v[52:55], v[8:11], a[148:151], v[52:55]\nv_mfma_f32_16x16x32_bf16 v[56:59], v[8:11], a[152:155], v[56:59]\nv_mfma_f32_16x16x32_bf16 v[60:63], v[8:11], a[156:159], v[60:63]\n"
"s_add_u32 s54, s54, 0x2000\nbuffer_load_dwordx4 v[132:135], v224, s[64:67], s54 offen offset:0 sc1\nbuffer_load_dwordx4 v[136:139], v224, s[64:67], s54 offen offset:1024 sc1\ns_add_u32 s54, s54, 0x2000\n"
"v_mfma_f32_16x16x32_bf16 v[64:67], v[12:15], a[144:147], v[64:67]\nv_mfma_f32_16x16x32_bf16 v[68:71], v[12:15], a[148:151], v[68:71]\nv_mfma_f32_16x16x32_bf16 v[72:75], v[12:15], a[152:155], v[72:75]\nv_mfma_f32_16x16x32_bf16 v[76:79], v[12:15], a[156:159], v[76:79]\n"
"v_mfma_f32_16x16x32_bf16 v[164:167], v[82:85], v[92:95], 0\nv_mfma_f32_16x16x32_bf16 v[168:171], v[80:83], v[92:95], 0\ns_waitcnt vmcnt(24)\nds_read_b64_tr_b16 v[0:1], v226 offset:32768\n"
"s_waitcnt vmcnt(22)\nds_read_b64_tr_b16 v[2:3], v226 offset:34816\nds_read_b64_tr_b16 v[4:5], v227 offset:32768\nds_read_b64_tr_b16 v[6:7], v227 offset:34816\n"
"ds_read_b64_tr_b16 v[8:9], v228 offset:32768\nds_read_b64_tr_b16 v[10:11], v228 offset:34816\nds_read_b64_tr_b16 v[12:13], v229 offset:32768\nds_read_b64_tr_b16 v[14:15], v229 offset:34816\n"
"buffer_load_dwordx4 v[140:143], v224, s[64:67], s54 offen offset:0 sc1\ns_waitcnt vmcnt(22) lgkmcnt(6)\nv_mfma_f32_16x16x32_bf16 v[16:19], v[0:3], a[224:227], v[16:19]\ns_waitcnt vmcnt(21)\n"
"v_mfma_f32_16x16x32_bf16 v[20:23], v[0:3], a[228:231], v[20:23]\ns_waitcnt vmcnt(20)\nv_mfma_f32_16x16x32_bf16 v[24:27], v[0:3], a[232:235], v[24:27]\ns_waitcnt vmcnt(19)\n"
"v_mfma_f32_16x16x32_bf16 v[28:31], v[0:3], a[236:239], v[28:31]\nbuffer_load_dwordx4 v[144:147], v224, s[64:67], s54 offen offset:1024 sc1\ns_waitcnt lgkmcnt(4)\nv_mfma_f32_16x16x32_bf16 v[32:35], v[4:7], a[224:227], v[32:35]\n"
"v_mfma_f32_16x16x32_bf16 v[36:39], v[4:7], a[228:231], v[36:39]\nv_mfma_f32_16x16x32_bf16 v[40:43], v[4:7], a[232:235], v[40:43]\nv_mfma_f32_16x16x32_bf16 v[44:47], v[4:7], a[236:239], v[44:47]\ns_add_u32 s54, s54, 0x2000\n"
"buffer_load_dwordx4 v[148:151], v224, s[64:67], s54 offen offset:0 sc1\ns_waitcnt lgkmcnt(0)\nv_mfma_f32_16x16x32_bf16 v[48:51], v[8:11], a[224:227], v[48:51]\nv_mfma_f32_16x16x32_bf16 v[52:55], v[8:11], a[228:231], v[52:55]\n"
"v_mfma_f32_16x16x32_bf16 v[56:59], v[8:11], a[232:235], v[56:59]\nv_mfma_f32_16x16x32_bf16 v[60:63], v[8:11], a[236:239], v[60:63]\nbuffer_load_dwordx4 v[152:155], v224, s[64:67], s54 offen offset:1024 sc1\ns_add_u32 s54, s54, 0x2000\n"
"v_mfma_f32_16x16x32_bf16 v[64:67], v[12:15], a[224:227], v[64:67]\nv_mfma_f32_16x16x32_bf16 v[68:71], v[12:15], a[228:231], v[68:71]\nv_mfma_f32_16x16x32_bf16 v[72:75], v[12:15], a[232:235], v[72:75]\nv_mfma_f32_16x16x32_bf16 v[76:79], v[12:15], a[236:239], v[76:79]\n"
"v_mfma_f32_16x16x32_bf16 v[172:175], v[82:85], v[96:99], 0\nv_mfma_f32_16x16x32_bf16 v[176:179], v[80:83], v[96:99], 0\ns_waitcnt vmcnt(19)\nds_read_b64_tr_b16 v[0:1], v226 offset:49152\n"
"s_waitcnt vmcnt(17)\nds_read_b64_tr_b16 v[2:3], v226 offset:51200\nds_read_b64_tr_b16 v[4:5], v227 offset:49152\nds_read_b64_tr_b16 v[6:7], v227 offset:51200\n"
"ds_read_b64_tr_b16 v[8:9], v228 offset:49152\nds_read_b64_tr_b16 v[10:11], v228 offset:51200\nds_read_b64_tr_b16 v[12:13], v229 offset:49152\nds_read_b64_tr_b16 v[14:15], v229 offset:51200\n"
"s_waitcnt vmcnt(16) lgkmcnt(6)\nv_mfma_f32_16x16x32_bf16 v[16:19], v[0:3], a[240:243], v[16:19]\ns_waitcnt vmcnt(15)\nv_mfma_f32_16x16x32_bf16 v[20:23], v[0:3], a[244:247], v[20:23]\n"
"s_waitcnt vmcnt(14)\nv_mfma_f32_16x16x32_bf16 v[24:27], v[0:3], a[248:251], v[24:27]\ns_waitcnt vmcnt(13)\nv_mfma_f32_16x16x32_bf16 v[28:31], v[0:3], a[252:255], v[28:31]\n"
"s_waitcnt lgkmcnt(4)\nv_mfma_f32_16x16x32_bf16 v[32:35], v[4:7], a[240:243], v[32:35]\nv_mfma_f32_16x16x32_bf16 v[36:39], v[4:7], a[244:247], v[36:39]\nv_mfma_f32_16x16x32_bf16 v[40:43], v[4:7], a[248:251], v[40:43]\n"
"v_mfma_f32_16x16x32_bf16 v[44:47], v[4:7], a[252:255], v[44:47]\nds_write_b128 v230, v[16:19] offset:0\nds_write_b128 v230, v[20:23] offset:1024\nds_write_b128 v230, v[24:27] offset:2048\n"
"ds_write_b128 v230, v[28:31] offset:3072\ns_waitcnt lgkmcnt(4)\nv_mfma_f32_16x16x32_bf16 v[48:51], v[8:11], a[240:243], v[48:51]\nv_mfma_f32_16x16x32_bf16 v[52:55], v[8:11], a[244:247], v[52:55]\n"
"v_mfma_f32_16x16x32_bf16 v[56:59], v[8:11], a[248:251], v[56:59]\nv_mfma_f32_16x16x32_bf16 v[60:63], v[8:11], a[252:255], v[60:63]\nds_write_b128 v231, v[32:35] offset:0\nds_write_b128 v231, v[36:39] offset:1024\n"
"ds_write_b128 v231, v[40:43] offset:2048\nds_write_b128 v231, v[44:47] offset:3072\nv_mfma_f32_16x16x32_bf16 v[64:67], v[12:15], a[240:243], v[64:67]\nv_mfma_f32_16x16x32_bf16 v[68:71], v[12:15], a[244:247], v[68:71]\n"
"v_mfma_f32_16x16x32_bf16 v[72:75], v[12:15], a[248:251], v[72:75]\nv_mfma_f32_16x16x32_bf16 v[76:79], v[12:15], a[252:255], v[76:79]\nds_write_b128 v232, v[48:51] offset:0\nds_write_b128 v232, v[52:55] offset:1024\n"
"ds_write_b128 v232, v[56:59] offset:2048\nds_write_b128 v232, v[60:63] offset:3072\nv_mfma_f32_16x16x32_bf16 v[164:167], v[82:85], v[100:103], v[164:167]\nv_mfma_f32_16x16x32_bf16 v[168:171], v[80:83], v[100:103], v[168:171]\n"
"s_waitcnt lgkmcnt(0)\ns_barrier\nds_read_b128 v[16:19], v241 offset:0\nds_read_b128 v[32:35], v241 offset:4096\n"
"ds_read_b128 v[48:51], v241 offset:8192\nds_read_b128 v[20:23], v241 offset:1024\nds_read_b128 v[36:39], v241 offset:5120\nds_read_b128 v[52:55], v241 offset:9216\n"
"ds_read_b128 v[24:27], v241 offset:2048\nds_read_b128 v[40:43], v241 offset:6144\nds_read_b128 v[56:59], v241 offset:10240\nds_read_b128 v[28:31], v241 offset:3072\n"
"ds_read_b128 v[44:47], v241 offset:7168\nds_read_b128 v[60:63], v241 offset:11264\ns_waitcnt vmcnt(12)\nv_mfma_f32_16x16x32_bf16 v[172:175], v[82:85], v[104:107], v[172:175]\n"
"v_mfma_f32_16x16x32_bf16 v[176:179], v[80:83], v[104:107], v[176:179]\ns_waitcnt vmcnt(11)\nv_mfma_f32_16x16x32_bf16 v[164:167], v[82:85], v[108:111], v[164:167]\nv_mfma_f32_16x16x32_bf16 v[168:171], v[80:83], v[108:111], v[168:171]\n"
"s_waitcnt vmcnt(10)\nv_mfma_f32_16x16x32_bf16 v[172:175], v[82:85], v[112:115], v[172:175]\nv_mfma_f32_16x16x32_bf16 v[176:179], v[80:83], v[112:115], v[176:179]\ns_waitcnt lgkmcnt(11)\n"
"v_pk_add_f32 v[64:65], v[64:65], v[16:17]\ns_waitcnt lgkmcnt(9)\nv_pk_add_f32 v[32:33], v[32:33], v[48:49]\nv_pk_add_f32 v[64:65], v[64:65], v[32:33]\n"
"v_pk_add_f32 v[66:67], v[66:67], v[18:19]\nv_pk_add_f32 v[34:35], v[34:35], v[50:51]\nv_pk_add_f32 v[66:67], v[66:67], v[34:35]\ns_waitcnt lgkmcnt(8)\n"
"v_pk_add_f32 v[68:69], v[68:69], v[20:21]\ns_waitcnt lgkmcnt(6)\nv_pk_add_f32 v[36:37], v[36:37], v[52:53]\nv_pk_add_f32 v[68:69], v[68:69], v[36:37]\n"
"v_pk_add_f32 v[70:71], v[70:71], v[22:23]\nv_pk_add_f32 v[38:39], v[38:39], v[54:55]\nv_pk_add_f32 v[70:71], v[70:71], v[38:39]\ns_waitcnt lgkmcnt(5)\n"
"v_pk_add_f32 v[72:73], v[72:73], v[24:25]\ns_waitcnt lgkmcnt(3)\nv_pk_add_f32 v[40:41], v[40:41], v[56:57]\nv_pk_add_f32 v[72:73], v[72:73], v[40:41]\n"
"v_pk_add_f32 v[74:75], v[74:75], v[26:27]\nv_pk_add_f32 v[42:43], v[42:43], v[58:59]\nv_pk_add_f32 v[74:75], v[74:75], v[42:43]\ns_waitcnt lgkmcnt(2)\n"
"v_pk_add_f32 v[76:77], v[76:77], v[28:29]\ns_waitcnt lgkmcnt(0)\nv_pk_add_f32 v[44:45], v[44:45], v[60:61]\nv_pk_add_f32 v[76:77], v[76:77], v[44:45]\n"
"v_pk_add_f32 v[78:79], v[78:79], v[30:31]\nv_pk_add_f32 v[46:47], v[46:47], v[62:63]\nv_pk_add_f32 v[78:79], v[78:79], v[46:47]\nv_exp_f32 v184, v64\n"
"v_exp_f32 v185, v65\nv_exp_f32 v186, v66\nv_exp_f32 v187, v67\ns_waitcnt vmcnt(9)\n"
"v_mfma_f32_16x16x32_bf16 v[164:167], v[82:85], v[116:119], v[164:167]\nv_mfma_f32_16x16x32_bf16 v[168:171], v[80:83], v[116:119], v[168:171]\nv_exp_f32 v188, v68\nv_exp_f32 v189, v69\n"
"v_exp_f32 v190, v70\nv_exp_f32 v191, v71\ns_waitcnt vmcnt(8)\nv_mfma_f32_16x16x32_bf16 v[172:175], v[82:85], v[120:123], v[172:175]\n"
"v_mfma_f32_16x16x32_bf16 v[176:179], v[80:83], v[120:123], v[176:179]\nv_exp_f32 v192, v72\nv_exp_f32 v193, v73\nv_exp_f32 v194, v74\n"
"v_exp_f32 v195, v75\ns_waitcnt vmcnt(7)\nv_mfma_f32_16x16x32_bf16 v[164:167], v[82:85], v[124:127], v[164:167]\nv_mfma_f32_16x16x32_bf16 v[168:171], v[80:83], v[124:127], v[168:171]\n"
"v_exp_f32 v196, v76\nv_exp_f32 v197, v77\nv_exp_f32 v198, v78\nv_exp_f32 v199, v79\n"
"s_waitcnt vmcnt(6)\nv_mfma_f32_16x16x32_bf16 v[172:175], v[82:85], v[128:131], v[172:175]\nv_mfma_f32_16x16x32_bf16 v[176:179], v[80:83], v[128:131], v[176:179]\nv_mov_b32 v16, 0xbfb8aa3b\n"
"v_pk_fma_f32 v[184:185], v[184:185], v[16:17], v[16:17] op_sel_hi:[1,0,0]\nv_pk_fma_f32 v[186:187], v[186:187], v[16:17], v[16:17] op_sel_hi:[1,0,0]\nv_pk_fma_f32 v[188:189], v[188:189], v[16:17], v[16:17] op_sel_hi:[1,0,0]\nv_pk_fma_f32 v[190:191], v[190:191], v[16:17], v[16:17] op_sel_hi:[1,0,0]\n"
"v_pk_fma_f32 v[192:193], v[192:193], v[16:17], v[16:17] op_sel_hi:[1,0,0]\nv_pk_fma_f32 v[194:195], v[194:195], v[16:17], v[16:17] op_sel_hi:[1,0,0]\nv_pk_fma_f32 v[196:197], v[196:197], v[16:17], v[16:17] op_sel_hi:[1,0,0]\nv_pk_fma_f32 v[198:199], v[198:199], v[16:17], v[16:17] op_sel_hi:[1,0,0]\n"
"v_rcp_f32 v184, v184\nv_rcp_f32 v185, v185\nv_rcp_f32 v186, v186\nv_rcp_f32 v187, v187\n"
"s_waitcnt vmcnt(5)\nv_mfma_f32_16x16x32_bf16 v[164:167], v[82:85], v[132:135], v[164:167]\nv_mfma_f32_16x16x32_bf16 v[168:171], v[80:83], v[132:135], v[168:171]\nv_rcp_f32 v188, v188\n"
"v_rcp_f32 v189, v189\nv_rcp_f32 v190, v190\nv_rcp_f32 v191, v191\ns_waitcnt vmcnt(4)\n"
"v_mfma_f32_16x16x32_bf16 v[172:175], v[82:85], v[136:139], v[172:175]\nv_mfma_f32_16x16x32_bf16 v[176:179], v[80:83], v[136:139], v[176:179]\nv_rcp_f32 v192, v192\nv_rcp_f32 v193, v193\n"
"v_rcp_f32 v194, v194\nv_rcp_f32 v195, v195\ns_waitcnt vmcnt(3)\nv_mfma_f32_16x16x32_bf16 v[164:167], v[82:85], v[140:143], v[164:167]\n"
"v_mfma_f32_16x16x32_bf16 v[168:171], v[80:83], v[140:143], v[168:171]\nv_rcp_f32 v196, v196\nv_rcp_f32 v197, v197\nv_rcp_f32 v198, v198\n"
"v_rcp_f32 v199, v199\ns_waitcnt vmcnt(2)\nv_mfma_f32_16x16x32_bf16 v[172:175], v[82:85], v[144:147], v[172:175]\nv_mfma_f32_16x16x32_bf16 v[176:179], v[80:83], v[144:147], v[176:179]\n"
"s_waitcnt vmcnt(1)\nv_mfma_f32_16x16x32_bf16 v[164:167], v[82:85], v[148:151], v[164:167]\nv_mfma_f32_16x16x32_bf16 v[168:171], v[80:83], v[148:151], v[168:171]\ns_waitcnt vmcnt(0)\n"
"v_mfma_f32_16x16x32_bf16 v[172:175], v[82:85], v[152:155], v[172:175]\nv_mfma_f32_16x16x32_bf16 v[176:179], v[80:83], v[152:155], v[176:179]\nv_pk_mul_f32 v[184:185], v[184:185], v[64:65]\nv_pk_mul_f32 v[186:187], v[186:187], v[66:67]\n"
"v_pk_mul_f32 v[188:189], v[188:189], v[68:69]\nv_pk_mul_f32 v[190:191], v[190:191], v[70:71]\nv_pk_mul_f32 v[192:193], v[192:193], v[72:73]\nv_pk_mul_f32 v[194:195], v[194:195], v[74:75]\n"
"v_pk_mul_f32 v[196:197], v[196:197], v[76:77]\nv_pk_mul_f32 v[198:199], v[198:199], v[78:79]\nv_pk_fma_f32 v[184:185], v[184:185], v[164:165], v[200:201]\nv_pk_fma_f32 v[186:187], v[186:187], v[166:167], v[202:203]\n"
"v_cvt_pk_bf16_f32 v156, v184, v185\nv_cvt_pk_bf16_f32 v157, v186, v187\nbuffer_store_dwordx2 v[156:157], v234, s[44:47], s76 offen\nv_pk_fma_f32 v[188:189], v[188:189], v[168:169], v[204:205]\n"
"v_pk_fma_f32 v[190:191], v[190:191], v[170:171], v[206:207]\nv_cvt_pk_bf16_f32 v158, v188, v189\nv_cvt_pk_bf16_f32 v159, v190, v191\nbuffer_store_dwordx2 v[158:159], v234, s[44:47], s77 offen\n"
"v_pk_fma_f32 v[192:193], v[192:193], v[172:173], v[208:209]\nv_pk_fma_f32 v[194:195], v[194:195], v[174:175], v[210:211]\nv_cvt_pk_bf16_f32 v160, v192, v193\nv_cvt_pk_bf16_f32 v161, v194, v195\n"
"buffer_store_dwordx2 v[160:161], v234, s[44:47], s78 offen\nv_pk_fma_f32 v[196:197], v[196:197], v[176:177], v[212:213]\nv_pk_fma_f32 v[198:199], v[198:199], v[178:179], v[214:215]\nv_cvt_pk_bf16_f32 v162, v196, v197\n"
"v_cvt_pk_bf16_f32 v163, v198, v199\nbuffer_store_dwordx2 v[162:163], v234, s[44:47], s79 offen\ns_branch L_end%=\nL_fb%=:\n"
"s_waitcnt vmcnt(0) lgkmcnt(0)\ns_nop 7\ns_barrier\ns_mov_b64 s[96:97], 0\n"
"v_subrev_u32 v225, 0x10000, v225\nv_subrev_u32 v226, 0x10000, v226\nv_subrev_u32 v227, 0x10000, v227\nv_subrev_u32 v228, 0x10000, v228\n"
"v_subrev_u32 v229, 0x10000, v229\nv_subrev_u32 v230, 0x10000, v230\nv_mov_b32 v240, 0xff800000\nv_mov_b32 v241, 0xff800000\n"
"v_mov_b32 v242, 0xff800000\nv_mov_b32 v243, 0xff800000\ns_lshr_b32 s87, s72, 1\ns_lshl_b32 s87, s87, 11\n"
"s_add_u32 s87, s87, 0x20000\nv_and_b32 v244, 15, v254\nv_lshl_add_u32 v244, v244, 4, s87\ns_mov_b32 s84, 32\n"
"s_mov_b32 s87, s88\nL_fbl%=:\ns_mov_b32 s79, s87\ns_add_u32 m0, s73, 0x0\n"
"s_add_u32 s79, s79, 0x0\nbuffer_load_dwordx4 v231, s[48:51], s79 offen lds\ns_add_u32 m0, s73, 0x2000\ns_add_u32 s79, s79, 0x80\n"
"buffer_load_dwordx4 v231, s[48:51], s79 offen lds\ns_add_u32 m0, s73, 0x4000\ns_add_u32 s79, s79, 0x80\nbuffer_load_dwordx4 v231, s[48:51], s79 offen lds\n"
"s_add_u32 m0, s73, 0x6000\ns_add_u32 s79, s79, 0x80\nbuffer_load_dwordx4 v231, s[48:51], s79 offen lds\ns_add_u32 m0, s73, 0x1000\n"
"s_add_u32 s79, s79, 0x3e80\nbuffer_load_dwordx4 v231, s[48:51], s79 offen lds\ns_add_u32 m0, s73, 0x3000\ns_add_u32 s79, s79, 0x80\n"
"buffer_load_dwordx4 v231, s[48:51], s79 offen lds\ns_add_u32 m0, s73, 0x5000\ns_add_u32 s79, s79, 0x80\nbuffer_load_dwordx4 v231, s[48:51], s79 offen lds\n"
"s_add_u32 m0, s73, 0x7000\ns_add_u32 s79, s79, 0x80\nbuffer_load_dwordx4 v231, s[48:51], s79 offen lds\ns_waitcnt vmcnt(0)\n"
"s_barrier\nds_read_b128 a[128:131], v225 offset:0\nds_read_b128 a[132:135], v226 offset:0\nds_read_b128 a[136:139], v225 offset:8192\n"
"ds_read_b128 a[140:143], v226 offset:8192\nds_read_b128 a[144:147], v225 offset:16384\nds_read_b128 a[148:151], v226 offset:16384\nds_read_b128 a[152:155], v225 offset:24576\n"
"ds_read_b128 a[156:159], v226 offset:24576\nds_read_b128 a[160:163], v225 offset:2048\nds_read_b128 a[164:167], v226 offset:2048\nds_read_b128 a[168:171], v225 offset:10240\n"
"ds_read_b128 a[172:175], v226 offset:10240\nds_read_b128 a[176:179], v225 offset:18432\nds_read_b128 a[180:183], v226 offset:18432\nds_read_b128 a[184:187], v225 offset:26624\n"
"ds_read_b128 a[188:191], v226 offset:26624\ns_waitcnt lgkmcnt(15)\nv_mfma_f32_16x16x32_bf16 v[128:131], a[128:131], a[0:3], 0\ns_waitcnt lgkmcnt(14)\n"
"v_mfma_f32_16x16x32_bf16 v[128:131], a[132:135], a[16:19], v[128:131]\ns_waitcnt lgkmcnt(13)\nv_mfma_f32_16x16x32_bf16 v[128:131], a[136:139], a[32:35], v[128:131]\ns_waitcnt lgkmcnt(12)\n"
"v_mfma_f32_16x16x32_bf16 v[128:131], a[140:143], a[48:51], v[128:131]\ns_waitcnt lgkmcnt(11)\nv_mfma_f32_16x16x32_bf16 v[128:131], a[144:147], a[64:67], v[128:131]\ns_waitcnt lgkmcnt(10)\n"
"v_mfma_f32_16x16x32_bf16 v[128:131], a[148:151], a[80:83], v[128:131]\ns_waitcnt lgkmcnt(9)\nv_mfma_f32_16x16x32_bf16 v[128:131], a[152:155], a[96:99], v[128:131]\ns_waitcnt lgkmcnt(0)\n"
"v_mfma_f32_16x16x32_bf16 v[128:131], a[156:159], a[112:115], v[128:131]\nv_mfma_f32_16x16x32_bf16 v[132:135], a[156:159], a[116:119], 0\nv_mfma_f32_16x16x32_bf16 v[132:135], a[152:155], a[100:103], v[132:135]\nv_mfma_f32_16x16x32_bf16 v[132:135], a[148:151], a[84:87], v[132:135]\n"
"v_mfma_f32_16x16x32_bf16 v[132:135], a[144:147], a[68:71], v[132:135]\nv_mfma_f32_16x16x32_bf16 v[132:135], a[140:143], a[52:55], v[132:135]\nv_mfma_f32_16x16x32_bf16 v[132:135], a[136:139], a[36:39], v[132:135]\nv_mfma_f32_16x16x32_bf16 v[132:135], a[132:135], a[20:23], v[132:135]\n"
"v_mfma_f32_16x16x32_bf16 v[132:135], a[128:131], a[4:7], v[132:135]\nv_mfma_f32_16x16x32_bf16 v[136:139], a[128:131], a[8:11], 0\nv_mfma_f32_16x16x32_bf16 v[136:139], a[132:135], a[24:27], v[136:139]\nv_mfma_f32_16x16x32_bf16 v[136:139], a[136:139], a[40:43], v[136:139]\n"
"v_mfma_f32_16x16x32_bf16 v[136:139], a[140:143], a[56:59], v[136:139]\nv_mfma_f32_16x16x32_bf16 v[136:139], a[144:147], a[72:75], v[136:139]\nv_mfma_f32_16x16x32_bf16 v[136:139], a[148:151], a[88:91], v[136:139]\nv_mfma_f32_16x16x32_bf16 v[136:139], a[152:155], a[104:107], v[136:139]\n"
"v_mfma_f32_16x16x32_bf16 v[136:139], a[156:159], a[120:123], v[136:139]\nv_mfma_f32_16x16x32_bf16 v[140:143], a[156:159], a[124:127], 0\nv_mfma_f32_16x16x32_bf16 v[140:143], a[152:155], a[108:111], v[140:143]\nv_mfma_f32_16x16x32_bf16 v[140:143], a[148:151], a[92:95], v[140:143]\n"
"v_mfma_f32_16x16x32_bf16 v[140:143], a[144:147], a[76:79], v[140:143]\nv_mfma_f32_16x16x32_bf16 v[140:143], a[140:143], a[60:63], v[140:143]\nv_mfma_f32_16x16x32_bf16 v[140:143], a[136:139], a[44:47], v[140:143]\nv_mfma_f32_16x16x32_bf16 v[140:143], a[132:135], a[28:31], v[140:143]\n"
"v_mfma_f32_16x16x32_bf16 v[140:143], a[128:131], a[12:15], v[140:143]\nv_mfma_f32_16x16x32_bf16 v[144:147], a[160:163], a[0:3], 0\nv_mfma_f32_16x16x32_bf16 v[144:147], a[164:167], a[16:19], v[144:147]\nv_mfma_f32_16x16x32_bf16 v[144:147], a[168:171], a[32:35], v[144:147]\n"
"v_mfma_f32_16x16x32_bf16 v[144:147], a[172:175], a[48:51], v[144:147]\nv_mfma_f32_16x16x32_bf16 v[144:147], a[176:179], a[64:67], v[144:147]\nv_mfma_f32_16x16x32_bf16 v[144:147], a[180:183], a[80:83], v[144:147]\nv_mfma_f32_16x16x32_bf16 v[144:147], a[184:187], a[96:99], v[144:147]\n"
"v_mfma_f32_16x16x32_bf16 v[144:147], a[188:191], a[112:115], v[144:147]\nv_mfma_f32_16x16x32_bf16 v[148:151], a[188:191], a[116:119], 0\nv_mfma_f32_16x16x32_bf16 v[148:151], a[184:187], a[100:103], v[148:151]\nv_mfma_f32_16x16x32_bf16 v[148:151], a[180:183], a[84:87], v[148:151]\n"
"v_mfma_f32_16x16x32_bf16 v[148:151], a[176:179], a[68:71], v[148:151]\nv_mfma_f32_16x16x32_bf16 v[148:151], a[172:175], a[52:55], v[148:151]\nv_mfma_f32_16x16x32_bf16 v[148:151], a[168:171], a[36:39], v[148:151]\nv_mfma_f32_16x16x32_bf16 v[148:151], a[164:167], a[20:23], v[148:151]\n"
"v_mfma_f32_16x16x32_bf16 v[148:151], a[160:163], a[4:7], v[148:151]\nv_mfma_f32_16x16x32_bf16 v[152:155], a[160:163], a[8:11], 0\nv_mfma_f32_16x16x32_bf16 v[152:155], a[164:167], a[24:27], v[152:155]\nv_mfma_f32_16x16x32_bf16 v[152:155], a[168:171], a[40:43], v[152:155]\n"
"v_mfma_f32_16x16x32_bf16 v[152:155], a[172:175], a[56:59], v[152:155]\nv_mfma_f32_16x16x32_bf16 v[152:155], a[176:179], a[72:75], v[152:155]\nv_mfma_f32_16x16x32_bf16 v[152:155], a[180:183], a[88:91], v[152:155]\nv_mfma_f32_16x16x32_bf16 v[152:155], a[184:187], a[104:107], v[152:155]\n"
"v_mfma_f32_16x16x32_bf16 v[152:155], a[188:191], a[120:123], v[152:155]\nv_mfma_f32_16x16x32_bf16 v[156:159], a[188:191], a[124:127], 0\nv_mfma_f32_16x16x32_bf16 v[156:159], a[184:187], a[108:111], v[156:159]\nv_mfma_f32_16x16x32_bf16 v[156:159], a[180:183], a[92:95], v[156:159]\n"
"v_mfma_f32_16x16x32_bf16 v[156:159], a[176:179], a[76:79], v[156:159]\nv_mfma_f32_16x16x32_bf16 v[156:159], a[172:175], a[60:63], v[156:159]\nv_mfma_f32_16x16x32_bf16 v[156:159], a[168:171], a[44:47], v[156:159]\nv_mfma_f32_16x16x32_bf16 v[156:159], a[164:167], a[28:31], v[156:159]\n"
"v_mfma_f32_16x16x32_bf16 v[156:159], a[160:163], a[12:15], v[156:159]\nv_max3_f32 v240, v240, v128, v129\nv_max3_f32 v240, v240, v130, v131\nv_max3_f32 v241, v241, v132, v133\n"
"v_max3_f32 v241, v241, v134, v135\nv_max3_f32 v242, v242, v136, v137\nv_max3_f32 v242, v242, v138, v139\nv_max3_f32 v243, v243, v140, v141\n"
"v_max3_f32 v243, v243, v142, v143\nv_max3_f32 v240, v240, v144, v145\nv_max3_f32 v240, v240, v146, v147\nv_max3_f32 v241, v241, v148, v149\n"
"v_max3_f32 v241, v241, v150, v151\nv_max3_f32 v242, v242, v152, v153\nv_max3_f32 v242, v242, v154, v155\nv_max3_f32 v243, v243, v156, v157\n"
"v_max3_f32 v243, v243, v158, v159\ns_add_u32 s87, s87, 0x8000\ns_barrier\ns_sub_u32 s84, s84, 1\n"
"s_cmp_lg_u32 s84, 0\ns_cbranch_scc1 L_fbl%=\ns_mov_b32 s79, s88\ns_mov_b32 s80, s88\n"
"s_mul_i32 s81, s74, 458752\ns_add_u32 m0, s73, 0x0\ns_add_u32 s79, s79, 0x0\nbuffer_load_dwordx4 v231, s[48:51], s79 offen lds\n"
"s_add_u32 m0, s73, 0x2000\ns_add_u32 s79, s79, 0x80\nbuffer_load_dwordx4 v231, s[48:51], s79 offen lds\ns_add_u32 m0, s73, 0x4000\n"
"s_add_u32 s79, s79, 0x80\nbuffer_load_dwordx4 v231, s[48:51], s79 offen lds\ns_add_u32 m0, s73, 0x6000\ns_add_u32 s79, s79, 0x80\n"
"buffer_load_dwordx4 v231, s[48:51], s79 offen lds\ns_add_u32 m0, s73, 0x1000\ns_add_u32 s79, s79, 0x3e80\nbuffer_load_dwordx4 v231, s[48:51], s79 offen lds\n"
"s_add_u32 m0, s73, 0x3000\ns_add_u32 s79, s79, 0x80\nbuffer_load_dwordx4 v231, s[48:51], s79 offen lds\ns_add_u32 m0, s73, 0x5000\n"
"s_add_u32 s79, s79, 0x80\nbuffer_load_dwordx4 v231, s[48:51], s79 offen lds\ns_add_u32 m0, s73, 0x7000\ns_add_u32 s79, s79, 0x80\n"
"buffer_load_dwordx4 v231, s[48:51], s79 offen lds\ns_add_u32 m0, s73, 0x10000\ns_add_u32 s79, s79, 0x3e80\nbuffer_load_dwordx4 v231, s[48:51], s79 offen lds\n"
"s_add_u32 m0, s73, 0x12000\ns_add_u32 s79, s79, 0x80\nbuffer_load_dwordx4 v231, s[48:51], s79 offen lds\ns_add_u32 m0, s73, 0x14000\n"
"s_add_u32 s79, s79, 0x80\nbuffer_load_dwordx4 v231, s[48:51], s79 offen lds\ns_add_u32 m0, s73, 0x16000\ns_add_u32 s79, s79, 0x80\n"
"buffer_load_dwordx4 v231, s[48:51], s79 offen lds\ns_add_u32 m0, s73, 0x11000\ns_add_u32 s79, s79, 0x3e80\nbuffer_load_dwordx4 v231, s[48:51], s79 offen lds\n"
"s_add_u32 m0, s73, 0x13000\ns_add_u32 s79, s79, 0x80\nbuffer_load_dwordx4 v231, s[48:51], s79 offen lds\ns_add_u32 m0, s73, 0x15000\n"
"s_add_u32 s79, s79, 0x80\nbuffer_load_dwordx4 v231, s[48:51], s79 offen lds\ns_add_u32 m0, s73, 0x17000\ns_add_u32 s79, s79, 0x80\n"
"buffer_load_dwordx4 v231, s[48:51], s79 offen lds\ns_add_u32 m0, s73, 0x18000\ns_add_u32 s79, s79, 0x3e80\nbuffer_load_dwordx4 v231, s[48:51], s79 offen lds\n"
"s_add_u32 m0, s73, 0x1a000\ns_add_u32 s79, s79, 0x80\nbuffer_load_dwordx4 v231, s[48:51], s79 offen lds\ns_add_u32 m0, s73, 0x1c000\n"
"s_add_u32 s79, s79, 0x80\nbuffer_load_dwordx4 v231, s[48:51], s79 offen lds\ns_add_u32 m0, s73, 0x1e000\ns_add_u32 s79, s79, 0x80\n"
"buffer_load_dwordx4 v231, s[48:51], s79 offen lds\ns_add_u32 m0, s73, 0x19000\ns_add_u32 s79, s79, 0x3e80\nbuffer_load_dwordx4 v231, s[48:51], s79 offen lds\n"
"s_add_u32 m0, s73, 0x1b000\ns_add_u32 s79, s79, 0x80\nbuffer_load_dwordx4 v231, s[48:51], s79 offen lds\ns_add_u32 m0, s73, 0x1d000\n"
"s_add_u32 s79, s79, 0x80\nbuffer_load_dwordx4 v231, s[48:51], s79 offen lds\ns_add_u32 m0, s73, 0x1f000\ns_add_u32 s79, s79, 0x80\n"
"buffer_load_dwordx4 v231, s[48:51], s79 offen lds\ns_waitcnt vmcnt(0)\ns_barrier\nds_read_b128 a[128:131], v225 offset:0\n"
"ds_read_b128 a[132:135], v226 offset:0\nds_read_b128 a[136:139], v225 offset:8192\nds_read_b128 a[140:143], v226 offset:8192\nds_read_b128 a[144:147], v225 offset:16384\n"
"ds_read_b128 a[148:151], v226 offset:16384\nds_read_b128 a[152:155], v225 offset:24576\nds_read_b128 a[156:159], v226 offset:24576\nds_read_b128 a[160:163], v225 offset:2048\n"
"ds_read_b128 a[164:167], v226 offset:2048\nds_read_b128 a[168:171], v225 offset:10240\nds_read_b128 a[172:175], v226 offset:10240\nds_read_b128 a[176:179], v225 offset:18432\n"
"ds_read_b128 a[180:183], v226 offset:18432\nds_read_b128 a[184:187], v225 offset:26624\nds_read_b128 a[188:191], v226 offset:26624\ns_waitcnt lgkmcnt(0)\n"
"s_barrier\nv_mfma_f32_16x16x32_bf16 v[76:79], a[128:131], a[0:3], 0\ns_add_u32 s80, s80, 0x0\ns_add_u32 s86, s73, 0x20000\n"
"v_add_u32 v245, s86, v224\ns_and_b32 s76, s96, 0xc2cc0000\nv_mfma_f32_16x16x32_bf16 v[80:83], a[128:131], a[4:7], 0\ns_add_u32 m0, s73, 0x8000\n"
"v_add_u32 v225, 0x10000, v225\nv_mfma_f32_16x16x32_bf16 v[84:87], a[128:131], a[8:11], 0\nbuffer_load_dwordx4 v232, s[52:55], s80 offen lds\nv_mfma_f32_16x16x32_bf16 v[88:91], a[128:131], a[12:15], 0\n"
"s_add_u32 s80, s80, 0x80\nds_read_b128 a[192:195], v225 offset:0\nv_mfma_f32_16x16x32_bf16 v[76:79], a[132:135], a[16:19], v[76:79]\ns_add_u32 m0, s73, 0xa000\n"
"ds_read_b128 a[200:203], v225 offset:8192\nv_mfma_f32_16x16x32_bf16 v[80:83], a[132:135], a[20:23], v[80:83]\nbuffer_load_dwordx4 v232, s[52:55], s80 offen lds\nv_mfma_f32_16x16x32_bf16 v[84:87], a[132:135], a[24:27], v[84:87]\n"
"s_add_u32 s80, s80, 0x4080\nds_read_b128 a[208:211], v225 offset:16384\nv_mfma_f32_16x16x32_bf16 v[88:91], a[132:135], a[28:31], v[88:91]\ns_add_u32 m0, s73, 0xc000\n"
"ds_read_b128 a[216:219], v225 offset:24576\nv_add_u32 v226, 0x10000, v226\nv_mfma_f32_16x16x32_bf16 v[76:79], a[136:139], a[32:35], v[76:79]\nbuffer_load_dwordx4 v232, s[52:55], s80 offen lds\n"
"v_mfma_f32_16x16x32_bf16 v[80:83], a[136:139], a[36:39], v[80:83]\ns_add_u32 s80, s80, 0x80\nds_read_b128 a[196:199], v226 offset:0\nv_mfma_f32_16x16x32_bf16 v[84:87], a[136:139], a[40:43], v[84:87]\n"
"s_add_u32 m0, s73, 0xe000\nds_read_b128 a[204:207], v226 offset:8192\nv_mfma_f32_16x16x32_bf16 v[88:91], a[136:139], a[44:47], v[88:91]\nbuffer_load_dwordx4 v232, s[52:55], s80 offen lds\n"
"v_mfma_f32_16x16x32_bf16 v[76:79], a[140:143], a[48:51], v[76:79]\ns_add_u32 s80, s80, 0xfffffe80\nds_read_b128 a[212:215], v226 offset:16384\nv_mfma_f32_16x16x32_bf16 v[80:83], a[140:143], a[52:55], v[80:83]\n"
"s_add_u32 m0, s73, 0x9000\nds_read_b128 a[220:223], v226 offset:24576\nv_mfma_f32_16x16x32_bf16 v[84:87], a[140:143], a[56:59], v[84:87]\nbuffer_load_dwordx4 v232, s[52:55], s80 offen lds\n"
"v_mfma_f32_16x16x32_bf16 v[88:91], a[140:143], a[60:63], v[88:91]\ns_add_u32 s80, s80, 0x80\nds_read_b128 a[224:227], v225 offset:2048\nv_mfma_f32_16x16x32_bf16 v[76:79], a[144:147], a[64:67], v[76:79]\n"
"s_add_u32 m0, s73, 0xb000\nds_read_b128 a[228:231], v226 offset:2048\nv_mfma_f32_16x16x32_bf16 v[80:83], a[144:147], a[68:71], v[80:83]\nbuffer_load_dwordx4 v232, s[52:55], s80 offen lds\n"
"v_mfma_f32_16x16x32_bf16 v[84:87], a[144:147], a[72:75], v[84:87]\ns_add_u32 s80, s80, 0xffffc080\nds_read_b128 a[232:235], v225 offset:10240\nv_mfma_f32_16x16x32_bf16 v[88:91], a[144:147], a[76:79], v[88:91]\n"
"s_add_u32 m0, s73, 0xd000\nds_read_b128 a[236:239], v226 offset:10240\nv_mfma_f32_16x16x32_bf16 v[76:79], a[148:151], a[80:83], v[76:79]\nbuffer_load_dwordx4 v232, s[52:55], s80 offen lds\n"
"v_mfma_f32_16x16x32_bf16 v[80:83], a[148:151], a[84:87], v[80:83]\ns_add_u32 s80, s80, 0x80\nds_read_b128 a[240:243], v225 offset:18432\nv_mfma_f32_16x16x32_bf16 v[84:87], a[148:151], a[88:91], v[84:87]\n"
"s_add_u32 m0, s73, 0xf000\nds_read_b128 a[244:247], v226 offset:18432\nv_mfma_f32_16x16x32_bf16 v[88:91], a[148:151], a[92:95], v[88:91]\nbuffer_load_dwordx4 v232, s[52:55], s80 offen lds\n"
"v_mfma_f32_16x16x32_bf16 v[76:79], a[152:155], a[96:99], v[76:79]\ns_add_u32 s80, s80, 0x7e80\nds_read_b128 a[248:251], v225 offset:26624\nv_mfma_f32_16x16x32_bf16 v[80:83], a[152:155], a[100:103], v[80:83]\n"
"s_add_u32 m0, s73, 0x0\nds_read_b128 a[252:255], v226 offset:26624\nv_mfma_f32_16x16x32_bf16 v[84:87], a[152:155], a[104:107], v[84:87]\nbuffer_load_dwordx4 v232, s[52:55], s80 offen lds\n"
"v_mfma_f32_16x16x32_bf16 v[88:91], a[152:155], a[108:111], v[88:91]\nv_mfma_f32_16x16x32_bf16 v[76:79], a[156:159], a[112:115], v[76:79]\nv_mfma_f32_16x16x32_bf16 v[80:83], a[156:159], a[116:119], v[80:83]\nv_mfma_f32_16x16x32_bf16 v[84:87], a[156:159], a[120:123], v[84:87]\n"
"v_mfma_f32_16x16x32_bf16 v[88:91], a[156:159], a[124:127], v[88:91]\nv_mfma_f32_16x16x32_bf16 v[92:95], a[160:163], a[0:3], 0\ns_nop 3\nv_max3_f32 v240, v240, v76, v77\n"
"v_mfma_f32_16x16x32_bf16 v[92:95], a[164:167], a[16:19], v[92:95]\nv_max3_f32 v241, v241, v80, v81\nv_max3_f32 v240, v240, v78, v79\nv_mfma_f32_16x16x32_bf16 v[92:95], a[168:171], a[32:35], v[92:95]\n"
"v_max3_f32 v242, v242, v84, v85\nv_max3_f32 v241, v241, v82, v83\nv_mfma_f32_16x16x32_bf16 v[92:95], a[172:175], a[48:51], v[92:95]\nv_max3_f32 v243, v243, v88, v89\n"
"v_max3_f32 v242, v242, v86, v87\nv_mfma_f32_16x16x32_bf16 v[92:95], a[176:179], a[64:67], v[92:95]\nv_max3_f32 v243, v243, v90, v91\nv_mfma_f32_16x16x32_bf16 v[92:95], a[180:183], a[80:83], v[92:95]\n"
"ds_write_b128 v245, v[240:243] offset:0\nv_mfma_f32_16x16x32_bf16 v[92:95], a[184:187], a[96:99], v[92:95]\nv_mfma_f32_16x16x32_bf16 v[92:95], a[188:191], a[112:115], v[92:95]\nv_mfma_f32_16x16x32_bf16 v[96:99], a[188:191], a[116:119], 0\n"
"v_mfma_f32_16x16x32_bf16 v[96:99], a[184:187], a[100:103], v[96:99]\ns_cmp_lt_u32 s72, 2\ns_cselect_b64 s[92:93], s[48:49], s[52:53]\nv_mfma_f32_16x16x32_bf16 v[96:99], a[180:183], a[84:87], v[96:99]\ns_waitcnt lgkmcnt(0)\ns_barrier\n"
"ds_read_b128 v[0:3], v244 offset:0\nv_mfma_f32_16x16x32_bf16 v[96:99], a[176:179], a[68:71], v[96:99]\ns_add_u32 s80, s80, 0x80\nds_read_b128 v[4:7], v244 offset:256\n"
"v_mfma_f32_16x16x32_bf16 v[96:99], a[172:175], a[52:55], v[96:99]\ns_add_u32 m0, s73, 0x2000\nds_read_b128 v[8:11], v244 offset:512\nv_mfma_f32_16x16x32_bf16 v[96:99], a[168:171], a[36:39], v[96:99]\n"
"buffer_load_dwordx4 v232, s[52:55], s80 offen lds\nv_mfma_f32_16x16x32_bf16 v[96:99], a[164:167], a[20:23], v[96:99]\ns_add_u32 s80, s80, 0x4080\nds_read_b128 v[12:15], v244 offset:768\n"
"v_mfma_f32_16x16x32_bf16 v[96:99], a[160:163], a[4:7], v[96:99]\ns_add_u32 m0, s73, 0x4000\nds_read_b128 v[16:19], v244 offset:1024\nv_mfma_f32_16x16x32_bf16 v[100:103], a[160:163], a[8:11], 0\n"
"buffer_load_dwordx4 v232, s[52:55], s80 offen lds\nv_mfma_f32_16x16x32_bf16 v[100:103], a[164:167], a[24:27], v[100:103]\ns_add_u32 s80, s80, 0x80\nds_read_b128 v[20:23], v244 offset:1280\n"
"v_mfma_f32_16x16x32_bf16 v[100:103], a[168:171], a[40:43], v[100:103]\ns_add_u32 m0, s73, 0x6000\nds_read_b128 v[24:27], v244 offset:1536\nv_mfma_f32_16x16x32_bf16 v[100:103], a[172:175], a[56:59], v[100:103]\n"
"buffer_load_dwordx4 v232, s[52:55], s80 offen lds\nv_mfma_f32_16x16x32_bf16 v[100:103], a[176:179], a[72:75], v[100:103]\ns_add_u32 s80, s80, 0xfffffe80\nds_read_b128 v[28:31], v244 offset:1792\n"
"v_mfma_f32_16x16x32_bf16 v[100:103], a[180:183], a[88:91], v[100:103]\ns_add_u32 m0, s73, 0x1000\ns_waitcnt lgkmcnt(4)\nv_max3_f32 v0, v0, v4, v8\n"
"v_max3_f32 v1, v1, v5, v9\nv_max3_f32 v2, v2, v6, v10\nv_mfma_f32_16x16x32_bf16 v[100:103], a[184:187], a[104:107], v[100:103]\nbuffer_load_dwordx4 v232, s[52:55], s80 offen lds\n"
"v_mfma_f32_16x16x32_bf16 v[100:103], a[188:191], a[120:123], v[100:103]\ns_add_u32 s80, s80, 0x80\nv_max3_f32 v3, v3, v7, v11\nv_mfma_f32_16x16x32_bf16 v[104:107], a[188:191], a[124:127], 0\n"
"s_add_u32 m0, s73, 0x3000\ns_waitcnt lgkmcnt(2)\nv_max3_f32 v0, v0, v12, v16\nv_max3_f32 v1, v1, v13, v17\n"
"v_max3_f32 v2, v2, v14, v18\nv_mfma_f32_16x16x32_bf16 v[104:107], a[184:187], a[108:111], v[104:107]\nbuffer_load_dwordx4 v232, s[52:55], s80 offen lds\nv_mfma_f32_16x16x32_bf16 v[104:107], a[180:183], a[92:95], v[104:107]\n"
"s_add_u32 s80, s80, 0xffffc080\nv_max3_f32 v3, v3, v15, v19\nv_mfma_f32_16x16x32_bf16 v[104:107], a[176:179], a[76:79], v[104:107]\ns_add_u32 m0, s73, 0x5000\n"
"s_waitcnt lgkmcnt(0)\nv_max3_f32 v0, v0, v20, v24\nv_max3_f32 v1, v1, v21, v25\nv_max3_f32 v2, v2, v22, v26\n"
"v_mfma_f32_16x16x32_bf16 v[104:107], a[172:175], a[60:63], v[104:107]\nbuffer_load_dwordx4 v232, s[52:55], s80 offen lds\nv_mfma_f32_16x16x32_bf16 v[104:107], a[168:171], a[44:47], v[104:107]\ns_add_u32 s80, s80, 0x80\n"
"v_max3_f32 v3, v3, v23, v27\nv_max_f32 v0, v0, v28\nv_max_f32 v1, v1, v29\nv_mfma_f32_16x16x32_bf16 v[104:107], a[164:167], a[28:31], v[104:107]\n"
"s_add_u32 m0, s73, 0x7000\nv_max_f32 v2, v2, v30\nv_max_f32 v3, v3, v31\nv_sub_f32 v208, s76, v0\n"
"v_mfma_f32_16x16x32_bf16 v[104:107], a[160:163], a[12:15], v[104:107]\nbuffer_load_dwordx4 v232, s[52:55], s80 offen lds\ns_waitcnt vmcnt(8)\ns_barrier\n"
"v_sub_f32 v209, s76, v0\nv_sub_f32 v210, s76, v0\nv_sub_f32 v211, s76, v0\ns_nop 1\n"
"v_mfma_f32_16x16x32_bf16 v[128:131], a[192:195], a[0:3], v[208:211]\ns_add_u32 s79, s79, 0x3e80\nds_read_b64_tr_b16 a[128:129], v227 offset:32768\nv_sub_f32 v212, s76, v1\n"
"v_sub_f32 v213, s76, v1\nv_mfma_f32_16x16x32_bf16 v[128:131], a[196:199], a[16:19], v[128:131]\ns_add_u32 m0, s73, 0x10000\nds_read_b64_tr_b16 a[130:131], v227 offset:34816\n"
"v_sub_f32 v214, s76, v1\nv_sub_f32 v215, s76, v1\nv_mfma_f32_16x16x32_bf16 v[128:131], a[200:203], a[32:35], v[128:131]\nbuffer_load_dwordx4 v231, s[48:51], s79 offen lds\n"
"v_mfma_f32_16x16x32_bf16 v[128:131], a[204:207], a[48:51], v[128:131]\ns_add_u32 s79, s79, 0x80\nds_read_b64_tr_b16 a[132:133], v227 offset:36864\nv_sub_f32 v216, s76, v2\n"
"v_sub_f32 v217, s76, v2\nv_mfma_f32_16x16x32_bf16 v[128:131], a[208:211], a[64:67], v[128:131]\ns_add_u32 m0, s73, 0x12000\nds_read_b64_tr_b16 a[134:135], v227 offset:38912\n"
"v_sub_f32 v218, s76, v2\nv_sub_f32 v219, s76, v2\nv_mfma_f32_16x16x32_bf16 v[128:131], a[212:215], a[80:83], v[128:131]\nbuffer_load_dwordx4 v231, s[48:51], s79 offen lds\n"
"v_mfma_f32_16x16x32_bf16 v[128:131], a[216:219], a[96:99], v[128:131]\ns_add_u32 s79, s79, 0x80\nds_read_b64_tr_b16 a[136:137], v228 offset:32768\nv_sub_f32 v220, s76, v3\n"
"v_sub_f32 v221, s76, v3\nv_mfma_f32_16x16x32_bf16 v[128:131], a[220:223], a[112:115], v[128:131]\ns_add_u32 m0, s73, 0x14000\nds_read_b64_tr_b16 a[138:139], v228 offset:34816\n"
"v_sub_f32 v222, s76, v3\nv_sub_f32 v223, s76, v3\nv_mfma_f32_16x16x32_bf16 v[132:135], a[220:223], a[116:119], v[212:215]\nbuffer_load_dwordx4 v231, s[48:51], s79 offen lds\n"
"v_mfma_f32_16x16x32_bf16 v[132:135], a[216:219], a[100:103], v[132:135]\ns_add_u32 s79, s79, 0x80\nds_read_b64_tr_b16 a[140:141], v228 offset:36864\nv_mfma_f32_16x16x32_bf16 v[132:135], a[212:215], a[84:87], v[132:135]\n"
"s_add_u32 m0, s73, 0x16000\nds_read_b64_tr_b16 a[142:143], v228 offset:38912\nv_add_f32 v76, v76, v208\nv_add_f32 v77, v77, v208\n"
"v_mfma_f32_16x16x32_bf16 v[132:135], a[208:211], a[68:71], v[132:135]\nbuffer_load_dwordx4 v231, s[48:51], s79 offen lds\nv_mfma_f32_16x16x32_bf16 v[132:135], a[204:207], a[52:55], v[132:135]\ns_add_u32 s79, s79, 0x3e80\n"
"ds_read_b64_tr_b16 a[144:145], v229 offset:32768\nv_add_f32 v78, v78, v208\nv_add_f32 v79, v79, v208\nv_mfma_f32_16x16x32_bf16 v[132:135], a[200:203], a[36:39], v[132:135]\n"
"s_add_u32 m0, s73, 0x11000\nds_read_b64_tr_b16 a[146:147], v229 offset:34816\nv_add_f32 v80, v80, v212\nv_add_f32 v81, v81, v212\n"
"v_mfma_f32_16x16x32_bf16 v[132:135], a[196:199], a[20:23], v[132:135]\nbuffer_load_dwordx4 v231, s[48:51], s79 offen lds\nv_mfma_f32_16x16x32_bf16 v[132:135], a[192:195], a[4:7], v[132:135]\ns_add_u32 s79, s79, 0x80\n"
"ds_read_b64_tr_b16 a[148:149], v229 offset:36864\nv_add_f32 v82, v82, v212\nv_add_f32 v83, v83, v212\nv_mfma_f32_16x16x32_bf16 v[136:139], a[192:195], a[8:11], v[216:219]\n"
"s_add_u32 m0, s73, 0x13000\nds_read_b64_tr_b16 a[150:151], v229 offset:38912\nv_add_f32 v84, v84, v216\nv_add_f32 v85, v85, v216\n"
"v_mfma_f32_16x16x32_bf16 v[136:139], a[196:199], a[24:27], v[136:139]\nbuffer_load_dwordx4 v231, s[48:51], s79 offen lds\nv_mfma_f32_16x16x32_bf16 v[136:139], a[200:203], a[40:43], v[136:139]\ns_add_u32 s79, s79, 0x80\n"
"ds_read_b64_tr_b16 a[152:153], v230 offset:32768\nv_add_f32 v92, v92, v208\nv_add_f32 v93, v93, v208\nv_mfma_f32_16x16x32_bf16 v[136:139], a[204:207], a[56:59], v[136:139]\n"
"s_add_u32 m0, s73, 0x15000\nds_read_b64_tr_b16 a[154:155], v230 offset:34816\nv_add_f32 v94, v94, v208\nv_add_f32 v95, v95, v208\n"
"v_mfma_f32_16x16x32_bf16 v[136:139], a[208:211], a[72:75], v[136:139]\nbuffer_load_dwordx4 v231, s[48:51], s79 offen lds\nv_mfma_f32_16x16x32_bf16 v[136:139], a[212:215], a[88:91], v[136:139]\ns_add_u32 s79, s79, 0x80\n"
"ds_read_b64_tr_b16 a[156:157], v230 offset:36864\nv_add_f32 v96, v96, v212\nv_add_f32 v97, v97, v212\nv_mfma_f32_16x16x32_bf16 v[136:139], a[216:219], a[104:107], v[136:139]\n"
"s_add_u32 m0, s73, 0x17000\nds_read_b64_tr_b16 a[158:159], v230 offset:38912\nv_add_f32 v98, v98, v212\nv_add_f32 v99, v99, v212\n"
"v_mfma_f32_16x16x32_bf16 v[136:139], a[220:223], a[120:123], v[136:139]\nbuffer_load_dwordx4 v231, s[48:51], s79 offen lds\nv_mfma_f32_16x16x32_bf16 v[140:143], a[220:223], a[124:127], v[220:223]\ns_add_u32 s94, s88, 0x100000\n"
"ds_read_b64_tr_b16 a[160:161], v227 offset:40960\nv_exp_f32 v76, v76\nv_mfma_f32_16x16x32_bf16 v[140:143], a[216:219], a[108:111], v[140:143]\ns_and_b32 s91, s85, 63\n"
"ds_read_b64_tr_b16 a[162:163], v227 offset:43008\nv_exp_f32 v77, v77\nv_mfma_f32_16x16x32_bf16 v[140:143], a[212:215], a[92:95], v[140:143]\ns_mul_i32 s91, s91, 1024\n"
"ds_read_b64_tr_b16 a[164:165], v227 offset:45056\nv_exp_f32 v78, v78\nv_mfma_f32_16x16x32_bf16 v[140:143], a[208:211], a[76:79], v[140:143]\ns_add_u32 s91, s91, s88\n"
"ds_read_b64_tr_b16 a[166:167], v227 offset:47104\nv_exp_f32 v79, v79\nv_mfma_f32_16x16x32_bf16 v[140:143], a[204:207], a[60:63], v[140:143]\ns_add_u32 s91, s91, 0x30000\n"
"ds_read_b64_tr_b16 a[168:169], v228 offset:40960\nv_exp_f32 v80, v80\nv_mfma_f32_16x16x32_bf16 v[140:143], a[200:203], a[44:47], v[140:143]\nds_read_b64_tr_b16 a[170:171], v228 offset:43008\n"
"v_exp_f32 v81, v81\nv_mfma_f32_16x16x32_bf16 v[140:143], a[196:199], a[28:31], v[140:143]\nds_read_b64_tr_b16 a[172:173], v228 offset:45056\nv_exp_f32 v82, v82\n"
"v_mfma_f32_16x16x32_bf16 v[140:143], a[192:195], a[12:15], v[140:143]\nds_read_b64_tr_b16 a[174:175], v228 offset:47104\nv_exp_f32 v83, v83\nv_mfma_f32_16x16x32_bf16 v[144:147], a[224:227], a[0:3], v[208:211]\n"
"ds_read_b64_tr_b16 a[176:177], v229 offset:40960\nv_exp_f32 v84, v84\nv_mfma_f32_16x16x32_bf16 v[144:147], a[228:231], a[16:19], v[144:147]\nds_read_b64_tr_b16 a[178:179], v229 offset:43008\n"
"v_exp_f32 v85, v85\nv_mfma_f32_16x16x32_bf16 v[144:147], a[232:235], a[32:35], v[144:147]\nds_read_b64_tr_b16 a[180:181], v229 offset:45056\nv_add_f32 v86, v86, v216\n"
"v_add_f32 v87, v87, v216\nv_mfma_f32_16x16x32_bf16 v[144:147], a[236:239], a[48:51], v[144:147]\nds_read_b64_tr_b16 a[182:183], v229 offset:47104\nv_exp_f32 v92, v92\n"
"v_mfma_f32_16x16x32_bf16 v[144:147], a[240:243], a[64:67], v[144:147]\nds_read_b64_tr_b16 a[184:185], v230 offset:40960\nv_exp_f32 v93, v93\nv_mfma_f32_16x16x32_bf16 v[144:147], a[244:247], a[80:83], v[144:147]\n"
"ds_read_b64_tr_b16 a[186:187], v230 offset:43008\nv_exp_f32 v94, v94\nv_mfma_f32_16x16x32_bf16 v[144:147], a[248:251], a[96:99], v[144:147]\nds_read_b64_tr_b16 a[188:189], v230 offset:45056\n"
"v_exp_f32 v95, v95\nv_mfma_f32_16x16x32_bf16 v[144:147], a[252:255], a[112:115], v[144:147]\nds_read_b64_tr_b16 a[190:191], v230 offset:47104\nv_exp_f32 v96, v96\n"
"v_mfma_f32_16x16x32_bf16 v[148:151], a[252:255], a[116:119], v[212:215]\nv_exp_f32 v97, v97\nv_add_f32 v100, v100, v216\nv_mfma_f32_16x16x32_bf16 v[148:151], a[248:251], a[100:103], v[148:151]\n"
"v_exp_f32 v98, v98\nv_add_f32 v101, v101, v216\nv_mfma_f32_16x16x32_bf16 v[148:151], a[244:247], a[84:87], v[148:151]\nv_exp_f32 v99, v99\n"
"v_add_f32 v102, v102, v216\nv_mfma_f32_16x16x32_bf16 v[148:151], a[240:243], a[68:71], v[148:151]\nv_exp_f32 v86, v86\nv_add_f32 v103, v103, v216\n"
"v_mfma_f32_16x16x32_bf16 v[148:151], a[236:239], a[52:55], v[148:151]\nv_exp_f32 v87, v87\nv_add_f32 v88, v88, v220\nv_mfma_f32_16x16x32_bf16 v[148:151], a[232:235], a[36:39], v[148:151]\n"
"v_exp_f32 v100, v100\nv_add_f32 v89, v89, v220\nv_mfma_f32_16x16x32_bf16 v[148:151], a[228:231], a[20:23], v[148:151]\nv_exp_f32 v101, v101\n"
"v_add_f32 v90, v90, v220\nv_mfma_f32_16x16x32_bf16 v[148:151], a[224:227], a[4:7], v[148:151]\nv_exp_f32 v102, v102\nv_add_f32 v91, v91, v220\n"
"v_mfma_f32_16x16x32_bf16 v[152:155], a[224:227], a[8:11], v[216:219]\nv_exp_f32 v103, v103\nv_add_f32 v104, v104, v220\nv_mfma_f32_16x16x32_bf16 v[152:155], a[228:231], a[24:27], v[152:155]\n"
"v_exp_f32 v88, v88\nv_add_f32 v105, v105, v220\nv_mfma_f32_16x16x32_bf16 v[152:155], a[232:235], a[40:43], v[152:155]\nv_exp_f32 v89, v89\n"
"v_cvt_pk_bf16_f32 v160, v76, v77\nv_mfma_f32_16x16x32_bf16 v[152:155], a[236:239], a[56:59], v[152:155]\nv_add_f32 v106, v106, v220\nv_add_f32 v107, v107, v220\n"
"v_cvt_pk_bf16_f32 v161, v78, v79\nv_mfma_f32_16x16x32_bf16 v[152:155], a[240:243], a[72:75], v[152:155]\nv_exp_f32 v90, v90\nv_cvt_pk_bf16_f32 v162, v92, v93\n"
"v_mfma_f32_16x16x32_bf16 v[152:155], a[244:247], a[88:91], v[152:155]\nv_exp_f32 v91, v91\nv_cvt_pk_bf16_f32 v164, v80, v81\nv_mfma_f32_16x16x32_bf16 v[152:155], a[248:251], a[104:107], v[152:155]\n"
"v_cvt_pk_bf16_f32 v165, v82, v83\nv_cvt_pk_bf16_f32 v163, v94, v95\nv_cvt_pk_bf16_f32 v166, v96, v97\nv_mfma_f32_16x16x32_bf16 v[152:155], a[252:255], a[120:123], v[152:155]\n"
"ds_write_b128 v233, v[160:163] offset:16384\nv_cvt_pk_bf16_f32 v167, v98, v99\nv_mfma_f32_16x16x32_bf16 v[156:159], a[252:255], a[124:127], v[220:223]\nv_exp_f32 v104, v104\n"
"v_cvt_pk_bf16_f32 v168, v84, v85\nv_mfma_f32_16x16x32_bf16 v[156:159], a[248:251], a[108:111], v[156:159]\nds_write_b128 v233, v[164:167] offset:17408\nv_cvt_pk_bf16_f32 v169, v86, v87\n"
"v_mfma_f32_16x16x32_bf16 v[156:159], a[244:247], a[92:95], v[156:159]\nv_exp_f32 v105, v105\nv_cvt_pk_bf16_f32 v170, v100, v101\nv_mfma_f32_16x16x32_bf16 v[156:159], a[240:243], a[76:79], v[156:159]\n"
"v_cvt_pk_bf16_f32 v171, v102, v103\nv_exp_f32 v106, v106\nv_mfma_f32_16x16x32_bf16 v[156:159], a[236:239], a[60:63], v[156:159]\nds_write_b128 v233, v[168:171] offset:18432\n"
"v_cvt_pk_bf16_f32 v172, v88, v89\nv_mfma_f32_16x16x32_bf16 v[156:159], a[232:235], a[44:47], v[156:159]\nv_exp_f32 v107, v107\nv_cvt_pk_bf16_f32 v173, v90, v91\n"
"v_mfma_f32_16x16x32_bf16 v[156:159], a[228:231], a[28:31], v[156:159]\nv_cvt_pk_bf16_f32 v174, v104, v105\nv_cvt_pk_bf16_f32 v175, v106, v107\nv_mfma_f32_16x16x32_bf16 v[156:159], a[224:227], a[12:15], v[156:159]\n"
"ds_write_b128 v233, v[172:175] offset:19456\ns_waitcnt vmcnt(0) lgkmcnt(0)\ns_nop 7\ns_barrier\n"
"s_branch L_att%=\nL_p2%=:\ns_mov_b32 s31, 0x8000\nL_p2l%=:\n"
"s_sleep 2\nbuffer_load_dword v251, v246, s[4:7], 0 offen offset:64 sc1\ns_waitcnt vmcnt(0)\nv_cmp_ne_u32 vcc, s30, v251\n"
"s_sub_u32 s31, s31, 1\ns_cbranch_scc1 L_p2r%=\ns_nop 2\ns_and_b32 vcc_lo, vcc_lo, 0xff\n"
"s_cbranch_scc1 L_p2l%=\ns_branch L_p2r%=\nL_nt%=:\ns_waitcnt vmcnt(0)\n"
"s_barrier\nbuffer_store_dword v252, v253, s[4:7], s33 offen offset:64 sc1\ns_branch L_end%=\nL_hs%=:\n"
"s_mov_b32 s84, 0x8000\nL_hsl%=:\nbuffer_load_dword v251, v246, s[4:7], 0 offen sc1\nbuffer_load_dword v252, v247, s[4:7], 0 offen offset:128 sc1\n"
"s_waitcnt vmcnt(0)\nbuffer_store_dword v252, v250, s[4:7], s33 offen sc1\nv_cmp_ne_u32 vcc, s30, v251\ns_nop 4\n"
"s_and_b32 vcc_lo, vcc_lo, s34\ns_cselect_b32 vcc_lo, 0, 1\ns_cmp_lg_u32 s31, 0\ns_cselect_b32 s31, 2, vcc_lo\n"
"s_cselect_b32 s33, 0x7f000000, s35\ns_sleep 8\ns_sub_u32 s84, s84, 1\ns_cbranch_scc1 L_hsr%=\n"
"s_cmp_lg_u32 s31, 2\ns_cbranch_scc1 L_hsl%=\ns_branch L_hsr%=\nL_varB%=:\n"
"ds_read_b64_tr_b16 a[64:65], v248 offset:0\nds_read_b64_tr_b16 a[66:67], v248 offset:512\nds_read_b128 a[128:131], v212 offset:0\nds_read_b64_tr_b16 a[68:69], v248 offset:4096\n"
"ds_read_b64_tr_b16 a[70:71], v248 offset:4608\nds_read_b128 a[144:147], v213 offset:0\nds_read_b64_tr_b16 a[72:73], v248 offset:8192\nds_read_b64_tr_b16 a[74:75], v248 offset:8704\n"
"ds_read_b128 a[160:163], v212 offset:8192\nds_read_b64_tr_b16 a[76:77], v248 offset:12288\nds_read_b64_tr_b16 a[78:79], v248 offset:12800\nds_read_b128 a[176:179], v213 offset:8192\n"
"ds_read_b64_tr_b16 a[80:81], v248 offset:16384\nds_read_b64_tr_b16 a[82:83], v248 offset:16896\ns_waitcnt lgkmcnt(11)\nv_mfma_f32_16x16x32_bf16 v[176:179], a[64:67], a[128:131], 0\n"
"s_add_u32 m0, s73, 0x8000\nds_read_b128 a[0:3], v212 offset:16384\nds_read_b64_tr_b16 a[84:85], v248 offset:20480\nds_read_b64_tr_b16 a[86:87], v248 offset:20992\n"
"v_pk_mul_f32 v[220:221], v[120:121], v[120:121]\ns_waitcnt lgkmcnt(11)\nv_mfma_f32_16x16x32_bf16 v[176:179], a[68:71], a[144:147], v[176:179]\ns_add_u32 s77, s77, 0x0\n"
"v_pk_mul_f32 v[222:223], v[122:123], v[122:123]\nv_pk_fma_f32 v[220:221], v[124:125], v[124:125], v[220:221]\nbuffer_load_dwordx4 v250, s[40:43], s77 offen lds\ns_waitcnt lgkmcnt(8)\n"
"v_mfma_f32_16x16x32_bf16 v[176:179], a[72:75], a[160:163], v[176:179]\ns_add_u32 m0, s73, 0x9000\nds_read_b128 a[16:19], v213 offset:16384\nds_read_b64_tr_b16 a[88:89], v248 offset:24576\n"
"ds_read_b64_tr_b16 a[90:91], v248 offset:25088\nv_pk_fma_f32 v[222:223], v[126:127], v[126:127], v[222:223]\ns_waitcnt lgkmcnt(8)\nv_mfma_f32_16x16x32_bf16 v[176:179], a[76:79], a[176:179], v[176:179]\n"
"s_add_u32 s77, s77, 0x1c000\nv_pk_fma_f32 v[220:221], v[128:129], v[128:129], v[220:221]\nv_pk_fma_f32 v[222:223], v[130:131], v[130:131], v[222:223]\nbuffer_load_dwordx4 v250, s[40:43], s77 offen lds\n"
"s_waitcnt lgkmcnt(5)\nv_mfma_f32_16x16x32_bf16 v[176:179], a[80:83], a[0:3], v[176:179]\ns_add_u32 m0, s73, 0xa000\nds_read_b128 a[32:35], v212 offset:24576\n"
"ds_read_b128 a[132:135], v212 offset:2048\nds_read_b128 a[148:151], v213 offset:2048\nv_pk_fma_f32 v[220:221], v[132:133], v[132:133], v[220:221]\ns_waitcnt lgkmcnt(5)\n"
"v_mfma_f32_16x16x32_bf16 v[176:179], a[84:87], a[16:19], v[176:179]\ns_add_u32 s77, s77, 0x1c000\nv_pk_fma_f32 v[222:223], v[134:135], v[134:135], v[222:223]\nv_pk_fma_f32 v[220:221], v[136:137], v[136:137], v[220:221]\n"
"buffer_load_dwordx4 v250, s[40:43], s77 offen lds\ns_waitcnt lgkmcnt(2)\nv_mfma_f32_16x16x32_bf16 v[176:179], a[88:91], a[32:35], v[176:179]\ns_add_u32 m0, s73, 0xb000\n"
"ds_read_b128 a[164:167], v212 offset:10240\nds_read_b128 a[180:183], v213 offset:10240\nds_read_b128 a[4:7], v212 offset:18432\nv_pk_fma_f32 v[222:223], v[138:139], v[138:139], v[222:223]\n"
"s_waitcnt lgkmcnt(4)\nv_mfma_f32_16x16x32_bf16 v[180:183], a[64:67], a[132:135], 0\ns_add_u32 s77, s77, 0x1c000\nv_pk_fma_f32 v[220:221], v[140:141], v[140:141], v[220:221]\n"
"v_pk_fma_f32 v[222:223], v[142:143], v[142:143], v[222:223]\nbuffer_load_dwordx4 v250, s[40:43], s77 offen lds\ns_waitcnt lgkmcnt(3)\nv_mfma_f32_16x16x32_bf16 v[180:183], a[68:71], a[148:151], v[180:183]\n"
"s_add_u32 m0, s73, 0xc000\nds_read_b128 a[20:23], v213 offset:18432\nds_read_b128 a[36:39], v212 offset:26624\nds_read_b128 a[136:139], v212 offset:4096\n"
"v_pk_fma_f32 v[220:221], v[144:145], v[144:145], v[220:221]\ns_waitcnt lgkmcnt(5)\nv_mfma_f32_16x16x32_bf16 v[180:183], a[72:75], a[164:167], v[180:183]\ns_add_u32 s77, s77, 0x1c000\n"
"v_pk_fma_f32 v[222:223], v[146:147], v[146:147], v[222:223]\nv_pk_fma_f32 v[220:221], v[148:149], v[148:149], v[220:221]\nbuffer_load_dwordx4 v250, s[40:43], s77 offen lds\ns_waitcnt lgkmcnt(4)\n"
"v_mfma_f32_16x16x32_bf16 v[180:183], a[76:79], a[180:183], v[180:183]\ns_add_u32 m0, s73, 0xd000\nds_read_b128 a[152:155], v213 offset:4096\nds_read_b128 a[168:171], v212 offset:12288\n"
"ds_read_b128 a[184:187], v213 offset:12288\nv_pk_fma_f32 v[222:223], v[150:151], v[150:151], v[222:223]\ns_waitcnt lgkmcnt(6)\nv_mfma_f32_16x16x32_bf16 v[180:183], a[80:83], a[4:7], v[180:183]\n"
"s_add_u32 s77, s77, 0x1c000\nv_pk_fma_f32 v[220:221], v[152:153], v[152:153], v[220:221]\nv_pk_fma_f32 v[222:223], v[154:155], v[154:155], v[222:223]\nbuffer_load_dwordx4 v250, s[40:43], s77 offen lds\n"
"s_waitcnt lgkmcnt(5)\nv_mfma_f32_16x16x32_bf16 v[180:183], a[84:87], a[20:23], v[180:183]\ns_add_u32 m0, s73, 0xe000\nds_read_b128 a[8:11], v212 offset:20480\n"
"ds_read_b128 a[24:27], v213 offset:20480\nds_read_b128 a[40:43], v212 offset:28672\nv_pk_fma_f32 v[220:221], v[156:157], v[156:157], v[220:221]\ns_waitcnt lgkmcnt(7)\n"
"v_mfma_f32_16x16x32_bf16 v[180:183], a[88:91], a[36:39], v[180:183]\ns_add_u32 s77, s77, 0x1c000\nv_pk_fma_f32 v[222:223], v[158:159], v[158:159], v[222:223]\nv_pk_fma_f32 v[220:221], v[160:161], v[160:161], v[220:221]\n"
"buffer_load_dwordx4 v250, s[40:43], s77 offen lds\nds_read_b128 a[140:143], v212 offset:6144\nds_read_b128 a[156:159], v213 offset:6144\nds_read_b128 a[172:175], v212 offset:14336\n"
"ds_read_b128 a[188:191], v213 offset:14336\nds_read_b128 a[12:15], v212 offset:22528\nds_read_b128 a[28:31], v213 offset:22528\nds_read_b128 a[44:47], v212 offset:30720\n"
"s_waitcnt vmcnt(9) lgkmcnt(0)\ns_barrier\nv_mfma_f32_16x16x32_bf16 v[184:187], a[64:67], a[136:139], 0\ns_add_u32 m0, s73, 0x18000\n"
"ds_read_b64_tr_b16 a[96:97], v249 offset:0\nds_read_b64_tr_b16 a[98:99], v249 offset:512\nds_read_b64_tr_b16 a[100:101], v249 offset:4096\nv_pk_fma_f32 v[222:223], v[162:163], v[162:163], v[222:223]\n"
"v_mfma_f32_16x16x32_bf16 v[184:187], a[68:71], a[152:155], v[184:187]\ns_add_u32 s77, s77, 0x1c000\nbuffer_load_dwordx4 v250, s[40:43], s77 offen lds\nv_mfma_f32_16x16x32_bf16 v[184:187], a[72:75], a[168:171], v[184:187]\n"
"s_add_u32 m0, s73, 0x19000\nds_read_b64_tr_b16 a[102:103], v249 offset:4608\nds_read_b64_tr_b16 a[104:105], v249 offset:8192\nds_read_b64_tr_b16 a[106:107], v249 offset:8704\n"
"v_pk_fma_f32 v[220:221], v[164:165], v[164:165], v[220:221]\nv_mfma_f32_16x16x32_bf16 v[184:187], a[76:79], a[184:187], v[184:187]\ns_add_u32 s77, s77, 0x1c000\nbuffer_load_dwordx4 v250, s[40:43], s77 offen lds\n"
"v_mfma_f32_16x16x32_bf16 v[184:187], a[80:83], a[8:11], v[184:187]\ns_add_u32 m0, s73, 0x1a000\nds_read_b64_tr_b16 a[108:109], v249 offset:12288\nds_read_b64_tr_b16 a[110:111], v249 offset:12800\n"
"ds_read_b64_tr_b16 a[112:113], v249 offset:16384\nv_pk_fma_f32 v[222:223], v[166:167], v[166:167], v[222:223]\nv_mfma_f32_16x16x32_bf16 v[184:187], a[84:87], a[24:27], v[184:187]\ns_add_u32 s77, s77, 0x1c000\n"
"buffer_load_dwordx4 v250, s[40:43], s77 offen lds\nv_mfma_f32_16x16x32_bf16 v[184:187], a[88:91], a[40:43], v[184:187]\ns_add_u32 m0, s73, 0x1b000\nds_read_b64_tr_b16 a[114:115], v249 offset:16896\n"
"ds_read_b64_tr_b16 a[116:117], v249 offset:20480\nds_read_b64_tr_b16 a[118:119], v249 offset:20992\nv_pk_fma_f32 v[220:221], v[168:169], v[168:169], v[220:221]\nv_mfma_f32_16x16x32_bf16 v[188:191], a[64:67], a[140:143], 0\n"
"s_add_u32 s77, s77, 0x1c000\nbuffer_load_dwordx4 v250, s[40:43], s77 offen lds\nv_mfma_f32_16x16x32_bf16 v[188:191], a[68:71], a[156:159], v[188:191]\ns_add_u32 m0, s73, 0x1c000\n"
"ds_read_b64_tr_b16 a[120:121], v249 offset:24576\nds_read_b64_tr_b16 a[122:123], v249 offset:25088\nds_read_b128 a[192:195], v213 offset:24576\nv_pk_fma_f32 v[222:223], v[170:171], v[170:171], v[222:223]\n"
"v_mfma_f32_16x16x32_bf16 v[188:191], a[72:75], a[172:175], v[188:191]\ns_add_u32 s77, s77, 0x1c000\nbuffer_load_dwordx4 v250, s[40:43], s77 offen lds\nv_mfma_f32_16x16x32_bf16 v[188:191], a[76:79], a[188:191], v[188:191]\n"
"s_add_u32 m0, s73, 0x1d000\nds_read_b128 a[208:211], v212 offset:32768\nds_read_b128 a[224:227], v213 offset:32768\nds_read_b128 a[240:243], v212 offset:40960\n"
"v_pk_fma_f32 v[220:221], v[172:173], v[172:173], v[220:221]\nv_mfma_f32_16x16x32_bf16 v[188:191], a[80:83], a[12:15], v[188:191]\ns_add_u32 s77, s77, 0x1c000\nbuffer_load_dwordx4 v250, s[40:43], s77 offen lds\n"
"v_mfma_f32_16x16x32_bf16 v[188:191], a[84:87], a[28:31], v[188:191]\ns_add_u32 m0, s73, 0x1e000\nds_read_b128 v[0:3], v213 offset:40960\nds_read_b128 v[16:19], v212 offset:49152\n"
"ds_read_b128 v[32:35], v213 offset:49152\nv_pk_fma_f32 v[222:223], v[174:175], v[174:175], v[222:223]\nv_mfma_f32_16x16x32_bf16 v[188:191], a[88:91], a[44:47], v[188:191]\ns_add_u32 s77, s77, 0x1c000\n"
"buffer_load_dwordx4 v250, s[40:43], s77 offen lds\nv_sub_f32 v198, v193, v218\nv_mul_f32 v198, v198, v198\nv_pk_add_f32 v[220:221], v[220:221], v[222:223]\n"
"v_add_f32 v196, v220, v221\ns_nop 1\nv_add_f32_dpp v196, v196, v196 quad_perm:[1,0,3,2] row_mask:0xf bank_mask:0xf\ns_nop 1\n"
"v_add_f32_dpp v196, v196, v196 quad_perm:[2,3,0,1] row_mask:0xf bank_mask:0xf\ns_nop 1\nv_add_f32_dpp v196, v196, v196 row_half_mirror row_mask:0xf bank_mask:0xf\nv_mul_f32 v196, 0x3b124925, v196\n"
"v_sub_f32 v196, v196, v198\nv_max_f32 v196, 0, v196\nv_cvt_pk_bf16_f32 v196, v196, v196\nv_lshlrev_b32 v196, 16, v196\n"
"v_add_f32 v196, 0x3727c5ac, v196\nv_cvt_pk_bf16_f32 v196, v196, v196\nv_lshlrev_b32 v196, 16, v196\nv_sqrt_f32 v196, v196\n"
"s_nop 0\nv_cvt_pk_bf16_f32 v196, v196, v196\nv_lshlrev_b32 v196, 16, v196\nv_rcp_f32 v196, v196\n"
"s_nop 0\nds_write_b32 v216, v196 offset:32\ns_waitcnt vmcnt(7) lgkmcnt(0)\ns_barrier\n"
"v_mfma_f32_16x16x32_bf16 v[176:179], a[96:99], a[192:195], v[176:179]\ns_add_u32 m0, s73, 0x0\nds_read_b128 a[196:199], v213 offset:26624\nds_read_b128 a[212:215], v212 offset:34816\n"
"ds_read_b128 a[228:231], v213 offset:34816\nv_mfma_f32_16x16x32_bf16 v[176:179], a[100:103], a[208:211], v[176:179]\ns_add_u32 s78, s78, 0x0\nbuffer_load_dwordx4 v251, s[44:47], s78 offen lds\n"
"v_mfma_f32_16x16x32_bf16 v[176:179], a[104:107], a[224:227], v[176:179]\ns_add_u32 m0, s73, 0x1000\nds_read_b128 a[244:247], v212 offset:43008\nds_read_b128 v[4:7], v213 offset:43008\n"
"ds_read_b128 v[20:23], v212 offset:51200\nv_mfma_f32_16x16x32_bf16 v[176:179], a[108:111], a[240:243], v[176:179]\ns_add_u32 s78, s78, 0x40000\nbuffer_load_dwordx4 v251, s[44:47], s78 offen lds\n"
"v_mfma_f32_16x16x32_bf16 v[176:179], a[112:115], v[0:3], v[176:179]\ns_add_u32 m0, s73, 0x2000\nds_read_b128 v[36:39], v213 offset:51200\nds_read_b128 a[200:203], v213 offset:28672\n"
"ds_read_b128 a[216:219], v212 offset:36864\nv_mfma_f32_16x16x32_bf16 v[176:179], a[116:119], v[16:19], v[176:179]\ns_add_u32 s78, s78, 0x40000\nbuffer_load_dwordx4 v251, s[44:47], s78 offen lds\n"
"v_mfma_f32_16x16x32_bf16 v[176:179], a[120:123], v[32:35], v[176:179]\ns_add_u32 m0, s73, 0x3000\nds_read_b128 a[232:235], v213 offset:36864\nds_read_b128 a[248:251], v212 offset:45056\n"
"ds_read_b128 v[8:11], v213 offset:45056\ns_waitcnt lgkmcnt(11)\nv_mfma_f32_16x16x32_bf16 v[180:183], a[96:99], a[196:199], v[180:183]\ns_add_u32 s78, s78, 0x40000\n"
"buffer_load_dwordx4 v251, s[44:47], s78 offen lds\ns_waitcnt lgkmcnt(9)\nv_mfma_f32_16x16x32_bf16 v[180:183], a[100:103], a[212:215], v[180:183]\ns_add_u32 m0, s73, 0x4000\n"
"ds_read_b128 v[24:27], v212 offset:53248\nds_read_b128 v[40:43], v213 offset:53248\nds_read_b128 a[204:207], v213 offset:30720\nv_mfma_f32_16x16x32_bf16 v[180:183], a[104:107], a[228:231], v[180:183]\n"
"s_add_u32 s78, s78, 0x40000\nbuffer_load_dwordx4 v251, s[44:47], s78 offen lds\ns_waitcnt lgkmcnt(9)\nv_mfma_f32_16x16x32_bf16 v[180:183], a[108:111], a[244:247], v[180:183]\n"
"s_add_u32 m0, s73, 0x5000\nds_read_b128 a[220:223], v212 offset:38912\nds_read_b128 a[236:239], v213 offset:38912\nds_read_b128 a[252:255], v212 offset:47104\n"
"v_mfma_f32_16x16x32_bf16 v[180:183], a[112:115], v[4:7], v[180:183]\ns_add_u32 s78, s78, 0x40000\nbuffer_load_dwordx4 v251, s[44:47], s78 offen lds\nv_mfma_f32_16x16x32_bf16 v[180:183], a[116:119], v[20:23], v[180:183]\n"
"s_add_u32 m0, s73, 0x6000\nds_read_b128 v[12:15], v213 offset:47104\nds_read_b128 v[28:31], v212 offset:55296\nds_read_b128 v[44:47], v213 offset:55296\n"
"s_waitcnt lgkmcnt(12)\nv_mfma_f32_16x16x32_bf16 v[180:183], a[120:123], v[36:39], v[180:183]\ns_add_u32 s78, s78, 0x40000\nbuffer_load_dwordx4 v251, s[44:47], s78 offen lds\n"
"v_mfma_f32_16x16x32_bf16 v[184:187], a[96:99], a[200:203], v[184:187]\ns_add_u32 m0, s73, 0x10000\nds_read_b64_tr_b16 a[64:65], v248 offset:32768\nds_read_b64_tr_b16 a[66:67], v248 offset:33280\n"
"ds_read_b64_tr_b16 a[68:69], v248 offset:36864\nv_mfma_f32_16x16x32_bf16 v[184:187], a[100:103], a[216:219], v[184:187]\ns_add_u32 s78, s78, 0x40000\nbuffer_load_dwordx4 v251, s[44:47], s78 offen lds\n"
"s_waitcnt lgkmcnt(9)\nv_mfma_f32_16x16x32_bf16 v[184:187], a[104:107], a[232:235], v[184:187]\ns_add_u32 m0, s73, 0x11000\nds_read_b64_tr_b16 a[70:71], v248 offset:37376\n"
"ds_read_b64_tr_b16 a[72:73], v248 offset:40960\nds_read_b64_tr_b16 a[74:75], v248 offset:41472\nv_mfma_f32_16x16x32_bf16 v[184:187], a[108:111], a[248:251], v[184:187]\ns_add_u32 s78, s78, 0x40000\n"
"buffer_load_dwordx4 v251, s[44:47], s78 offen lds\nv_mfma_f32_16x16x32_bf16 v[184:187], a[112:115], v[8:11], v[184:187]\ns_add_u32 m0, s73, 0x12000\nds_read_b64_tr_b16 a[76:77], v248 offset:45056\n"
"ds_read_b64_tr_b16 a[78:79], v248 offset:45568\nds_read_b64_tr_b16 a[80:81], v248 offset:49152\nv_mfma_f32_16x16x32_bf16 v[184:187], a[116:119], v[24:27], v[184:187]\ns_add_u32 s78, s78, 0x40000\n"
"buffer_load_dwordx4 v251, s[44:47], s78 offen lds\nv_mfma_f32_16x16x32_bf16 v[184:187], a[120:123], v[40:43], v[184:187]\ns_add_u32 m0, s73, 0x13000\nds_read_b64_tr_b16 a[82:83], v248 offset:49664\n"
"ds_read_b64_tr_b16 a[84:85], v248 offset:53248\nds_read_b64_tr_b16 a[86:87], v248 offset:53760\nv_mfma_f32_16x16x32_bf16 v[188:191], a[96:99], a[204:207], v[188:191]\ns_add_u32 s78, s78, 0x40000\n"
"buffer_load_dwordx4 v251, s[44:47], s78 offen lds\ns_waitcnt lgkmcnt(9)\nv_mfma_f32_16x16x32_bf16 v[188:191], a[100:103], a[220:223], v[188:191]\ns_add_u32 m0, s73, 0x14000\n"
"ds_read_b64_tr_b16 a[88:89], v248 offset:57344\nds_read_b64_tr_b16 a[90:91], v248 offset:57856\nds_read_b32 v144, v217 offset:28672\nv_mfma_f32_16x16x32_bf16 v[188:191], a[104:107], a[236:239], v[188:191]\n"
"s_add_u32 s78, s78, 0x40000\nbuffer_load_dwordx4 v251, s[44:47], s78 offen lds\nv_mfma_f32_16x16x32_bf16 v[188:191], a[108:111], a[252:255], v[188:191]\ns_add_u32 m0, s73, 0x15000\n"
"ds_read_b32 v145, v217 offset:28736\nds_read_b32 v146, v217 offset:28800\nds_read_b32 v147, v217 offset:28864\nv_mov_b32 v106, 0\n"
"v_mfma_f32_16x16x32_bf16 v[188:191], a[112:115], v[12:15], v[188:191]\ns_add_u32 s78, s78, 0x40000\nbuffer_load_dwordx4 v251, s[44:47], s78 offen lds\nv_mfma_f32_16x16x32_bf16 v[188:191], a[116:119], v[28:31], v[188:191]\n"
"s_add_u32 m0, s73, 0x16000\nds_read_b32 v106, v106 offset:28928\nv_mul_f32 v128, v176, v176\nv_mfma_f32_16x16x32_bf16 v[188:191], a[120:123], v[44:47], v[188:191]\n"
"s_add_u32 s78, s78, 0x40000\nbuffer_load_dwordx4 v251, s[44:47], s78 offen lds\ns_waitcnt vmcnt(14) lgkmcnt(0)\ns_barrier\n"
"v_mfma_f32_16x16x32_bf16 v[160:163], a[64:67], a[128:131], 0\ns_add_u32 s78, s78, 0xffcc0200\nds_read_b64_tr_b16 a[96:97], v249 offset:32768\nv_mul_f32 v129, v177, v177\n"
"v_mfma_f32_16x16x32_bf16 v[160:163], a[68:71], a[144:147], v[160:163]\ns_add_u32 m0, s73, 0x8000\nds_read_b64_tr_b16 a[98:99], v249 offset:33280\nv_mul_f32 v130, v178, v178\n"
"v_mfma_f32_16x16x32_bf16 v[160:163], a[72:75], a[160:163], v[160:163]\nbuffer_load_dwordx4 v251, s[44:47], s78 offen lds\nv_mfma_f32_16x16x32_bf16 v[160:163], a[76:79], a[176:179], v[160:163]\ns_add_u32 s78, s78, 0x40000\n"
"ds_read_b64_tr_b16 a[100:101], v249 offset:36864\nv_mul_f32 v152, 0xc0135761, v144\nv_mfma_f32_16x16x32_bf16 v[160:163], a[80:83], a[0:3], v[160:163]\ns_add_u32 m0, s73, 0x9000\n"
"ds_read_b64_tr_b16 a[102:103], v249 offset:37376\nv_mul_f32 v156, v144, v144\nv_mfma_f32_16x16x32_bf16 v[160:163], a[84:87], a[16:19], v[160:163]\nbuffer_load_dwordx4 v251, s[44:47], s78 offen lds\n"
"v_mfma_f32_16x16x32_bf16 v[160:163], a[88:91], a[32:35], v[160:163]\ns_lshr_b32 s87, s74, 2\nds_read_b64_tr_b16 a[104:105], v249 offset:40960\nv_mul_f32 v148, v152, v156\n"
"v_mfma_f32_16x16x32_bf16 v[164:167], a[88:91], a[36:39], 0\ns_add_u32 s78, s78, 0x40000\nds_read_b64_tr_b16 a[106:107], v249 offset:41472\nv_mul_f32 v153, 0xc0135761, v145\n"
"v_mfma_f32_16x16x32_bf16 v[164:167], a[84:87], a[20:23], v[164:167]\ns_add_u32 m0, s73, 0xa000\nds_read_b64_tr_b16 a[108:109], v249 offset:45056\nv_mul_f32 v157, v145, v145\n"
"v_mfma_f32_16x16x32_bf16 v[164:167], a[80:83], a[4:7], v[164:167]\nbuffer_load_dwordx4 v251, s[44:47], s78 offen lds\nv_mfma_f32_16x16x32_bf16 v[164:167], a[76:79], a[180:183], v[164:167]\nds_read_b64_tr_b16 a[110:111], v249 offset:45568\n"
"v_mul_f32 v154, 0xc0135761, v146\nv_mfma_f32_16x16x32_bf16 v[164:167], a[72:75], a[164:167], v[164:167]\ns_add_u32 s78, s78, 0x40000\nds_read_b64_tr_b16 a[112:113], v249 offset:49152\n"
"v_mul_f32 v148, 0x3d372713, v148\nv_mfma_f32_16x16x32_bf16 v[164:167], a[68:71], a[148:151], v[164:167]\ns_add_u32 m0, s73, 0xb000\nds_read_b64_tr_b16 a[114:115], v249 offset:49664\n"
"v_mul_f32 v158, v146, v146\nv_mfma_f32_16x16x32_bf16 v[164:167], a[64:67], a[132:135], v[164:167]\nbuffer_load_dwordx4 v251, s[44:47], s78 offen lds\nv_mfma_f32_16x16x32_bf16 v[168:171], a[64:67], a[136:139], 0\n"
"s_add_u32 s78, s78, 0x40000\nds_read_b64_tr_b16 a[116:117], v249 offset:53248\nv_mul_f32 v155, 0xc0135761, v147\nv_mfma_f32_16x16x32_bf16 v[168:171], a[68:71], a[152:155], v[168:171]\n"
"s_add_u32 m0, s73, 0xc000\nds_read_b64_tr_b16 a[118:119], v249 offset:53760\nv_mul_f32 v159, v147, v147\nv_mfma_f32_16x16x32_bf16 v[168:171], a[72:75], a[168:171], v[168:171]\n"
"buffer_load_dwordx4 v251, s[44:47], s78 offen lds\nv_mfma_f32_16x16x32_bf16 v[168:171], a[76:79], a[184:187], v[168:171]\nds_read_b64_tr_b16 a[120:121], v249 offset:57344\nv_mul_f32 v149, v153, v157\n"
"v_mfma_f32_16x16x32_bf16 v[168:171], a[80:83], a[8:11], v[168:171]\ns_add_u32 s78, s78, 0x40000\nds_read_b64_tr_b16 a[122:123], v249 offset:57856\nv_mul_f32 v131, v179, v179\n"
"v_mfma_f32_16x16x32_bf16 v[168:171], a[84:87], a[24:27], v[168:171]\ns_add_u32 m0, s73, 0xd000\nv_mul_f32 v150, v154, v158\nv_mul_f32 v151, v155, v159\n"
"v_mfma_f32_16x16x32_bf16 v[168:171], a[88:91], a[40:43], v[168:171]\nbuffer_load_dwordx4 v251, s[44:47], s78 offen lds\nv_mfma_f32_16x16x32_bf16 v[172:175], a[88:91], a[44:47], 0\nv_fma_f32 v128, v128, v148, v152\n"
"v_fma_f32 v129, v129, v148, v152\nv_mfma_f32_16x16x32_bf16 v[172:175], a[84:87], a[28:31], v[172:175]\ns_add_u32 s78, s78, 0x40000\nv_fma_f32 v130, v130, v148, v152\n"
"v_mul_f32 v149, 0x3d372713, v149\nv_mfma_f32_16x16x32_bf16 v[172:175], a[80:83], a[12:15], v[172:175]\ns_add_u32 m0, s73, 0xe000\nv_fma_f32 v131, v131, v148, v152\n"
"v_mul_f32 v132, v180, v180\nv_mfma_f32_16x16x32_bf16 v[172:175], a[76:79], a[188:191], v[172:175]\nbuffer_load_dwordx4 v251, s[44:47], s78 offen lds\nv_mfma_f32_16x16x32_bf16 v[172:175], a[72:75], a[172:175], v[172:175]\n"
"v_mul_f32 v133, v181, v181\nv_mul_f32 v134, v182, v182\nv_mfma_f32_16x16x32_bf16 v[172:175], a[68:71], a[156:159], v[172:175]\nv_mul_f32 v135, v183, v183\n"
"v_mul_f32 v150, 0x3d372713, v150\nv_mfma_f32_16x16x32_bf16 v[172:175], a[64:67], a[140:143], v[172:175]\nv_mul_f32 v136, v184, v184\nv_mul_f32 v137, v185, v185\n"
"s_waitcnt vmcnt(14) lgkmcnt(0)\ns_barrier\nv_mfma_f32_16x16x32_bf16 v[160:163], a[96:99], a[192:195], v[160:163]\ns_add_u32 s78, s78, 0x40000\n"
"ds_read_b64_tr_b16 a[64:65], v248 offset:0\ns_cmp_lt_u32 s75, 28\ns_cbranch_scc1 L_kvB%=\nbuffer_load_dword a127, v247, s[92:95], s35 offen\ns_add_u32 s35, s35, 0x10000\nbuffer_load_dword a127, v247, s[92:95], s35 offen\ns_add_u32 s35, s35, 0x10000\nbuffer_load_dword a127, v247, s[92:95], s35 offen\ns_add_u32 s35, s35, 0x10000\nL_kvB%=:\nv_mfma_f32_16x16x32_bf16 v[160:163], a[100:103], a[208:211], v[160:163]\ns_add_u32 m0, s73, 0x18000\n"
"ds_read_b64_tr_b16 a[66:67], v248 offset:512\nv_mul_f32 v138, v186, v186\nv_mfma_f32_16x16x32_bf16 v[160:163], a[104:107], a[224:227], v[160:163]\nbuffer_load_dwordx4 v251, s[44:47], s78 offen lds\n"
"v_mfma_f32_16x16x32_bf16 v[160:163], a[108:111], a[240:243], v[160:163]\ns_add_u32 s78, s78, 0x40000\nds_read_b64_tr_b16 a[68:69], v248 offset:4096\nv_mul_f32 v151, 0x3d372713, v151\n"
"v_mfma_f32_16x16x32_bf16 v[160:163], a[112:115], v[0:3], v[160:163]\ns_add_u32 m0, s73, 0x19000\nds_read_b64_tr_b16 a[70:71], v248 offset:4608\nv_mul_f32 v139, v187, v187\n"
"v_mfma_f32_16x16x32_bf16 v[160:163], a[116:119], v[16:19], v[160:163]\nbuffer_load_dwordx4 v251, s[44:47], s78 offen lds\nv_mfma_f32_16x16x32_bf16 v[160:163], a[120:123], v[32:35], v[160:163]\nds_read_b64_tr_b16 a[72:73], v248 offset:8192\n"
"v_mul_f32 v140, v188, v188\nv_mfma_f32_16x16x32_bf16 v[164:167], a[120:123], v[36:39], v[164:167]\ns_add_u32 s78, s78, 0x40000\nds_read_b64_tr_b16 a[74:75], v248 offset:8704\n"
"v_mul_f32 v141, v189, v189\nv_mfma_f32_16x16x32_bf16 v[164:167], a[116:119], v[20:23], v[164:167]\ns_add_u32 m0, s73, 0x1a000\nds_read_b64_tr_b16 a[76:77], v248 offset:12288\n"
"v_mul_f32 v156, 0xbfb8aa3b, v156\nv_mfma_f32_16x16x32_bf16 v[164:167], a[112:115], v[4:7], v[164:167]\nbuffer_load_dwordx4 v251, s[44:47], s78 offen lds\nv_mfma_f32_16x16x32_bf16 v[164:167], a[108:111], a[244:247], v[164:167]\n"
"ds_read_b64_tr_b16 a[78:79], v248 offset:12800\nv_mul_f32 v142, v190, v190\nv_mfma_f32_16x16x32_bf16 v[164:167], a[104:107], a[228:231], v[164:167]\ns_add_u32 s78, s78, 0x40000\n"
"ds_read_b64_tr_b16 a[80:81], v248 offset:16384\nv_mul_f32 v128, v128, v176\nv_mfma_f32_16x16x32_bf16 v[164:167], a[100:103], a[212:215], v[164:167]\ns_add_u32 m0, s73, 0x1b000\n"
"ds_read_b64_tr_b16 a[82:83], v248 offset:16896\nv_mul_f32 v129, v129, v177\nv_mfma_f32_16x16x32_bf16 v[164:167], a[96:99], a[196:199], v[164:167]\nbuffer_load_dwordx4 v251, s[44:47], s78 offen lds\n"
"v_mfma_f32_16x16x32_bf16 v[168:171], a[96:99], a[200:203], v[168:171]\ns_add_u32 s78, s78, 0x40000\nds_read_b64_tr_b16 a[84:85], v248 offset:20480\nv_mul_f32 v130, v130, v178\n"
"v_mfma_f32_16x16x32_bf16 v[168:171], a[100:103], a[216:219], v[168:171]\ns_add_u32 m0, s73, 0x1c000\nds_read_b64_tr_b16 a[86:87], v248 offset:20992\nv_mul_f32 v131, v131, v179\n"
"v_mfma_f32_16x16x32_bf16 v[168:171], a[104:107], a[232:235], v[168:171]\nbuffer_load_dwordx4 v251, s[44:47], s78 offen lds\nv_mfma_f32_16x16x32_bf16 v[168:171], a[108:111], a[248:251], v[168:171]\nds_read_b64_tr_b16 a[88:89], v248 offset:24576\n"
"v_fma_f32 v132, v132, v149, v153\nv_mfma_f32_16x16x32_bf16 v[168:171], a[112:115], v[8:11], v[168:171]\ns_add_u32 s78, s78, 0x40000\nds_read_b64_tr_b16 a[90:91], v248 offset:25088\n"
"v_mul_f32 v143, v191, v191\nv_mfma_f32_16x16x32_bf16 v[168:171], a[116:119], v[24:27], v[168:171]\ns_add_u32 m0, s73, 0x1d000\nv_fma_f32 v133, v133, v149, v153\n"
"v_fma_f32 v134, v134, v149, v153\nv_mfma_f32_16x16x32_bf16 v[168:171], a[120:123], v[40:43], v[168:171]\nbuffer_load_dwordx4 v251, s[44:47], s78 offen lds\nv_mfma_f32_16x16x32_bf16 v[172:175], a[120:123], v[44:47], v[172:175]\n"
"v_fma_f32 v135, v135, v149, v153\nv_fma_f32 v136, v136, v150, v154\nv_mfma_f32_16x16x32_bf16 v[172:175], a[116:119], v[28:31], v[172:175]\ns_add_u32 s78, s78, 0x40000\n"
"v_fma_f32 v137, v137, v150, v154\nv_fma_f32 v138, v138, v150, v154\nv_mfma_f32_16x16x32_bf16 v[172:175], a[112:115], v[12:15], v[172:175]\ns_add_u32 m0, s73, 0x1e000\n"
"v_fma_f32 v139, v139, v150, v154\nv_fma_f32 v140, v140, v151, v155\nv_mfma_f32_16x16x32_bf16 v[172:175], a[108:111], a[252:255], v[172:175]\nbuffer_load_dwordx4 v251, s[44:47], s78 offen lds\n"
"v_mfma_f32_16x16x32_bf16 v[172:175], a[104:107], a[236:239], v[172:175]\nv_fma_f32 v141, v141, v151, v155\nv_fma_f32 v142, v142, v151, v155\nv_mfma_f32_16x16x32_bf16 v[172:175], a[100:103], a[220:223], v[172:175]\n"
"v_rcp_f32 v156, v156\nv_mfma_f32_16x16x32_bf16 v[172:175], a[96:99], a[204:207], v[172:175]\nv_exp_f32 v128, v128\ns_waitcnt vmcnt(14) lgkmcnt(0)\n"
"s_barrier\nv_mfma_f32_16x16x32_bf16 v[192:195], a[64:67], a[128:131], 0\ns_add_u32 s76, s76, 0xffe94080\nds_read_b64_tr_b16 a[96:97], v249 offset:0\n"
"v_mul_f32 v157, 0xbfb8aa3b, v157\nv_mfma_f32_16x16x32_bf16 v[192:195], a[68:71], a[144:147], v[192:195]\ns_add_u32 m0, s73, 0x0\nds_read_b64_tr_b16 a[98:99], v249 offset:512\n"
"v_fma_f32 v143, v143, v151, v155\nv_mfma_f32_16x16x32_bf16 v[192:195], a[72:75], a[160:163], v[192:195]\nbuffer_load_dwordx4 v250, s[36:39], s76 offen lds\nv_mfma_f32_16x16x32_bf16 v[192:195], a[76:79], a[176:179], v[192:195]\n"
"s_add_u32 s76, s76, 0x1c000\nds_read_b64_tr_b16 a[100:101], v249 offset:4096\nv_mul_f32 v132, v132, v180\nv_mfma_f32_16x16x32_bf16 v[192:195], a[80:83], a[0:3], v[192:195]\n"
"s_add_u32 m0, s73, 0x1000\nds_read_b64_tr_b16 a[102:103], v249 offset:4608\nv_mul_f32 v133, v133, v181\nv_mfma_f32_16x16x32_bf16 v[192:195], a[84:87], a[16:19], v[192:195]\n"
"buffer_load_dwordx4 v250, s[36:39], s76 offen lds\nv_mfma_f32_16x16x32_bf16 v[192:195], a[88:91], a[32:35], v[192:195]\nds_read_b64_tr_b16 a[104:105], v249 offset:8192\nv_mul_f32 v134, v134, v182\n"
"v_mfma_f32_16x16x32_bf16 v[196:199], a[88:91], a[36:39], 0\ns_add_u32 s76, s76, 0x1c000\nds_read_b64_tr_b16 a[106:107], v249 offset:8704\nv_mul_f32 v135, v135, v183\n"
"v_mfma_f32_16x16x32_bf16 v[196:199], a[84:87], a[20:23], v[196:199]\ns_add_u32 m0, s73, 0x2000\nds_read_b64_tr_b16 a[108:109], v249 offset:12288\nv_mul_f32 v158, 0xbfb8aa3b, v158\n"
"v_mfma_f32_16x16x32_bf16 v[196:199], a[80:83], a[4:7], v[196:199]\nbuffer_load_dwordx4 v250, s[36:39], s76 offen lds\nv_mfma_f32_16x16x32_bf16 v[196:199], a[76:79], a[180:183], v[196:199]\nds_read_b64_tr_b16 a[110:111], v249 offset:12800\n"
"v_mul_f32 v136, v136, v184\nv_mfma_f32_16x16x32_bf16 v[196:199], a[72:75], a[164:167], v[196:199]\ns_add_u32 s76, s76, 0x1c000\nds_read_b64_tr_b16 a[112:113], v249 offset:16384\n"
"v_mul_f32 v137, v137, v185\nv_mfma_f32_16x16x32_bf16 v[196:199], a[68:71], a[148:151], v[196:199]\ns_add_u32 m0, s73, 0x3000\nds_read_b64_tr_b16 a[114:115], v249 offset:16896\n"
"v_mul_f32 v138, v138, v186\nv_mfma_f32_16x16x32_bf16 v[196:199], a[64:67], a[132:135], v[196:199]\nbuffer_load_dwordx4 v250, s[36:39], s76 offen lds\nv_mfma_f32_16x16x32_bf16 v[200:203], a[64:67], a[136:139], 0\n"
"s_add_u32 s76, s76, 0x1c000\nds_read_b64_tr_b16 a[116:117], v249 offset:20480\nv_mul_f32 v159, 0xbfb8aa3b, v159\nv_mfma_f32_16x16x32_bf16 v[200:203], a[68:71], a[152:155], v[200:203]\n"
"s_add_u32 m0, s73, 0x4000\nds_read_b64_tr_b16 a[118:119], v249 offset:20992\nv_mul_f32 v139, v139, v187\nv_mfma_f32_16x16x32_bf16 v[200:203], a[72:75], a[168:171], v[200:203]\n"
"buffer_load_dwordx4 v250, s[36:39], s76 offen lds\nv_mfma_f32_16x16x32_bf16 v[200:203], a[76:79], a[184:187], v[200:203]\nds_read_b64_tr_b16 a[120:121], v249 offset:24576\nv_mul_f32 v140, v140, v188\n"
"v_mfma_f32_16x16x32_bf16 v[200:203], a[80:83], a[8:11], v[200:203]\ns_add_u32 s76, s76, 0x1c000\nds_read_b64_tr_b16 a[122:123], v249 offset:25088\nv_mul_f32 v141, v141, v189\n"
"v_mfma_f32_16x16x32_bf16 v[200:203], a[84:87], a[24:27], v[200:203]\ns_add_u32 m0, s73, 0x5000\nv_exp_f32 v129, v129\nv_mfma_f32_16x16x32_bf16 v[200:203], a[88:91], a[40:43], v[200:203]\n"
"buffer_load_dwordx4 v250, s[36:39], s76 offen lds\nv_mfma_f32_16x16x32_bf16 v[204:207], a[88:91], a[44:47], 0\nv_exp_f32 v130, v130\nv_mfma_f32_16x16x32_bf16 v[204:207], a[84:87], a[28:31], v[204:207]\n"
"s_add_u32 s76, s76, 0x1c000\nv_exp_f32 v131, v131\nv_mfma_f32_16x16x32_bf16 v[204:207], a[80:83], a[12:15], v[204:207]\ns_add_u32 m0, s73, 0x6000\n"
"v_mul_f32 v142, v142, v190\nv_mul_f32 v143, v143, v191\nv_mfma_f32_16x16x32_bf16 v[204:207], a[76:79], a[188:191], v[204:207]\nbuffer_load_dwordx4 v250, s[36:39], s76 offen lds\n"
"v_mfma_f32_16x16x32_bf16 v[204:207], a[72:75], a[172:175], v[204:207]\nv_fma_f32 v128, v128, v156, v156\nv_fma_f32 v129, v129, v156, v156\nv_mfma_f32_16x16x32_bf16 v[204:207], a[68:71], a[156:159], v[204:207]\n"
"v_fma_f32 v130, v130, v156, v156\nv_fma_f32 v131, v131, v156, v156\nv_mfma_f32_16x16x32_bf16 v[204:207], a[64:67], a[140:143], v[204:207]\nv_rcp_f32 v157, v157\n"
"s_waitcnt vmcnt(14) lgkmcnt(0)\ns_barrier\nv_mfma_f32_16x16x32_bf16 v[192:195], a[96:99], a[192:195], v[192:195]\ns_add_u32 s76, s76, 0x1c000\n"
"ds_read_b64_tr_b16 a[64:65], v248 offset:32768\nv_mul_f32 v236, 0x3db8aa3b, v144\nv_mfma_f32_16x16x32_bf16 v[192:195], a[100:103], a[208:211], v[192:195]\ns_add_u32 m0, s73, 0x10000\n"
"ds_read_b64_tr_b16 a[66:67], v248 offset:33280\nv_mul_f32 v237, 0x3db8aa3b, v145\nv_mfma_f32_16x16x32_bf16 v[192:195], a[104:107], a[224:227], v[192:195]\nbuffer_load_dwordx4 v250, s[36:39], s76 offen lds\n"
"v_mfma_f32_16x16x32_bf16 v[192:195], a[108:111], a[240:243], v[192:195]\ns_add_u32 s76, s76, 0x1c000\nds_read_b64_tr_b16 a[68:69], v248 offset:36864\nv_mul_f32 v238, 0x3db8aa3b, v146\n"
"v_mfma_f32_16x16x32_bf16 v[192:195], a[112:115], v[0:3], v[192:195]\ns_add_u32 m0, s73, 0x11000\nds_read_b64_tr_b16 a[70:71], v248 offset:37376\nv_mul_f32 v239, 0x3db8aa3b, v147\n"
"v_mfma_f32_16x16x32_bf16 v[192:195], a[116:119], v[16:19], v[192:195]\nbuffer_load_dwordx4 v250, s[36:39], s76 offen lds\nv_mfma_f32_16x16x32_bf16 v[192:195], a[120:123], v[32:35], v[192:195]\nds_read_b64_tr_b16 a[72:73], v248 offset:40960\n"
"v_subrev_u32 v104, 0x18000, v252\nv_mfma_f32_16x16x32_bf16 v[196:199], a[120:123], v[36:39], v[196:199]\ns_add_u32 s76, s76, 0x1c000\nds_read_b64_tr_b16 a[74:75], v248 offset:41472\n"
"v_subrev_u32 v105, 0x18000, v253\nv_mfma_f32_16x16x32_bf16 v[196:199], a[116:119], v[20:23], v[196:199]\ns_add_u32 m0, s73, 0x12000\nds_read_b64_tr_b16 a[76:77], v248 offset:45056\n"
"v_lshlrev_b32 v110, 3, v254\nv_mfma_f32_16x16x32_bf16 v[196:199], a[112:115], v[4:7], v[196:199]\nbuffer_load_dwordx4 v250, s[36:39], s76 offen lds\nv_mfma_f32_16x16x32_bf16 v[196:199], a[108:111], a[244:247], v[196:199]\n"
"ds_read_b64_tr_b16 a[78:79], v248 offset:45568\nv_mul_f32 v192, v192, v236\nv_mfma_f32_16x16x32_bf16 v[196:199], a[104:107], a[228:231], v[196:199]\ns_add_u32 s76, s76, 0x1c000\n"
"ds_read_b64_tr_b16 a[80:81], v248 offset:49152\nv_mul_f32 v193, v193, v236\nv_mfma_f32_16x16x32_bf16 v[196:199], a[100:103], a[212:215], v[196:199]\ns_add_u32 m0, s73, 0x13000\n"
"ds_read_b64_tr_b16 a[82:83], v248 offset:49664\nv_mul_f32 v194, v194, v236\nv_mfma_f32_16x16x32_bf16 v[196:199], a[96:99], a[196:199], v[196:199]\nbuffer_load_dwordx4 v250, s[36:39], s76 offen lds\n"
"v_mfma_f32_16x16x32_bf16 v[200:203], a[96:99], a[200:203], v[200:203]\ns_add_u32 s76, s76, 0x1c000\nds_read_b64_tr_b16 a[84:85], v248 offset:53248\nv_mul_f32 v195, v195, v236\n"
"v_mfma_f32_16x16x32_bf16 v[200:203], a[100:103], a[216:219], v[200:203]\ns_add_u32 m0, s73, 0x14000\nds_read_b64_tr_b16 a[86:87], v248 offset:53760\nv_cvt_pk_bf16_f32 v48, v192, v193\n"
"v_mfma_f32_16x16x32_bf16 v[200:203], a[104:107], a[232:235], v[200:203]\nbuffer_load_dwordx4 v250, s[36:39], s76 offen lds\nv_mfma_f32_16x16x32_bf16 v[200:203], a[108:111], a[248:251], v[200:203]\nds_read_b64_tr_b16 a[88:89], v248 offset:57344\n"
"v_mul_f32 v196, v196, v237\nv_mfma_f32_16x16x32_bf16 v[200:203], a[112:115], v[8:11], v[200:203]\ns_add_u32 s76, s76, 0x1c000\nds_read_b64_tr_b16 a[90:91], v248 offset:57856\n"
"v_mul_f32 v197, v197, v237\nv_mfma_f32_16x16x32_bf16 v[200:203], a[116:119], v[24:27], v[200:203]\ns_add_u32 m0, s73, 0x15000\nv_exp_f32 v132, v132\n"
"v_mfma_f32_16x16x32_bf16 v[200:203], a[120:123], v[40:43], v[200:203]\nbuffer_load_dwordx4 v250, s[36:39], s76 offen lds\nv_mfma_f32_16x16x32_bf16 v[204:207], a[120:123], v[44:47], v[204:207]\nv_exp_f32 v133, v133\n"
"v_mfma_f32_16x16x32_bf16 v[204:207], a[116:119], v[28:31], v[204:207]\ns_add_u32 s76, s76, 0x1c000\nv_exp_f32 v134, v134\nv_mfma_f32_16x16x32_bf16 v[204:207], a[112:115], v[12:15], v[204:207]\n"
"s_add_u32 m0, s73, 0x16000\nv_exp_f32 v135, v135\nv_mfma_f32_16x16x32_bf16 v[204:207], a[108:111], a[252:255], v[204:207]\nbuffer_load_dwordx4 v250, s[36:39], s76 offen lds\n"
"v_mfma_f32_16x16x32_bf16 v[204:207], a[104:107], a[236:239], v[204:207]\nv_rcp_f32 v158, v158\nv_mfma_f32_16x16x32_bf16 v[204:207], a[100:103], a[220:223], v[204:207]\nv_exp_f32 v136, v136\n"
"v_mfma_f32_16x16x32_bf16 v[204:207], a[96:99], a[204:207], v[204:207]\nv_exp_f32 v137, v137\ns_waitcnt vmcnt(14) lgkmcnt(0)\ns_barrier\n"
"v_mfma_f32_16x16x32_bf16 v[208:211], a[64:67], a[128:131], 0\ns_add_u32 s77, s77, 0xffe94080\nds_read_b64_tr_b16 a[96:97], v249 offset:32768\nv_fma_f32 v132, v132, v157, v157\n"
"v_mfma_f32_16x16x32_bf16 v[208:211], a[68:71], a[144:147], v[208:211]\ns_add_u32 m0, s73, 0x8000\nds_read_b64_tr_b16 a[98:99], v249 offset:33280\nv_fma_f32 v133, v133, v157, v157\n"
"v_mfma_f32_16x16x32_bf16 v[208:211], a[72:75], a[160:163], v[208:211]\nbuffer_load_dwordx4 v250, s[40:43], s77 offen lds\nv_mfma_f32_16x16x32_bf16 v[208:211], a[76:79], a[176:179], v[208:211]\ns_add_u32 s77, s77, 0x1c000\n"
"ds_read_b64_tr_b16 a[100:101], v249 offset:36864\nv_fma_f32 v134, v134, v157, v157\nv_mfma_f32_16x16x32_bf16 v[208:211], a[80:83], a[0:3], v[208:211]\ns_add_u32 m0, s73, 0x9000\n"
"ds_read_b64_tr_b16 a[102:103], v249 offset:37376\nv_fma_f32 v135, v135, v157, v157\nv_mfma_f32_16x16x32_bf16 v[208:211], a[84:87], a[16:19], v[208:211]\nbuffer_load_dwordx4 v250, s[40:43], s77 offen lds\n"
"v_mfma_f32_16x16x32_bf16 v[208:211], a[88:91], a[32:35], v[208:211]\nds_read_b64_tr_b16 a[104:105], v249 offset:40960\nv_fma_f32 v136, v136, v158, v158\nv_mfma_f32_16x16x32_bf16 v[212:215], a[88:91], a[36:39], 0\n"
"s_add_u32 s77, s77, 0x1c000\nds_read_b64_tr_b16 a[106:107], v249 offset:41472\nv_fma_f32 v137, v137, v158, v158\nv_mfma_f32_16x16x32_bf16 v[212:215], a[84:87], a[20:23], v[212:215]\n"
"s_add_u32 m0, s73, 0xa000\nds_read_b64_tr_b16 a[108:109], v249 offset:45056\nv_mul_f32 v198, v198, v237\nv_mfma_f32_16x16x32_bf16 v[212:215], a[80:83], a[4:7], v[212:215]\n"
"buffer_load_dwordx4 v250, s[40:43], s77 offen lds\nv_mfma_f32_16x16x32_bf16 v[212:215], a[76:79], a[180:183], v[212:215]\nds_read_b64_tr_b16 a[110:111], v249 offset:45568\nv_mul_f32 v199, v199, v237\n"
"v_mfma_f32_16x16x32_bf16 v[212:215], a[72:75], a[164:167], v[212:215]\ns_add_u32 s77, s77, 0x1c000\nds_read_b64_tr_b16 a[112:113], v249 offset:49152\nv_mul_f32 v200, v200, v238\n"
"v_mfma_f32_16x16x32_bf16 v[212:215], a[68:71], a[148:151], v[212:215]\ns_add_u32 m0, s73, 0xb000\nds_read_b64_tr_b16 a[114:115], v249 offset:49664\nv_mul_f32 v201, v201, v238\n"
"v_mfma_f32_16x16x32_bf16 v[212:215], a[64:67], a[132:135], v[212:215]\nbuffer_load_dwordx4 v250, s[40:43], s77 offen lds\nv_mfma_f32_16x16x32_bf16 v[216:219], a[64:67], a[136:139], 0\ns_add_u32 s77, s77, 0x1c000\n"
"ds_read_b64_tr_b16 a[116:117], v249 offset:53248\nv_cvt_pk_bf16_f32 v49, v194, v195\nv_mfma_f32_16x16x32_bf16 v[216:219], a[68:71], a[152:155], v[216:219]\ns_add_u32 m0, s73, 0xc000\n"
"ds_read_b64_tr_b16 a[118:119], v249 offset:53760\nv_mul_f32 v202, v202, v238\nv_mfma_f32_16x16x32_bf16 v[216:219], a[72:75], a[168:171], v[216:219]\nbuffer_load_dwordx4 v250, s[40:43], s77 offen lds\n"
"v_mfma_f32_16x16x32_bf16 v[216:219], a[76:79], a[184:187], v[216:219]\nds_read_b64_tr_b16 a[120:121], v249 offset:57344\nv_cvt_pk_bf16_f32 v50, v196, v197\nv_mfma_f32_16x16x32_bf16 v[216:219], a[80:83], a[8:11], v[216:219]\n"
"s_add_u32 s77, s77, 0x1c000\nds_read_b64_tr_b16 a[122:123], v249 offset:57856\nv_mul_f32 v203, v203, v238\nv_mfma_f32_16x16x32_bf16 v[216:219], a[84:87], a[24:27], v[216:219]\n"
"s_add_u32 m0, s73, 0xd000\nv_exp_f32 v138, v138\nv_mfma_f32_16x16x32_bf16 v[216:219], a[88:91], a[40:43], v[216:219]\nbuffer_load_dwordx4 v250, s[40:43], s77 offen lds\n"
"v_mfma_f32_16x16x32_bf16 v[220:223], a[88:91], a[44:47], 0\nv_exp_f32 v139, v139\nv_mfma_f32_16x16x32_bf16 v[220:223], a[84:87], a[28:31], v[220:223]\ns_add_u32 s77, s77, 0x1c000\n"
"v_rcp_f32 v159, v159\nv_mfma_f32_16x16x32_bf16 v[220:223], a[80:83], a[12:15], v[220:223]\ns_add_u32 m0, s73, 0xe000\nv_exp_f32 v140, v140\n"
"v_mfma_f32_16x16x32_bf16 v[220:223], a[76:79], a[188:191], v[220:223]\nbuffer_load_dwordx4 v250, s[40:43], s77 offen lds\nv_mfma_f32_16x16x32_bf16 v[220:223], a[72:75], a[172:175], v[220:223]\nv_exp_f32 v141, v141\n"
"v_mfma_f32_16x16x32_bf16 v[220:223], a[68:71], a[156:159], v[220:223]\nv_exp_f32 v142, v142\nv_mfma_f32_16x16x32_bf16 v[220:223], a[64:67], a[140:143], v[220:223]\nv_exp_f32 v143, v143\n"
"s_waitcnt vmcnt(14) lgkmcnt(0)\ns_barrier\nv_mfma_f32_16x16x32_bf16 v[208:211], a[96:99], a[192:195], v[208:211]\ns_add_u32 s77, s77, 0x1c000\n"
"v_rcp_f32 v128, v128\nv_mfma_f32_16x16x32_bf16 v[208:211], a[100:103], a[208:211], v[208:211]\ns_add_u32 m0, s73, 0x18000\nv_rcp_f32 v129, v129\n"
"v_mfma_f32_16x16x32_bf16 v[208:211], a[104:107], a[224:227], v[208:211]\nbuffer_load_dwordx4 v250, s[40:43], s77 offen lds\nv_mfma_f32_16x16x32_bf16 v[208:211], a[108:111], a[240:243], v[208:211]\ns_add_u32 s77, s77, 0x1c000\n"
"v_rcp_f32 v130, v130\nv_mfma_f32_16x16x32_bf16 v[208:211], a[112:115], v[0:3], v[208:211]\ns_add_u32 m0, s73, 0x19000\nv_rcp_f32 v131, v131\n"
"v_mfma_f32_16x16x32_bf16 v[208:211], a[116:119], v[16:19], v[208:211]\nbuffer_load_dwordx4 v250, s[40:43], s77 offen lds\nv_mfma_f32_16x16x32_bf16 v[208:211], a[120:123], v[32:35], v[208:211]\nds_read_b64_tr_b16 a[64:65], v248 offset:0\n"
"v_fma_f32 v138, v138, v158, v158\nv_mfma_f32_16x16x32_bf16 v[212:215], a[120:123], v[36:39], v[212:215]\ns_add_u32 s77, s77, 0x1c000\nds_read_b64_tr_b16 a[66:67], v248 offset:512\n"
"v_fma_f32 v139, v139, v158, v158\nv_mfma_f32_16x16x32_bf16 v[212:215], a[116:119], v[20:23], v[212:215]\ns_add_u32 m0, s73, 0x1a000\nds_read_b64_tr_b16 a[68:69], v248 offset:4096\n"
"v_fma_f32 v140, v140, v159, v159\nv_mfma_f32_16x16x32_bf16 v[212:215], a[112:115], v[4:7], v[212:215]\nbuffer_load_dwordx4 v250, s[40:43], s77 offen lds\nv_mfma_f32_16x16x32_bf16 v[212:215], a[108:111], a[244:247], v[212:215]\n"
"ds_read_b64_tr_b16 a[70:71], v248 offset:4608\nv_fma_f32 v141, v141, v159, v159\nv_mfma_f32_16x16x32_bf16 v[212:215], a[104:107], a[228:231], v[212:215]\ns_add_u32 s77, s77, 0x1c000\n"
"ds_read_b64_tr_b16 a[72:73], v248 offset:8192\nv_fma_f32 v142, v142, v159, v159\nv_mfma_f32_16x16x32_bf16 v[212:215], a[100:103], a[212:215], v[212:215]\ns_add_u32 m0, s73, 0x1b000\n"
"ds_read_b64_tr_b16 a[74:75], v248 offset:8704\nv_fma_f32 v143, v143, v159, v159\nv_mfma_f32_16x16x32_bf16 v[212:215], a[96:99], a[196:199], v[212:215]\nbuffer_load_dwordx4 v250, s[40:43], s77 offen lds\n"
"v_mfma_f32_16x16x32_bf16 v[216:219], a[96:99], a[200:203], v[216:219]\ns_add_u32 s77, s77, 0x1c000\nds_read_b64_tr_b16 a[76:77], v248 offset:12288\nv_mul_f32 v128, v128, v176\n"
"v_mfma_f32_16x16x32_bf16 v[216:219], a[100:103], a[216:219], v[216:219]\ns_add_u32 m0, s73, 0x1c000\nds_read_b64_tr_b16 a[78:79], v248 offset:12800\nv_mul_f32 v129, v129, v177\n"
"v_mfma_f32_16x16x32_bf16 v[216:219], a[104:107], a[232:235], v[216:219]\nbuffer_load_dwordx4 v250, s[40:43], s77 offen lds\nv_mfma_f32_16x16x32_bf16 v[216:219], a[108:111], a[248:251], v[216:219]\nds_read_b64_tr_b16 a[80:81], v248 offset:16384\n"
"v_mul_f32 v130, v130, v178\nv_mfma_f32_16x16x32_bf16 v[216:219], a[112:115], v[8:11], v[216:219]\ns_add_u32 s77, s77, 0x1c000\nds_read_b64_tr_b16 a[82:83], v248 offset:16896\n"
"v_mul_f32 v131, v131, v179\nv_mfma_f32_16x16x32_bf16 v[216:219], a[116:119], v[24:27], v[216:219]\ns_add_u32 m0, s73, 0x1d000\nds_read_b64_tr_b16 a[84:85], v248 offset:20480\n"
"v_mul_f32 v128, v128, v160\nv_mfma_f32_16x16x32_bf16 v[216:219], a[120:123], v[40:43], v[216:219]\nbuffer_load_dwordx4 v250, s[40:43], s77 offen lds\nv_mfma_f32_16x16x32_bf16 v[220:223], a[120:123], v[44:47], v[220:223]\n"
"ds_read_b64_tr_b16 a[86:87], v248 offset:20992\nv_mul_f32 v129, v129, v161\nv_mfma_f32_16x16x32_bf16 v[220:223], a[116:119], v[28:31], v[220:223]\ns_add_u32 s77, s77, 0x1c000\n"
"ds_read_b64_tr_b16 a[88:89], v248 offset:24576\nv_mul_f32 v130, v130, v162\nv_mfma_f32_16x16x32_bf16 v[220:223], a[112:115], v[12:15], v[220:223]\ns_add_u32 m0, s73, 0x1e000\n"
"ds_read_b64_tr_b16 a[90:91], v248 offset:25088\nv_mul_f32 v131, v131, v163\nv_mfma_f32_16x16x32_bf16 v[220:223], a[108:111], a[252:255], v[220:223]\nbuffer_load_dwordx4 v250, s[40:43], s77 offen lds\n"
"v_mfma_f32_16x16x32_bf16 v[220:223], a[104:107], a[236:239], v[220:223]\nv_rcp_f32 v132, v132\nv_mfma_f32_16x16x32_bf16 v[220:223], a[100:103], a[220:223], v[220:223]\nv_rcp_f32 v133, v133\n"
"v_mfma_f32_16x16x32_bf16 v[220:223], a[96:99], a[204:207], v[220:223]\nv_rcp_f32 v134, v134\ns_waitcnt vmcnt(14) lgkmcnt(0)\ns_barrier\n"
"v_mfma_f32_16x16x32_bf16 v[176:179], a[64:67], a[128:131], 0\ns_add_u32 s78, s78, 0xffcbfe80\nv_rcp_f32 v135, v135\nv_mfma_f32_16x16x32_bf16 v[176:179], a[68:71], a[144:147], v[176:179]\n"
"s_add_u32 m0, s73, 0x0\nv_rcp_f32 v136, v136\nv_mfma_f32_16x16x32_bf16 v[176:179], a[72:75], a[160:163], v[176:179]\nbuffer_load_dwordx4 v251, s[44:47], s78 offen lds\n"
"v_mfma_f32_16x16x32_bf16 v[176:179], a[76:79], a[176:179], v[176:179]\ns_add_u32 s78, s78, 0x40000\nv_rcp_f32 v137, v137\nv_mfma_f32_16x16x32_bf16 v[176:179], a[80:83], a[0:3], v[176:179]\n"
"s_add_u32 m0, s73, 0x1000\nv_rcp_f32 v138, v138\nv_mfma_f32_16x16x32_bf16 v[176:179], a[84:87], a[16:19], v[176:179]\nbuffer_load_dwordx4 v251, s[44:47], s78 offen lds\n"
"v_mfma_f32_16x16x32_bf16 v[176:179], a[88:91], a[32:35], v[176:179]\nv_rcp_f32 v139, v139\nv_mul_f32 v132, v132, v180\nv_mul_f32 v133, v133, v181\n"
"v_mul_f32 v134, v134, v182\nv_mul_f32 v135, v135, v183\nv_mfma_f32_16x16x32_bf16 v[180:183], a[88:91], a[36:39], 0\ns_add_u32 s78, s78, 0x40000\n"
"v_rcp_f32 v140, v140\nv_mfma_f32_16x16x32_bf16 v[180:183], a[84:87], a[20:23], v[180:183]\ns_add_u32 m0, s73, 0x2000\nv_rcp_f32 v141, v141\n"
"v_mfma_f32_16x16x32_bf16 v[180:183], a[80:83], a[4:7], v[180:183]\nbuffer_load_dwordx4 v251, s[44:47], s78 offen lds\nv_mfma_f32_16x16x32_bf16 v[180:183], a[76:79], a[180:183], v[180:183]\nv_rcp_f32 v142, v142\n"
"v_mfma_f32_16x16x32_bf16 v[180:183], a[72:75], a[164:167], v[180:183]\ns_add_u32 s78, s78, 0x40000\nv_rcp_f32 v143, v143\nv_mfma_f32_16x16x32_bf16 v[180:183], a[68:71], a[148:151], v[180:183]\n"
"s_add_u32 m0, s73, 0x3000\nds_read_b64_tr_b16 a[96:97], v249 offset:0\nv_mul_f32 v136, v136, v184\nv_mfma_f32_16x16x32_bf16 v[180:183], a[64:67], a[132:135], v[180:183]\n"
"buffer_load_dwordx4 v251, s[44:47], s78 offen lds\nv_mul_f32 v137, v137, v185\nv_mul_f32 v138, v138, v186\nv_mul_f32 v139, v139, v187\n"
"v_mfma_f32_16x16x32_bf16 v[184:187], a[64:67], a[136:139], 0\ns_add_u32 s78, s78, 0x40000\nds_read_b64_tr_b16 a[98:99], v249 offset:512\nv_mul_f32 v140, v140, v188\n"
"v_mfma_f32_16x16x32_bf16 v[184:187], a[68:71], a[152:155], v[184:187]\ns_add_u32 m0, s73, 0x4000\nds_read_b64_tr_b16 a[100:101], v249 offset:4096\nv_mul_f32 v141, v141, v189\n"
"v_mfma_f32_16x16x32_bf16 v[184:187], a[72:75], a[168:171], v[184:187]\nbuffer_load_dwordx4 v251, s[44:47], s78 offen lds\nv_mfma_f32_16x16x32_bf16 v[184:187], a[76:79], a[184:187], v[184:187]\nds_read_b64_tr_b16 a[102:103], v249 offset:4608\n"
"v_mul_f32 v142, v142, v190\nv_mfma_f32_16x16x32_bf16 v[184:187], a[80:83], a[8:11], v[184:187]\ns_add_u32 s78, s78, 0x40000\nds_read_b64_tr_b16 a[104:105], v249 offset:8192\n"
"v_mul_f32 v143, v143, v191\nv_mfma_f32_16x16x32_bf16 v[184:187], a[84:87], a[24:27], v[184:187]\ns_add_u32 m0, s73, 0x5000\nds_read_b64_tr_b16 a[106:107], v249 offset:8704\n"
"v_cvt_pk_bf16_f32 v112, v128, v129\nv_mfma_f32_16x16x32_bf16 v[184:187], a[88:91], a[40:43], v[184:187]\nbuffer_load_dwordx4 v251, s[44:47], s78 offen lds\nv_mfma_f32_16x16x32_bf16 v[188:191], a[88:91], a[44:47], 0\n"
"ds_read_b64_tr_b16 a[108:109], v249 offset:12288\nv_cvt_pk_bf16_f32 v113, v130, v131\nv_mfma_f32_16x16x32_bf16 v[188:191], a[84:87], a[28:31], v[188:191]\ns_add_u32 s78, s78, 0x40000\n"
"ds_read_b64_tr_b16 a[110:111], v249 offset:12800\nv_mul_f32 v132, v132, v164\nv_mfma_f32_16x16x32_bf16 v[188:191], a[80:83], a[12:15], v[188:191]\ns_add_u32 m0, s73, 0x6000\n"
"ds_read_b64_tr_b16 a[112:113], v249 offset:16384\nv_mul_f32 v133, v133, v165\nv_mfma_f32_16x16x32_bf16 v[188:191], a[76:79], a[188:191], v[188:191]\nbuffer_load_dwordx4 v251, s[44:47], s78 offen lds\n"
"v_mfma_f32_16x16x32_bf16 v[188:191], a[72:75], a[172:175], v[188:191]\nds_read_b64_tr_b16 a[114:115], v249 offset:16896\nv_mul_f32 v134, v134, v166\nv_mfma_f32_16x16x32_bf16 v[188:191], a[68:71], a[156:159], v[188:191]\n"
"ds_read_b64_tr_b16 a[116:117], v249 offset:20480\nv_mul_f32 v135, v135, v167\nv_mfma_f32_16x16x32_bf16 v[188:191], a[64:67], a[140:143], v[188:191]\nds_read_b64_tr_b16 a[118:119], v249 offset:20992\n"
"v_cvt_pk_bf16_f32 v116, v132, v133\nds_read_b64_tr_b16 a[120:121], v249 offset:24576\nds_read_b64_tr_b16 a[122:123], v249 offset:25088\ns_waitcnt vmcnt(14) lgkmcnt(0)\n"
"s_barrier\nv_mfma_f32_16x16x32_bf16 v[176:179], a[96:99], a[192:195], v[176:179]\ns_add_u32 s78, s78, 0x40000\nds_read_b64_tr_b16 a[64:65], v248 offset:32768\n"
"v_cvt_pk_bf16_f32 v117, v134, v135\nv_mfma_f32_16x16x32_bf16 v[176:179], a[100:103], a[208:211], v[176:179]\ns_add_u32 m0, s73, 0x10000\nds_read_b64_tr_b16 a[66:67], v248 offset:33280\n"
"v_mul_f32 v136, v136, v168\nv_mfma_f32_16x16x32_bf16 v[176:179], a[104:107], a[224:227], v[176:179]\nbuffer_load_dwordx4 v251, s[44:47], s78 offen lds\nv_mfma_f32_16x16x32_bf16 v[176:179], a[108:111], a[240:243], v[176:179]\n"
"s_add_u32 s78, s78, 0x40000\nds_read_b64_tr_b16 a[68:69], v248 offset:36864\nv_mul_f32 v137, v137, v169\nv_mfma_f32_16x16x32_bf16 v[176:179], a[112:115], v[0:3], v[176:179]\n"
"s_add_u32 m0, s73, 0x11000\nds_read_b64_tr_b16 a[70:71], v248 offset:37376\nv_mul_f32 v138, v138, v170\nv_mfma_f32_16x16x32_bf16 v[176:179], a[116:119], v[16:19], v[176:179]\n"
"buffer_load_dwordx4 v251, s[44:47], s78 offen lds\nv_mfma_f32_16x16x32_bf16 v[176:179], a[120:123], v[32:35], v[176:179]\nds_read_b64_tr_b16 a[72:73], v248 offset:40960\nv_mul_f32 v139, v139, v171\n"
"v_mfma_f32_16x16x32_bf16 v[180:183], a[120:123], v[36:39], v[180:183]\ns_add_u32 s78, s78, 0x40000\nds_read_b64_tr_b16 a[74:75], v248 offset:41472\nv_mul_f32 v140, v140, v172\n"
"v_mfma_f32_16x16x32_bf16 v[180:183], a[116:119], v[20:23], v[180:183]\ns_add_u32 m0, s73, 0x12000\nds_read_b64_tr_b16 a[76:77], v248 offset:45056\nv_mul_f32 v141, v141, v173\n"
"v_mfma_f32_16x16x32_bf16 v[180:183], a[112:115], v[4:7], v[180:183]\nbuffer_load_dwordx4 v251, s[44:47], s78 offen lds\nv_mfma_f32_16x16x32_bf16 v[180:183], a[108:111], a[244:247], v[180:183]\nds_read_b64_tr_b16 a[78:79], v248 offset:45568\n"
"v_mul_f32 v128, v176, v176\nv_mfma_f32_16x16x32_bf16 v[180:183], a[104:107], a[228:231], v[180:183]\ns_add_u32 s78, s78, 0x40000\nds_read_b64_tr_b16 a[80:81], v248 offset:49152\n"
"v_mul_f32 v129, v177, v177\nv_mfma_f32_16x16x32_bf16 v[180:183], a[100:103], a[212:215], v[180:183]\ns_add_u32 m0, s73, 0x13000\nds_read_b64_tr_b16 a[82:83], v248 offset:49664\n"
"v_fma_f32 v128, v128, v148, v152\nv_mfma_f32_16x16x32_bf16 v[180:183], a[96:99], a[196:199], v[180:183]\nbuffer_load_dwordx4 v251, s[44:47], s78 offen lds\nv_mfma_f32_16x16x32_bf16 v[184:187], a[96:99], a[200:203], v[184:187]\n"
"s_add_u32 s78, s78, 0x40000\nds_read_b64_tr_b16 a[84:85], v248 offset:53248\nv_mul_f32 v130, v178, v178\nv_mfma_f32_16x16x32_bf16 v[184:187], a[100:103], a[216:219], v[184:187]\n"
"s_add_u32 m0, s73, 0x14000\nds_read_b64_tr_b16 a[86:87], v248 offset:53760\nv_mul_f32 v131, v179, v179\nv_mfma_f32_16x16x32_bf16 v[184:187], a[104:107], a[232:235], v[184:187]\n"
"buffer_load_dwordx4 v251, s[44:47], s78 offen lds\nv_mfma_f32_16x16x32_bf16 v[184:187], a[108:111], a[248:251], v[184:187]\nds_read_b64_tr_b16 a[88:89], v248 offset:57344\nv_fma_f32 v129, v129, v148, v152\n"
"v_mfma_f32_16x16x32_bf16 v[184:187], a[112:115], v[8:11], v[184:187]\ns_add_u32 s78, s78, 0x40000\nds_read_b64_tr_b16 a[90:91], v248 offset:57856\nv_mul_f32 v128, v128, v176\n"
"v_mfma_f32_16x16x32_bf16 v[184:187], a[116:119], v[24:27], v[184:187]\ns_add_u32 m0, s73, 0x15000\nv_fma_f32 v130, v130, v148, v152\nv_fma_f32 v131, v131, v148, v152\n"
"v_mfma_f32_16x16x32_bf16 v[184:187], a[120:123], v[40:43], v[184:187]\nbuffer_load_dwordx4 v251, s[44:47], s78 offen lds\nv_mfma_f32_16x16x32_bf16 v[188:191], a[120:123], v[44:47], v[188:191]\nv_mul_f32 v129, v129, v177\n"
"v_mul_f32 v130, v130, v178\nv_mfma_f32_16x16x32_bf16 v[188:191], a[116:119], v[28:31], v[188:191]\ns_add_u32 s78, s78, 0x40000\nv_exp_f32 v128, v128\n"
"v_mfma_f32_16x16x32_bf16 v[188:191], a[112:115], v[12:15], v[188:191]\ns_add_u32 m0, s73, 0x16000\nv_mul_f32 v131, v131, v179\nv_mul_f32 v132, v180, v180\n"
"v_mfma_f32_16x16x32_bf16 v[188:191], a[108:111], a[252:255], v[188:191]\nbuffer_load_dwordx4 v251, s[44:47], s78 offen lds\nv_mfma_f32_16x16x32_bf16 v[188:191], a[104:107], a[236:239], v[188:191]\nv_mul_f32 v133, v181, v181\n"
"v_mul_f32 v134, v182, v182\nv_mfma_f32_16x16x32_bf16 v[188:191], a[100:103], a[220:223], v[188:191]\nv_mul_f32 v135, v183, v183\nv_mul_f32 v142, v142, v174\n"
"v_mfma_f32_16x16x32_bf16 v[188:191], a[96:99], a[204:207], v[188:191]\nv_exp_f32 v129, v129\ns_waitcnt vmcnt(14) lgkmcnt(0)\ns_barrier\n"
"v_mfma_f32_16x16x32_bf16 v[160:163], a[64:67], a[128:131], 0\ns_add_u32 s78, s78, 0xffcc0200\nds_read_b64_tr_b16 a[96:97], v249 offset:32768\nv_mul_f32 v143, v143, v175\n"
"v_mfma_f32_16x16x32_bf16 v[160:163], a[68:71], a[144:147], v[160:163]\ns_add_u32 m0, s73, 0x8000\nds_read_b64_tr_b16 a[98:99], v249 offset:33280\nv_fma_f32 v132, v132, v149, v153\n"
"v_mfma_f32_16x16x32_bf16 v[160:163], a[72:75], a[160:163], v[160:163]\nbuffer_load_dwordx4 v251, s[44:47], s78 offen lds\nv_mfma_f32_16x16x32_bf16 v[160:163], a[76:79], a[176:179], v[160:163]\ns_add_u32 s78, s78, 0x40000\n"
"ds_read_b64_tr_b16 a[100:101], v249 offset:36864\nv_fma_f32 v133, v133, v149, v153\nv_mfma_f32_16x16x32_bf16 v[160:163], a[80:83], a[0:3], v[160:163]\ns_add_u32 m0, s73, 0x9000\n"
"ds_read_b64_tr_b16 a[102:103], v249 offset:37376\nv_fma_f32 v134, v134, v149, v153\nv_mfma_f32_16x16x32_bf16 v[160:163], a[84:87], a[16:19], v[160:163]\nbuffer_load_dwordx4 v251, s[44:47], s78 offen lds\n"
"v_mfma_f32_16x16x32_bf16 v[160:163], a[88:91], a[32:35], v[160:163]\nds_read_b64_tr_b16 a[104:105], v249 offset:40960\nv_fma_f32 v135, v135, v149, v153\nv_mfma_f32_16x16x32_bf16 v[164:167], a[88:91], a[36:39], 0\n"
"s_add_u32 s78, s78, 0x40000\nds_read_b64_tr_b16 a[106:107], v249 offset:41472\nv_fma_f32 v128, v128, v156, v156\nv_mfma_f32_16x16x32_bf16 v[164:167], a[84:87], a[20:23], v[164:167]\n"
"s_add_u32 m0, s73, 0xa000\nds_read_b64_tr_b16 a[108:109], v249 offset:45056\nv_fma_f32 v129, v129, v156, v156\nv_mfma_f32_16x16x32_bf16 v[164:167], a[80:83], a[4:7], v[164:167]\n"
"buffer_load_dwordx4 v251, s[44:47], s78 offen lds\nv_mfma_f32_16x16x32_bf16 v[164:167], a[76:79], a[180:183], v[164:167]\nds_read_b64_tr_b16 a[110:111], v249 offset:45568\nv_mul_f32 v132, v132, v180\n"
"v_mfma_f32_16x16x32_bf16 v[164:167], a[72:75], a[164:167], v[164:167]\ns_add_u32 s78, s78, 0x40000\nds_read_b64_tr_b16 a[112:113], v249 offset:49152\nv_mul_f32 v133, v133, v181\n"
"v_mfma_f32_16x16x32_bf16 v[164:167], a[68:71], a[148:151], v[164:167]\ns_add_u32 m0, s73, 0xb000\nds_read_b64_tr_b16 a[114:115], v249 offset:49664\nv_mul_f32 v134, v134, v182\n"
"v_mfma_f32_16x16x32_bf16 v[164:167], a[64:67], a[132:135], v[164:167]\nbuffer_load_dwordx4 v251, s[44:47], s78 offen lds\nv_mfma_f32_16x16x32_bf16 v[168:171], a[64:67], a[136:139], 0\ns_add_u32 s78, s78, 0x40000\n"
"ds_read_b64_tr_b16 a[116:117], v249 offset:53248\nv_mul_f32 v135, v135, v183\nv_mfma_f32_16x16x32_bf16 v[168:171], a[68:71], a[152:155], v[168:171]\ns_add_u32 m0, s73, 0xc000\n"
"ds_read_b64_tr_b16 a[118:119], v249 offset:53760\nv_cvt_pk_bf16_f32 v120, v136, v137\nv_mfma_f32_16x16x32_bf16 v[168:171], a[72:75], a[168:171], v[168:171]\nbuffer_load_dwordx4 v251, s[44:47], s78 offen lds\n"
"v_mfma_f32_16x16x32_bf16 v[168:171], a[76:79], a[184:187], v[168:171]\nds_read_b64_tr_b16 a[120:121], v249 offset:57344\nv_cvt_pk_bf16_f32 v51, v198, v199\nv_mfma_f32_16x16x32_bf16 v[168:171], a[80:83], a[8:11], v[168:171]\n"
"s_add_u32 s78, s78, 0x40000\nds_read_b64_tr_b16 a[122:123], v249 offset:57856\nv_mul_f32 v204, v204, v239\nv_mfma_f32_16x16x32_bf16 v[168:171], a[84:87], a[24:27], v[168:171]\n"
"s_add_u32 m0, s73, 0xd000\nv_exp_f32 v130, v130\nv_mfma_f32_16x16x32_bf16 v[168:171], a[88:91], a[40:43], v[168:171]\nbuffer_load_dwordx4 v251, s[44:47], s78 offen lds\n"
"v_mfma_f32_16x16x32_bf16 v[172:175], a[88:91], a[44:47], 0\nv_exp_f32 v131, v131\nv_mfma_f32_16x16x32_bf16 v[172:175], a[84:87], a[28:31], v[172:175]\ns_add_u32 s78, s78, 0x40000\n"
"v_fma_f32 v130, v130, v156, v156\nv_fma_f32 v131, v131, v156, v156\nv_mfma_f32_16x16x32_bf16 v[172:175], a[80:83], a[12:15], v[172:175]\ns_add_u32 m0, s73, 0xe000\n"
"v_rcp_f32 v128, v128\nv_mfma_f32_16x16x32_bf16 v[172:175], a[76:79], a[188:191], v[172:175]\nbuffer_load_dwordx4 v251, s[44:47], s78 offen lds\nv_mfma_f32_16x16x32_bf16 v[172:175], a[72:75], a[172:175], v[172:175]\n"
"v_rcp_f32 v129, v129\nv_mfma_f32_16x16x32_bf16 v[172:175], a[68:71], a[156:159], v[172:175]\nv_rcp_f32 v130, v130\nv_mfma_f32_16x16x32_bf16 v[172:175], a[64:67], a[140:143], v[172:175]\n"
"v_rcp_f32 v131, v131\ns_waitcnt vmcnt(14) lgkmcnt(0)\ns_barrier\nv_mfma_f32_16x16x32_bf16 v[160:163], a[96:99], a[192:195], v[160:163]\n"
"s_add_u32 s78, s78, 0x40000\nds_read_b64_tr_b16 a[64:65], v248 offset:0\nv_mul_f32 v205, v205, v239\nv_mfma_f32_16x16x32_bf16 v[160:163], a[100:103], a[208:211], v[160:163]\n"
"s_add_u32 m0, s73, 0x18000\nds_read_b64_tr_b16 a[66:67], v248 offset:512\nv_mul_f32 v206, v206, v239\nv_mfma_f32_16x16x32_bf16 v[160:163], a[104:107], a[224:227], v[160:163]\n"
"buffer_load_dwordx4 v251, s[44:47], s78 offen lds\nv_mfma_f32_16x16x32_bf16 v[160:163], a[108:111], a[240:243], v[160:163]\ns_add_u32 s78, s78, 0x40000\nds_read_b64_tr_b16 a[68:69], v248 offset:4096\n"
"v_mul_f32 v207, v207, v239\nv_mfma_f32_16x16x32_bf16 v[160:163], a[112:115], v[0:3], v[160:163]\ns_add_u32 m0, s73, 0x19000\nds_read_b64_tr_b16 a[70:71], v248 offset:4608\n"
"v_cvt_pk_bf16_f32 v121, v138, v139\nv_mfma_f32_16x16x32_bf16 v[160:163], a[116:119], v[16:19], v[160:163]\nbuffer_load_dwordx4 v251, s[44:47], s78 offen lds\nv_mfma_f32_16x16x32_bf16 v[160:163], a[120:123], v[32:35], v[160:163]\n"
"ds_read_b64_tr_b16 a[72:73], v248 offset:8192\nv_mul_f32 v128, v128, v176\nv_mfma_f32_16x16x32_bf16 v[164:167], a[120:123], v[36:39], v[164:167]\ns_add_u32 s78, s78, 0x40000\n"
"ds_read_b64_tr_b16 a[74:75], v248 offset:8704\nv_mul_f32 v129, v129, v177\nv_mfma_f32_16x16x32_bf16 v[164:167], a[116:119], v[20:23], v[164:167]\ns_add_u32 m0, s73, 0x1a000\n"
"ds_read_b64_tr_b16 a[76:77], v248 offset:12288\nv_mul_f32 v130, v130, v178\nv_mfma_f32_16x16x32_bf16 v[164:167], a[112:115], v[4:7], v[164:167]\nbuffer_load_dwordx4 v251, s[44:47], s78 offen lds\n"
"v_mfma_f32_16x16x32_bf16 v[164:167], a[108:111], a[244:247], v[164:167]\nds_read_b64_tr_b16 a[78:79], v248 offset:12800\nv_cvt_pk_bf16_f32 v52, v200, v201\nv_mfma_f32_16x16x32_bf16 v[164:167], a[104:107], a[228:231], v[164:167]\n"
"s_add_u32 s78, s78, 0x40000\nds_read_b64_tr_b16 a[80:81], v248 offset:16384\nv_mul_f32 v131, v131, v179\nv_mfma_f32_16x16x32_bf16 v[164:167], a[100:103], a[212:215], v[164:167]\n"
"s_add_u32 m0, s73, 0x1b000\nds_read_b64_tr_b16 a[82:83], v248 offset:16896\nv_cvt_pk_bf16_f32 v53, v202, v203\nv_mfma_f32_16x16x32_bf16 v[164:167], a[96:99], a[196:199], v[164:167]\n"
"buffer_load_dwordx4 v251, s[44:47], s78 offen lds\nv_mfma_f32_16x16x32_bf16 v[168:171], a[96:99], a[200:203], v[168:171]\ns_add_u32 s78, s78, 0x40000\nds_read_b64_tr_b16 a[84:85], v248 offset:20480\n"
"v_mul_f32 v136, v184, v184\nv_mfma_f32_16x16x32_bf16 v[168:171], a[100:103], a[216:219], v[168:171]\ns_add_u32 m0, s73, 0x1c000\nds_read_b64_tr_b16 a[86:87], v248 offset:20992\n"
"v_cvt_pk_bf16_f32 v54, v204, v205\nv_mfma_f32_16x16x32_bf16 v[168:171], a[104:107], a[232:235], v[168:171]\nbuffer_load_dwordx4 v251, s[44:47], s78 offen lds\nv_mfma_f32_16x16x32_bf16 v[168:171], a[108:111], a[248:251], v[168:171]\n"
"ds_read_b64_tr_b16 a[88:89], v248 offset:24576\nv_cvt_pk_bf16_f32 v55, v206, v207\nv_mfma_f32_16x16x32_bf16 v[168:171], a[112:115], v[8:11], v[168:171]\ns_add_u32 s78, s78, 0x40000\n"
"ds_read_b64_tr_b16 a[90:91], v248 offset:25088\nv_mul_f32 v137, v185, v185\nv_mfma_f32_16x16x32_bf16 v[168:171], a[116:119], v[24:27], v[168:171]\ns_add_u32 m0, s73, 0x1d000\n"
"v_exp_f32 v132, v132\nv_mfma_f32_16x16x32_bf16 v[168:171], a[120:123], v[40:43], v[168:171]\nbuffer_load_dwordx4 v251, s[44:47], s78 offen lds\nv_mfma_f32_16x16x32_bf16 v[172:175], a[120:123], v[44:47], v[172:175]\n"
"v_exp_f32 v133, v133\nv_mfma_f32_16x16x32_bf16 v[172:175], a[116:119], v[28:31], v[172:175]\ns_add_u32 s78, s78, 0x40000\nv_exp_f32 v134, v134\n"
"v_mfma_f32_16x16x32_bf16 v[172:175], a[112:115], v[12:15], v[172:175]\ns_add_u32 m0, s73, 0x1e000\nv_exp_f32 v135, v135\nv_mfma_f32_16x16x32_bf16 v[172:175], a[108:111], a[252:255], v[172:175]\n"
"buffer_load_dwordx4 v251, s[44:47], s78 offen lds\nv_mfma_f32_16x16x32_bf16 v[172:175], a[104:107], a[236:239], v[172:175]\nv_fma_f32 v132, v132, v157, v157\nv_fma_f32 v133, v133, v157, v157\n"
"v_mfma_f32_16x16x32_bf16 v[172:175], a[100:103], a[220:223], v[172:175]\nv_fma_f32 v134, v134, v157, v157\nv_fma_f32 v135, v135, v157, v157\nv_mfma_f32_16x16x32_bf16 v[172:175], a[96:99], a[204:207], v[172:175]\n"
"v_mul_f32 v138, v186, v186\nv_mul_f32 v128, v128, v160\ns_waitcnt vmcnt(14) lgkmcnt(0)\ns_barrier\n"
"v_mfma_f32_16x16x32_bf16 v[192:195], a[64:67], a[128:131], 0\ns_add_u32 s76, s76, 0xffe94080\nds_read_b64_tr_b16 a[96:97], v249 offset:0\nv_mul_f32 v129, v129, v161\n"
"v_mfma_f32_16x16x32_bf16 v[192:195], a[68:71], a[144:147], v[192:195]\ns_add_u32 m0, s73, 0x0\nds_read_b64_tr_b16 a[98:99], v249 offset:512\nv_mul_f32 v130, v130, v162\n"
"v_mfma_f32_16x16x32_bf16 v[192:195], a[72:75], a[160:163], v[192:195]\nbuffer_load_dwordx4 v250, s[36:39], s76 offen lds\nv_mfma_f32_16x16x32_bf16 v[192:195], a[76:79], a[176:179], v[192:195]\ns_add_u32 s76, s76, 0x1c000\n"
"ds_read_b64_tr_b16 a[100:101], v249 offset:4096\nv_mul_f32 v131, v131, v163\nv_mfma_f32_16x16x32_bf16 v[192:195], a[80:83], a[0:3], v[192:195]\ns_add_u32 m0, s73, 0x1000\n"
"ds_read_b64_tr_b16 a[102:103], v249 offset:4608\nv_fma_f32 v136, v136, v150, v154\nv_mfma_f32_16x16x32_bf16 v[192:195], a[84:87], a[16:19], v[192:195]\nbuffer_load_dwordx4 v250, s[36:39], s76 offen lds\n"
"v_mfma_f32_16x16x32_bf16 v[192:195], a[88:91], a[32:35], v[192:195]\nds_read_b64_tr_b16 a[104:105], v249 offset:8192\nv_mul_f32 v139, v187, v187\nv_mfma_f32_16x16x32_bf16 v[196:199], a[88:91], a[36:39], 0\n"
"s_add_u32 s76, s76, 0x1c000\nds_read_b64_tr_b16 a[106:107], v249 offset:8704\nv_fma_f32 v137, v137, v150, v154\nv_mfma_f32_16x16x32_bf16 v[196:199], a[84:87], a[20:23], v[196:199]\n"
"s_add_u32 m0, s73, 0x2000\nds_read_b64_tr_b16 a[108:109], v249 offset:12288\nv_fma_f32 v138, v138, v150, v154\nv_mfma_f32_16x16x32_bf16 v[196:199], a[80:83], a[4:7], v[196:199]\n"
"buffer_load_dwordx4 v250, s[36:39], s76 offen lds\nv_mfma_f32_16x16x32_bf16 v[196:199], a[76:79], a[180:183], v[196:199]\nds_read_b64_tr_b16 a[110:111], v249 offset:12800\nv_cvt_pk_bf16_f32 v114, v128, v129\n"
"v_mfma_f32_16x16x32_bf16 v[196:199], a[72:75], a[164:167], v[196:199]\ns_add_u32 s76, s76, 0x1c000\nds_read_b64_tr_b16 a[112:113], v249 offset:16384\nv_cvt_pk_bf16_f32 v115, v130, v131\n"
"v_mfma_f32_16x16x32_bf16 v[196:199], a[68:71], a[148:151], v[196:199]\ns_add_u32 m0, s73, 0x3000\nds_read_b64_tr_b16 a[114:115], v249 offset:16896\nv_mul_f32 v208, v208, v236\n"
"v_mfma_f32_16x16x32_bf16 v[196:199], a[64:67], a[132:135], v[196:199]\nbuffer_load_dwordx4 v250, s[36:39], s76 offen lds\nv_mfma_f32_16x16x32_bf16 v[200:203], a[64:67], a[136:139], 0\ns_add_u32 s76, s76, 0x1c000\n"
"ds_read_b64_tr_b16 a[116:117], v249 offset:20480\nv_mul_f32 v209, v209, v236\nv_mfma_f32_16x16x32_bf16 v[200:203], a[68:71], a[152:155], v[200:203]\ns_add_u32 m0, s73, 0x4000\n"
"ds_read_b64_tr_b16 a[118:119], v249 offset:20992\nv_mul_f32 v210, v210, v236\nv_mfma_f32_16x16x32_bf16 v[200:203], a[72:75], a[168:171], v[200:203]\nbuffer_load_dwordx4 v250, s[36:39], s76 offen lds\n"
"v_mfma_f32_16x16x32_bf16 v[200:203], a[76:79], a[184:187], v[200:203]\nds_read_b64_tr_b16 a[120:121], v249 offset:24576\nv_fma_f32 v139, v139, v150, v154\nv_mfma_f32_16x16x32_bf16 v[200:203], a[80:83], a[8:11], v[200:203]\n"
"s_add_u32 s76, s76, 0x1c000\nds_read_b64_tr_b16 a[122:123], v249 offset:25088\nv_cvt_pk_bf16_f32 v124, v140, v141\nv_mfma_f32_16x16x32_bf16 v[200:203], a[84:87], a[24:27], v[200:203]\n"
"s_add_u32 m0, s73, 0x5000\nv_rcp_f32 v132, v132\nv_mfma_f32_16x16x32_bf16 v[200:203], a[88:91], a[40:43], v[200:203]\nbuffer_load_dwordx4 v250, s[36:39], s76 offen lds\n"
"v_mfma_f32_16x16x32_bf16 v[204:207], a[88:91], a[44:47], 0\nv_rcp_f32 v133, v133\nv_mfma_f32_16x16x32_bf16 v[204:207], a[84:87], a[28:31], v[204:207]\ns_add_u32 s76, s76, 0x1c000\n"
"v_rcp_f32 v134, v134\nv_mfma_f32_16x16x32_bf16 v[204:207], a[80:83], a[12:15], v[204:207]\ns_add_u32 m0, s73, 0x6000\nv_rcp_f32 v135, v135\n"
"v_mfma_f32_16x16x32_bf16 v[204:207], a[76:79], a[188:191], v[204:207]\nbuffer_load_dwordx4 v250, s[36:39], s76 offen lds\nv_mfma_f32_16x16x32_bf16 v[204:207], a[72:75], a[172:175], v[204:207]\nv_mul_f32 v211, v211, v236\n"
"v_mul_f32 v212, v212, v237\nv_mfma_f32_16x16x32_bf16 v[204:207], a[68:71], a[156:159], v[204:207]\nv_mul_f32 v213, v213, v237\nv_mul_f32 v214, v214, v237\n"
"v_mfma_f32_16x16x32_bf16 v[204:207], a[64:67], a[140:143], v[204:207]\nv_mul_f32 v215, v215, v237\nv_mul_f32 v132, v132, v180\ns_waitcnt vmcnt(14) lgkmcnt(0)\n"
"s_barrier\nv_mfma_f32_16x16x32_bf16 v[192:195], a[96:99], a[192:195], v[192:195]\ns_add_u32 s76, s76, 0x1c000\nds_read_b64_tr_b16 a[64:65], v248 offset:32768\n"
"v_mul_f32 v133, v133, v181\nv_mfma_f32_16x16x32_bf16 v[192:195], a[100:103], a[208:211], v[192:195]\ns_add_u32 m0, s73, 0x10000\nds_read_b64_tr_b16 a[66:67], v248 offset:33280\n"
"v_mul_f32 v134, v134, v182\nv_mfma_f32_16x16x32_bf16 v[192:195], a[104:107], a[224:227], v[192:195]\nbuffer_load_dwordx4 v250, s[36:39], s76 offen lds\nv_mfma_f32_16x16x32_bf16 v[192:195], a[108:111], a[240:243], v[192:195]\n"
"s_add_u32 s76, s76, 0x1c000\nds_read_b64_tr_b16 a[68:69], v248 offset:36864\nv_mul_f32 v135, v135, v183\nv_mfma_f32_16x16x32_bf16 v[192:195], a[112:115], v[0:3], v[192:195]\n"
"s_add_u32 m0, s73, 0x11000\nds_read_b64_tr_b16 a[70:71], v248 offset:37376\nv_mul_f32 v136, v136, v184\nv_mfma_f32_16x16x32_bf16 v[192:195], a[116:119], v[16:19], v[192:195]\n"
"buffer_load_dwordx4 v250, s[36:39], s76 offen lds\nv_mfma_f32_16x16x32_bf16 v[192:195], a[120:123], v[32:35], v[192:195]\nds_read_b64_tr_b16 a[72:73], v248 offset:40960\nv_mul_f32 v137, v137, v185\n"
"v_mfma_f32_16x16x32_bf16 v[196:199], a[120:123], v[36:39], v[196:199]\ns_add_u32 s76, s76, 0x1c000\nds_read_b64_tr_b16 a[74:75], v248 offset:41472\nv_mul_f32 v138, v138, v186\n"
"v_mfma_f32_16x16x32_bf16 v[196:199], a[116:119], v[20:23], v[196:199]\ns_add_u32 m0, s73, 0x12000\nds_read_b64_tr_b16 a[76:77], v248 offset:45056\nv_cvt_pk_bf16_f32 v56, v208, v209\n"
"v_mfma_f32_16x16x32_bf16 v[196:199], a[112:115], v[4:7], v[196:199]\nbuffer_load_dwordx4 v250, s[36:39], s76 offen lds\nv_mfma_f32_16x16x32_bf16 v[196:199], a[108:111], a[244:247], v[196:199]\nds_read_b64_tr_b16 a[78:79], v248 offset:45568\n"
"v_mul_f32 v139, v139, v187\nv_mfma_f32_16x16x32_bf16 v[196:199], a[104:107], a[228:231], v[196:199]\ns_add_u32 s76, s76, 0x1c000\nds_read_b64_tr_b16 a[80:81], v248 offset:49152\n"
"v_cvt_pk_bf16_f32 v57, v210, v211\nv_mfma_f32_16x16x32_bf16 v[196:199], a[100:103], a[212:215], v[196:199]\ns_add_u32 m0, s73, 0x13000\nds_read_b64_tr_b16 a[82:83], v248 offset:49664\n"
"v_mul_f32 v216, v216, v238\nv_mfma_f32_16x16x32_bf16 v[196:199], a[96:99], a[196:199], v[196:199]\nbuffer_load_dwordx4 v250, s[36:39], s76 offen lds\nv_mfma_f32_16x16x32_bf16 v[200:203], a[96:99], a[200:203], v[200:203]\n"
"s_add_u32 s76, s76, 0x1c000\nds_read_b64_tr_b16 a[84:85], v248 offset:53248\nv_mul_f32 v217, v217, v238\nv_mfma_f32_16x16x32_bf16 v[200:203], a[100:103], a[216:219], v[200:203]\n"
"s_add_u32 m0, s73, 0x14000\nds_read_b64_tr_b16 a[86:87], v248 offset:53760\nv_mul_f32 v218, v218, v238\nv_mfma_f32_16x16x32_bf16 v[200:203], a[104:107], a[232:235], v[200:203]\n"
"buffer_load_dwordx4 v250, s[36:39], s76 offen lds\nv_mfma_f32_16x16x32_bf16 v[200:203], a[108:111], a[248:251], v[200:203]\nds_read_b64_tr_b16 a[88:89], v248 offset:57344\nv_mul_f32 v219, v219, v238\n"
"v_mfma_f32_16x16x32_bf16 v[200:203], a[112:115], v[8:11], v[200:203]\ns_add_u32 s76, s76, 0x1c000\nds_read_b64_tr_b16 a[90:91], v248 offset:57856\nv_mul_f32 v140, v188, v188\n"
"v_mfma_f32_16x16x32_bf16 v[200:203], a[116:119], v[24:27], v[200:203]\ns_add_u32 m0, s73, 0x15000\nv_cvt_pk_bf16_f32 v58, v212, v213\nv_cvt_pk_bf16_f32 v59, v214, v215\n"
"v_mfma_f32_16x16x32_bf16 v[200:203], a[120:123], v[40:43], v[200:203]\nbuffer_load_dwordx4 v250, s[36:39], s76 offen lds\nv_mfma_f32_16x16x32_bf16 v[204:207], a[120:123], v[44:47], v[204:207]\nv_mul_f32 v141, v189, v189\n"
"v_mul_f32 v132, v132, v164\nv_mfma_f32_16x16x32_bf16 v[204:207], a[116:119], v[28:31], v[204:207]\ns_add_u32 s76, s76, 0x1c000\nv_mul_f32 v133, v133, v165\n"
"v_mul_f32 v134, v134, v166\nv_mfma_f32_16x16x32_bf16 v[204:207], a[112:115], v[12:15], v[204:207]\ns_add_u32 m0, s73, 0x16000\nv_exp_f32 v136, v136\n"
"v_mfma_f32_16x16x32_bf16 v[204:207], a[108:111], a[252:255], v[204:207]\nbuffer_load_dwordx4 v250, s[36:39], s76 offen lds\nv_mfma_f32_16x16x32_bf16 v[204:207], a[104:107], a[236:239], v[204:207]\nbuffer_store_dwordx4 v[112:115], v224, s[60:63], s82 offen offset:0 sc1\n"
"v_mfma_f32_16x16x32_bf16 v[204:207], a[100:103], a[220:223], v[204:207]\nv_mul_f32 v220, v220, v239\nv_mul_f32 v135, v135, v167\nv_mfma_f32_16x16x32_bf16 v[204:207], a[96:99], a[204:207], v[204:207]\n"
"v_mul_f32 v221, v221, v239\nv_mul_f32 v222, v222, v239\ns_waitcnt vmcnt(15) lgkmcnt(0)\ns_barrier\n"
"v_mfma_f32_16x16x32_bf16 v[208:211], a[64:67], a[128:131], 0\ns_add_u32 s77, s77, 0xffe94080\nv_exp_f32 v137, v137\nv_mfma_f32_16x16x32_bf16 v[208:211], a[68:71], a[144:147], v[208:211]\n"
"s_add_u32 m0, s73, 0x8000\nv_exp_f32 v138, v138\nv_mfma_f32_16x16x32_bf16 v[208:211], a[72:75], a[160:163], v[208:211]\nbuffer_load_dwordx4 v250, s[40:43], s77 offen lds\n"
"v_mfma_f32_16x16x32_bf16 v[208:211], a[76:79], a[176:179], v[208:211]\ns_add_u32 s77, s77, 0x1c000\nv_exp_f32 v139, v139\nv_mfma_f32_16x16x32_bf16 v[208:211], a[80:83], a[0:3], v[208:211]\n"
"s_add_u32 m0, s73, 0x9000\nds_read_b64_tr_b16 a[96:97], v249 offset:32768\nv_mul_f32 v223, v223, v239\nv_mfma_f32_16x16x32_bf16 v[208:211], a[84:87], a[16:19], v[208:211]\n"
"buffer_load_dwordx4 v250, s[40:43], s77 offen lds\nv_mfma_f32_16x16x32_bf16 v[208:211], a[88:91], a[32:35], v[208:211]\nds_read_b64_tr_b16 a[98:99], v249 offset:33280\nv_cvt_pk_bf16_f32 v60, v216, v217\n"
"v_mfma_f32_16x16x32_bf16 v[212:215], a[88:91], a[36:39], 0\ns_add_u32 s77, s77, 0x1c000\nds_read_b64_tr_b16 a[100:101], v249 offset:36864\nv_cvt_pk_bf16_f32 v61, v218, v219\n"
"v_mfma_f32_16x16x32_bf16 v[212:215], a[84:87], a[20:23], v[212:215]\ns_add_u32 m0, s73, 0xa000\nds_read_b64_tr_b16 a[102:103], v249 offset:37376\nv_cvt_pk_bf16_f32 v125, v142, v143\n"
"v_mfma_f32_16x16x32_bf16 v[212:215], a[80:83], a[4:7], v[212:215]\nbuffer_load_dwordx4 v250, s[40:43], s77 offen lds\nv_mfma_f32_16x16x32_bf16 v[212:215], a[76:79], a[180:183], v[212:215]\nds_read_b64_tr_b16 a[104:105], v249 offset:40960\n"
"v_fma_f32 v140, v140, v151, v155\nv_mfma_f32_16x16x32_bf16 v[212:215], a[72:75], a[164:167], v[212:215]\ns_add_u32 s77, s77, 0x1c000\nds_read_b64_tr_b16 a[106:107], v249 offset:41472\n"
"v_fma_f32 v141, v141, v151, v155\nv_mfma_f32_16x16x32_bf16 v[212:215], a[68:71], a[148:151], v[212:215]\ns_add_u32 m0, s73, 0xb000\nds_read_b64_tr_b16 a[108:109], v249 offset:45056\n"
"v_cvt_pk_bf16_f32 v118, v132, v133\nv_mfma_f32_16x16x32_bf16 v[212:215], a[64:67], a[132:135], v[212:215]\nbuffer_load_dwordx4 v250, s[40:43], s77 offen lds\nv_mfma_f32_16x16x32_bf16 v[216:219], a[64:67], a[136:139], 0\n"
"s_add_u32 s77, s77, 0x1c000\nds_read_b64_tr_b16 a[110:111], v249 offset:45568\nv_cvt_pk_bf16_f32 v119, v134, v135\nv_mfma_f32_16x16x32_bf16 v[216:219], a[68:71], a[152:155], v[216:219]\n"
"s_add_u32 m0, s73, 0xc000\nds_read_b64_tr_b16 a[112:113], v249 offset:49152\nv_fma_f32 v136, v136, v158, v158\nv_mfma_f32_16x16x32_bf16 v[216:219], a[72:75], a[168:171], v[216:219]\n"
"buffer_load_dwordx4 v250, s[40:43], s77 offen lds\nv_mfma_f32_16x16x32_bf16 v[216:219], a[76:79], a[184:187], v[216:219]\nds_read_b64_tr_b16 a[114:115], v249 offset:49664\nv_cvt_pk_bf16_f32 v62, v220, v221\n"
"v_mfma_f32_16x16x32_bf16 v[216:219], a[80:83], a[8:11], v[216:219]\ns_add_u32 s77, s77, 0x1c000\nds_read_b64_tr_b16 a[116:117], v249 offset:53248\nv_cvt_pk_bf16_f32 v63, v222, v223\n"
"v_mfma_f32_16x16x32_bf16 v[216:219], a[84:87], a[24:27], v[216:219]\ns_add_u32 m0, s73, 0xd000\nds_read_b64_tr_b16 a[118:119], v249 offset:53760\nv_fma_f32 v137, v137, v158, v158\n"
"v_mfma_f32_16x16x32_bf16 v[216:219], a[88:91], a[40:43], v[216:219]\nbuffer_load_dwordx4 v250, s[40:43], s77 offen lds\nv_mfma_f32_16x16x32_bf16 v[220:223], a[88:91], a[44:47], 0\nds_read_b64_tr_b16 a[120:121], v249 offset:57344\n"
"v_fma_f32 v138, v138, v158, v158\nv_mfma_f32_16x16x32_bf16 v[220:223], a[84:87], a[28:31], v[220:223]\ns_add_u32 s77, s77, 0x1c000\nds_read_b64_tr_b16 a[122:123], v249 offset:57856\n"
"v_fma_f32 v139, v139, v158, v158\nv_mfma_f32_16x16x32_bf16 v[220:223], a[80:83], a[12:15], v[220:223]\ns_add_u32 m0, s73, 0xe000\nv_mul_f32 v142, v190, v190\n"
"v_mul_f32 v140, v140, v188\nv_mfma_f32_16x16x32_bf16 v[220:223], a[76:79], a[188:191], v[220:223]\nbuffer_load_dwordx4 v250, s[40:43], s77 offen lds\nv_mfma_f32_16x16x32_bf16 v[220:223], a[72:75], a[172:175], v[220:223]\n"
"v_mul_f32 v141, v141, v189\nv_fma_f32 v142, v142, v151, v155\nv_mfma_f32_16x16x32_bf16 v[220:223], a[68:71], a[156:159], v[220:223]\nbuffer_store_dwordx4 v[116:119], v224, s[60:63], s82 offen offset:1024 sc1\n"
"v_mfma_f32_16x16x32_bf16 v[220:223], a[64:67], a[140:143], v[220:223]\nv_rcp_f32 v136, v136\ns_waitcnt vmcnt(16) lgkmcnt(0)\ns_barrier\n"
"v_mfma_f32_16x16x32_bf16 v[208:211], a[96:99], a[192:195], v[208:211]\ns_add_u32 s77, s77, 0x1c000\nv_rcp_f32 v137, v137\nv_mfma_f32_16x16x32_bf16 v[208:211], a[100:103], a[208:211], v[208:211]\n"
"s_add_u32 m0, s73, 0x18000\nv_rcp_f32 v138, v138\nv_mfma_f32_16x16x32_bf16 v[208:211], a[104:107], a[224:227], v[208:211]\nbuffer_load_dwordx4 v250, s[40:43], s77 offen lds\n"
"v_mfma_f32_16x16x32_bf16 v[208:211], a[108:111], a[240:243], v[208:211]\ns_add_u32 s77, s77, 0x1c000\nv_rcp_f32 v139, v139\nv_mfma_f32_16x16x32_bf16 v[208:211], a[112:115], v[0:3], v[208:211]\n"
"s_add_u32 m0, s73, 0x19000\nv_mul_f32 v143, v191, v191\nds_read_b64_tr_b16 a[64:65], v248 offset:0\nv_mfma_f32_16x16x32_bf16 v[208:211], a[116:119], v[16:19], v[208:211]\n"
"buffer_load_dwordx4 v250, s[40:43], s77 offen lds\nv_mfma_f32_16x16x32_bf16 v[208:211], a[120:123], v[32:35], v[208:211]\nv_exp_f32 v140, v140\nv_mfma_f32_16x16x32_bf16 v[212:215], a[120:123], v[36:39], v[212:215]\n"
"s_add_u32 s77, s77, 0x1c000\nv_exp_f32 v141, v141\nv_mfma_f32_16x16x32_bf16 v[212:215], a[116:119], v[20:23], v[212:215]\ns_add_u32 m0, s73, 0x1a000\n"
"ds_read_b64_tr_b16 a[66:67], v248 offset:512\nv_fma_f32 v143, v143, v151, v155\nv_mfma_f32_16x16x32_bf16 v[212:215], a[112:115], v[4:7], v[212:215]\nbuffer_load_dwordx4 v250, s[40:43], s77 offen lds\n"
"v_mfma_f32_16x16x32_bf16 v[212:215], a[108:111], a[244:247], v[212:215]\nds_read_b64_tr_b16 a[68:69], v248 offset:4096\nv_mul_f32 v136, v136, v184\nv_mfma_f32_16x16x32_bf16 v[212:215], a[104:107], a[228:231], v[212:215]\n"
"s_add_u32 s77, s77, 0x1c000\nds_read_b64_tr_b16 a[70:71], v248 offset:4608\nv_mul_f32 v137, v137, v185\nv_mfma_f32_16x16x32_bf16 v[212:215], a[100:103], a[212:215], v[212:215]\n"
"s_add_u32 m0, s73, 0x1b000\nds_read_b64_tr_b16 a[72:73], v248 offset:8192\nv_mul_f32 v138, v138, v186\nv_mfma_f32_16x16x32_bf16 v[212:215], a[96:99], a[196:199], v[212:215]\n"
"buffer_load_dwordx4 v250, s[40:43], s77 offen lds\nv_mfma_f32_16x16x32_bf16 v[216:219], a[96:99], a[200:203], v[216:219]\ns_add_u32 s77, s77, 0x1c000\nds_read_b64_tr_b16 a[74:75], v248 offset:8704\n"
"v_mul_f32 v139, v139, v187\nv_mfma_f32_16x16x32_bf16 v[216:219], a[100:103], a[216:219], v[216:219]\ns_add_u32 m0, s73, 0x1c000\nds_read_b64_tr_b16 a[76:77], v248 offset:12288\n"
"v_mul_f32 v142, v142, v190\nv_mfma_f32_16x16x32_bf16 v[216:219], a[104:107], a[232:235], v[216:219]\nbuffer_load_dwordx4 v250, s[40:43], s77 offen lds\nv_mfma_f32_16x16x32_bf16 v[216:219], a[108:111], a[248:251], v[216:219]\n"
"ds_read_b64_tr_b16 a[78:79], v248 offset:12800\nv_mul_f32 v143, v143, v191\nv_mfma_f32_16x16x32_bf16 v[216:219], a[112:115], v[8:11], v[216:219]\ns_add_u32 s77, s77, 0x1c000\n"
"ds_read_b64_tr_b16 a[80:81], v248 offset:16384\nv_mul_f32 v136, v136, v168\nv_mfma_f32_16x16x32_bf16 v[216:219], a[116:119], v[24:27], v[216:219]\ns_add_u32 m0, s73, 0x1d000\n"
"ds_read_b64_tr_b16 a[82:83], v248 offset:16896\nv_mul_f32 v137, v137, v169\nv_mfma_f32_16x16x32_bf16 v[216:219], a[120:123], v[40:43], v[216:219]\nbuffer_load_dwordx4 v250, s[40:43], s77 offen lds\n"
"v_mfma_f32_16x16x32_bf16 v[220:223], a[120:123], v[44:47], v[220:223]\nds_read_b64_tr_b16 a[84:85], v248 offset:20480\nv_mul_f32 v138, v138, v170\nv_mfma_f32_16x16x32_bf16 v[220:223], a[116:119], v[28:31], v[220:223]\n"
"s_add_u32 s77, s77, 0x1c000\nds_read_b64_tr_b16 a[86:87], v248 offset:20992\nv_mul_f32 v139, v139, v171\nv_mfma_f32_16x16x32_bf16 v[220:223], a[112:115], v[12:15], v[220:223]\n"
"s_add_u32 m0, s73, 0x1e000\nds_read_b64_tr_b16 a[88:89], v248 offset:24576\nv_cvt_pk_bf16_f32 v122, v136, v137\nv_mfma_f32_16x16x32_bf16 v[220:223], a[108:111], a[252:255], v[220:223]\n"
"buffer_load_dwordx4 v250, s[40:43], s77 offen lds\nv_mfma_f32_16x16x32_bf16 v[220:223], a[104:107], a[236:239], v[220:223]\nds_read_b64_tr_b16 a[90:91], v248 offset:25088\nv_cvt_pk_bf16_f32 v123, v138, v139\n"
"v_mfma_f32_16x16x32_bf16 v[220:223], a[100:103], a[220:223], v[220:223]\nv_exp_f32 v142, v142\nv_mfma_f32_16x16x32_bf16 v[220:223], a[96:99], a[204:207], v[220:223]\nv_exp_f32 v143, v143\n"
"s_waitcnt vmcnt(16) lgkmcnt(0)\ns_barrier\nv_mfma_f32_16x16x32_bf16 v[176:179], a[64:67], a[128:131], 0\ns_add_u32 s78, s78, 0xffcbfe80\n"
"v_fma_f32 v140, v140, v159, v159\nv_fma_f32 v141, v141, v159, v159\nv_mfma_f32_16x16x32_bf16 v[176:179], a[68:71], a[144:147], v[176:179]\ns_add_u32 m0, s73, 0x0\n"
"v_fma_f32 v142, v142, v159, v159\nv_fma_f32 v143, v143, v159, v159\nv_mfma_f32_16x16x32_bf16 v[176:179], a[72:75], a[160:163], v[176:179]\nbuffer_load_dwordx4 v251, s[44:47], s78 offen lds\n"
"v_mfma_f32_16x16x32_bf16 v[176:179], a[76:79], a[176:179], v[176:179]\ns_add_u32 s78, s78, 0x40000\nv_rcp_f32 v140, v140\nv_mfma_f32_16x16x32_bf16 v[176:179], a[80:83], a[0:3], v[176:179]\n"
"s_add_u32 m0, s73, 0x1000\nv_rcp_f32 v141, v141\nv_mfma_f32_16x16x32_bf16 v[176:179], a[84:87], a[16:19], v[176:179]\nbuffer_load_dwordx4 v251, s[44:47], s78 offen lds\n"
"v_mfma_f32_16x16x32_bf16 v[176:179], a[88:91], a[32:35], v[176:179]\nv_rcp_f32 v142, v142\nv_mfma_f32_16x16x32_bf16 v[180:183], a[88:91], a[36:39], 0\ns_add_u32 s78, s78, 0x40000\n"
"v_rcp_f32 v143, v143\nv_mfma_f32_16x16x32_bf16 v[180:183], a[84:87], a[20:23], v[180:183]\ns_add_u32 m0, s73, 0x2000\nds_read_b64_tr_b16 a[96:97], v249 offset:0\n"
"v_mul_f32 v140, v140, v188\nv_mfma_f32_16x16x32_bf16 v[180:183], a[80:83], a[4:7], v[180:183]\nbuffer_load_dwordx4 v251, s[44:47], s78 offen lds\nv_mfma_f32_16x16x32_bf16 v[180:183], a[76:79], a[180:183], v[180:183]\n"
"ds_read_b64_tr_b16 a[98:99], v249 offset:512\nv_mul_f32 v141, v141, v189\nv_mfma_f32_16x16x32_bf16 v[180:183], a[72:75], a[164:167], v[180:183]\ns_add_u32 s78, s78, 0x40000\n"
"ds_read_b64_tr_b16 a[100:101], v249 offset:4096\nv_mul_f32 v142, v142, v190\nv_mfma_f32_16x16x32_bf16 v[180:183], a[68:71], a[148:151], v[180:183]\ns_add_u32 m0, s73, 0x3000\n"
"ds_read_b64_tr_b16 a[102:103], v249 offset:4608\nv_mul_f32 v143, v143, v191\nv_mfma_f32_16x16x32_bf16 v[180:183], a[64:67], a[132:135], v[180:183]\nbuffer_load_dwordx4 v251, s[44:47], s78 offen lds\n"
"v_mfma_f32_16x16x32_bf16 v[184:187], a[64:67], a[136:139], 0\ns_add_u32 s78, s78, 0x40000\nds_read_b64_tr_b16 a[104:105], v249 offset:8192\nv_mul_f32 v140, v140, v172\n"
"v_mfma_f32_16x16x32_bf16 v[184:187], a[68:71], a[152:155], v[184:187]\ns_add_u32 m0, s73, 0x4000\nds_read_b64_tr_b16 a[106:107], v249 offset:8704\nv_mul_f32 v141, v141, v173\n"
"v_mfma_f32_16x16x32_bf16 v[184:187], a[72:75], a[168:171], v[184:187]\nbuffer_load_dwordx4 v251, s[44:47], s78 offen lds\nv_mfma_f32_16x16x32_bf16 v[184:187], a[76:79], a[184:187], v[184:187]\nds_read_b64_tr_b16 a[108:109], v249 offset:12288\n"
"v_mul_f32 v142, v142, v174\nv_mfma_f32_16x16x32_bf16 v[184:187], a[80:83], a[8:11], v[184:187]\ns_add_u32 s78, s78, 0x40000\nds_read_b64_tr_b16 a[110:111], v249 offset:12800\n"
"v_mul_f32 v143, v143, v175\nv_mfma_f32_16x16x32_bf16 v[184:187], a[84:87], a[24:27], v[184:187]\ns_add_u32 m0, s73, 0x5000\nds_read_b64_tr_b16 a[112:113], v249 offset:16384\n"
"v_cvt_pk_bf16_f32 v126, v140, v141\nv_mfma_f32_16x16x32_bf16 v[184:187], a[88:91], a[40:43], v[184:187]\nbuffer_load_dwordx4 v251, s[44:47], s78 offen lds\nv_mfma_f32_16x16x32_bf16 v[188:191], a[88:91], a[44:47], 0\n"
"ds_read_b64_tr_b16 a[114:115], v249 offset:16896\nv_cvt_pk_bf16_f32 v127, v142, v143\nv_mfma_f32_16x16x32_bf16 v[188:191], a[84:87], a[28:31], v[188:191]\ns_add_u32 s78, s78, 0x40000\n"
"ds_read_b64_tr_b16 a[116:117], v249 offset:20480\nv_mul_f32 v192, v192, v236\nv_mfma_f32_16x16x32_bf16 v[188:191], a[80:83], a[12:15], v[188:191]\ns_add_u32 m0, s73, 0x6000\n"
"ds_read_b64_tr_b16 a[118:119], v249 offset:20992\nv_mul_f32 v193, v193, v236\nv_mfma_f32_16x16x32_bf16 v[188:191], a[76:79], a[188:191], v[188:191]\nbuffer_load_dwordx4 v251, s[44:47], s78 offen lds\n"
"v_mfma_f32_16x16x32_bf16 v[188:191], a[72:75], a[172:175], v[188:191]\nbuffer_store_dwordx4 v[120:123], v224, s[60:63], s82 offen offset:2048 sc1\nv_mfma_f32_16x16x32_bf16 v[188:191], a[68:71], a[156:159], v[188:191]\nds_read_b64_tr_b16 a[120:121], v249 offset:24576\n"
"v_mul_f32 v194, v194, v236\nv_mfma_f32_16x16x32_bf16 v[188:191], a[64:67], a[140:143], v[188:191]\nds_read_b64_tr_b16 a[122:123], v249 offset:25088\nv_mul_f32 v195, v195, v236\n"
"s_waitcnt vmcnt(16) lgkmcnt(0)\ns_barrier\nv_mfma_f32_16x16x32_bf16 v[176:179], a[96:99], a[192:195], v[176:179]\ns_add_u32 s78, s78, 0x40000\n"
"ds_read_b64_tr_b16 a[64:65], v248 offset:32768\nv_cvt_pk_bf16_f32 v64, v192, v193\nv_mfma_f32_16x16x32_bf16 v[176:179], a[100:103], a[208:211], v[176:179]\ns_add_u32 m0, s73, 0x10000\n"
"ds_read_b64_tr_b16 a[66:67], v248 offset:33280\nv_cvt_pk_bf16_f32 v65, v194, v195\nv_mfma_f32_16x16x32_bf16 v[176:179], a[104:107], a[224:227], v[176:179]\nbuffer_load_dwordx4 v251, s[44:47], s78 offen lds\n"
"v_mfma_f32_16x16x32_bf16 v[176:179], a[108:111], a[240:243], v[176:179]\ns_add_u32 s78, s78, 0x40000\nds_read_b64_tr_b16 a[68:69], v248 offset:36864\nv_mul_f32 v196, v196, v237\n"
"v_mfma_f32_16x16x32_bf16 v[176:179], a[112:115], v[0:3], v[176:179]\ns_add_u32 m0, s73, 0x11000\nds_read_b64_tr_b16 a[70:71], v248 offset:37376\nv_mul_f32 v197, v197, v237\n"
"v_mfma_f32_16x16x32_bf16 v[176:179], a[116:119], v[16:19], v[176:179]\nbuffer_load_dwordx4 v251, s[44:47], s78 offen lds\nv_mfma_f32_16x16x32_bf16 v[176:179], a[120:123], v[32:35], v[176:179]\nds_read_b64_tr_b16 a[72:73], v248 offset:40960\n"
"v_mul_f32 v198, v198, v237\nv_mfma_f32_16x16x32_bf16 v[180:183], a[120:123], v[36:39], v[180:183]\ns_add_u32 s78, s78, 0x40000\nds_read_b64_tr_b16 a[74:75], v248 offset:41472\n"
"v_mul_f32 v199, v199, v237\nv_mfma_f32_16x16x32_bf16 v[180:183], a[116:119], v[20:23], v[180:183]\ns_add_u32 m0, s73, 0x12000\nds_read_b64_tr_b16 a[76:77], v248 offset:45056\n"
"v_cvt_pk_bf16_f32 v66, v196, v197\nv_mfma_f32_16x16x32_bf16 v[180:183], a[112:115], v[4:7], v[180:183]\nbuffer_load_dwordx4 v251, s[44:47], s78 offen lds\nv_mfma_f32_16x16x32_bf16 v[180:183], a[108:111], a[244:247], v[180:183]\n"
"ds_read_b64_tr_b16 a[78:79], v248 offset:45568\nv_cvt_pk_bf16_f32 v67, v198, v199\nv_mfma_f32_16x16x32_bf16 v[180:183], a[104:107], a[228:231], v[180:183]\ns_add_u32 s78, s78, 0x40000\n"
"ds_read_b64_tr_b16 a[80:81], v248 offset:49152\nv_mul_f32 v200, v200, v238\nv_mfma_f32_16x16x32_bf16 v[180:183], a[100:103], a[212:215], v[180:183]\ns_add_u32 m0, s73, 0x13000\n"
"ds_read_b64_tr_b16 a[82:83], v248 offset:49664\nv_mul_f32 v201, v201, v238\nv_mfma_f32_16x16x32_bf16 v[180:183], a[96:99], a[196:199], v[180:183]\nbuffer_load_dwordx4 v251, s[44:47], s78 offen lds\n"
"v_mfma_f32_16x16x32_bf16 v[184:187], a[96:99], a[200:203], v[184:187]\ns_add_u32 s78, s78, 0x40000\nds_read_b64_tr_b16 a[84:85], v248 offset:53248\nv_mul_f32 v202, v202, v238\n"
"v_mfma_f32_16x16x32_bf16 v[184:187], a[100:103], a[216:219], v[184:187]\ns_add_u32 m0, s73, 0x14000\nds_read_b64_tr_b16 a[86:87], v248 offset:53760\nv_mul_f32 v203, v203, v238\n"
"v_mfma_f32_16x16x32_bf16 v[184:187], a[104:107], a[232:235], v[184:187]\nbuffer_load_dwordx4 v251, s[44:47], s78 offen lds\nv_mfma_f32_16x16x32_bf16 v[184:187], a[108:111], a[248:251], v[184:187]\nds_read_b64_tr_b16 a[88:89], v248 offset:57344\n"
"v_cvt_pk_bf16_f32 v68, v200, v201\nv_mfma_f32_16x16x32_bf16 v[184:187], a[112:115], v[8:11], v[184:187]\ns_add_u32 s78, s78, 0x40000\nds_read_b64_tr_b16 a[90:91], v248 offset:57856\n"
"v_cvt_pk_bf16_f32 v69, v202, v203\nv_mfma_f32_16x16x32_bf16 v[184:187], a[116:119], v[24:27], v[184:187]\ns_add_u32 m0, s73, 0x15000\nv_mul_f32 v204, v204, v239\n"
"v_mul_f32 v205, v205, v239\nv_mfma_f32_16x16x32_bf16 v[184:187], a[120:123], v[40:43], v[184:187]\nbuffer_load_dwordx4 v251, s[44:47], s78 offen lds\nv_mfma_f32_16x16x32_bf16 v[188:191], a[120:123], v[44:47], v[188:191]\n"
"v_mul_f32 v206, v206, v239\nv_mul_f32 v207, v207, v239\nv_mfma_f32_16x16x32_bf16 v[188:191], a[116:119], v[28:31], v[188:191]\ns_add_u32 s78, s78, 0x40000\n"
"v_cvt_pk_bf16_f32 v70, v204, v205\nv_cvt_pk_bf16_f32 v71, v206, v207\nv_mfma_f32_16x16x32_bf16 v[188:191], a[112:115], v[12:15], v[188:191]\ns_add_u32 m0, s73, 0x16000\n"
"v_mul_f32 v208, v208, v236\nv_mul_f32 v209, v209, v236\nv_mfma_f32_16x16x32_bf16 v[188:191], a[108:111], a[252:255], v[188:191]\nbuffer_load_dwordx4 v251, s[44:47], s78 offen lds\n"
"v_mfma_f32_16x16x32_bf16 v[188:191], a[104:107], a[236:239], v[188:191]\nbuffer_store_dwordx4 v[124:127], v224, s[60:63], s82 offen offset:3072 sc1\nv_mfma_f32_16x16x32_bf16 v[188:191], a[100:103], a[220:223], v[188:191]\ns_nop 0\n"
"s_mul_i32 s82, s75, 14\nv_mul_f32 v210, v210, v236\nv_mul_f32 v211, v211, v236\nv_mfma_f32_16x16x32_bf16 v[188:191], a[96:99], a[204:207], v[188:191]\n"
"s_add_u32 s82, s82, s87\nv_cvt_pk_bf16_f32 v72, v208, v209\nv_cvt_pk_bf16_f32 v73, v210, v211\ns_waitcnt vmcnt(16) lgkmcnt(0)\n"
"s_barrier\nv_mfma_f32_16x16x32_bf16 v[160:163], a[64:67], a[128:131], 0\ns_add_u32 s78, s78, 0xffcc0200\nds_read_b64_tr_b16 a[96:97], v249 offset:32768\n"
"v_mul_f32 v212, v212, v237\nv_mfma_f32_16x16x32_bf16 v[160:163], a[68:71], a[144:147], v[160:163]\ns_add_u32 m0, s73, 0x8000\nds_read_b64_tr_b16 a[98:99], v249 offset:33280\n"
"v_mul_f32 v213, v213, v237\nv_mfma_f32_16x16x32_bf16 v[160:163], a[72:75], a[160:163], v[160:163]\nbuffer_load_dwordx4 v251, s[44:47], s78 offen lds\nv_mfma_f32_16x16x32_bf16 v[160:163], a[76:79], a[176:179], v[160:163]\n"
"s_add_u32 s78, s78, 0x40000\nds_read_b64_tr_b16 a[100:101], v249 offset:36864\nv_mul_f32 v214, v214, v237\nv_mfma_f32_16x16x32_bf16 v[160:163], a[80:83], a[0:3], v[160:163]\n"
"s_add_u32 m0, s73, 0x9000\nds_read_b64_tr_b16 a[102:103], v249 offset:37376\nv_mul_f32 v215, v215, v237\nv_mfma_f32_16x16x32_bf16 v[160:163], a[84:87], a[16:19], v[160:163]\n"
"buffer_load_dwordx4 v251, s[44:47], s78 offen lds\nv_mfma_f32_16x16x32_bf16 v[160:163], a[88:91], a[32:35], v[160:163]\ns_add_u32 s82, s82, 12\nds_read_b64_tr_b16 a[104:105], v249 offset:40960\n"
"v_cvt_pk_bf16_f32 v74, v212, v213\nv_mfma_f32_16x16x32_bf16 v[164:167], a[88:91], a[36:39], 0\ns_add_u32 s78, s78, 0x40000\nds_read_b64_tr_b16 a[106:107], v249 offset:41472\n"
"v_cvt_pk_bf16_f32 v75, v214, v215\nv_mfma_f32_16x16x32_bf16 v[164:167], a[84:87], a[20:23], v[164:167]\ns_add_u32 m0, s73, 0xa000\nds_read_b64_tr_b16 a[108:109], v249 offset:45056\n"
"v_mul_f32 v216, v216, v238\nv_mfma_f32_16x16x32_bf16 v[164:167], a[80:83], a[4:7], v[164:167]\nbuffer_load_dwordx4 v251, s[44:47], s78 offen lds\nv_mfma_f32_16x16x32_bf16 v[164:167], a[76:79], a[180:183], v[164:167]\n"
"s_lshl_b32 s82, s82, 2\nds_read_b64_tr_b16 a[110:111], v249 offset:45568\nv_mul_f32 v217, v217, v238\nv_mfma_f32_16x16x32_bf16 v[164:167], a[72:75], a[164:167], v[164:167]\n"
"s_add_u32 s78, s78, 0x40000\nds_read_b64_tr_b16 a[112:113], v249 offset:49152\nv_mul_f32 v218, v218, v238\nv_mfma_f32_16x16x32_bf16 v[164:167], a[68:71], a[148:151], v[164:167]\n"
"s_add_u32 m0, s73, 0xb000\nds_read_b64_tr_b16 a[114:115], v249 offset:49664\nv_mul_f32 v219, v219, v238\nv_mfma_f32_16x16x32_bf16 v[164:167], a[64:67], a[132:135], v[164:167]\n"
"buffer_load_dwordx4 v251, s[44:47], s78 offen lds\nv_mfma_f32_16x16x32_bf16 v[168:171], a[64:67], a[136:139], 0\ns_add_u32 s78, s78, 0x40000\nds_read_b64_tr_b16 a[116:117], v249 offset:53248\n"
"v_cvt_pk_bf16_f32 v76, v216, v217\nv_mfma_f32_16x16x32_bf16 v[168:171], a[68:71], a[152:155], v[168:171]\ns_add_u32 m0, s73, 0xc000\nds_read_b64_tr_b16 a[118:119], v249 offset:53760\n"
"v_cvt_pk_bf16_f32 v77, v218, v219\nv_mfma_f32_16x16x32_bf16 v[168:171], a[72:75], a[168:171], v[168:171]\nbuffer_load_dwordx4 v251, s[44:47], s78 offen lds\nv_mfma_f32_16x16x32_bf16 v[168:171], a[76:79], a[184:187], v[168:171]\n"
"s_add_u32 s82, s82, s72\nds_read_b64_tr_b16 a[120:121], v249 offset:57344\nv_mul_f32 v220, v220, v239\nv_mfma_f32_16x16x32_bf16 v[168:171], a[80:83], a[8:11], v[168:171]\n"
"s_add_u32 s78, s78, 0x40000\nds_read_b64_tr_b16 a[122:123], v249 offset:57856\nv_mul_f32 v221, v221, v239\nv_mfma_f32_16x16x32_bf16 v[168:171], a[84:87], a[24:27], v[168:171]\n"
"s_add_u32 m0, s73, 0xd000\nv_mul_f32 v222, v222, v239\nv_mul_f32 v223, v223, v239\nv_mfma_f32_16x16x32_bf16 v[168:171], a[88:91], a[40:43], v[168:171]\n"
"buffer_load_dwordx4 v251, s[44:47], s78 offen lds\nv_mfma_f32_16x16x32_bf16 v[172:175], a[88:91], a[44:47], 0\ns_lshl_b32 s82, s82, 12\nv_cvt_pk_bf16_f32 v78, v220, v221\n"
"v_cvt_pk_bf16_f32 v79, v222, v223\nv_mfma_f32_16x16x32_bf16 v[172:175], a[84:87], a[28:31], v[172:175]\ns_add_u32 s78, s78, 0x40000\nv_mul_f32 v128, v176, v176\n"
"v_mul_f32 v129, v177, v177\nv_mfma_f32_16x16x32_bf16 v[172:175], a[80:83], a[12:15], v[172:175]\ns_add_u32 m0, s73, 0xe000\nv_permlane16_swap_b32 v48, v50\n"
"v_permlane16_swap_b32 v49, v51\nv_mfma_f32_16x16x32_bf16 v[172:175], a[76:79], a[188:191], v[172:175]\nbuffer_load_dwordx4 v251, s[44:47], s78 offen lds\nv_mfma_f32_16x16x32_bf16 v[172:175], a[72:75], a[172:175], v[172:175]\n"
"s_and_b32 s87, s74, 2\nv_permlane16_swap_b32 v52, v54\nv_permlane16_swap_b32 v53, v55\nv_mfma_f32_16x16x32_bf16 v[172:175], a[68:71], a[156:159], v[172:175]\n"
"s_lshl_b32 s87, s87, 8\nv_permlane16_swap_b32 v56, v58\nv_permlane16_swap_b32 v57, v59\nv_mfma_f32_16x16x32_bf16 v[172:175], a[64:67], a[140:143], v[172:175]\n"
"s_add_u32 s82, s82, s87\nv_permlane16_swap_b32 v60, v62\nv_permlane16_swap_b32 v61, v63\ns_waitcnt vmcnt(16) lgkmcnt(0)\n"
"s_barrier\nv_mfma_f32_16x16x32_bf16 v[160:163], a[96:99], a[192:195], v[160:163]\ns_add_u32 s78, s78, 0x40000\nds_read_b64_tr_b16 a[64:65], v248 offset:0\n"
"v_permlane16_swap_b32 v64, v66\nv_mfma_f32_16x16x32_bf16 v[160:163], a[100:103], a[208:211], v[160:163]\ns_add_u32 m0, s73, 0x18000\nds_read_b64_tr_b16 a[66:67], v248 offset:512\n"
"v_permlane16_swap_b32 v65, v67\nv_mfma_f32_16x16x32_bf16 v[160:163], a[104:107], a[224:227], v[160:163]\nbuffer_load_dwordx4 v251, s[44:47], s78 offen lds\nv_mfma_f32_16x16x32_bf16 v[160:163], a[108:111], a[240:243], v[160:163]\n"
"s_add_u32 s78, s78, 0x40000\nds_read_b64_tr_b16 a[68:69], v248 offset:4096\nv_permlane16_swap_b32 v68, v70\nv_mfma_f32_16x16x32_bf16 v[160:163], a[112:115], v[0:3], v[160:163]\n"
"s_add_u32 m0, s73, 0x19000\nds_read_b64_tr_b16 a[70:71], v248 offset:4608\nv_permlane16_swap_b32 v69, v71\nv_mfma_f32_16x16x32_bf16 v[160:163], a[116:119], v[16:19], v[160:163]\n"
"buffer_load_dwordx4 v251, s[44:47], s78 offen lds\nv_mfma_f32_16x16x32_bf16 v[160:163], a[120:123], v[32:35], v[160:163]\nds_read_b64_tr_b16 a[72:73], v248 offset:8192\nv_permlane16_swap_b32 v72, v74\n"
"v_mfma_f32_16x16x32_bf16 v[164:167], a[120:123], v[36:39], v[164:167]\ns_add_u32 s78, s78, 0x40000\nds_read_b64_tr_b16 a[74:75], v248 offset:8704\nv_permlane16_swap_b32 v73, v75\n"
"v_mfma_f32_16x16x32_bf16 v[164:167], a[116:119], v[20:23], v[164:167]\ns_add_u32 m0, s73, 0x1a000\nds_read_b64_tr_b16 a[76:77], v248 offset:12288\nv_permlane16_swap_b32 v76, v78\n"
"v_mfma_f32_16x16x32_bf16 v[164:167], a[112:115], v[4:7], v[164:167]\nbuffer_load_dwordx4 v251, s[44:47], s78 offen lds\nv_mfma_f32_16x16x32_bf16 v[164:167], a[108:111], a[244:247], v[164:167]\nds_read_b64_tr_b16 a[78:79], v248 offset:12800\n"
"v_permlane16_swap_b32 v77, v79\nv_mfma_f32_16x16x32_bf16 v[164:167], a[104:107], a[228:231], v[164:167]\ns_add_u32 s78, s78, 0x40000\nds_read_b64_tr_b16 a[80:81], v248 offset:16384\n"
"v_mul_f32 v130, v178, v178\nv_mfma_f32_16x16x32_bf16 v[164:167], a[100:103], a[212:215], v[164:167]\ns_add_u32 m0, s73, 0x1b000\nds_read_b64_tr_b16 a[82:83], v248 offset:16896\n"
"v_mul_f32 v131, v179, v179\nv_mfma_f32_16x16x32_bf16 v[164:167], a[96:99], a[196:199], v[164:167]\nbuffer_load_dwordx4 v251, s[44:47], s78 offen lds\nv_mfma_f32_16x16x32_bf16 v[168:171], a[96:99], a[200:203], v[168:171]\n"
"s_add_u32 s78, s78, 0x40000\nds_read_b64_tr_b16 a[84:85], v248 offset:20480\nv_mul_f32 v132, v180, v180\nv_mfma_f32_16x16x32_bf16 v[168:171], a[100:103], a[216:219], v[168:171]\n"
"s_add_u32 m0, s73, 0x1c000\nds_read_b64_tr_b16 a[86:87], v248 offset:20992\nv_fma_f32 v128, v128, v148, v152\nv_mfma_f32_16x16x32_bf16 v[168:171], a[104:107], a[232:235], v[168:171]\n"
"buffer_load_dwordx4 v251, s[44:47], s78 offen lds\nv_mfma_f32_16x16x32_bf16 v[168:171], a[108:111], a[248:251], v[168:171]\nds_read_b64_tr_b16 a[88:89], v248 offset:24576\nv_mul_f32 v133, v181, v181\n"
"v_mfma_f32_16x16x32_bf16 v[168:171], a[112:115], v[8:11], v[168:171]\ns_add_u32 s78, s78, 0x40000\nds_read_b64_tr_b16 a[90:91], v248 offset:25088\nv_fma_f32 v129, v129, v148, v152\n"
"v_mfma_f32_16x16x32_bf16 v[168:171], a[116:119], v[24:27], v[168:171]\ns_add_u32 m0, s73, 0x1d000\nv_mul_f32 v134, v182, v182\nv_mul_f32 v135, v183, v183\n"
"v_mfma_f32_16x16x32_bf16 v[168:171], a[120:123], v[40:43], v[168:171]\nbuffer_load_dwordx4 v251, s[44:47], s78 offen lds\nv_mfma_f32_16x16x32_bf16 v[172:175], a[120:123], v[44:47], v[172:175]\nv_fma_f32 v130, v130, v148, v152\n"
"v_fma_f32 v131, v131, v148, v152\nv_mfma_f32_16x16x32_bf16 v[172:175], a[116:119], v[28:31], v[172:175]\ns_add_u32 s78, s78, 0x40000\nv_fma_f32 v132, v132, v149, v153\n"
"v_fma_f32 v133, v133, v149, v153\nv_mfma_f32_16x16x32_bf16 v[172:175], a[112:115], v[12:15], v[172:175]\ns_add_u32 m0, s73, 0x1e000\nv_mul_f32 v128, v128, v176\n"
"v_mul_f32 v136, v184, v184\nv_mfma_f32_16x16x32_bf16 v[172:175], a[108:111], a[252:255], v[172:175]\nbuffer_load_dwordx4 v251, s[44:47], s78 offen lds\nv_mfma_f32_16x16x32_bf16 v[172:175], a[104:107], a[236:239], v[172:175]\n"
"v_mul_f32 v129, v129, v177\nv_fma_f32 v134, v134, v149, v153\nv_mfma_f32_16x16x32_bf16 v[172:175], a[100:103], a[220:223], v[172:175]\nv_fma_f32 v135, v135, v149, v153\n"
"v_mul_f32 v137, v185, v185\nv_mfma_f32_16x16x32_bf16 v[172:175], a[96:99], a[204:207], v[172:175]\nv_mul_f32 v138, v186, v186\nv_mul_f32 v130, v130, v178\n"
"s_waitcnt vmcnt(15) lgkmcnt(0)\ns_barrier\nv_mfma_f32_16x16x32_bf16 v[192:195], a[64:67], a[128:131], 0\ns_add_u32 s78, s78, 0xffcbfe80\n"
"ds_read_b64_tr_b16 a[96:97], v249 offset:0\nv_mul_f32 v131, v131, v179\nv_mfma_f32_16x16x32_bf16 v[192:195], a[68:71], a[144:147], v[192:195]\ns_add_u32 m0, s73, 0x0\n"
"ds_read_b64_tr_b16 a[98:99], v249 offset:512\nv_mul_f32 v132, v132, v180\nv_mfma_f32_16x16x32_bf16 v[192:195], a[72:75], a[160:163], v[192:195]\nbuffer_load_dwordx4 v251, s[44:47], s78 offen lds\n"
"v_mfma_f32_16x16x32_bf16 v[192:195], a[76:79], a[176:179], v[192:195]\ns_add_u32 s78, s78, 0x40000\nds_read_b64_tr_b16 a[100:101], v249 offset:4096\nv_mul_f32 v133, v133, v181\n"
"v_mfma_f32_16x16x32_bf16 v[192:195], a[80:83], a[0:3], v[192:195]\ns_add_u32 m0, s73, 0x1000\nds_read_b64_tr_b16 a[102:103], v249 offset:4608\nv_mul_f32 v139, v187, v187\n"
"v_mfma_f32_16x16x32_bf16 v[192:195], a[84:87], a[16:19], v[192:195]\nbuffer_load_dwordx4 v251, s[44:47], s78 offen lds\nv_mfma_f32_16x16x32_bf16 v[192:195], a[88:91], a[32:35], v[192:195]\nds_read_b64_tr_b16 a[104:105], v249 offset:8192\n"
"v_fma_f32 v136, v136, v150, v154\nv_mfma_f32_16x16x32_bf16 v[196:199], a[88:91], a[36:39], 0\ns_add_u32 s78, s78, 0x40000\nds_read_b64_tr_b16 a[106:107], v249 offset:8704\n"
"v_mul_f32 v134, v134, v182\nv_mfma_f32_16x16x32_bf16 v[196:199], a[84:87], a[20:23], v[196:199]\ns_add_u32 m0, s73, 0x2000\nds_read_b64_tr_b16 a[108:109], v249 offset:12288\n"
"v_mul_f32 v135, v135, v183\nv_mfma_f32_16x16x32_bf16 v[196:199], a[80:83], a[4:7], v[196:199]\nbuffer_load_dwordx4 v251, s[44:47], s78 offen lds\nv_mfma_f32_16x16x32_bf16 v[196:199], a[76:79], a[180:183], v[196:199]\n"
"ds_read_b64_tr_b16 a[110:111], v249 offset:12800\nv_fma_f32 v137, v137, v150, v154\nv_mfma_f32_16x16x32_bf16 v[196:199], a[72:75], a[164:167], v[196:199]\ns_add_u32 s78, s78, 0x40000\n"
"ds_read_b64_tr_b16 a[112:113], v249 offset:16384\nv_fma_f32 v138, v138, v150, v154\nv_mfma_f32_16x16x32_bf16 v[196:199], a[68:71], a[148:151], v[196:199]\ns_add_u32 m0, s73, 0x3000\n"
"ds_read_b64_tr_b16 a[114:115], v249 offset:16896\nv_fma_f32 v139, v139, v150, v154\nv_mfma_f32_16x16x32_bf16 v[196:199], a[64:67], a[132:135], v[196:199]\nbuffer_load_dwordx4 v251, s[44:47], s78 offen lds\n"
"v_mfma_f32_16x16x32_bf16 v[200:203], a[64:67], a[136:139], 0\ns_add_u32 s78, s78, 0x40000\nds_read_b64_tr_b16 a[116:117], v249 offset:20480\nv_mul_f32 v136, v136, v184\n"
"v_mfma_f32_16x16x32_bf16 v[200:203], a[68:71], a[152:155], v[200:203]\ns_add_u32 m0, s73, 0x4000\nds_read_b64_tr_b16 a[118:119], v249 offset:20992\nv_mul_f32 v140, v188, v188\n"
"v_mfma_f32_16x16x32_bf16 v[200:203], a[72:75], a[168:171], v[200:203]\nbuffer_load_dwordx4 v251, s[44:47], s78 offen lds\nv_mfma_f32_16x16x32_bf16 v[200:203], a[76:79], a[184:187], v[200:203]\nds_read_b64_tr_b16 a[120:121], v249 offset:24576\n"
"v_mul_f32 v137, v137, v185\nv_mfma_f32_16x16x32_bf16 v[200:203], a[80:83], a[8:11], v[200:203]\ns_add_u32 s78, s78, 0x40000\nds_read_b64_tr_b16 a[122:123], v249 offset:25088\n"
"v_mul_f32 v141, v189, v189\nv_mfma_f32_16x16x32_bf16 v[200:203], a[84:87], a[24:27], v[200:203]\ns_add_u32 m0, s73, 0x5000\nv_exp_f32 v128, v128\n"
"v_mfma_f32_16x16x32_bf16 v[200:203], a[88:91], a[40:43], v[200:203]\nbuffer_load_dwordx4 v251, s[44:47], s78 offen lds\nv_mfma_f32_16x16x32_bf16 v[204:207], a[88:91], a[44:47], 0\nv_exp_f32 v129, v129\n"
"v_mfma_f32_16x16x32_bf16 v[204:207], a[84:87], a[28:31], v[204:207]\ns_add_u32 s78, s78, 0x40000\nv_exp_f32 v130, v130\nv_mfma_f32_16x16x32_bf16 v[204:207], a[80:83], a[12:15], v[204:207]\n"
"s_add_u32 m0, s73, 0x6000\nv_exp_f32 v131, v131\nv_mfma_f32_16x16x32_bf16 v[204:207], a[76:79], a[188:191], v[204:207]\nbuffer_load_dwordx4 v251, s[44:47], s78 offen lds\n"
"v_mfma_f32_16x16x32_bf16 v[204:207], a[72:75], a[172:175], v[204:207]\nv_exp_f32 v132, v132\nv_mfma_f32_16x16x32_bf16 v[204:207], a[68:71], a[156:159], v[204:207]\nv_exp_f32 v133, v133\n"
"v_mfma_f32_16x16x32_bf16 v[204:207], a[64:67], a[140:143], v[204:207]\nv_fma_f32 v128, v128, v156, v156\nv_fma_f32 v129, v129, v156, v156\ns_waitcnt vmcnt(14) lgkmcnt(0)\n"
"s_barrier\nv_mfma_f32_16x16x32_bf16 v[192:195], a[96:99], a[192:195], v[192:195]\ns_add_u32 s78, s78, 0x40000\nds_read_b64_tr_b16 a[64:65], v248 offset:32768\n"
"v_mul_f32 v138, v138, v186\nv_mfma_f32_16x16x32_bf16 v[192:195], a[100:103], a[208:211], v[192:195]\ns_add_u32 m0, s73, 0x10000\nds_read_b64_tr_b16 a[66:67], v248 offset:33280\n"
"v_fma_f32 v130, v130, v156, v156\nv_mfma_f32_16x16x32_bf16 v[192:195], a[104:107], a[224:227], v[192:195]\nbuffer_load_dwordx4 v251, s[44:47], s78 offen lds\nv_mfma_f32_16x16x32_bf16 v[192:195], a[108:111], a[240:243], v[192:195]\n"
"s_add_u32 s78, s78, 0x40000\nds_read_b64_tr_b16 a[68:69], v248 offset:36864\nv_fma_f32 v131, v131, v156, v156\nv_mfma_f32_16x16x32_bf16 v[192:195], a[112:115], v[0:3], v[192:195]\n"
"s_add_u32 m0, s73, 0x11000\nds_read_b64_tr_b16 a[70:71], v248 offset:37376\nv_fma_f32 v132, v132, v157, v157\nv_mfma_f32_16x16x32_bf16 v[192:195], a[116:119], v[16:19], v[192:195]\n"
"buffer_load_dwordx4 v251, s[44:47], s78 offen lds\nv_mfma_f32_16x16x32_bf16 v[192:195], a[120:123], v[32:35], v[192:195]\nds_read_b64_tr_b16 a[72:73], v248 offset:40960\nv_fma_f32 v133, v133, v157, v157\n"
"v_mfma_f32_16x16x32_bf16 v[196:199], a[120:123], v[36:39], v[196:199]\ns_add_u32 s78, s78, 0x40000\nds_read_b64_tr_b16 a[74:75], v248 offset:41472\nv_mul_f32 v139, v139, v187\n"
"v_mfma_f32_16x16x32_bf16 v[196:199], a[116:119], v[20:23], v[196:199]\ns_add_u32 m0, s73, 0x12000\nds_read_b64_tr_b16 a[76:77], v248 offset:45056\nv_mul_f32 v142, v190, v190\n"
"v_mfma_f32_16x16x32_bf16 v[196:199], a[112:115], v[4:7], v[196:199]\nbuffer_load_dwordx4 v251, s[44:47], s78 offen lds\nv_mfma_f32_16x16x32_bf16 v[196:199], a[108:111], a[244:247], v[196:199]\nds_read_b64_tr_b16 a[78:79], v248 offset:45568\n"
"v_mul_f32 v192, v192, v236\nv_mfma_f32_16x16x32_bf16 v[196:199], a[104:107], a[228:231], v[196:199]\ns_add_u32 s78, s78, 0x40000\nds_read_b64_tr_b16 a[80:81], v248 offset:49152\n"
"v_mul_f32 v193, v193, v236\nv_mfma_f32_16x16x32_bf16 v[196:199], a[100:103], a[212:215], v[196:199]\ns_add_u32 m0, s73, 0x13000\nds_read_b64_tr_b16 a[82:83], v248 offset:49664\n"
"v_mul_f32 v194, v194, v236\nv_mfma_f32_16x16x32_bf16 v[196:199], a[96:99], a[196:199], v[196:199]\nbuffer_load_dwordx4 v251, s[44:47], s78 offen lds\nv_mfma_f32_16x16x32_bf16 v[200:203], a[96:99], a[200:203], v[200:203]\n"
"s_add_u32 s78, s78, 0x40000\nds_read_b64_tr_b16 a[84:85], v248 offset:53248\nv_mul_f32 v195, v195, v236\nv_mfma_f32_16x16x32_bf16 v[200:203], a[100:103], a[216:219], v[200:203]\n"
"s_add_u32 m0, s73, 0x14000\nds_read_b64_tr_b16 a[86:87], v248 offset:53760\nv_cvt_pk_bf16_f32 v80, v192, v193\nv_mfma_f32_16x16x32_bf16 v[200:203], a[104:107], a[232:235], v[200:203]\n"
"buffer_load_dwordx4 v251, s[44:47], s78 offen lds\nv_mfma_f32_16x16x32_bf16 v[200:203], a[108:111], a[248:251], v[200:203]\nds_read_b64_tr_b16 a[88:89], v248 offset:57344\nv_cvt_pk_bf16_f32 v81, v194, v195\n"
"v_mfma_f32_16x16x32_bf16 v[200:203], a[112:115], v[8:11], v[200:203]\ns_add_u32 s78, s78, 0x40000\nds_read_b64_tr_b16 a[90:91], v248 offset:57856\nv_mul_f32 v196, v196, v237\n"
"v_mfma_f32_16x16x32_bf16 v[200:203], a[116:119], v[24:27], v[200:203]\ns_add_u32 m0, s73, 0x15000\nv_mul_f32 v197, v197, v237\nv_mul_f32 v198, v198, v237\n"
"v_mfma_f32_16x16x32_bf16 v[200:203], a[120:123], v[40:43], v[200:203]\nbuffer_load_dwordx4 v251, s[44:47], s78 offen lds\nv_mfma_f32_16x16x32_bf16 v[204:207], a[120:123], v[44:47], v[204:207]\nv_mul_f32 v199, v199, v237\n"
"v_cvt_pk_bf16_f32 v82, v196, v197\nv_mfma_f32_16x16x32_bf16 v[204:207], a[116:119], v[28:31], v[204:207]\ns_add_u32 s78, s78, 0x40000\nv_cvt_pk_bf16_f32 v83, v198, v199\n"
"v_permlane16_swap_b32 v80, v82\nv_mfma_f32_16x16x32_bf16 v[204:207], a[112:115], v[12:15], v[204:207]\ns_add_u32 m0, s73, 0x16000\nv_permlane16_swap_b32 v81, v83\n"
"v_mul_f32 v143, v191, v191\nv_mfma_f32_16x16x32_bf16 v[204:207], a[108:111], a[252:255], v[204:207]\nbuffer_load_dwordx4 v251, s[44:47], s78 offen lds\nv_mfma_f32_16x16x32_bf16 v[204:207], a[104:107], a[236:239], v[204:207]\n"
"v_mul_f32 v200, v200, v238\nv_mul_f32 v201, v201, v238\nv_mfma_f32_16x16x32_bf16 v[204:207], a[100:103], a[220:223], v[204:207]\nv_mul_f32 v202, v202, v238\n"
"v_mul_f32 v203, v203, v238\nv_mfma_f32_16x16x32_bf16 v[204:207], a[96:99], a[204:207], v[204:207]\nv_cvt_pk_bf16_f32 v84, v200, v201\nv_cvt_pk_bf16_f32 v85, v202, v203\n"
"s_waitcnt vmcnt(14) lgkmcnt(0)\ns_barrier\nv_mfma_f32_16x16x32_bf16 v[208:211], a[64:67], a[128:131], 0\ns_add_u32 s78, s78, 0xffcc0200\n"
"ds_read_b64_tr_b16 a[96:97], v249 offset:32768\nv_fma_f32 v140, v140, v151, v155\nv_mfma_f32_16x16x32_bf16 v[208:211], a[68:71], a[144:147], v[208:211]\ns_add_u32 m0, s73, 0x8000\n"
"ds_read_b64_tr_b16 a[98:99], v249 offset:33280\nv_fma_f32 v141, v141, v151, v155\nv_mfma_f32_16x16x32_bf16 v[208:211], a[72:75], a[160:163], v[208:211]\nbuffer_load_dwordx4 v251, s[44:47], s78 offen lds\n"
"v_mfma_f32_16x16x32_bf16 v[208:211], a[76:79], a[176:179], v[208:211]\ns_add_u32 s78, s78, 0x40000\nds_read_b64_tr_b16 a[100:101], v249 offset:36864\nv_mul_f32 v204, v204, v239\n"
"v_mfma_f32_16x16x32_bf16 v[208:211], a[80:83], a[0:3], v[208:211]\ns_add_u32 m0, s73, 0x9000\nds_read_b64_tr_b16 a[102:103], v249 offset:37376\nv_mul_f32 v205, v205, v239\n"
"v_mfma_f32_16x16x32_bf16 v[208:211], a[84:87], a[16:19], v[208:211]\nbuffer_load_dwordx4 v251, s[44:47], s78 offen lds\nv_mfma_f32_16x16x32_bf16 v[208:211], a[88:91], a[32:35], v[208:211]\nds_read_b64_tr_b16 a[104:105], v249 offset:40960\n"
"v_mul_f32 v206, v206, v239\nv_mfma_f32_16x16x32_bf16 v[212:215], a[88:91], a[36:39], 0\ns_add_u32 s78, s78, 0x40000\nds_read_b64_tr_b16 a[106:107], v249 offset:41472\n"
"v_mul_f32 v207, v207, v239\nv_mfma_f32_16x16x32_bf16 v[212:215], a[84:87], a[20:23], v[212:215]\ns_add_u32 m0, s73, 0xa000\nds_read_b64_tr_b16 a[108:109], v249 offset:45056\n"
"v_cvt_pk_bf16_f32 v86, v204, v205\nv_mfma_f32_16x16x32_bf16 v[212:215], a[80:83], a[4:7], v[212:215]\nbuffer_load_dwordx4 v251, s[44:47], s78 offen lds\nv_mfma_f32_16x16x32_bf16 v[212:215], a[76:79], a[180:183], v[212:215]\n"
"ds_read_b64_tr_b16 a[110:111], v249 offset:45568\nv_cvt_pk_bf16_f32 v87, v206, v207\nv_mfma_f32_16x16x32_bf16 v[212:215], a[72:75], a[164:167], v[212:215]\ns_add_u32 s78, s78, 0x40000\n"
"ds_read_b64_tr_b16 a[112:113], v249 offset:49152\nv_permlane16_swap_b32 v84, v86\nv_mfma_f32_16x16x32_bf16 v[212:215], a[68:71], a[148:151], v[212:215]\ns_add_u32 m0, s73, 0xb000\n"
"ds_read_b64_tr_b16 a[114:115], v249 offset:49664\nv_permlane16_swap_b32 v85, v87\nv_mfma_f32_16x16x32_bf16 v[212:215], a[64:67], a[132:135], v[212:215]\nbuffer_load_dwordx4 v251, s[44:47], s78 offen lds\n"
"v_mfma_f32_16x16x32_bf16 v[216:219], a[64:67], a[136:139], 0\ns_add_u32 s78, s78, 0x40000\nds_read_b64_tr_b16 a[116:117], v249 offset:53248\nv_fma_f32 v142, v142, v151, v155\n"
"v_mfma_f32_16x16x32_bf16 v[216:219], a[68:71], a[152:155], v[216:219]\ns_add_u32 m0, s73, 0xc000\nds_read_b64_tr_b16 a[118:119], v249 offset:53760\nv_fma_f32 v143, v143, v151, v155\n"
"v_mfma_f32_16x16x32_bf16 v[216:219], a[72:75], a[168:171], v[216:219]\nbuffer_load_dwordx4 v251, s[44:47], s78 offen lds\nv_mfma_f32_16x16x32_bf16 v[216:219], a[76:79], a[184:187], v[216:219]\nds_read_b64_tr_b16 a[120:121], v249 offset:57344\n"
"v_mul_f32 v140, v140, v188\nv_mfma_f32_16x16x32_bf16 v[216:219], a[80:83], a[8:11], v[216:219]\ns_add_u32 s78, s78, 0x40000\nds_read_b64_tr_b16 a[122:123], v249 offset:57856\n"
"v_mul_f32 v141, v141, v189\nv_mfma_f32_16x16x32_bf16 v[216:219], a[84:87], a[24:27], v[216:219]\ns_add_u32 m0, s73, 0xd000\nv_exp_f32 v134, v134\n"
"v_mfma_f32_16x16x32_bf16 v[216:219], a[88:91], a[40:43], v[216:219]\nbuffer_load_dwordx4 v251, s[44:47], s78 offen lds\nv_mfma_f32_16x16x32_bf16 v[220:223], a[88:91], a[44:47], 0\nv_exp_f32 v135, v135\n"
"v_mfma_f32_16x16x32_bf16 v[220:223], a[84:87], a[28:31], v[220:223]\ns_add_u32 s78, s78, 0x40000\nv_exp_f32 v136, v136\nv_mfma_f32_16x16x32_bf16 v[220:223], a[80:83], a[12:15], v[220:223]\n"
"s_add_u32 m0, s73, 0xe000\nv_rcp_f32 v128, v128\nv_mfma_f32_16x16x32_bf16 v[220:223], a[76:79], a[188:191], v[220:223]\nbuffer_load_dwordx4 v251, s[44:47], s78 offen lds\n"
"v_mfma_f32_16x16x32_bf16 v[220:223], a[72:75], a[172:175], v[220:223]\nv_fma_f32 v134, v134, v157, v157\nv_fma_f32 v135, v135, v157, v157\nv_mfma_f32_16x16x32_bf16 v[220:223], a[68:71], a[156:159], v[220:223]\n"
"v_rcp_f32 v129, v129\nv_mfma_f32_16x16x32_bf16 v[220:223], a[64:67], a[140:143], v[220:223]\nv_exp_f32 v137, v137\ns_waitcnt vmcnt(14) lgkmcnt(0)\n"
"s_barrier\nv_mfma_f32_16x16x32_bf16 v[208:211], a[96:99], a[192:195], v[208:211]\ns_add_u32 s78, s78, 0x40000\nds_read_b64_tr_b16 a[64:65], v248 offset:0\n"
"v_fma_f32 v136, v136, v158, v158\nv_mfma_f32_16x16x32_bf16 v[208:211], a[100:103], a[208:211], v[208:211]\ns_add_u32 m0, s73, 0x18000\nds_read_b64_tr_b16 a[66:67], v248 offset:512\n"
"v_mul_f32 v128, v128, v176\nv_mfma_f32_16x16x32_bf16 v[208:211], a[104:107], a[224:227], v[208:211]\nbuffer_load_dwordx4 v251, s[44:47], s78 offen lds\nv_mfma_f32_16x16x32_bf16 v[208:211], a[108:111], a[240:243], v[208:211]\n"
"s_add_u32 s78, s78, 0x40000\nds_read_b64_tr_b16 a[68:69], v248 offset:4096\nv_mul_f32 v129, v129, v177\nv_mfma_f32_16x16x32_bf16 v[208:211], a[112:115], v[0:3], v[208:211]\n"
"s_add_u32 m0, s73, 0x19000\nds_read_b64_tr_b16 a[70:71], v248 offset:4608\nv_fma_f32 v137, v137, v158, v158\nv_mfma_f32_16x16x32_bf16 v[208:211], a[116:119], v[16:19], v[208:211]\n"
"buffer_load_dwordx4 v251, s[44:47], s78 offen lds\nv_mfma_f32_16x16x32_bf16 v[208:211], a[120:123], v[32:35], v[208:211]\nds_read_b64_tr_b16 a[72:73], v248 offset:8192\nv_mul_f32 v142, v142, v190\n"
"v_mfma_f32_16x16x32_bf16 v[212:215], a[120:123], v[36:39], v[212:215]\ns_add_u32 s78, s78, 0x40000\nds_read_b64_tr_b16 a[74:75], v248 offset:8704\nv_mul_f32 v143, v143, v191\n"
"v_mfma_f32_16x16x32_bf16 v[212:215], a[116:119], v[20:23], v[212:215]\ns_add_u32 m0, s73, 0x1a000\nds_read_b64_tr_b16 a[76:77], v248 offset:12288\nv_mul_f32 v128, v128, v160\n"
"v_mfma_f32_16x16x32_bf16 v[212:215], a[112:115], v[4:7], v[212:215]\nbuffer_load_dwordx4 v251, s[44:47], s78 offen lds\nv_mfma_f32_16x16x32_bf16 v[212:215], a[108:111], a[244:247], v[212:215]\nds_read_b64_tr_b16 a[78:79], v248 offset:12800\n"
"v_mul_f32 v208, v208, v236\nv_mfma_f32_16x16x32_bf16 v[212:215], a[104:107], a[228:231], v[212:215]\ns_add_u32 s78, s78, 0x40000\nds_read_b64_tr_b16 a[80:81], v248 offset:16384\n"
"v_mul_f32 v209, v209, v236\nv_mfma_f32_16x16x32_bf16 v[212:215], a[100:103], a[212:215], v[212:215]\ns_add_u32 m0, s73, 0x1b000\nds_read_b64_tr_b16 a[82:83], v248 offset:16896\n"
"v_mul_f32 v210, v210, v236\nv_mfma_f32_16x16x32_bf16 v[212:215], a[96:99], a[196:199], v[212:215]\nbuffer_load_dwordx4 v251, s[44:47], s78 offen lds\nv_mfma_f32_16x16x32_bf16 v[216:219], a[96:99], a[200:203], v[216:219]\n"
"s_add_u32 s78, s78, 0x40000\nds_read_b64_tr_b16 a[84:85], v248 offset:20480\nv_mul_f32 v211, v211, v236\nv_mfma_f32_16x16x32_bf16 v[216:219], a[100:103], a[216:219], v[216:219]\n"
"s_add_u32 m0, s73, 0x1c000\nds_read_b64_tr_b16 a[86:87], v248 offset:20992\nv_cvt_pk_bf16_f32 v88, v208, v209\nv_mfma_f32_16x16x32_bf16 v[216:219], a[104:107], a[232:235], v[216:219]\n"
"buffer_load_dwordx4 v251, s[44:47], s78 offen lds\nv_mfma_f32_16x16x32_bf16 v[216:219], a[108:111], a[248:251], v[216:219]\nds_read_b64_tr_b16 a[88:89], v248 offset:24576\nv_mul_f32 v212, v212, v237\n"
"v_mfma_f32_16x16x32_bf16 v[216:219], a[112:115], v[8:11], v[216:219]\ns_add_u32 s78, s78, 0x40000\nds_read_b64_tr_b16 a[90:91], v248 offset:25088\nv_mul_f32 v213, v213, v237\n"
"v_mfma_f32_16x16x32_bf16 v[216:219], a[116:119], v[24:27], v[216:219]\ns_add_u32 m0, s73, 0x1d000\nv_mul_f32 v214, v214, v237\nv_mul_f32 v215, v215, v237\n"
"v_mfma_f32_16x16x32_bf16 v[216:219], a[120:123], v[40:43], v[216:219]\nbuffer_load_dwordx4 v251, s[44:47], s78 offen lds\nv_mfma_f32_16x16x32_bf16 v[220:223], a[120:123], v[44:47], v[220:223]\nv_cvt_pk_bf16_f32 v89, v210, v211\n"
"v_cvt_pk_bf16_f32 v90, v212, v213\nv_mfma_f32_16x16x32_bf16 v[220:223], a[116:119], v[28:31], v[220:223]\ns_add_u32 s78, s78, 0x40000\nv_cvt_pk_bf16_f32 v91, v214, v215\n"
"v_permlane16_swap_b32 v88, v90\nv_mfma_f32_16x16x32_bf16 v[220:223], a[112:115], v[12:15], v[220:223]\ns_add_u32 m0, s73, 0x1e000\nv_permlane16_swap_b32 v89, v91\n"
"v_mul_f32 v129, v129, v161\nv_mfma_f32_16x16x32_bf16 v[220:223], a[108:111], a[252:255], v[220:223]\nbuffer_load_dwordx4 v251, s[44:47], s78 offen lds\nv_mfma_f32_16x16x32_bf16 v[220:223], a[104:107], a[236:239], v[220:223]\n"
"v_mul_f32 v216, v216, v238\nv_mul_f32 v217, v217, v238\nv_mfma_f32_16x16x32_bf16 v[220:223], a[100:103], a[220:223], v[220:223]\nv_mul_f32 v218, v218, v238\n"
"v_mul_f32 v219, v219, v238\nv_mfma_f32_16x16x32_bf16 v[220:223], a[96:99], a[204:207], v[220:223]\nv_cvt_pk_bf16_f32 v92, v216, v217\nv_cvt_pk_bf16_f32 v93, v218, v219\n"
"s_waitcnt vmcnt(14) lgkmcnt(0)\ns_barrier\nv_mfma_f32_16x16x32_bf16 v[192:195], a[64:67], a[128:131], 0\ns_add_u32 s79, s79, 0x0\n"
"ds_read_b64_tr_b16 a[96:97], v249 offset:0\nv_cvt_pk_bf16_f32 v112, v128, v129\nv_mfma_f32_16x16x32_bf16 v[192:195], a[68:71], a[144:147], v[192:195]\ns_add_u32 m0, s73, 0x0\n"
"ds_read_b64_tr_b16 a[98:99], v249 offset:512\nv_readfirstlane_b32 s30, v106\nv_mfma_f32_16x16x32_bf16 v[192:195], a[72:75], a[160:163], v[192:195]\nbuffer_load_dwordx4 v231, s[48:51], s79 offen lds\n"
"v_mfma_f32_16x16x32_bf16 v[192:195], a[76:79], a[176:179], v[192:195]\ns_add_u32 s79, s79, 0x80\nds_read_b64_tr_b16 a[100:101], v249 offset:4096\nv_mul_f32 v220, v220, v239\n"
"v_mfma_f32_16x16x32_bf16 v[192:195], a[80:83], a[0:3], v[192:195]\ns_add_u32 m0, s73, 0x2000\nds_read_b64_tr_b16 a[102:103], v249 offset:4608\nv_mul_f32 v221, v221, v239\n"
"v_mfma_f32_16x16x32_bf16 v[192:195], a[84:87], a[16:19], v[192:195]\nbuffer_load_dwordx4 v231, s[48:51], s79 offen lds\nv_mfma_f32_16x16x32_bf16 v[192:195], a[88:91], a[32:35], v[192:195]\ns_add_u32 s79, s79, 0x80\n"
"ds_read_b64_tr_b16 a[104:105], v249 offset:8192\nv_mul_f32 v222, v222, v239\nv_mfma_f32_16x16x32_bf16 v[196:199], a[88:91], a[36:39], 0\ns_add_u32 m0, s73, 0x4000\n"
"ds_read_b64_tr_b16 a[106:107], v249 offset:8704\nv_mul_f32 v223, v223, v239\nv_mfma_f32_16x16x32_bf16 v[196:199], a[84:87], a[20:23], v[196:199]\nbuffer_load_dwordx4 v231, s[48:51], s79 offen lds\n"
"v_mfma_f32_16x16x32_bf16 v[196:199], a[80:83], a[4:7], v[196:199]\ns_add_u32 s79, s79, 0x80\nds_read_b64_tr_b16 a[108:109], v249 offset:12288\nv_cvt_pk_bf16_f32 v94, v220, v221\n"
"v_mfma_f32_16x16x32_bf16 v[196:199], a[76:79], a[180:183], v[196:199]\ns_add_u32 m0, s73, 0x6000\nds_read_b64_tr_b16 a[110:111], v249 offset:12800\nv_cvt_pk_bf16_f32 v95, v222, v223\n"
"v_mfma_f32_16x16x32_bf16 v[196:199], a[72:75], a[164:167], v[196:199]\nbuffer_load_dwordx4 v231, s[48:51], s79 offen lds\nv_mfma_f32_16x16x32_bf16 v[196:199], a[68:71], a[148:151], v[196:199]\nds_read_b64_tr_b16 a[112:113], v249 offset:16384\n"
"v_permlane16_swap_b32 v92, v94\nv_mfma_f32_16x16x32_bf16 v[196:199], a[64:67], a[132:135], v[196:199]\ns_add_u32 s79, s79, 0x3e80\nds_read_b64_tr_b16 a[114:115], v249 offset:16896\n"
"v_permlane16_swap_b32 v93, v95\nv_mfma_f32_16x16x32_bf16 v[200:203], a[64:67], a[136:139], 0\ns_add_u32 m0, s73, 0x1000\nds_read_b64_tr_b16 a[116:117], v249 offset:20480\n"
"ds_read_b64_tr_b16 a[118:119], v249 offset:20992\nv_mfma_f32_16x16x32_bf16 v[200:203], a[68:71], a[152:155], v[200:203]\nbuffer_load_dwordx4 v231, s[48:51], s79 offen lds\nv_mfma_f32_16x16x32_bf16 v[200:203], a[72:75], a[168:171], v[200:203]\n"
"s_add_u32 s79, s79, 0x80\nds_read_b64_tr_b16 a[120:121], v249 offset:24576\nds_read_b64_tr_b16 a[122:123], v249 offset:25088\nv_mfma_f32_16x16x32_bf16 v[200:203], a[76:79], a[184:187], v[200:203]\n"
"s_add_u32 m0, s73, 0x3000\nv_exp_f32 v138, v138\nv_mfma_f32_16x16x32_bf16 v[200:203], a[80:83], a[8:11], v[200:203]\nbuffer_load_dwordx4 v231, s[48:51], s79 offen lds\n"
"v_mfma_f32_16x16x32_bf16 v[200:203], a[84:87], a[24:27], v[200:203]\ns_add_u32 s79, s79, 0x80\nv_rcp_f32 v130, v130\nv_mfma_f32_16x16x32_bf16 v[200:203], a[88:91], a[40:43], v[200:203]\n"
"s_add_u32 m0, s73, 0x5000\nv_rcp_f32 v131, v131\nv_mfma_f32_16x16x32_bf16 v[204:207], a[88:91], a[44:47], 0\nbuffer_load_dwordx4 v231, s[48:51], s79 offen lds\n"
"v_mfma_f32_16x16x32_bf16 v[204:207], a[84:87], a[28:31], v[204:207]\ns_add_u32 s79, s79, 0x80\nv_rcp_f32 v132, v132\nv_mfma_f32_16x16x32_bf16 v[204:207], a[80:83], a[12:15], v[204:207]\n"
"s_add_u32 m0, s73, 0x7000\nv_exp_f32 v139, v139\nv_mfma_f32_16x16x32_bf16 v[204:207], a[76:79], a[188:191], v[204:207]\nbuffer_load_dwordx4 v231, s[48:51], s79 offen lds\n"
"v_mfma_f32_16x16x32_bf16 v[204:207], a[72:75], a[172:175], v[204:207]\nv_rcp_f32 v133, v133\nv_mfma_f32_16x16x32_bf16 v[204:207], a[68:71], a[156:159], v[204:207]\nv_rcp_f32 v134, v134\n"
"v_mfma_f32_16x16x32_bf16 v[204:207], a[64:67], a[140:143], v[204:207]\nv_rcp_f32 v135, v135\ns_waitcnt vmcnt(15) lgkmcnt(0)\ns_barrier\n"
"v_mfma_f32_16x16x32_bf16 v[192:195], a[96:99], a[192:195], v[192:195]\ns_add_u32 m0, s73, 0x10000\nds_read_b64_tr_b16 a[64:65], v248 offset:32768\nv_fma_f32 v138, v138, v158, v158\n"
"v_mfma_f32_16x16x32_bf16 v[192:195], a[100:103], a[208:211], v[192:195]\ns_add_u32 s79, s79, 0x3e80\nbuffer_load_dwordx4 v231, s[48:51], s79 offen lds\nv_mfma_f32_16x16x32_bf16 v[192:195], a[104:107], a[224:227], v[192:195]\n"
"s_add_u32 m0, s73, 0x12000\nds_read_b64_tr_b16 a[66:67], v248 offset:33280\nv_mul_f32 v130, v130, v178\nv_mfma_f32_16x16x32_bf16 v[192:195], a[108:111], a[240:243], v[192:195]\n"
"s_add_u32 s79, s79, 0x80\nbuffer_load_dwordx4 v231, s[48:51], s79 offen lds\nv_mfma_f32_16x16x32_bf16 v[192:195], a[112:115], v[0:3], v[192:195]\ns_add_u32 m0, s73, 0x14000\n"
"ds_read_b64_tr_b16 a[68:69], v248 offset:36864\nv_mul_f32 v131, v131, v179\nv_mfma_f32_16x16x32_bf16 v[192:195], a[116:119], v[16:19], v[192:195]\ns_add_u32 s79, s79, 0x80\n"
"buffer_load_dwordx4 v231, s[48:51], s79 offen lds\nv_mfma_f32_16x16x32_bf16 v[192:195], a[120:123], v[32:35], v[192:195]\ns_add_u32 m0, s73, 0x16000\nds_read_b64_tr_b16 a[70:71], v248 offset:37376\n"
"v_mul_f32 v132, v132, v180\nv_mfma_f32_16x16x32_bf16 v[196:199], a[120:123], v[36:39], v[196:199]\ns_add_u32 s79, s79, 0x80\nbuffer_load_dwordx4 v231, s[48:51], s79 offen lds\n"
"v_mfma_f32_16x16x32_bf16 v[196:199], a[116:119], v[20:23], v[196:199]\ns_add_u32 m0, s73, 0x11000\nds_read_b64_tr_b16 a[72:73], v248 offset:40960\nv_fma_f32 v139, v139, v158, v158\n"
"v_mfma_f32_16x16x32_bf16 v[196:199], a[112:115], v[4:7], v[196:199]\ns_add_u32 s79, s79, 0x3e80\nbuffer_load_dwordx4 v231, s[48:51], s79 offen lds\nv_mfma_f32_16x16x32_bf16 v[196:199], a[108:111], a[244:247], v[196:199]\n"
"s_add_u32 m0, s73, 0x13000\nds_read_b64_tr_b16 a[74:75], v248 offset:41472\nv_mul_f32 v192, v192, v236\nv_mfma_f32_16x16x32_bf16 v[196:199], a[104:107], a[228:231], v[196:199]\n"
"s_add_u32 s79, s79, 0x80\nbuffer_load_dwordx4 v231, s[48:51], s79 offen lds\nv_mfma_f32_16x16x32_bf16 v[196:199], a[100:103], a[212:215], v[196:199]\ns_add_u32 m0, s73, 0x15000\n"
"ds_read_b64_tr_b16 a[76:77], v248 offset:45056\nv_mul_f32 v193, v193, v236\nv_mfma_f32_16x16x32_bf16 v[196:199], a[96:99], a[196:199], v[196:199]\ns_add_u32 s79, s79, 0x80\n"
"buffer_load_dwordx4 v231, s[48:51], s79 offen lds\nv_mfma_f32_16x16x32_bf16 v[200:203], a[96:99], a[200:203], v[200:203]\ns_add_u32 m0, s73, 0x17000\nds_read_b64_tr_b16 a[78:79], v248 offset:45568\n"
"v_mul_f32 v194, v194, v236\nv_mfma_f32_16x16x32_bf16 v[200:203], a[100:103], a[216:219], v[200:203]\ns_add_u32 s79, s79, 0x80\nbuffer_load_dwordx4 v231, s[48:51], s79 offen lds\n"
"v_mfma_f32_16x16x32_bf16 v[200:203], a[104:107], a[232:235], v[200:203]\nds_read_b64_tr_b16 a[80:81], v248 offset:49152\nv_mul_f32 v195, v195, v236\nv_mfma_f32_16x16x32_bf16 v[200:203], a[108:111], a[248:251], v[200:203]\n"
"ds_read_b64_tr_b16 a[82:83], v248 offset:49664\nv_mul_f32 v196, v196, v237\nv_mfma_f32_16x16x32_bf16 v[200:203], a[112:115], v[8:11], v[200:203]\nds_read_b64_tr_b16 a[84:85], v248 offset:53248\n"
"v_mul_f32 v197, v197, v237\nv_mfma_f32_16x16x32_bf16 v[200:203], a[116:119], v[24:27], v[200:203]\nds_read_b64_tr_b16 a[86:87], v248 offset:53760\nv_mul_f32 v198, v198, v237\n"
"v_mfma_f32_16x16x32_bf16 v[200:203], a[120:123], v[40:43], v[200:203]\nds_read_b64_tr_b16 a[88:89], v248 offset:57344\nv_mul_f32 v199, v199, v237\nv_mfma_f32_16x16x32_bf16 v[204:207], a[120:123], v[44:47], v[204:207]\n"
"ds_read_b64_tr_b16 a[90:91], v248 offset:57856\nv_cvt_pk_bf16_f32 v96, v192, v193\nv_mfma_f32_16x16x32_bf16 v[204:207], a[116:119], v[28:31], v[204:207]\nv_cvt_pk_bf16_f32 v97, v194, v195\n"
"v_cvt_pk_bf16_f32 v98, v196, v197\nv_mfma_f32_16x16x32_bf16 v[204:207], a[112:115], v[12:15], v[204:207]\nv_cvt_pk_bf16_f32 v99, v198, v199\nv_permlane16_swap_b32 v96, v98\n"
"v_mfma_f32_16x16x32_bf16 v[204:207], a[108:111], a[252:255], v[204:207]\nv_mul_f32 v200, v200, v238\nv_mul_f32 v201, v201, v238\nv_mfma_f32_16x16x32_bf16 v[204:207], a[104:107], a[236:239], v[204:207]\n"
"v_mul_f32 v202, v202, v238\nv_mul_f32 v203, v203, v238\nv_mfma_f32_16x16x32_bf16 v[204:207], a[100:103], a[220:223], v[204:207]\nv_cvt_pk_bf16_f32 v100, v200, v201\n"
"v_cvt_pk_bf16_f32 v101, v202, v203\nv_mfma_f32_16x16x32_bf16 v[204:207], a[96:99], a[204:207], v[204:207]\nv_permlane16_swap_b32 v97, v99\nv_mul_f32 v133, v133, v181\n"
"s_waitcnt vmcnt(16) lgkmcnt(0)\ns_barrier\nv_mfma_f32_16x16x32_bf16 v[208:211], a[64:67], a[128:131], 0\nds_read_b64_tr_b16 a[96:97], v249 offset:32768\n"
"ds_read_b64_tr_b16 a[98:99], v249 offset:33280\nv_mfma_f32_16x16x32_bf16 v[208:211], a[68:71], a[144:147], v[208:211]\nds_read_b64_tr_b16 a[100:101], v249 offset:36864\nds_read_b64_tr_b16 a[102:103], v249 offset:37376\n"
"v_mfma_f32_16x16x32_bf16 v[208:211], a[72:75], a[160:163], v[208:211]\nds_write_b128 v252, v[48:51] offset:0\nds_read_b64_tr_b16 a[104:105], v249 offset:40960\nv_mfma_f32_16x16x32_bf16 v[208:211], a[76:79], a[176:179], v[208:211]\n"
"ds_read_b64_tr_b16 a[106:107], v249 offset:41472\nds_read_b64_tr_b16 a[108:109], v249 offset:45056\nv_mfma_f32_16x16x32_bf16 v[208:211], a[80:83], a[0:3], v[208:211]\nds_write_b128 v252, v[52:55] offset:2048\n"
"ds_read_b64_tr_b16 a[110:111], v249 offset:45568\nv_mfma_f32_16x16x32_bf16 v[208:211], a[84:87], a[16:19], v[208:211]\nds_read_b64_tr_b16 a[112:113], v249 offset:49152\nds_read_b64_tr_b16 a[114:115], v249 offset:49664\n"
"v_mfma_f32_16x16x32_bf16 v[208:211], a[88:91], a[32:35], v[208:211]\nds_write_b128 v252, v[56:59] offset:8192\nds_read_b64_tr_b16 a[116:117], v249 offset:53248\nv_mfma_f32_16x16x32_bf16 v[212:215], a[88:91], a[36:39], 0\n"
"ds_read_b64_tr_b16 a[118:119], v249 offset:53760\nds_read_b64_tr_b16 a[120:121], v249 offset:57344\nv_mfma_f32_16x16x32_bf16 v[212:215], a[84:87], a[20:23], v[212:215]\nds_write_b128 v252, v[60:63] offset:10240\n"
"ds_read_b64_tr_b16 a[122:123], v249 offset:57856\nv_mfma_f32_16x16x32_bf16 v[212:215], a[80:83], a[4:7], v[212:215]\nv_mul_f32 v204, v204, v239\nv_mul_f32 v205, v205, v239\n"
"v_mfma_f32_16x16x32_bf16 v[212:215], a[76:79], a[180:183], v[212:215]\nds_write_b128 v104, v[64:67] offset:0\nv_mfma_f32_16x16x32_bf16 v[212:215], a[72:75], a[164:167], v[212:215]\nv_mul_f32 v206, v206, v239\n"
"v_mul_f32 v207, v207, v239\nv_mfma_f32_16x16x32_bf16 v[212:215], a[68:71], a[148:151], v[212:215]\nds_write_b128 v104, v[68:71] offset:2048\nv_mfma_f32_16x16x32_bf16 v[212:215], a[64:67], a[132:135], v[212:215]\n"
"v_cvt_pk_bf16_f32 v102, v204, v205\nv_cvt_pk_bf16_f32 v103, v206, v207\nv_mfma_f32_16x16x32_bf16 v[216:219], a[64:67], a[136:139], 0\nds_write_b128 v104, v[72:75] offset:8192\n"
"v_mfma_f32_16x16x32_bf16 v[216:219], a[68:71], a[152:155], v[216:219]\nv_permlane16_swap_b32 v100, v102\nv_permlane16_swap_b32 v101, v103\nv_mfma_f32_16x16x32_bf16 v[216:219], a[72:75], a[168:171], v[216:219]\n"
"ds_write_b128 v104, v[76:79] offset:10240\nv_mfma_f32_16x16x32_bf16 v[216:219], a[76:79], a[184:187], v[216:219]\nv_exp_f32 v140, v140\nv_mfma_f32_16x16x32_bf16 v[216:219], a[80:83], a[8:11], v[216:219]\n"
"ds_write_b128 v104, v[80:83] offset:16384\nv_mfma_f32_16x16x32_bf16 v[216:219], a[84:87], a[24:27], v[216:219]\nv_mul_f32 v134, v134, v182\nv_mul_f32 v135, v135, v183\n"
"v_mfma_f32_16x16x32_bf16 v[216:219], a[88:91], a[40:43], v[216:219]\nds_write_b128 v104, v[84:87] offset:18432\nv_mfma_f32_16x16x32_bf16 v[220:223], a[88:91], a[44:47], 0\nv_rcp_f32 v136, v136\n"
"v_mfma_f32_16x16x32_bf16 v[220:223], a[84:87], a[28:31], v[220:223]\nds_write_b128 v104, v[88:91] offset:24576\nv_mfma_f32_16x16x32_bf16 v[220:223], a[80:83], a[12:15], v[220:223]\nv_exp_f32 v141, v141\n"
"v_mfma_f32_16x16x32_bf16 v[220:223], a[76:79], a[188:191], v[220:223]\nds_write_b128 v104, v[92:95] offset:26624\nv_mfma_f32_16x16x32_bf16 v[220:223], a[72:75], a[172:175], v[220:223]\nv_rcp_f32 v137, v137\n"
"v_mfma_f32_16x16x32_bf16 v[220:223], a[68:71], a[156:159], v[220:223]\nds_write_b128 v252, v[96:99] offset:16384\nv_mfma_f32_16x16x32_bf16 v[220:223], a[64:67], a[140:143], v[220:223]\nv_rcp_f32 v138, v138\n"
"s_waitcnt vmcnt(8) lgkmcnt(0)\ns_barrier\nv_mfma_f32_16x16x32_bf16 v[208:211], a[96:99], a[192:195], v[208:211]\ns_add_u32 s79, s79, 0x3e80\n"
"ds_read_b128 a[128:131], v225 offset:0\nds_read_b128 a[132:135], v226 offset:0\nv_mfma_f32_16x16x32_bf16 v[208:211], a[100:103], a[208:211], v[208:211]\ns_add_u32 m0, s73, 0x18000\n"
"ds_read_b128 a[136:139], v225 offset:8192\nds_read_b128 a[140:143], v226 offset:8192\nv_mfma_f32_16x16x32_bf16 v[208:211], a[104:107], a[224:227], v[208:211]\nbuffer_load_dwordx4 v231, s[48:51], s79 offen lds\n"
"v_mfma_f32_16x16x32_bf16 v[208:211], a[108:111], a[240:243], v[208:211]\ns_add_u32 s79, s79, 0x80\nds_read_b128 a[144:147], v225 offset:16384\nds_read_b128 a[148:151], v226 offset:16384\n"
"v_mfma_f32_16x16x32_bf16 v[208:211], a[112:115], v[0:3], v[208:211]\ns_add_u32 m0, s73, 0x1a000\nds_read_b128 a[152:155], v225 offset:24576\nds_read_b128 a[156:159], v226 offset:24576\n"
"v_mfma_f32_16x16x32_bf16 v[208:211], a[116:119], v[16:19], v[208:211]\nbuffer_load_dwordx4 v231, s[48:51], s79 offen lds\nv_mfma_f32_16x16x32_bf16 v[208:211], a[120:123], v[32:35], v[208:211]\ns_add_u32 s79, s79, 0x80\n"
"ds_read_b128 a[160:163], v225 offset:2048\nds_read_b128 a[164:167], v226 offset:2048\nv_mfma_f32_16x16x32_bf16 v[212:215], a[120:123], v[36:39], v[212:215]\ns_add_u32 m0, s73, 0x1c000\n"
"ds_read_b128 a[168:171], v225 offset:10240\nds_read_b128 a[172:175], v226 offset:10240\nv_mfma_f32_16x16x32_bf16 v[212:215], a[116:119], v[20:23], v[212:215]\nbuffer_load_dwordx4 v231, s[48:51], s79 offen lds\n"
"v_mfma_f32_16x16x32_bf16 v[212:215], a[112:115], v[4:7], v[212:215]\ns_add_u32 s79, s79, 0x80\nds_read_b128 a[176:179], v225 offset:18432\nds_read_b128 a[180:183], v226 offset:18432\n"
"v_mfma_f32_16x16x32_bf16 v[212:215], a[108:111], a[244:247], v[212:215]\ns_add_u32 m0, s73, 0x1e000\nds_read_b128 a[184:187], v225 offset:26624\nds_read_b128 a[188:191], v226 offset:26624\n"
"v_mul_f32 v208, v208, v236\nv_mul_f32 v209, v209, v236\nv_mfma_f32_16x16x32_bf16 v[212:215], a[104:107], a[228:231], v[212:215]\nbuffer_load_dwordx4 v231, s[48:51], s79 offen lds\n"
"v_mfma_f32_16x16x32_bf16 v[212:215], a[100:103], a[212:215], v[212:215]\nds_read_b128 a[0:3], v253 offset:0\nds_read_b128 a[4:7], v253 offset:1024\nv_mul_f32 v210, v210, v236\n"
"v_mul_f32 v211, v211, v236\nv_mfma_f32_16x16x32_bf16 v[212:215], a[96:99], a[196:199], v[212:215]\ns_add_u32 s79, s79, 0x3e80\nds_read_b128 a[8:11], v253 offset:2048\n"
"ds_read_b128 a[12:15], v253 offset:3072\nv_cvt_pk_bf16_f32 v208, v208, v209\nv_cvt_pk_bf16_f32 v209, v210, v211\nv_mfma_f32_16x16x32_bf16 v[216:219], a[96:99], a[200:203], v[216:219]\n"
"s_add_u32 m0, s73, 0x19000\nds_read_b128 a[16:19], v253 offset:4096\nds_read_b128 a[20:23], v253 offset:5120\nv_mfma_f32_16x16x32_bf16 v[216:219], a[100:103], a[216:219], v[216:219]\n"
"buffer_load_dwordx4 v231, s[48:51], s79 offen lds\nv_mfma_f32_16x16x32_bf16 v[216:219], a[104:107], a[232:235], v[216:219]\ns_add_u32 s79, s79, 0x80\nds_read_b128 a[24:27], v253 offset:6144\n"
"ds_read_b128 a[28:31], v253 offset:7168\nv_mfma_f32_16x16x32_bf16 v[216:219], a[108:111], a[248:251], v[216:219]\ns_add_u32 m0, s73, 0x1b000\nds_read_b128 a[32:35], v105 offset:0\n"
"ds_read_b128 a[36:39], v105 offset:1024\nv_mul_f32 v212, v212, v237\nv_mul_f32 v213, v213, v237\nv_mfma_f32_16x16x32_bf16 v[216:219], a[112:115], v[8:11], v[216:219]\n"
"buffer_load_dwordx4 v231, s[48:51], s79 offen lds\nv_mfma_f32_16x16x32_bf16 v[216:219], a[116:119], v[24:27], v[216:219]\ns_add_u32 s79, s79, 0x80\nds_read_b128 a[40:43], v105 offset:2048\n"
"ds_read_b128 a[44:47], v105 offset:3072\nv_mul_f32 v214, v214, v237\nv_mul_f32 v215, v215, v237\nv_mfma_f32_16x16x32_bf16 v[216:219], a[120:123], v[40:43], v[216:219]\n"
"s_add_u32 m0, s73, 0x1d000\nds_read_b128 a[48:51], v105 offset:4096\nds_read_b128 a[52:55], v105 offset:5120\nv_cvt_pk_bf16_f32 v210, v212, v213\n"
"v_cvt_pk_bf16_f32 v211, v214, v215\nv_mfma_f32_16x16x32_bf16 v[220:223], a[120:123], v[44:47], v[220:223]\nbuffer_load_dwordx4 v231, s[48:51], s79 offen lds\nv_mfma_f32_16x16x32_bf16 v[220:223], a[116:119], v[28:31], v[220:223]\n"
"s_add_u32 s79, s79, 0x80\nds_read_b128 a[56:59], v105 offset:6144\nds_read_b128 a[60:63], v105 offset:7168\nv_permlane16_swap_b32 v208, v210\n"
"v_permlane16_swap_b32 v209, v211\nv_mfma_f32_16x16x32_bf16 v[220:223], a[112:115], v[12:15], v[220:223]\ns_add_u32 m0, s73, 0x1f000\nds_read_b128 a[64:67], v105 offset:16384\n"
"ds_read_b128 a[68:71], v105 offset:17408\nv_mfma_f32_16x16x32_bf16 v[220:223], a[108:111], a[252:255], v[220:223]\nbuffer_load_dwordx4 v231, s[48:51], s79 offen lds\nv_mfma_f32_16x16x32_bf16 v[220:223], a[104:107], a[236:239], v[220:223]\n"
"ds_read_b128 a[72:75], v105 offset:18432\nds_read_b128 a[76:79], v105 offset:19456\nv_mul_f32 v216, v216, v238\nv_mul_f32 v217, v217, v238\n"
"v_mfma_f32_16x16x32_bf16 v[220:223], a[100:103], a[220:223], v[220:223]\nds_read_b128 a[80:83], v105 offset:20480\nds_read_b128 a[84:87], v105 offset:21504\nv_mul_f32 v218, v218, v238\n"
"v_mul_f32 v219, v219, v238\nv_mfma_f32_16x16x32_bf16 v[220:223], a[96:99], a[204:207], v[220:223]\nds_read_b128 a[88:91], v105 offset:22528\nds_read_b128 a[92:95], v105 offset:23552\n"
"v_cvt_pk_bf16_f32 v216, v216, v217\nv_cvt_pk_bf16_f32 v217, v218, v219\nds_write_b128 v252, v[100:103] offset:18432\nds_write_b128 v252, v[208:211] offset:24576\n"
"s_nop 1\nv_mul_f32 v220, v220, v239\nv_mul_f32 v221, v221, v239\nv_cvt_pk_bf16_f32 v218, v220, v221\n"
"s_nop 1\nv_permlane16_swap_b32 v216, v218\nv_mul_f32 v222, v222, v239\nv_mul_f32 v223, v223, v239\n"
"v_cvt_pk_bf16_f32 v219, v222, v223\ns_nop 1\nv_permlane16_swap_b32 v217, v219\nds_write_b128 v252, v[216:219] offset:26624\n"
"s_waitcnt vmcnt(8) lgkmcnt(0)\ns_barrier\nds_read_b128 a[96:99], v253 offset:16384\nds_read_b128 a[100:103], v253 offset:17408\n"
"ds_read_b128 a[104:107], v253 offset:18432\nds_read_b128 a[108:111], v253 offset:19456\nds_read_b128 a[112:115], v253 offset:20480\nds_read_b128 a[116:119], v253 offset:21504\n"
"ds_read_b128 a[120:123], v253 offset:22528\nds_read_b128 a[124:127], v253 offset:23552\nv_mfma_f32_16x16x32_bf16 v[76:79], a[128:131], a[0:3], 0\ns_add_u32 s80, s80, 0x0\n"
"s_add_u32 s86, s73, 0x20000\nv_add_u32 v245, s86, v224\ns_and_b32 s76, s96, 0xc2cc0000\nv_mfma_f32_16x16x32_bf16 v[80:83], a[128:131], a[4:7], 0\n"
"s_add_u32 m0, s73, 0x8000\nv_add_u32 v225, 0x10000, v225\nv_add_u32 v226, 0x10000, v226\nv_mfma_f32_16x16x32_bf16 v[84:87], a[128:131], a[8:11], 0\n"
"buffer_load_dwordx4 v232, s[52:55], s80 offen lds\nv_mfma_f32_16x16x32_bf16 v[88:91], a[128:131], a[12:15], 0\ns_add_u32 s80, s80, 0x80\nds_read_b128 a[192:195], v225 offset:0\n"
"v_mul_f32 v130, v130, v162\nv_mfma_f32_16x16x32_bf16 v[76:79], a[132:135], a[16:19], v[76:79]\ns_add_u32 m0, s73, 0xa000\nds_read_b128 a[196:199], v226 offset:0\n"
"v_mul_f32 v131, v131, v163\nv_mfma_f32_16x16x32_bf16 v[80:83], a[132:135], a[20:23], v[80:83]\nbuffer_load_dwordx4 v232, s[52:55], s80 offen lds\nv_mfma_f32_16x16x32_bf16 v[84:87], a[132:135], a[24:27], v[84:87]\n"
"s_add_u32 s80, s80, 0x4080\nds_read_b128 a[200:203], v225 offset:8192\nv_mul_f32 v132, v132, v164\nv_mfma_f32_16x16x32_bf16 v[88:91], a[132:135], a[28:31], v[88:91]\n"
"s_add_u32 m0, s73, 0xc000\nds_read_b128 a[204:207], v226 offset:8192\nv_fma_f32 v140, v140, v159, v159\nv_mfma_f32_16x16x32_bf16 v[76:79], a[136:139], a[32:35], v[76:79]\n"
"buffer_load_dwordx4 v232, s[52:55], s80 offen lds\nv_mfma_f32_16x16x32_bf16 v[80:83], a[136:139], a[36:39], v[80:83]\ns_add_u32 s80, s80, 0x80\nds_read_b128 a[208:211], v225 offset:16384\n"
"v_mul_f32 v133, v133, v165\nv_mfma_f32_16x16x32_bf16 v[84:87], a[136:139], a[40:43], v[84:87]\ns_add_u32 m0, s73, 0xe000\nds_read_b128 a[212:215], v226 offset:16384\n"
"v_exp_f32 v142, v142\nv_mfma_f32_16x16x32_bf16 v[88:91], a[136:139], a[44:47], v[88:91]\nbuffer_load_dwordx4 v232, s[52:55], s80 offen lds\nv_mfma_f32_16x16x32_bf16 v[76:79], a[140:143], a[48:51], v[76:79]\n"
"s_add_u32 s80, s80, 0xfffffe80\nds_read_b128 a[216:219], v225 offset:24576\nv_rcp_f32 v139, v139\nv_mfma_f32_16x16x32_bf16 v[80:83], a[140:143], a[52:55], v[80:83]\n"
"s_add_u32 m0, s73, 0x9000\nds_read_b128 a[220:223], v226 offset:24576\nv_exp_f32 v143, v143\nv_mfma_f32_16x16x32_bf16 v[84:87], a[140:143], a[56:59], v[84:87]\n"
"buffer_load_dwordx4 v232, s[52:55], s80 offen lds\nv_mfma_f32_16x16x32_bf16 v[88:91], a[140:143], a[60:63], v[88:91]\ns_add_u32 s80, s80, 0x80\nds_read_b128 a[224:227], v225 offset:2048\n"
"v_mul_f32 v134, v134, v166\nv_mfma_f32_16x16x32_bf16 v[76:79], a[144:147], a[64:67], v[76:79]\ns_add_u32 m0, s73, 0xb000\nds_read_b128 a[228:231], v226 offset:2048\n"
"v_mul_f32 v136, v136, v184\nv_fma_f32 v141, v141, v159, v159\nv_mfma_f32_16x16x32_bf16 v[80:83], a[144:147], a[68:71], v[80:83]\nbuffer_load_dwordx4 v232, s[52:55], s80 offen lds\n"
"v_mfma_f32_16x16x32_bf16 v[84:87], a[144:147], a[72:75], v[84:87]\ns_add_u32 s80, s80, 0xffffc080\nds_read_b128 a[232:235], v225 offset:10240\nv_mul_f32 v135, v135, v167\n"
"v_cvt_pk_bf16_f32 v113, v130, v131\nv_mfma_f32_16x16x32_bf16 v[88:91], a[144:147], a[76:79], v[88:91]\ns_add_u32 m0, s73, 0xd000\nds_read_b128 a[236:239], v226 offset:10240\n"
"v_mul_f32 v137, v137, v185\nv_mul_f32 v138, v138, v186\nv_mfma_f32_16x16x32_bf16 v[76:79], a[148:151], a[80:83], v[76:79]\nbuffer_load_dwordx4 v232, s[52:55], s80 offen lds\n"
"v_mfma_f32_16x16x32_bf16 v[80:83], a[148:151], a[84:87], v[80:83]\ns_add_u32 s80, s80, 0x80\nds_read_b128 a[240:243], v225 offset:18432\nv_fma_f32 v142, v142, v159, v159\n"
"v_mul_f32 v139, v139, v187\nv_mfma_f32_16x16x32_bf16 v[84:87], a[148:151], a[88:91], v[84:87]\ns_add_u32 m0, s73, 0xf000\nds_read_b128 a[244:247], v226 offset:18432\n"
"v_fma_f32 v143, v143, v159, v159\nv_cvt_pk_bf16_f32 v116, v132, v133\nv_mfma_f32_16x16x32_bf16 v[88:91], a[148:151], a[92:95], v[88:91]\nbuffer_load_dwordx4 v232, s[52:55], s80 offen lds\n"
"s_waitcnt lgkmcnt(4)\nv_mfma_f32_16x16x32_bf16 v[76:79], a[152:155], a[96:99], v[76:79]\ns_add_u32 s80, s80, 0x7e80\nds_read_b128 a[248:251], v225 offset:26624\n"
"v_rcp_f32 v140, v140\nv_mfma_f32_16x16x32_bf16 v[80:83], a[152:155], a[100:103], v[80:83]\ns_add_u32 m0, s73, 0x0\nds_read_b128 a[252:255], v226 offset:26624\n"
"v_cvt_pk_bf16_f32 v117, v134, v135\nv_mul_f32 v136, v136, v168\nv_mfma_f32_16x16x32_bf16 v[84:87], a[152:155], a[104:107], v[84:87]\nbuffer_load_dwordx4 v232, s[52:55], s80 offen lds\n"
"v_mfma_f32_16x16x32_bf16 v[88:91], a[152:155], a[108:111], v[88:91]\ns_mul_i32 s34, s75, 37\nv_rcp_f32 v141, v141\nv_mul_f32 v137, v137, v169\n"
"v_mfma_f32_16x16x32_bf16 v[76:79], a[156:159], a[112:115], v[76:79]\ns_lshr_b32 s34, s34, 8\nbuffer_store_dwordx2 v[112:113], v110, s[60:63], s82 offen offset:0 sc1\nv_mul_f32 v138, v138, v170\n"
"v_mfma_f32_16x16x32_bf16 v[80:83], a[156:159], a[116:119], v[80:83]\ns_mul_i32 s35, s34, 7\nv_rcp_f32 v142, v142\nv_mul_f32 v139, v139, v171\n"
"v_mfma_f32_16x16x32_bf16 v[84:87], a[156:159], a[120:123], v[84:87]\ns_lshl_b32 s34, s34, 3\nv_rcp_f32 v143, v143\nv_mul_f32 v140, v140, v188\n"
"v_mfma_f32_16x16x32_bf16 v[88:91], a[156:159], a[124:127], v[88:91]\ns_add_u32 s34, s34, s74\nbuffer_store_dwordx2 v[116:117], v110, s[60:63], s82 offen offset:1024 sc1\nv_cvt_pk_bf16_f32 v120, v136, v137\n"
"v_mfma_f32_16x16x32_bf16 v[92:95], a[160:163], a[0:3], 0\ns_lshl_b32 s34, s34, 3\nv_max3_f32 v240, v240, v76, v77\nv_cvt_pk_bf16_f32 v121, v138, v139\n"
"v_mul_f32 v141, v141, v189\nv_mfma_f32_16x16x32_bf16 v[92:95], a[164:167], a[16:19], v[92:95]\ns_lshr_b32 s33, s75, 3\nv_max3_f32 v241, v241, v80, v81\n"
"v_max3_f32 v240, v240, v78, v79\nv_mul_f32 v142, v142, v190\nv_mfma_f32_16x16x32_bf16 v[92:95], a[168:171], a[32:35], v[92:95]\ns_mul_i32 s33, s33, 56\n"
"v_max3_f32 v242, v242, v84, v85\nv_max3_f32 v241, v241, v82, v83\nv_mul_f32 v143, v143, v191\nv_mfma_f32_16x16x32_bf16 v[92:95], a[172:175], a[48:51], v[92:95]\n"
"s_and_b32 s87, s75, 7\nv_max3_f32 v243, v243, v88, v89\nv_max3_f32 v242, v242, v86, v87\nv_mul_f32 v140, v140, v172\n"
"v_mfma_f32_16x16x32_bf16 v[92:95], a[176:179], a[64:67], v[92:95]\ns_add_u32 s33, s33, s87\nv_max3_f32 v243, v243, v90, v91\nbuffer_store_dwordx2 v[120:121], v110, s[60:63], s82 offen offset:2048 sc1\n"
"v_mfma_f32_16x16x32_bf16 v[92:95], a[180:183], a[80:83], v[92:95]\ns_sub_u32 s33, s33, 64\nds_write_b128 v245, v[240:243] offset:0\nv_mul_f32 v141, v141, v173\n"
"v_mfma_f32_16x16x32_bf16 v[92:95], a[184:187], a[96:99], v[92:95]\ns_sub_u32 s35, s75, s35\nv_mul_f32 v142, v142, v174\nv_mul_f32 v143, v143, v175\n"
"v_cvt_pk_bf16_f32 v124, v140, v141\nv_mfma_f32_16x16x32_bf16 v[92:95], a[188:191], a[112:115], v[92:95]\ns_add_u32 s35, s35, 8\nv_cvt_pk_bf16_f32 v125, v142, v143\n"
"v_add_u32 v247, s34, v254\nv_mfma_f32_16x16x32_bf16 v[96:99], a[188:191], a[116:119], 0\ns_lshl_b32 s87, %1, 8\nbuffer_store_dwordx2 v[124:125], v110, s[60:63], s82 offen offset:3072 sc1\n"
"v_mfma_f32_16x16x32_bf16 v[96:99], a[184:187], a[100:103], v[96:99]\ns_cmp_lt_u32 s72, 2\ns_cselect_b64 s[92:93], s[48:49], s[52:53]\nv_lshl_add_u32 v251, v254, 3, s33\nv_cmp_gt_u32 vcc, 8, v254\n"
"v_mfma_f32_16x16x32_bf16 v[96:99], a[180:183], a[84:87], v[96:99]\ns_waitcnt lgkmcnt(0)\ns_barrier\nds_read_b128 v[0:3], v244 offset:0\nv_cndmask_b32 v247, v251, v247, vcc\n"
"v_mfma_f32_16x16x32_bf16 v[96:99], a[176:179], a[68:71], v[96:99]\ns_add_u32 s80, s80, 0x80\nds_read_b128 v[4:7], v244 offset:256\nv_mov_b32 v251, 0x7f0000\n"
"v_mfma_f32_16x16x32_bf16 v[96:99], a[172:175], a[52:55], v[96:99]\ns_add_u32 m0, s73, 0x2000\nds_read_b128 v[8:11], v244 offset:512\nv_mov_b32 v250, s35\n"
"v_mfma_f32_16x16x32_bf16 v[96:99], a[168:171], a[36:39], v[96:99]\nbuffer_load_dwordx4 v232, s[52:55], s80 offen lds\nv_mfma_f32_16x16x32_bf16 v[96:99], a[164:167], a[20:23], v[96:99]\ns_add_u32 s80, s80, 0x4080\n"
"ds_read_b128 v[12:15], v244 offset:768\nv_mov_b32 v252, s74\nv_mfma_f32_16x16x32_bf16 v[96:99], a[160:163], a[4:7], v[96:99]\ns_add_u32 m0, s73, 0x4000\n"
"ds_read_b128 v[16:19], v244 offset:1024\ns_cmp_lt_u32 s75, 28\ns_cselect_b32 s34, -1, 0\ns_and_b32 s34, s34, 0xff\ns_or_b32 s34, s34, 0x7f00\ns_mov_b32 s35, 0\nv_mfma_f32_16x16x32_bf16 v[100:103], a[160:163], a[8:11], 0\nbuffer_load_dwordx4 v232, s[52:55], s80 offen lds\n"
"v_mfma_f32_16x16x32_bf16 v[100:103], a[164:167], a[24:27], v[100:103]\ns_add_u32 s80, s80, 0x80\nds_read_b128 v[20:23], v244 offset:1280\nv_cndmask_b32 v247, v251, v247, s[34:35]\n"
"v_mfma_f32_16x16x32_bf16 v[100:103], a[168:171], a[40:43], v[100:103]\ns_add_u32 m0, s73, 0x6000\nds_read_b128 v[24:27], v244 offset:1536\nv_cndmask_b32 v250, v252, v250, vcc\n"
"v_mfma_f32_16x16x32_bf16 v[100:103], a[172:175], a[56:59], v[100:103]\nbuffer_load_dwordx4 v232, s[52:55], s80 offen lds\nv_mfma_f32_16x16x32_bf16 v[100:103], a[176:179], a[72:75], v[100:103]\ns_add_u32 s80, s80, 0xfffffe80\n"
"ds_read_b128 v[28:31], v244 offset:1792\nv_lshlrev_b32 v247, 8, v247\nv_mfma_f32_16x16x32_bf16 v[100:103], a[180:183], a[88:91], v[100:103]\ns_add_u32 m0, s73, 0x1000\n"
"s_waitcnt lgkmcnt(4)\nv_max3_f32 v0, v0, v4, v8\nv_max3_f32 v1, v1, v5, v9\nv_max3_f32 v2, v2, v6, v10\n"
"v_mfma_f32_16x16x32_bf16 v[100:103], a[184:187], a[104:107], v[100:103]\nbuffer_load_dwordx4 v232, s[52:55], s80 offen lds\nv_mfma_f32_16x16x32_bf16 v[100:103], a[188:191], a[120:123], v[100:103]\ns_add_u32 s80, s80, 0x80\n"
"v_max3_f32 v3, v3, v7, v11\nv_lshl_add_u32 v250, v250, 2, v247\nv_mfma_f32_16x16x32_bf16 v[104:107], a[188:191], a[124:127], 0\ns_add_u32 m0, s73, 0x3000\n"
"s_waitcnt lgkmcnt(2)\nv_max3_f32 v0, v0, v12, v16\nv_max3_f32 v1, v1, v13, v17\nv_max3_f32 v2, v2, v14, v18\n"
"v_mfma_f32_16x16x32_bf16 v[104:107], a[184:187], a[108:111], v[104:107]\nbuffer_load_dwordx4 v232, s[52:55], s80 offen lds\nv_mfma_f32_16x16x32_bf16 v[104:107], a[180:183], a[92:95], v[104:107]\ns_add_u32 s80, s80, 0xffffc080\n"
"v_max3_f32 v3, v3, v15, v19\nv_lshl_add_u32 v246, v254, 2, s87\nv_mfma_f32_16x16x32_bf16 v[104:107], a[176:179], a[76:79], v[104:107]\ns_add_u32 m0, s73, 0x5000\n"
"s_waitcnt lgkmcnt(0)\nv_max3_f32 v0, v0, v20, v24\nv_max3_f32 v1, v1, v21, v25\nv_max3_f32 v2, v2, v22, v26\n"
"v_mfma_f32_16x16x32_bf16 v[104:107], a[172:175], a[60:63], v[104:107]\nbuffer_load_dwordx4 v232, s[52:55], s80 offen lds\nv_mfma_f32_16x16x32_bf16 v[104:107], a[168:171], a[44:47], v[104:107]\ns_add_u32 s80, s80, 0x80\n"
"v_max3_f32 v3, v3, v23, v27\nv_max_f32 v0, v0, v28\nv_max_f32 v1, v1, v29\nv_mfma_f32_16x16x32_bf16 v[104:107], a[164:167], a[28:31], v[104:107]\n"
"s_add_u32 m0, s73, 0x7000\nv_max_f32 v2, v2, v30\nv_max_f32 v3, v3, v31\nv_sub_f32 v208, s76, v0\n"
"v_mfma_f32_16x16x32_bf16 v[104:107], a[160:163], a[12:15], v[104:107]\nbuffer_load_dwordx4 v232, s[52:55], s80 offen lds\ns_waitcnt vmcnt(12)\ns_barrier\n"
"v_sub_f32 v209, s76, v0\nv_sub_f32 v210, s76, v0\nv_sub_f32 v211, s76, v0\ns_nop 1\n"
"v_mfma_f32_16x16x32_bf16 v[128:131], a[192:195], a[0:3], v[208:211]\ns_add_u32 s79, s79, 0x3e80\nds_read_b64_tr_b16 a[128:129], v227 offset:32768\nv_sub_f32 v212, s76, v1\n"
"v_sub_f32 v213, s76, v1\nv_mfma_f32_16x16x32_bf16 v[128:131], a[196:199], a[16:19], v[128:131]\ns_add_u32 m0, s73, 0x10000\nds_read_b64_tr_b16 a[130:131], v227 offset:34816\n"
"v_sub_f32 v214, s76, v1\nv_sub_f32 v215, s76, v1\nv_mfma_f32_16x16x32_bf16 v[128:131], a[200:203], a[32:35], v[128:131]\nbuffer_load_dwordx4 v231, s[48:51], s79 offen lds\n"
"v_mfma_f32_16x16x32_bf16 v[128:131], a[204:207], a[48:51], v[128:131]\ns_add_u32 s79, s79, 0x80\nds_read_b64_tr_b16 a[132:133], v227 offset:36864\nv_sub_f32 v216, s76, v2\n"
"v_sub_f32 v217, s76, v2\nv_mfma_f32_16x16x32_bf16 v[128:131], a[208:211], a[64:67], v[128:131]\ns_add_u32 m0, s73, 0x12000\nds_read_b64_tr_b16 a[134:135], v227 offset:38912\n"
"v_sub_f32 v218, s76, v2\nv_sub_f32 v219, s76, v2\nv_mfma_f32_16x16x32_bf16 v[128:131], a[212:215], a[80:83], v[128:131]\nbuffer_load_dwordx4 v231, s[48:51], s79 offen lds\n"
"v_mfma_f32_16x16x32_bf16 v[128:131], a[216:219], a[96:99], v[128:131]\ns_add_u32 s79, s79, 0x80\nds_read_b64_tr_b16 a[136:137], v228 offset:32768\nv_sub_f32 v220, s76, v3\n"
"v_sub_f32 v221, s76, v3\nv_mfma_f32_16x16x32_bf16 v[128:131], a[220:223], a[112:115], v[128:131]\ns_add_u32 m0, s73, 0x14000\nds_read_b64_tr_b16 a[138:139], v228 offset:34816\n"
"v_sub_f32 v222, s76, v3\nv_sub_f32 v223, s76, v3\nv_mfma_f32_16x16x32_bf16 v[132:135], a[220:223], a[116:119], v[212:215]\nbuffer_load_dwordx4 v231, s[48:51], s79 offen lds\n"
"v_mfma_f32_16x16x32_bf16 v[132:135], a[216:219], a[100:103], v[132:135]\ns_add_u32 s79, s79, 0x80\nds_read_b64_tr_b16 a[140:141], v228 offset:36864\nv_add_f32 v76, v76, v208\n"
"v_add_f32 v77, v77, v208\nv_mfma_f32_16x16x32_bf16 v[132:135], a[212:215], a[84:87], v[132:135]\ns_add_u32 m0, s73, 0x16000\nds_read_b64_tr_b16 a[142:143], v228 offset:38912\n"
"v_add_f32 v78, v78, v208\nv_add_f32 v79, v79, v208\nv_mfma_f32_16x16x32_bf16 v[132:135], a[208:211], a[68:71], v[132:135]\nbuffer_load_dwordx4 v231, s[48:51], s79 offen lds\n"
"v_mfma_f32_16x16x32_bf16 v[132:135], a[204:207], a[52:55], v[132:135]\ns_add_u32 s79, s79, 0x3e80\nds_read_b64_tr_b16 a[144:145], v229 offset:32768\nv_add_f32 v80, v80, v212\n"
"v_add_f32 v81, v81, v212\nv_mfma_f32_16x16x32_bf16 v[132:135], a[200:203], a[36:39], v[132:135]\ns_add_u32 m0, s73, 0x11000\nds_read_b64_tr_b16 a[146:147], v229 offset:34816\n"
"v_add_f32 v82, v82, v212\nv_add_f32 v83, v83, v212\nv_mfma_f32_16x16x32_bf16 v[132:135], a[196:199], a[20:23], v[132:135]\nbuffer_load_dwordx4 v231, s[48:51], s79 offen lds\n"
"v_mfma_f32_16x16x32_bf16 v[132:135], a[192:195], a[4:7], v[132:135]\ns_add_u32 s79, s79, 0x80\nds_read_b64_tr_b16 a[148:149], v229 offset:36864\nv_add_f32 v84, v84, v216\n"
"v_add_f32 v85, v85, v216\nv_mfma_f32_16x16x32_bf16 v[136:139], a[192:195], a[8:11], v[216:219]\ns_add_u32 m0, s73, 0x13000\nds_read_b64_tr_b16 a[150:151], v229 offset:38912\n"
"v_add_f32 v92, v92, v208\nv_add_f32 v93, v93, v208\nv_mfma_f32_16x16x32_bf16 v[136:139], a[196:199], a[24:27], v[136:139]\nbuffer_load_dwordx4 v231, s[48:51], s79 offen lds\n"
"v_mfma_f32_16x16x32_bf16 v[136:139], a[200:203], a[40:43], v[136:139]\ns_add_u32 s79, s79, 0x80\nds_read_b64_tr_b16 a[152:153], v230 offset:32768\nv_add_f32 v94, v94, v208\n"
"v_add_f32 v95, v95, v208\nv_mfma_f32_16x16x32_bf16 v[136:139], a[204:207], a[56:59], v[136:139]\ns_add_u32 m0, s73, 0x15000\nds_read_b64_tr_b16 a[154:155], v230 offset:34816\n"
"v_add_f32 v96, v96, v212\nv_add_f32 v97, v97, v212\nv_mfma_f32_16x16x32_bf16 v[136:139], a[208:211], a[72:75], v[136:139]\nbuffer_load_dwordx4 v231, s[48:51], s79 offen lds\n"
"v_mfma_f32_16x16x32_bf16 v[136:139], a[212:215], a[88:91], v[136:139]\ns_add_u32 s79, s79, 0x80\nds_read_b64_tr_b16 a[156:157], v230 offset:36864\nv_add_f32 v98, v98, v212\n"
"v_add_f32 v99, v99, v212\nv_mfma_f32_16x16x32_bf16 v[136:139], a[216:219], a[104:107], v[136:139]\ns_add_u32 m0, s73, 0x17000\nds_read_b64_tr_b16 a[158:159], v230 offset:38912\n"
"v_exp_f32 v76, v76\nv_mfma_f32_16x16x32_bf16 v[136:139], a[220:223], a[120:123], v[136:139]\nbuffer_load_dwordx4 v231, s[48:51], s79 offen lds\nv_mfma_f32_16x16x32_bf16 v[140:143], a[220:223], a[124:127], v[220:223]\n"
"s_cmp_eq_u32 s72, 0\ns_cselect_b32 s35, 0, 0x7f000000\ns_mov_b32 s33, 0x7f000000\nds_read_b64_tr_b16 a[160:161], v227 offset:40960\nv_exp_f32 v77, v77\nv_mfma_f32_16x16x32_bf16 v[140:143], a[216:219], a[108:111], v[140:143]\n"
"s_add_u32 s94, s88, 0x100000\nds_read_b64_tr_b16 a[162:163], v227 offset:43008\nv_exp_f32 v78, v78\nv_mfma_f32_16x16x32_bf16 v[140:143], a[212:215], a[92:95], v[140:143]\n"
"s_and_b32 s91, s85, 63\nds_read_b64_tr_b16 a[164:165], v227 offset:45056\nv_exp_f32 v79, v79\nv_mfma_f32_16x16x32_bf16 v[140:143], a[208:211], a[76:79], v[140:143]\n"
"s_mul_i32 s91, s91, 1024\nds_read_b64_tr_b16 a[166:167], v227 offset:47104\nv_exp_f32 v80, v80\nv_mfma_f32_16x16x32_bf16 v[140:143], a[204:207], a[60:63], v[140:143]\n"
"s_add_u32 s91, s91, s88\nds_read_b64_tr_b16 a[168:169], v228 offset:40960\nv_exp_f32 v81, v81\nv_mfma_f32_16x16x32_bf16 v[140:143], a[200:203], a[44:47], v[140:143]\n"
"s_add_u32 s91, s91, 0x30000\nds_read_b64_tr_b16 a[170:171], v228 offset:43008\nv_exp_f32 v82, v82\nv_mfma_f32_16x16x32_bf16 v[140:143], a[196:199], a[28:31], v[140:143]\n"
"ds_read_b64_tr_b16 a[172:173], v228 offset:45056\nv_exp_f32 v83, v83\nv_mfma_f32_16x16x32_bf16 v[140:143], a[192:195], a[12:15], v[140:143]\nds_read_b64_tr_b16 a[174:175], v228 offset:47104\n"
"v_exp_f32 v84, v84\nv_mfma_f32_16x16x32_bf16 v[144:147], a[224:227], a[0:3], v[208:211]\nds_read_b64_tr_b16 a[176:177], v229 offset:40960\nv_exp_f32 v85, v85\n"
"v_mfma_f32_16x16x32_bf16 v[144:147], a[228:231], a[16:19], v[144:147]\nds_read_b64_tr_b16 a[178:179], v229 offset:43008\nv_add_f32 v86, v86, v216\nv_add_f32 v87, v87, v216\n"
"v_mfma_f32_16x16x32_bf16 v[144:147], a[232:235], a[32:35], v[144:147]\nds_read_b64_tr_b16 a[180:181], v229 offset:45056\nv_exp_f32 v92, v92\nv_mfma_f32_16x16x32_bf16 v[144:147], a[236:239], a[48:51], v[144:147]\n"
"ds_read_b64_tr_b16 a[182:183], v229 offset:47104\nv_exp_f32 v93, v93\nv_mfma_f32_16x16x32_bf16 v[144:147], a[240:243], a[64:67], v[144:147]\nds_read_b64_tr_b16 a[184:185], v230 offset:40960\n"
"v_exp_f32 v94, v94\nv_mfma_f32_16x16x32_bf16 v[144:147], a[244:247], a[80:83], v[144:147]\nds_read_b64_tr_b16 a[186:187], v230 offset:43008\nv_exp_f32 v95, v95\n"
"v_mfma_f32_16x16x32_bf16 v[144:147], a[248:251], a[96:99], v[144:147]\nds_read_b64_tr_b16 a[188:189], v230 offset:45056\nv_exp_f32 v96, v96\nv_mfma_f32_16x16x32_bf16 v[144:147], a[252:255], a[112:115], v[144:147]\n"
"ds_read_b64_tr_b16 a[190:191], v230 offset:47104\nv_exp_f32 v97, v97\nv_mfma_f32_16x16x32_bf16 v[148:151], a[252:255], a[116:119], v[212:215]\nv_exp_f32 v98, v98\n"
"v_add_f32 v100, v100, v216\nv_mfma_f32_16x16x32_bf16 v[148:151], a[248:251], a[100:103], v[148:151]\nv_exp_f32 v99, v99\nv_add_f32 v101, v101, v216\n"
"v_mfma_f32_16x16x32_bf16 v[148:151], a[244:247], a[84:87], v[148:151]\nv_exp_f32 v86, v86\nv_add_f32 v102, v102, v216\nv_mfma_f32_16x16x32_bf16 v[148:151], a[240:243], a[68:71], v[148:151]\n"
"v_exp_f32 v87, v87\nv_add_f32 v103, v103, v216\nv_mfma_f32_16x16x32_bf16 v[148:151], a[236:239], a[52:55], v[148:151]\nv_exp_f32 v100, v100\n"
"v_add_f32 v88, v88, v220\nv_mfma_f32_16x16x32_bf16 v[148:151], a[232:235], a[36:39], v[148:151]\nv_exp_f32 v101, v101\nv_add_f32 v89, v89, v220\n"
"v_mfma_f32_16x16x32_bf16 v[148:151], a[228:231], a[20:23], v[148:151]\nv_exp_f32 v102, v102\nv_add_f32 v90, v90, v220\nv_mfma_f32_16x16x32_bf16 v[148:151], a[224:227], a[4:7], v[148:151]\n"
"v_exp_f32 v103, v103\nv_add_f32 v91, v91, v220\nv_mfma_f32_16x16x32_bf16 v[152:155], a[224:227], a[8:11], v[216:219]\nv_exp_f32 v88, v88\n"
"v_add_f32 v104, v104, v220\nv_mfma_f32_16x16x32_bf16 v[152:155], a[228:231], a[24:27], v[152:155]\nv_exp_f32 v89, v89\nv_add_f32 v105, v105, v220\n"
"v_mfma_f32_16x16x32_bf16 v[152:155], a[232:235], a[40:43], v[152:155]\nv_cvt_pk_bf16_f32 v160, v76, v77\nv_add_f32 v106, v106, v220\nv_add_f32 v107, v107, v220\n"
"v_mfma_f32_16x16x32_bf16 v[152:155], a[236:239], a[56:59], v[152:155]\nv_cvt_pk_bf16_f32 v161, v78, v79\nv_exp_f32 v90, v90\nv_mfma_f32_16x16x32_bf16 v[152:155], a[240:243], a[72:75], v[152:155]\n"
"v_exp_f32 v91, v91\nv_cvt_pk_bf16_f32 v162, v92, v93\nv_mfma_f32_16x16x32_bf16 v[152:155], a[244:247], a[88:91], v[152:155]\nv_cvt_pk_bf16_f32 v164, v80, v81\n"
"v_cvt_pk_bf16_f32 v165, v82, v83\nv_cvt_pk_bf16_f32 v163, v94, v95\nv_mfma_f32_16x16x32_bf16 v[152:155], a[248:251], a[104:107], v[152:155]\nds_write_b128 v233, v[160:163] offset:16384\n"
"v_cvt_pk_bf16_f32 v166, v96, v97\nv_mfma_f32_16x16x32_bf16 v[152:155], a[252:255], a[120:123], v[152:155]\nv_exp_f32 v104, v104\nv_cvt_pk_bf16_f32 v167, v98, v99\n"
"v_mfma_f32_16x16x32_bf16 v[156:159], a[252:255], a[124:127], v[220:223]\nds_write_b128 v233, v[164:167] offset:17408\nv_cvt_pk_bf16_f32 v168, v84, v85\nv_mfma_f32_16x16x32_bf16 v[156:159], a[248:251], a[108:111], v[156:159]\n"
"v_exp_f32 v105, v105\nv_cvt_pk_bf16_f32 v169, v86, v87\nv_mfma_f32_16x16x32_bf16 v[156:159], a[244:247], a[92:95], v[156:159]\nv_cvt_pk_bf16_f32 v170, v100, v101\n"
"v_cvt_pk_bf16_f32 v171, v102, v103\nv_cvt_pk_bf16_f32 v172, v88, v89\nv_mfma_f32_16x16x32_bf16 v[156:159], a[240:243], a[76:79], v[156:159]\nds_write_b128 v233, v[168:171] offset:18432\n"
"v_cvt_pk_bf16_f32 v173, v90, v91\nv_mfma_f32_16x16x32_bf16 v[156:159], a[236:239], a[60:63], v[156:159]\nv_exp_f32 v106, v106\nv_cvt_pk_bf16_f32 v174, v104, v105\n"
"v_mfma_f32_16x16x32_bf16 v[156:159], a[232:235], a[44:47], v[156:159]\nv_exp_f32 v107, v107\nv_mfma_f32_16x16x32_bf16 v[156:159], a[228:231], a[28:31], v[156:159]\nv_cvt_pk_bf16_f32 v175, v106, v107\n"
"v_exp_f32 v128, v128\nv_mfma_f32_16x16x32_bf16 v[156:159], a[224:227], a[12:15], v[156:159]\nds_write_b128 v233, v[172:175] offset:19456\ns_waitcnt lgkmcnt(0)\n"
"s_barrier\nv_mfma_f32_16x16x32_bf16 v[0:3], a[128:131], v[160:163], 0\ns_add_u32 s80, s80, 0x7e80\nds_read_b128 v[192:195], v234 offset:16384\n"
"v_exp_f32 v129, v129\nv_mfma_f32_16x16x32_bf16 v[4:7], a[128:131], v[164:167], 0\ns_add_u32 m0, s73, 0x8000\nds_read_b128 v[196:199], v234 offset:17408\n"
"v_exp_f32 v130, v130\nv_mfma_f32_16x16x32_bf16 v[8:11], a[128:131], v[168:171], 0\nbuffer_load_dwordx4 v232, s[52:55], s80 offen lds\nv_mfma_f32_16x16x32_bf16 v[12:15], a[128:131], v[172:175], 0\n"
"s_add_u32 s80, s80, 0x80\nds_read_b128 v[200:203], v234 offset:18432\nv_exp_f32 v131, v131\nv_mfma_f32_16x16x32_bf16 v[16:19], a[136:139], v[160:163], 0\n"
"s_add_u32 m0, s73, 0xa000\nds_read_b128 v[204:207], v234 offset:19456\nv_exp_f32 v144, v144\nv_mfma_f32_16x16x32_bf16 v[20:23], a[136:139], v[164:167], 0\n"
"buffer_load_dwordx4 v232, s[52:55], s80 offen lds\nv_mfma_f32_16x16x32_bf16 v[24:27], a[136:139], v[168:171], 0\ns_add_u32 s80, s80, 0x4080\nds_read_b128 a[192:195], v225 offset:32768\n"
"v_exp_f32 v145, v145\nv_mfma_f32_16x16x32_bf16 v[28:31], a[136:139], v[172:175], 0\ns_add_u32 m0, s73, 0xc000\nds_read_b128 a[196:199], v226 offset:32768\n"
"v_exp_f32 v146, v146\nv_mfma_f32_16x16x32_bf16 v[32:35], a[144:147], v[160:163], 0\nbuffer_load_dwordx4 v232, s[52:55], s80 offen lds\nv_mfma_f32_16x16x32_bf16 v[36:39], a[144:147], v[164:167], 0\n"
"s_add_u32 s80, s80, 0x80\nds_read_b128 a[200:203], v225 offset:40960\nv_exp_f32 v147, v147\nv_mfma_f32_16x16x32_bf16 v[40:43], a[144:147], v[168:171], 0\n"
"s_add_u32 m0, s73, 0xe000\nds_read_b128 a[204:207], v226 offset:40960\nv_exp_f32 v132, v132\nv_mfma_f32_16x16x32_bf16 v[44:47], a[144:147], v[172:175], 0\n"
"buffer_load_dwordx4 v232, s[52:55], s80 offen lds\ns_waitcnt lgkmcnt(4)\nv_mfma_f32_16x16x32_bf16 v[0:3], a[132:135], v[192:195], v[0:3]\ns_add_u32 s80, s80, 0xfffffe80\n"
"ds_read_b128 a[208:211], v225 offset:49152\nv_exp_f32 v133, v133\nv_mfma_f32_16x16x32_bf16 v[4:7], a[132:135], v[196:199], v[4:7]\ns_add_u32 m0, s73, 0x9000\n"
"ds_read_b128 a[212:215], v226 offset:49152\nv_exp_f32 v134, v134\nv_mfma_f32_16x16x32_bf16 v[8:11], a[132:135], v[200:203], v[8:11]\nbuffer_load_dwordx4 v232, s[52:55], s80 offen lds\n"
"v_mfma_f32_16x16x32_bf16 v[12:15], a[132:135], v[204:207], v[12:15]\ns_add_u32 s80, s80, 0x80\nds_read_b128 a[216:219], v225 offset:57344\nv_exp_f32 v135, v135\n"
"v_mfma_f32_16x16x32_bf16 v[16:19], a[140:143], v[192:195], v[16:19]\ns_add_u32 m0, s73, 0xb000\nds_read_b128 a[220:223], v226 offset:57344\nv_exp_f32 v148, v148\n"
"v_mfma_f32_16x16x32_bf16 v[20:23], a[140:143], v[196:199], v[20:23]\nbuffer_load_dwordx4 v232, s[52:55], s80 offen lds\nv_mfma_f32_16x16x32_bf16 v[24:27], a[140:143], v[200:203], v[24:27]\ns_add_u32 s80, s80, 0xffffc080\n"
"ds_read_b128 a[224:227], v225 offset:34816\nv_exp_f32 v149, v149\nv_mfma_f32_16x16x32_bf16 v[28:31], a[140:143], v[204:207], v[28:31]\ns_add_u32 m0, s73, 0xd000\n"
"ds_read_b128 a[228:231], v226 offset:34816\nv_exp_f32 v150, v150\nv_mfma_f32_16x16x32_bf16 v[32:35], a[148:151], v[192:195], v[32:35]\nbuffer_load_dwordx4 v232, s[52:55], s80 offen lds\n"
"v_mfma_f32_16x16x32_bf16 v[36:39], a[148:151], v[196:199], v[36:39]\ns_add_u32 s80, s80, 0x80\nds_read_b128 a[232:235], v225 offset:43008\nv_exp_f32 v151, v151\n"
"v_mfma_f32_16x16x32_bf16 v[40:43], a[148:151], v[200:203], v[40:43]\ns_add_u32 m0, s73, 0xf000\nds_read_b128 a[236:239], v226 offset:43008\nv_exp_f32 v136, v136\n"
"v_mfma_f32_16x16x32_bf16 v[44:47], a[148:151], v[204:207], v[44:47]\nbuffer_load_dwordx4 v232, s[52:55], s80 offen lds\nv_mfma_f32_16x16x32_bf16 v[48:51], a[152:155], v[160:163], 0\nds_read_b128 a[240:243], v225 offset:51200\n"
"v_exp_f32 v137, v137\nv_mfma_f32_16x16x32_bf16 v[48:51], a[156:159], v[192:195], v[48:51]\nds_read_b128 a[244:247], v226 offset:51200\nv_exp_f32 v138, v138\n"
"v_mfma_f32_16x16x32_bf16 v[52:55], a[152:155], v[164:167], 0\nds_read_b128 a[248:251], v225 offset:59392\nv_exp_f32 v139, v139\nv_mfma_f32_16x16x32_bf16 v[52:55], a[156:159], v[196:199], v[52:55]\n"
"ds_read_b128 a[252:255], v226 offset:59392\nv_exp_f32 v152, v152\nv_mfma_f32_16x16x32_bf16 v[56:59], a[152:155], v[168:171], 0\nv_exp_f32 v153, v153\n"
"v_mov_b32 v236, 0\nv_mfma_f32_16x16x32_bf16 v[56:59], a[156:159], v[200:203], v[56:59]\nv_exp_f32 v154, v154\nv_mov_b32 v237, 0\n"
"v_mfma_f32_16x16x32_bf16 v[60:63], a[152:155], v[172:175], 0\nv_exp_f32 v155, v155\nv_add_f32 v236, v236, v76\nv_mfma_f32_16x16x32_bf16 v[60:63], a[156:159], v[204:207], v[60:63]\n"
"v_exp_f32 v140, v140\nv_add_f32 v237, v237, v80\nv_mfma_f32_16x16x32_bf16 v[64:67], a[160:163], v[160:163], 0\nv_exp_f32 v141, v141\n"
"v_add_f32 v236, v236, v77\nv_mfma_f32_16x16x32_bf16 v[64:67], a[164:167], v[192:195], v[64:67]\nv_exp_f32 v142, v142\nv_add_f32 v237, v237, v81\n"
"v_mfma_f32_16x16x32_bf16 v[68:71], a[160:163], v[164:167], 0\nv_exp_f32 v143, v143\nv_add_f32 v236, v236, v78\nv_mfma_f32_16x16x32_bf16 v[68:71], a[164:167], v[196:199], v[68:71]\n"
"v_exp_f32 v156, v156\nv_mov_b32 v238, 0\nv_mfma_f32_16x16x32_bf16 v[72:75], a[160:163], v[168:171], 0\nv_exp_f32 v157, v157\n"
"v_cvt_pk_bf16_f32 v176, v128, v129\nv_mfma_f32_16x16x32_bf16 v[72:75], a[164:167], v[200:203], v[72:75]\nv_cvt_pk_bf16_f32 v177, v130, v131\nv_cvt_pk_bf16_f32 v178, v144, v145\n"
"v_cvt_pk_bf16_f32 v179, v146, v147\nv_add_f32 v236, v236, v79\nv_mfma_f32_16x16x32_bf16 v[76:79], a[160:163], v[172:175], 0\nds_write_b128 v233, v[176:179] offset:0\n"
"v_add_f32 v237, v237, v82\nv_mfma_f32_16x16x32_bf16 v[76:79], a[164:167], v[204:207], v[76:79]\nv_exp_f32 v158, v158\nv_cvt_pk_bf16_f32 v180, v132, v133\n"
"v_add_f32 v237, v237, v83\nv_mfma_f32_16x16x32_bf16 v[80:83], a[168:171], v[160:163], 0\nv_exp_f32 v159, v159\nv_cvt_pk_bf16_f32 v181, v134, v135\n"
"v_mfma_f32_16x16x32_bf16 v[80:83], a[172:175], v[192:195], v[80:83]\nv_cvt_pk_bf16_f32 v182, v148, v149\nv_cvt_pk_bf16_f32 v183, v150, v151\nv_add_f32 v238, v238, v84\n"
"v_add_f32 v238, v238, v85\nv_add_f32 v238, v238, v86\nv_add_f32 v238, v238, v87\nv_mfma_f32_16x16x32_bf16 v[84:87], a[168:171], v[164:167], 0\n"
"ds_write_b128 v233, v[180:183] offset:1024\nv_cvt_pk_bf16_f32 v184, v136, v137\nv_mfma_f32_16x16x32_bf16 v[84:87], a[172:175], v[196:199], v[84:87]\nv_cvt_pk_bf16_f32 v185, v138, v139\n"
"v_mov_b32 v239, 0\nv_cvt_pk_bf16_f32 v186, v152, v153\nv_add_f32 v239, v239, v88\nv_add_f32 v239, v239, v89\n"
"v_add_f32 v239, v239, v90\nv_add_f32 v239, v239, v91\nv_mfma_f32_16x16x32_bf16 v[88:91], a[168:171], v[168:171], 0\nv_cvt_pk_bf16_f32 v187, v154, v155\n"
"v_cvt_pk_bf16_f32 v188, v140, v141\nv_cvt_pk_bf16_f32 v189, v142, v143\nv_mfma_f32_16x16x32_bf16 v[88:91], a[172:175], v[200:203], v[88:91]\nds_write_b128 v233, v[184:187] offset:2048\n"
"v_cvt_pk_bf16_f32 v190, v156, v157\nv_add_f32 v236, v236, v92\nv_add_f32 v236, v236, v93\nv_add_f32 v236, v236, v94\n"
"v_add_f32 v236, v236, v95\nv_mfma_f32_16x16x32_bf16 v[92:95], a[168:171], v[172:175], 0\nv_cvt_pk_bf16_f32 v191, v158, v159\nv_add_f32 v237, v237, v96\n"
"v_add_f32 v238, v238, v100\nv_mfma_f32_16x16x32_bf16 v[92:95], a[172:175], v[204:207], v[92:95]\nds_write_b128 v233, v[188:191] offset:3072\nv_add_f32 v237, v237, v97\n"
"v_add_f32 v237, v237, v98\nv_add_f32 v237, v237, v99\nv_mfma_f32_16x16x32_bf16 v[96:99], a[176:179], v[160:163], 0\nv_add_f32 v238, v238, v101\n"
"v_add_f32 v239, v239, v104\nv_add_f32 v236, v236, v128\nv_mfma_f32_16x16x32_bf16 v[96:99], a[180:183], v[192:195], v[96:99]\nv_add_f32 v238, v238, v102\n"
"v_add_f32 v239, v239, v105\nv_add_f32 v236, v236, v129\nv_add_f32 v238, v238, v103\nv_mfma_f32_16x16x32_bf16 v[100:103], a[176:179], v[164:167], 0\n"
"v_add_f32 v239, v239, v106\nv_add_f32 v237, v237, v132\nv_add_f32 v238, v238, v136\nv_mfma_f32_16x16x32_bf16 v[100:103], a[180:183], v[196:199], v[100:103]\n"
"v_add_f32 v239, v239, v107\nv_add_f32 v237, v237, v133\nv_add_f32 v236, v236, v130\nv_mfma_f32_16x16x32_bf16 v[104:107], a[176:179], v[168:171], 0\n"
"v_add_f32 v238, v238, v137\nv_add_f32 v237, v237, v134\nv_mfma_f32_16x16x32_bf16 v[104:107], a[180:183], v[200:203], v[104:107]\nv_add_f32 v238, v238, v138\n"
"v_add_f32 v236, v236, v131\nv_add_f32 v239, v239, v140\nv_mfma_f32_16x16x32_bf16 v[108:111], a[176:179], v[172:175], 0\nv_add_f32 v237, v237, v135\n"
"v_add_f32 v238, v238, v139\nv_add_f32 v239, v239, v141\nv_mfma_f32_16x16x32_bf16 v[108:111], a[180:183], v[204:207], v[108:111]\nv_add_f32 v239, v239, v142\n"
"v_add_f32 v236, v236, v144\nv_add_f32 v237, v237, v148\nv_mfma_f32_16x16x32_bf16 v[112:115], a[184:187], v[160:163], 0\nv_lshrrev_b32 v249, 3, v254\n"
"v_add_f32 v239, v239, v143\nv_add_f32 v238, v238, v152\nv_mfma_f32_16x16x32_bf16 v[112:115], a[188:191], v[192:195], v[112:115]\nv_add_f32 v236, v236, v145\n"
"v_add_f32 v237, v237, v149\nv_add_f32 v238, v238, v153\nv_mfma_f32_16x16x32_bf16 v[116:119], a[184:187], v[164:167], 0\nv_lshlrev_b32 v249, 28, v249\n"
"v_add_f32 v236, v236, v146\nv_add_f32 v239, v239, v156\nv_mfma_f32_16x16x32_bf16 v[116:119], a[188:191], v[196:199], v[116:119]\nv_add_f32 v237, v237, v150\n"
"v_add_f32 v238, v238, v154\nv_add_f32 v239, v239, v157\nv_mfma_f32_16x16x32_bf16 v[120:123], a[184:187], v[168:171], 0\nv_lshl_add_u32 v249, v254, 7, v249\n"
"v_add_f32 v236, v236, v147\nv_add_f32 v239, v239, v158\nv_mfma_f32_16x16x32_bf16 v[120:123], a[188:191], v[200:203], v[120:123]\nv_add_f32 v237, v237, v151\n"
"v_add_f32 v238, v238, v155\nv_add_f32 v239, v239, v159\nv_mfma_f32_16x16x32_bf16 v[124:127], a[184:187], v[172:175], 0\nv_mfma_f32_16x16x32_bf16 v[124:127], a[188:191], v[204:207], v[124:127]\n"
"s_waitcnt vmcnt(16) lgkmcnt(0)\ns_barrier\ns_branch L_join%=\nL_end%=:\n"
  : "+v"(tid)
  : "s"(blockIdx.x), "s"(__builtin_amdgcn_kernarg_segment_ptr()), "s"((unsigned)(size_t)lds)
  : "s4", "s5", "s6", "s7", "s8", "s9", "s10", "s11", "s12", "s13", "s14", "s15", "s16", "s17", "s18", "s19", "s20", "s21", "s22", "s23", "s24", "s25", "s26", "s27", "s28", "s29", "s30", "s31", "v1", "v2", "v3", "v4", "v5", "v6", "v7", "v8", "v9", "v10", "v11", "v12", "v13", "v14", "v15", "v16", "v17", "v18", "v19", "v20", "v21", "v22", "v23", "v24", "v25", "v26", "v27", "v28", "v29", "v30", "v31", "v32", "v33", "v34", "v35", "v36", "v37", "v38", "v39", "v40", "v41", "v42", "v43", "v44", "v45", "v46", "v47", "v48", "v49", "v50", "v51", "v52", "v53", "v54", "v55", "v56", "v57", "v58", "v59", "v60", "v61", "v62", "v63", "v64", "v65", "v66", "v67", "v68", "v69", "v70", "v71", "v72", "v73", "v74", "v75", "v76", "v77", "v78", "v79", "v80", "v81", "v82", "v83", "v84", "v85", "v86", "v87", "v88", "v89", "v90", "v91", "v92", "v93", "v94", "v95", "v96", "v97", "v98", "v99", "v100", "v101", "v102", "v103", "v104", "v105", "v106", "v107", "v108", "v109", "v110", "v111", "v112", "v113", "v114", "v115", "v116", "v117", "v118", "v119", "v120", "v121", "v122", "v123", "v124", "v125", "v126", "v127", "v128", "v129", "v130", "v131", "v132", "v133", "v134", "v135", "v136", "v137", "v138", "v139", "v140", "v141", "v142", "v143", "v144", "v145", "v146", "v147", "v148", "v149", "v150", "v151", "v152", "v153", "v154", "v155", "v156", "v157", "v158", "v159", "v160", "v161", "v162", "v163", "v164", "v165", "v166", "v167", "v168", "v169", "v170", "v171", "v172", "v173", "v174", "v175", "v176", "v177", "v178", "v179", "v180", "v181", "v182", "v183", "v184", "v185", "v186", "v187", "v188", "v189", "v190", "v191", "v192", "v193", "v194", "v195", "v196", "v197", "v198", "v199", "v200", "v201", "v202", "v203", "v204", "v205", "v206", "v207", "v208", "v209", "v210", "v211", "v212", "v213", "v214", "v215", "v216", "v217", "v218", "v219", "v220", "v221", "v222", "v223", "v224", "v225", "v226", "v227", "v228", "v229", "v230", "v231", "v232", "v233", "v234", "v235", "v236", "v237", "v238", "v239", "v240", "v241", "v242", "v243", "v244", "v245", "v246", "v247", "v248", "v249", "v250", "v251", "v252", "v253", "v254", "v255", "a0", "a1", "a2", "a3", "a4", "a5", "a6", "a7", "a8", "a9", "a10", "a11", "a12", "a13", "a14", "a15", "a16", "a17", "a18", "a19", "a20", "a21", "a22", "a23", "a24", "a25", "a26", "a27", "a28", "a29", "a30", "a31", "a32", "a33", "a34", "a35", "a36", "a37", "a38", "a39", "a40", "a41", "a42", "a43", "a44", "a45", "a46", "a47", "a48", "a49", "a50", "a51", "a52", "a53", "a54", "a55", "a56", "a57", "a58", "a59", "a60", "a61", "a62", "a63", "a64", "a65", "a66", "a67", "a68", "a69", "a70", "a71", "a72", "a73", "a74", "a75", "a76", "a77", "a78", "a79", "a80", "a81", "a82", "a83", "a84", "a85", "a86", "a87", "a88", "a89", "a90", "a91", "a92", "a93", "a94", "a95", "a96", "a97", "a98", "a99", "a100", "a101", "a102", "a103", "a104", "a105", "a106", "a107", "a108", "a109", "a110", "a111", "a112", "a113", "a114", "a115", "a116", "a117", "a118", "a119", "a120", "a121", "a122", "a123", "a124", "a125", "a126", "a127", "a128", "a129", "a130", "a131", "a132", "a133", "a134", "a135", "a136", "a137", "a138", "a139", "a140", "a141", "a142", "a143", "a144", "a145", "a146", "a147", "a148", "a149", "a150", "a151", "a152", "a153", "a154", "a155", "a156", "a157", "a158", "a159", "a160", "a161", "a162", "a163", "a164", "a165", "a166", "a167", "a168", "a169", "a170", "a171", "a172", "a173", "a174", "a175", "a176", "a177", "a178", "a179", "a180", "a181", "a182", "a183", "a184", "a185", "a186", "a187", "a188", "a189", "a190", "a191", "a192", "a193", "a194", "a195", "a196", "a197", "a198", "a199", "a200", "a201", "a202", "a203", "a204", "a205", "a206", "a207", "a208", "a209", "a210", "a211", "a212", "a213", "a214", "a215", "a216", "a217", "a218", "a219", "a220", "a221", "a222", "a223", "a224", "a225", "a226", "a227", "a228", "a229", "a230", "a231", "a232", "a233", "a234", "a235", "a236", "a237", "a238", "a239", "a240", "a241", "a242", "a243", "a244", "a245", "a246", "a247", "a248", "a249", "a250", "a251", "a252", "a253", "a254", "a255", "s33", "s34", "s35", "s36", "s37", "s38", "s39", "s40", "s41", "s42", "s43", "s44", "s45", "s46", "s47", "s48", "s49", "s50", "s51", "s52", "s53", "s54", "s55", "s56", "s57", "s58", "s59", "s60", "s61", "s62", "s63", "s64", "s65", "s66", "s67", "s68", "s69", "s70", "s71", "s72", "s73", "s74", "s75", "s76", "s77", "s78", "s79", "s80", "s81", "s82", "s83", "s84", "s85", "s86", "s87", "s88", "s89", "s90", "s91", "s92", "s93", "s94", "s95", "s96", "s97", "vcc", "scc", "memory");
}

extern "C" void kernel_launch(void* const* d_in, const int* in_sizes, int n_in, void* d_out, int out_size, void* d_ws, size_t ws_size,
                              hipStream_t stream) {
  if (n_in != 9 || in_sizes[0] != 2048 * 448 || in_sizes[1] != 8 * 2048 * 256 || out_size != 2048 * 448 || ws_size < (40u << 20)) return;
  char* ws = (char*)d_ws;
  void* hbuf = ws; void* sbuf = ws + (8u << 20); void* dbg = ws + (32u << 20);
  fused_main<<<256, 256, 0, stream>>>(d_in[0], d_in[1], d_in[2], d_in[3], d_in[4], d_in[6], d_in[7], hbuf, sbuf, dbg, d_in[8], ws + (24u << 20), d_out);
}
```
